```python
import torch

def _softmax(x, axis=-1):
    return torch.softmax(x, dim=axis)
def _gelu(x):
    return torch.nn.functional.gelu(x, approximate="tanh")
def _silu(x):
    return torch.nn.functional.silu(x)
def _sigmoid(x):
    return torch.sigmoid(x)
def _layernorm(x, axis=-1, eps=1e-5):
    m = x.mean(dim=axis, keepdim=True)
    v = x.var(dim=axis, keepdim=True, unbiased=False)
    return (x - m) / torch.sqrt(v + eps)
def _scatter_add(tgt, idx, src, axis=0):
    return tgt.clone().index_add_(axis, idx, src)
def _segment_sum(src, seg, n):
    out = torch.zeros((n,) + src.shape[1:], dtype=src.dtype)
    return out.index_add_(0, seg, src)
def _pad(x, pw):
    flat = tuple(v for pair in reversed(pw) for v in pair)
    return torch.nn.functional.pad(x, flat)

kernel_name = 'interleave_layers__linear_kernel_hedgehog__mixer_s_mlp__prompt'

def setup_inputs():

    _w = (lambda a: (lambda t: t.to(torch.bfloat16) if t.is_floating_point() else t)(torch.as_tensor(a)))
    def _A_setup():
        B, S, D, H = 1, 2048, 1024, 32
        Dh = D // H
        return {
            "x": _w((lambda *s: torch.randn(*s, dtype=torch.bfloat16) * 0.05)(B, S, D)),
            "wqkv": _w((lambda *s: torch.randn(*s, dtype=torch.bfloat16) * 0.2)(D, 3 * D)),
            "w_phi": _w((lambda *s: torch.randn(*s, dtype=torch.bfloat16) * 0.2)(H, Dh, Dh) * (Dh ** -0.5)),
            "wo": _w((lambda *s: torch.randn(*s, dtype=torch.bfloat16) * 0.05)(D, D)),
        }
    _A_in = _A_setup()

    def _B_setup():
        B, S, D, F = 1, 2048, 1024, 3584
        return {
            "x":  _w((lambda *s: torch.randn(*s, dtype=torch.bfloat16) * 0.05)(B, S, D)),
            "w1": _w((lambda *s: torch.randn(*s, dtype=torch.bfloat16) * 3.2)(D, D) * (D ** -0.5)),
            "w2": _w((lambda *s: torch.randn(*s, dtype=torch.bfloat16) * 3.2)(D, D) * (D ** -0.5)),
            "w3": _w((lambda *s: torch.randn(*s, dtype=torch.bfloat16) * 3.2)(D, F) * (D ** -0.5)),
            "w4": _w((lambda *s: torch.randn(*s, dtype=torch.bfloat16) * 1.6)(F, D) * (F ** -0.5)),
        }
    _B_in = _B_setup()

    return {
        "A_x": _A_in["x"],
        "A_wqkv": _A_in["wqkv"],
        "A_w_phi": _A_in["w_phi"],
        "A_wo": _A_in["wo"],
        "B_x": _B_in["x"],
        "B_w1": _B_in["w1"],
        "B_w2": _B_in["w2"],
        "B_w3": _B_in["w3"],
        "B_w4": _B_in["w4"],
    }

def reference(A_x, A_wqkv, A_w_phi, A_wo, B_x, B_w1, B_w2, B_w3, B_w4):
    def _A_ref(x, wqkv, w_phi, wo):
        B, S, D = x.shape
        H = 32; Dh = D // H
        qkv = (lambda x, *d: x.permute(*d))((x @ wqkv).reshape(B, S, 3, H, Dh), 2, 0, 3, 1, 4)
        q, k, v = qkv[0], qkv[1], qkv[2]
        pq = torch.einsum("bhsd,hdf->bhsf", q, w_phi)
        pk = torch.einsum("bhsd,hdf->bhsf", k, w_phi)
        phi_q = torch.concatenate([_softmax(pq, axis=-1), _softmax(-pq, axis=-1)], axis=-1)
        phi_k = torch.concatenate([_softmax(pk, axis=-1), _softmax(-pk, axis=-1)], axis=-1)
        kv = torch.einsum("bhsf,bhsd->bhfd", phi_k, v)
        z  = torch.einsum("bhsf->bhf", phi_k)
        num = torch.einsum("bhqf,bhfd->bhqd", phi_q, kv)
        den = torch.einsum("bhqf,bhf->bhq", phi_q, z)[..., None] + 1e-6
        o = num / den
        return (lambda x, *d: x.permute(*d))(o, 0, 2, 1, 3).reshape(B, S, D) @ wo

    def _B_ref(x, w1, w2, w3, w4):
        B, S, D = x.shape
        h = _gelu(_layernorm(x, axis=-1) @ w1)
        c = D // 4
        g0 = _pad(h[:, :, 0*c:1*c], ((0, 0), (1, 0), (0, 0)))[:, :S, :]
        g1 = _pad(h[:, :, 1*c:2*c], ((0, 0), (0, 1), (0, 0)))[:, 1:, :]
        g2 = _pad(h[:, :, 2*c:3*c], ((0, 0), (2, 0), (0, 0)))[:, :S, :]
        g3 = _pad(h[:, :, 3*c:   ], ((0, 0), (0, 2), (0, 0)))[:, 2:, :]
        hs = torch.concatenate([g0, g1, g2, g3], axis=-1) @ w2
        x1 = x + hs
        h2 = _gelu(_layernorm(x1, axis=-1) @ w3) @ w4
        return x1 + h2

    x = A_x
    x = x + _A_ref(_layernorm(x, axis=-1), A_wqkv, A_w_phi, A_wo)
    x = x + _B_ref(_layernorm(x, axis=-1), B_w1, B_w2, B_w3, B_w4)
    x = x + _A_ref(_layernorm(x, axis=-1), A_wqkv, A_w_phi, A_wo)
    x = x + _B_ref(_layernorm(x, axis=-1), B_w1, B_w2, B_w3, B_w4)
    return x
```

```cpp
#include <hip/hip_runtime.h>
#include <hip/hip_bf16.h>
#include <stdint.h>

typedef __attribute__((ext_vector_type(8))) short bf16x8;
typedef __attribute__((ext_vector_type(4))) short s16x4;
typedef __attribute__((ext_vector_type(4))) float f32x4;
typedef __attribute__((ext_vector_type(2))) float f32x2;
typedef __attribute__((ext_vector_type(4))) unsigned u32x4;
typedef __attribute__((ext_vector_type(2))) unsigned u32x2;
typedef __attribute__((ext_vector_type(2))) __bf16 bf16x2_t;
typedef unsigned short u16;
#define LDS_AS __attribute__((address_space(3)))
#define DI __device__ __forceinline__

static constexpr int SEQ = 2048, DM = 1024, NH = 32, FF = 3584;

DI float bf2f(unsigned v) { return __uint_as_float(v << 16); }
DI float bflo(unsigned v) { return __uint_as_float(v << 16); }
DI float bfhi(unsigned v) { return __uint_as_float(v & 0xffff0000u); }
DI unsigned pk2(float a, float b) {
  f32x2 v = {a, b};
  return __builtin_bit_cast(unsigned, __builtin_convertvector(v, bf16x2_t));
}
DI float rbf(float a) { return bflo(pk2(a, a)); }

template <int I> struct IC { static constexpr int value = I; };
template <int B, int E, class F> DI void static_for(F&& f) {
  if constexpr (B < E) { f(IC<B>{}); static_for<B + 1, E>(f); }
}
template <int N> DI void wait_vm() { asm volatile("s_waitcnt vmcnt(%0)" ::"n"(N) : "memory"); }
DI void wait_lgkm0() { asm volatile("s_waitcnt lgkmcnt(0)" ::: "memory"); }
DI void bar() { __builtin_amdgcn_s_barrier(); }
DI void glds16(const void* src, char* lds_dst) {
  __builtin_amdgcn_global_load_lds((const unsigned*)src, (unsigned*)lds_dst, 16, 0, 0);
}
DI s16x4 trread(const char* p) {
  return __builtin_bit_cast(s16x4, __builtin_amdgcn_ds_read_tr16_b64_v4i16((LDS_AS s16x4*)p));
}
template <int OFF> DI s16x4 lds_tr(unsigned addr) {
  s16x4 r; asm volatile("ds_read_b64_tr_b16 %0, %1 offset:%2" : "=v"(r) : "v"(addr), "n"(OFF) : "memory"); return r;
}
template <int OFF> DI bf16x8 lds_b128(unsigned addr) {
  bf16x8 r; asm volatile("ds_read_b128 %0, %1 offset:%2" : "=v"(r) : "v"(addr), "n"(OFF) : "memory"); return r;
}
DI bf16x8 cat8(s16x4 lo, s16x4 hi) { return (bf16x8){lo[0], lo[1], lo[2], lo[3], hi[0], hi[1], hi[2], hi[3]}; }
DI f32x4 mfma16(bf16x8 a, bf16x8 b, f32x4 c) { return __builtin_amdgcn_mfma_f32_16x16x32_bf16(a, b, c, 0, 0, 0); }

DI float gelu_tanh(float x) {
  const float u = 0.7978845608028654f * (x + 0.044715f * x * x * x);
  const float e = __builtin_amdgcn_exp2f(u * 2.885390081777927f);
  const float t = 1.0f - 2.0f * __builtin_amdgcn_rcpf(e + 1.0f);
  return 0.5f * x * (1.0f + t);
}

struct GemmArgs {
  const u16* A;
  const u16* B;
  u16* out;
  const u16* res1;
  const u16* res2;
  const float* stats_in;
  float* stats_out;
  const float* colsum;
  const u16* wphi;
  float* kvpart;
  int ln_mode;
  int unused;
};

enum { EPI_QKV = 0, EPI_RES = 1, EPI_GELU_SHIFT = 2, EPI_W2 = 3, EPI_GELU = 4, EPI_W4 = 5 };

struct RowP { float alpha, beta, mhat, dhat; };
DI RowP row_params(float mean, float var, int mode) {
  RowP r;
  const float mh = rbf(mean);
  const float dh = rbf(__builtin_sqrtf(rbf(rbf(var) + 1e-5f)));
  r.mhat = mh; r.dhat = dh;
  if (mode == 2) {
    const float m2 = (mean - mh) / dh;
    const float v2 = var / (dh * dh);
    const float mh2 = rbf(m2);
    const float dh2 = rbf(__builtin_sqrtf(rbf(rbf(v2) + 1e-5f)));
    r.alpha = 1.0f / (dh * dh2);
    r.beta = mh / (dh * dh2) + mh2 / dh2;
  } else {
    r.alpha = 1.0f / dh;
    r.beta = mh / dh;
  }
  return r;
}

template <int BN, int S, int KD, int EPI>
__global__ __launch_bounds__(256, 1) void gemm_k(GemmArgs g) {
  constexpr int NK = KD / 64;
  constexpr int NPF = BN / 64;
  constexpr bool HALF = (BN % 64) != 0;
  constexpr int NI = BN / 32;
  constexpr int A_BYTES = 16384;
  constexpr int STAGE = A_BYTES + BN * 128;
  constexpr int L = 4 + NPF * 2 + (HALF ? 1 : 0);
  constexpr int NTOT = (EPI == EPI_QKV) ? 3 * DM : (EPI == EPI_GELU ? FF : DM);
  constexpr bool USE_STATS = (EPI == EPI_QKV || EPI == EPI_GELU_SHIFT || EPI == EPI_W2 || EPI == EPI_GELU);
  constexpr int NRES = (EPI == EPI_RES || EPI == EPI_W2) ? 1 : (EPI == EPI_W4 ? 2 : 0);
  constexpr int OFF_STATS = S * STAGE;
  constexpr int OFF_RES = OFF_STATS + (USE_STATS ? 16384 : 0);
  constexpr int OFF_WPHI = OFF_RES + NRES * 16384;
  constexpr int OFF_ROWP = OFF_WPHI + (EPI == EPI_QKV ? 4096 : 0);

  extern __shared__ __attribute__((aligned(16))) char smem[];
  const int tid = threadIdx.x, lane = tid & 63;
  const int w = __builtin_amdgcn_readfirstlane(tid >> 6);
  const int wr = w >> 1, wc = w & 1;

  const int b = blockIdx.x, xcd = b & 7, j = b >> 3;
  int rp, ct;
  if constexpr (EPI == EPI_QKV || EPI == EPI_GELU) { rp = (xcd >> 2) * 8 + (j >> 2); ct = (xcd & 3) * 4 + (j & 3); }
  else { rp = (xcd >> 1) * 4 + (j >> 3); ct = (xcd & 1) * 8 + (j & 7); }
  const int m0 = rp * 128;
  const int n0 = (EPI == EPI_QKV) ? ct * 64 : ct * BN;
  constexpr int PSTRIDE = (EPI == EPI_QKV) ? DM : 64;

  if constexpr (USE_STATS) {
    if (g.ln_mode != 0 || EPI == EPI_W2) {
#pragma unroll
      for (int e = 0; e < 4; ++e) {
        const int p = w * 4 + e;
        glds16(g.stats_in + ((size_t)p * SEQ + m0) * 2 + lane * 4, smem + OFF_STATS + p * 1024);
      }
    }
  }
  if constexpr (NRES >= 1) {
#pragma unroll
    for (int e = 0; e < 4; ++e) {
      const int r = (w * 4 + e) * 8 + (lane >> 3);
      glds16(g.res1 + (size_t)(m0 + r) * DM + n0 + (lane & 7) * 8, smem + OFF_RES + (w * 4 + e) * 1024);
    }
  }
  if constexpr (NRES >= 2) {
#pragma unroll
    for (int e = 0; e < 4; ++e) {
      const int r = (w * 4 + e) * 8 + (lane >> 3);
      glds16(g.res2 + (size_t)(m0 + r) * DM + n0 + (lane & 7) * 8, smem + OFF_RES + 16384 + (w * 4 + e) * 1024);
    }
  }
  if constexpr (EPI == EPI_QKV) {
    glds16(g.wphi + (size_t)ct * 2048 + w * 512 + lane * 8, smem + OFF_WPHI + w * 1024);
  }

  const u16* a_src = g.A + (size_t)(m0 + w * 32 + (lane >> 3)) * KD + (((lane & 7) ^ (lane >> 3)) * 8);
  const u16* b_src[2];
#pragma unroll
  for (int e = 0; e < 2; ++e) {
    const int r = lane >> 3;
    const int f = ((r >> 1) & 1) | (e << 1);
    const int c = ((((lane & 7) >> 1) ^ f) << 1) | (lane & 1);
    b_src[e] = g.B + (size_t)((w * 2 + e) * 8 + r) * NTOT + n0 + c * 8;
  }
  const u16* bh_src = nullptr;
  if constexpr (HALF) {
    const int c = ((((lane >> 1) & 1) ^ ((lane >> 5) & 1)) << 1) | (lane & 1);
    bh_src = g.B + (size_t)(w * 16 + (lane >> 2)) * NTOT + n0 + NPF * 64 + c * 8;
  }
  auto issue = [&](int kt, int slot) {
    char* base = smem + slot * STAGE;
#pragma unroll
    for (int e = 0; e < 4; ++e) glds16(a_src + (size_t)e * 8 * KD + kt * 64, base + (w * 4 + e) * 1024);
#pragma unroll
    for (int p = 0; p < NPF; ++p)
#pragma unroll
      for (int e = 0; e < 2; ++e)
        glds16(b_src[e] + (size_t)kt * 64 * NTOT + p * PSTRIDE, base + A_BYTES + p * 8192 + (w * 2 + e) * 1024);
    if constexpr (HALF) glds16(bh_src + (size_t)kt * 64 * NTOT, base + A_BYTES + NPF * 8192 + w * 1024);
  };

  const int g4 = lane >> 4, q4 = (lane >> 2) & 3, pp = lane & 3;
  const int fsw = ((q4 >> 1) & 1) | ((g4 & 1) << 1);
  int a_off[2], b_off[2], bh_off = 0;
#pragma unroll
  for (int k = 0; k < 2; ++k) {
    a_off[k] = (wr * 64 + (lane & 15)) * 128 + (((k * 4 + g4) ^ (lane & 7)) * 16);
    b_off[k] = A_BYTES + (8 * g4 + q4) * 128 + (((2 * k + wc) ^ fsw) * 32) + pp * 8;
  }
  if constexpr (HALF) bh_off = A_BYTES + NPF * 8192 + (8 * g4 + q4) * 64 + ((wc ^ (g4 & 1)) * 32) + pp * 8;

  f32x4 acc[4][NI];
#pragma unroll
  for (int m = 0; m < 4; ++m)
#pragma unroll
    for (int i = 0; i < NI; ++i) acc[m][i] = (f32x4){0.f, 0.f, 0.f, 0.f};
  bf16x8 xa[2][4];
  s16x4 wlo[2][NI], whi[2][NI];
  const unsigned lbase = (unsigned)(size_t)smem;

  auto ldfrag = [&](auto setc, auto slotc, auto kc) {
    constexpr int set = decltype(setc)::value, slot = decltype(slotc)::value, k32 = decltype(kc)::value;
    const unsigned sb = lbase + slot * STAGE;
    const unsigned ba0 = sb + b_off[0], ba1 = sb + b_off[1], bah = sb + bh_off, aa = sb + a_off[k32];
    static_for<0, NI>([&](auto ic) {
      constexpr int i = decltype(ic)::value;
      if constexpr (HALF && i == NI - 1) {
        wlo[set][i] = lds_tr<k32 * 2048>(bah);
        whi[set][i] = lds_tr<k32 * 2048 + 256>(bah);
      } else {
        wlo[set][i] = lds_tr<(i >> 1) * 8192 + k32 * 4096>((i & 1) ? ba1 : ba0);
        whi[set][i] = lds_tr<(i >> 1) * 8192 + k32 * 4096 + 512>((i & 1) ? ba1 : ba0);
      }
    });
    static_for<0, 4>([&](auto mc) { constexpr int m = decltype(mc)::value; xa[set][m] = lds_b128<m * 2048>(aa); });
  };
  auto wait_set = [&](auto setc) {
    constexpr int set = decltype(setc)::value;
    asm volatile("s_waitcnt lgkmcnt(0)" : "+v"(xa[set][0]), "+v"(xa[set][1]), "+v"(xa[set][2]), "+v"(xa[set][3]) :: "memory");
#pragma unroll
    for (int i = 0; i < NI; ++i) asm volatile("" : "+v"(wlo[set][i]), "+v"(whi[set][i]));
    __builtin_amdgcn_sched_barrier(0);
  };
  auto mma = [&](int set) {
#pragma unroll
    for (int i = 0; i < NI; ++i) {
      const bf16x8 wbv = cat8(wlo[set][i], whi[set][i]);
#pragma unroll
      for (int m = 0; m < 4; ++m) acc[m][i] = mfma16(wbv, xa[set][m], acc[m][i]);
    }
  };

  static_for<0, S>([&](auto ic) { issue(ic.value, ic.value); });
  wait_vm<(S - 1) * L>();
  bar();
  ldfrag(IC<0>{}, IC<0>{}, IC<0>{});
  static_for<0, NK>([&](auto ic) {
    constexpr int kt = ic.value;
    constexpr int slot = kt % S;
    wait_set(IC<0>{});
    ldfrag(IC<1>{}, IC<slot>{}, IC<1>{});
    __builtin_amdgcn_sched_barrier(0);
    mma(0);
    __builtin_amdgcn_sched_barrier(0);
    wait_set(IC<1>{});
    if constexpr (kt + 1 < NK) {
      constexpr int young = (S - 2 < NK - 2 - kt) ? S - 2 : NK - 2 - kt;
      wait_vm<young * L>();
      bar();
      if constexpr (kt + S < NK) issue(kt + S, slot);
      ldfrag(IC<0>{}, IC<(kt + 1) % S>{}, IC<0>{});
      __builtin_amdgcn_sched_barrier(0);
    }
    mma(1);
    __builtin_amdgcn_sched_barrier(0);
  });
  wait_lgkm0();
  bar();

  RowP* rowp = (RowP*)(smem + OFF_ROWP);
  const bool ln_on = USE_STATS && (g.ln_mode != 0 || EPI == EPI_W2);
  if constexpr (USE_STATS) {
    if (ln_on) {
      if (tid < 128) {
        const f32x2* sp = (const f32x2*)(smem + OFF_STATS);
        float s = 0.f;
        f32x2 v[16];
#pragma unroll
        for (int p = 0; p < 16; ++p) { v[p] = sp[p * 128 + tid]; s += v[p][0]; }
        const float mean = s * (1.0f / 1024.0f);
        float m2 = 0.f;
#pragma unroll
        for (int p = 0; p < 16; ++p) { const float d = v[p][0] * (1.0f / 64.0f) - mean; m2 += v[p][1] + 64.0f * d * d; }
        rowp[tid] = row_params(mean, m2 * (1.0f / 1024.0f), EPI == EPI_W2 ? 1 : g.ln_mode);
      }
      __syncthreads();
    }
  }

  constexpr int TSTR = BN * 2 + 16;
  char* T = smem;
  constexpr bool FOLD = (EPI == EPI_QKV || EPI == EPI_GELU_SHIFT || EPI == EPI_GELU);
  {
    float al[4] = {1.f, 1.f, 1.f, 1.f}, be[4] = {0.f, 0.f, 0.f, 0.f};
    if constexpr (FOLD) {
      if (ln_on) {
#pragma unroll
        for (int m = 0; m < 4; ++m) { const RowP r = rowp[wr * 64 + m * 16 + (lane & 15)]; al[m] = r.alpha; be[m] = r.beta; }
      }
    }
#pragma unroll
    for (int i = 0; i < NI; ++i) {
      const int nb = 2 * i + wc;
      const int ncol = nb * 16 + g4 * 4;
      f32x4 cs = {0.f, 0.f, 0.f, 0.f};
      if constexpr (FOLD) {
        if (ln_on) {
          const int gcol = (EPI == EPI_QKV) ? (ncol >> 6) * DM + n0 + (ncol & 63) : n0 + ncol;
          cs = *(const f32x4*)(g.colsum + gcol);
        }
      }
#pragma unroll
      for (int m = 0; m < 4; ++m) {
        float y[4];
#pragma unroll
        for (int r = 0; r < 4; ++r) {
          float v = acc[m][i][r];
          if constexpr (FOLD) v = v * al[m] - cs[r] * be[m];
          if constexpr (EPI == EPI_GELU_SHIFT || EPI == EPI_GELU) v = gelu_tanh(rbf(v));
          y[r] = v;
        }
        u32x2 pkd = {pk2(y[0], y[1]), pk2(y[2], y[3])};
        *(u32x2*)(T + (wr * 64 + m * 16 + (lane & 15)) * TSTR + ncol * 2) = pkd;
      }
    }
  }
  __syncthreads();

  if constexpr (EPI == EPI_GELU) {
    constexpr int CH = BN / 8;
#pragma unroll
    for (int it = 0; it < (128 * CH) / 256; ++it) {
      const int idx = it * 256 + tid, r = idx / CH, c = idx % CH;
      const u32x4 v = *(const u32x4*)(T + r * TSTR + c * 16);
      *(u32x4*)(g.out + (size_t)(m0 + r) * FF + n0 + c * 8) = v;
    }
  } else if constexpr (EPI == EPI_GELU_SHIFT) {
    const int grp = n0 >> 8;
    const int sh = (grp == 0) ? 1 : (grp == 1) ? -1 : (grp == 2) ? 2 : -2;
#pragma unroll
    for (int it = 0; it < 4; ++it) {
      const int r = it * 32 + (tid >> 3), c = tid & 7;
      const u32x4 v = *(const u32x4*)(T + r * TSTR + c * 16);
      const int dr = m0 + r + sh;
      if (dr >= 0 && dr < SEQ) *(u32x4*)(g.out + (size_t)dr * DM + n0 + c * 8) = v;
    }
    if (tid < 16) {
      const int zr = tid >> 3, c = tid & 7;
      const int az = sh > 0 ? sh : -sh;
      if (zr < az) {
        if (sh > 0 && rp == 0) *(u32x4*)(g.out + (size_t)zr * DM + n0 + c * 8) = (u32x4){0u, 0u, 0u, 0u};
        if (sh < 0 && rp == 15) *(u32x4*)(g.out + (size_t)(SEQ - 1 - zr) * DM + n0 + c * 8) = (u32x4){0u, 0u, 0u, 0u};
      }
    }
  } else if constexpr (EPI == EPI_RES || EPI == EPI_W2 || EPI == EPI_W4) {
#pragma unroll
    for (int it = 0; it < 4; ++it) {
      const int r = it * 32 + (tid >> 3), c = tid & 7;
      const u32x4 v = *(const u32x4*)(T + r * TSTR + c * 16);
      const u32x4 x = *(const u32x4*)(smem + OFF_RES + r * 128 + c * 16);
      float y[8];
      if constexpr (EPI == EPI_RES) {
#pragma unroll
        for (int e = 0; e < 4; ++e) { y[2 * e] = rbf(bflo(x[e]) + bflo(v[e])); y[2 * e + 1] = rbf(bfhi(x[e]) + bfhi(v[e])); }
      } else if constexpr (EPI == EPI_W2) {
        const RowP rr = rowp[r];
#pragma unroll
        for (int e = 0; e < 4; ++e) {
          const float l0 = rbf(rbf(bflo(x[e]) - rr.mhat) / rr.dhat), l1 = rbf(rbf(bfhi(x[e]) - rr.mhat) / rr.dhat);
          y[2 * e] = rbf(l0 + bflo(v[e])); y[2 * e + 1] = rbf(l1 + bfhi(v[e]));
        }
      } else {
        const u32x4 x1 = *(const u32x4*)(smem + OFF_RES + 16384 + r * 128 + c * 16);
#pragma unroll
        for (int e = 0; e < 4; ++e) {
          y[2 * e] = rbf(bflo(x[e]) + rbf(bflo(x1[e]) + bflo(v[e])));
          y[2 * e + 1] = rbf(bfhi(x[e]) + rbf(bfhi(x1[e]) + bfhi(v[e])));
        }
      }
      u32x4 o;
#pragma unroll
      for (int e = 0; e < 4; ++e) o[e] = pk2(y[2 * e], y[2 * e + 1]);
      *(u32x4*)(g.out + (size_t)(m0 + r) * DM + n0 + c * 8) = o;
      float s = 0.f;
#pragma unroll
      for (int e = 0; e < 8; ++e) s += y[e];
      s += __shfl_xor(s, 1); s += __shfl_xor(s, 2); s += __shfl_xor(s, 4);
      const float mc = s * (1.0f / 64.0f);
      float m2 = 0.f;
#pragma unroll
      for (int e = 0; e < 8; ++e) { const float d = y[e] - mc; m2 += d * d; }
      m2 += __shfl_xor(m2, 1); m2 += __shfl_xor(m2, 2); m2 += __shfl_xor(m2, 4);
      if (c == 0) *(f32x2*)(g.stats_out + ((size_t)ct * SEQ + m0 + r) * 2) = (f32x2){s, m2};
    }
  } else {
    constexpr int PKSTR = 144;
    char* PK = smem + 128 * TSTR;
    {
      const int hh = w & 1, tk = w >> 1;
      bf16x8 wf[2];
#pragma unroll
      for (int fb = 0; fb < 2; ++fb) {
        const char* p = smem + OFF_WPHI + hh * 2048 + (8 * g4 + q4) * 64 + fb * 32 + pp * 8;
        wf[fb] = cat8(trread(p), trread(p + 256));
      }
#pragma unroll 2
      for (int mb = 0; mb < 8; ++mb) {
        const int row = mb * 16 + (lane & 15);
        const bf16x8 xf = *(const bf16x8*)(T + row * TSTR + (tk * 64 + hh * 32 + g4 * 8) * 2);
        float p[8];
#pragma unroll
        for (int fb = 0; fb < 2; ++fb) {
          const f32x4 d = mfma16(wf[fb], xf, (f32x4){0.f, 0.f, 0.f, 0.f});
#pragma unroll
          for (int r = 0; r < 4; ++r) p[fb * 4 + r] = rbf(d[r]);
        }
        float mx = p[0], mn = p[0];
#pragma unroll
        for (int e = 1; e < 8; ++e) { mx = fmaxf(mx, p[e]); mn = fminf(mn, p[e]); }
        mx = fmaxf(mx, __shfl_xor(mx, 16)); mx = fmaxf(mx, __shfl_xor(mx, 32));
        mn = fminf(mn, __shfl_xor(mn, 16)); mn = fminf(mn, __shfl_xor(mn, 32));
        float ep[8], en[8], sp = 0.f, sn = 0.f;
#pragma unroll
        for (int e = 0; e < 8; ++e) {
          ep[e] = __builtin_amdgcn_exp2f((p[e] - mx) * 1.4426950408889634f); sp += ep[e];
          en[e] = __builtin_amdgcn_exp2f((mn - p[e]) * 1.4426950408889634f); sn += en[e];
        }
        sp += __shfl_xor(sp, 16); sp += __shfl_xor(sp, 32);
        sn += __shfl_xor(sn, 16); sn += __shfl_xor(sn, 32);
        const float ip = 1.0f / sp, in_ = 1.0f / sn;
#pragma unroll
        for (int fb = 0; fb < 2; ++fb) {
          const u32x2 a = {pk2(ep[fb * 4] * ip, ep[fb * 4 + 1] * ip), pk2(ep[fb * 4 + 2] * ip, ep[fb * 4 + 3] * ip)};
          const u32x2 c = {pk2(en[fb * 4] * in_, en[fb * 4 + 1] * in_), pk2(en[fb * 4 + 2] * in_, en[fb * 4 + 3] * in_)};
          const int f = fb * 16 + g4 * 4;
          if (tk == 0) {
            u16* dst = g.out + ((size_t)(m0 + row) * NH + ct * 2 + hh) * 64 + f;
            *(u32x2*)dst = a; *(u32x2*)(dst + 32) = c;
          } else {
            char* dst = PK + (hh * 128 + row) * PKSTR + f * 2;
            *(u32x2*)dst = a; *(u32x2*)(dst + 64) = c;
          }
        }
      }
    }
    __syncthreads();
    {
      const int hh = w >> 1;
      f32x4 kv[2][3];
#pragma unroll
      for (int a = 0; a < 2; ++a)
#pragma unroll
        for (int d = 0; d < 3; ++d) kv[a][d] = (f32x4){0.f, 0.f, 0.f, 0.f};
      const short one = (short)0x3F80;
      const bf16x8 ones = {one, one, one, one, one, one, one, one};
#pragma unroll
      for (int ks = 0; ks < 4; ++ks) {
        const int row = ks * 32 + 8 * g4 + q4;
        bf16x8 vf[2], pf[2];
#pragma unroll
        for (int d = 0; d < 2; ++d) {
          const char* p = T + row * TSTR + (128 + hh * 32 + d * 16) * 2 + pp * 8;
          vf[d] = cat8(trread(p), trread(p + 4 * TSTR));
        }
#pragma unroll
        for (int a = 0; a < 2; ++a) {
          const char* p = PK + (hh * 128 + row) * PKSTR + (((w & 1) * 2 + a) * 16) * 2 + pp * 8;
          pf[a] = cat8(trread(p), trread(p + 4 * PKSTR));
        }
#pragma unroll
        for (int a = 0; a < 2; ++a) {
          kv[a][0] = mfma16(pf[a], vf[0], kv[a][0]);
          kv[a][1] = mfma16(pf[a], vf[1], kv[a][1]);
          kv[a][2] = mfma16(pf[a], ones, kv[a][2]);
        }
      }
      float* slab = g.kvpart + ((size_t)rp * NH + ct * 2 + hh) * (33 * 64);
#pragma unroll
      for (int a = 0; a < 2; ++a) {
        const int f = ((w & 1) * 2 + a) * 16 + g4 * 4;
        *(f32x4*)(slab + (lane & 15) * 64 + f) = kv[a][0];
        *(f32x4*)(slab + (16 + (lane & 15)) * 64 + f) = kv[a][1];
        if ((lane & 15) == 0) *(f32x4*)(slab + 32 * 64 + f) = kv[a][2];
      }
    }
  }
}

__global__ __launch_bounds__(256) void ln0_k(const u16* __restrict__ x, u16* __restrict__ y) {
  const int lane = threadIdx.x & 63, row = blockIdx.x * 4 + (threadIdx.x >> 6);
  const u16* src = x + (size_t)row * DM;
  const u32x4 a = *(const u32x4*)(src + lane * 8), b = *(const u32x4*)(src + 512 + lane * 8);
  float v[16];
#pragma unroll
  for (int e = 0; e < 4; ++e) { v[2 * e] = bflo(a[e]); v[2 * e + 1] = bfhi(a[e]); v[8 + 2 * e] = bflo(b[e]); v[9 + 2 * e] = bfhi(b[e]); }
  float s = 0.f;
#pragma unroll
  for (int e = 0; e < 16; ++e) s += v[e];
#pragma unroll
  for (int o = 1; o < 64; o <<= 1) s += __shfl_xor(s, o);
  const float mean = s * (1.0f / 1024.0f);
  float m2 = 0.f;
#pragma unroll
  for (int e = 0; e < 16; ++e) { const float d = v[e] - mean; m2 += d * d; }
#pragma unroll
  for (int o = 1; o < 64; o <<= 1) m2 += __shfl_xor(m2, o);
  const RowP r = row_params(mean, m2 * (1.0f / 1024.0f), 1);
  u32x4 oa, ob;
#pragma unroll
  for (int e = 0; e < 4; ++e) {
    oa[e] = pk2(rbf(v[2 * e] - r.mhat) / r.dhat, rbf(v[2 * e + 1] - r.mhat) / r.dhat);
    ob[e] = pk2(rbf(v[8 + 2 * e] - r.mhat) / r.dhat, rbf(v[9 + 2 * e] - r.mhat) / r.dhat);
  }
  u16* dst = y + (size_t)row * DM;
  *(u32x4*)(dst + lane * 8) = oa;
  *(u32x4*)(dst + 512 + lane * 8) = ob;
}

__global__ __launch_bounds__(256) void attn_o_k(const u16* __restrict__ phiq, const float* __restrict__ kvpart, u16* __restrict__ o,
                                                const u16* __restrict__ wqkv, const u16* __restrict__ w1, const u16* __restrict__ w3,
                                                float* __restrict__ colsum) {
  __shared__ __attribute__((aligned(16))) char sm[8192 + 64];
  const int tid = threadIdx.x, lane = tid & 63, w = tid >> 6;
  if (blockIdx.x >= 256) {
    const int cg = blockIdx.x - 256;
    const u16* W; int N, c0;
    if (cg < 48) { W = wqkv; N = 3 * DM; c0 = cg * 64; }
    else if (cg < 64) { W = w1; N = DM; c0 = (cg - 48) * 64; }
    else { W = w3; N = FF; c0 = (cg - 64) * 64; }
    const int c = tid & 7, rg = tid >> 3;
    float s[8] = {0.f, 0.f, 0.f, 0.f, 0.f, 0.f, 0.f, 0.f};
#pragma unroll 8
    for (int i = 0; i < 32; ++i) {
      const u32x4 v = *(const u32x4*)(W + (size_t)(rg + i * 32) * N + c0 + c * 8);
#pragma unroll
      for (int e = 0; e < 4; ++e) { s[2 * e] += bflo(v[e]); s[2 * e + 1] += bfhi(v[e]); }
    }
    float* red = (float*)sm;
#pragma unroll
    for (int e = 0; e < 8; ++e) red[rg * 64 + c * 8 + e] = s[e];
    __syncthreads();
    if (tid < 64) {
      float t = 0.f;
#pragma unroll
      for (int i = 0; i < 32; ++i) t += red[i * 64 + tid];
      colsum[cg * 64 + tid] = t;
    }
    return;
  }
  const int h = blockIdx.x & 31, rb = blockIdx.x >> 5;
  const int g4 = lane >> 4;
  bf16x8 pq[4][2];
#pragma unroll
  for (int mb = 0; mb < 4; ++mb)
#pragma unroll
    for (int ks = 0; ks < 2; ++ks)
      pq[mb][ks] = *(const bf16x8*)(phiq + ((size_t)(rb * 256 + w * 64 + mb * 16 + (lane & 15)) * NH + h) * 64 + ks * 32 + g4 * 8);
  u16* kvt = (u16*)sm;
#pragma unroll
  for (int it = 0; it < 3; ++it) {
    const int idx = it * 256 + tid;
    if (idx < 528) {
      f32x4 s = {0.f, 0.f, 0.f, 0.f};
#pragma unroll
      for (int p = 0; p < 16; ++p) s += *(const f32x4*)(kvpart + ((size_t)p * NH + h) * (33 * 64) + idx * 4);
      *(u32x2*)(kvt + idx * 4) = (u32x2){pk2(s[0], s[1]), pk2(s[2], s[3])};
    }
  }
  __syncthreads();
  bf16x8 kf[3][2];
#pragma unroll
  for (int db = 0; db < 3; ++db)
#pragma unroll
    for (int ks = 0; ks < 2; ++ks) {
      const int d = (db < 2) ? db * 16 + (lane & 15) : 32;
      kf[db][ks] = *(const bf16x8*)(kvt + d * 64 + ks * 32 + g4 * 8);
    }
#pragma unroll
  for (int mb = 0; mb < 4; ++mb) {
    f32x4 a[3];
#pragma unroll
    for (int db = 0; db < 3; ++db) {
      a[db] = mfma16(kf[db][0], pq[mb][0], (f32x4){0.f, 0.f, 0.f, 0.f});
      a[db] = mfma16(kf[db][1], pq[mb][1], a[db]);
    }
    const float den = rbf(rbf(a[2][0]) + 1e-6f);
    u16* dst = o + (size_t)(rb * 256 + w * 64 + mb * 16 + (lane & 15)) * DM + h * 32 + g4 * 4;
#pragma unroll
    for (int db = 0; db < 2; ++db) {
      const u32x2 v = {pk2(rbf(a[db][0]) / den, rbf(a[db][1]) / den), pk2(rbf(a[db][2]) / den, rbf(a[db][3]) / den)};
      *(u32x2*)(dst + db * 16) = v;
    }
  }
}

template <int BN, int S, int KD, int EPI> static constexpr int smem_bytes() {
  constexpr bool USE_STATS = (EPI == EPI_QKV || EPI == EPI_GELU_SHIFT || EPI == EPI_W2 || EPI == EPI_GELU);
  constexpr int NRES = (EPI == EPI_RES || EPI == EPI_W2) ? 1 : (EPI == EPI_W4 ? 2 : 0);
  return S * (16384 + BN * 128) + (USE_STATS ? 16384 : 0) + NRES * 16384 + (EPI == EPI_QKV ? 4096 : 0) + 2048;
}
template <int BN, int S, int KD, int EPI> static void launch_gemm(const GemmArgs& a, hipStream_t st) {
  constexpr int sb = smem_bytes<BN, S, KD, EPI>();
  static_assert(sb <= 163840, "LDS");
  (void)hipFuncSetAttribute((const void*)gemm_k<BN, S, KD, EPI>, hipFuncAttributeMaxDynamicSharedMemorySize, sb);
  gemm_k<BN, S, KD, EPI><<<256, 256, sb, st>>>(a);
}

extern "C" void kernel_launch(void* const* d_in, const int* in_sizes, int n_in, void* d_out, int out_size, void* d_ws,
                              size_t ws_size, hipStream_t stream) {
  const u16* x0 = (const u16*)d_in[0];
  const u16* wqkv = (const u16*)d_in[1];
  const u16* wphi = (const u16*)d_in[2];
  const u16* wo = (const u16*)d_in[3];
  const u16* w1 = (const u16*)d_in[5];
  const u16* w2 = (const u16*)d_in[6];
  const u16* w3 = (const u16*)d_in[7];
  const u16* w4 = (const u16*)d_in[8];
  char* ws = (char*)d_ws;
  const size_t MB = 1 << 20;
  u16* xln0 = (u16*)(ws + 0 * MB);
  u16* phiq = (u16*)(ws + 4 * MB);
  float* kvpart = (float*)(ws + 12 * MB);
  u16* obuf = (u16*)(ws + 17 * MB);
  u16* xa = (u16*)(ws + 21 * MB);
  u16* hsh = (u16*)(ws + 25 * MB);
  u16* x1 = (u16*)(ws + 29 * MB);
  u16* gbuf = (u16*)(ws + 33 * MB);
  u16* xb = (u16*)(ws + 48 * MB);
  float* st_xa = (float*)(ws + 52 * MB);
  float* st_x1 = (float*)(ws + 53 * MB);
  float* st_xb = (float*)(ws + 54 * MB);
  float* colsum = (float*)(ws + 55 * MB);

  ln0_k<<<SEQ / 4, 256, 0, stream>>>(x0, xln0);
  for (int layer = 0; layer < 2; ++layer) {
    const u16* xin = layer == 0 ? x0 : xb;
    u16* xout = layer == 0 ? xb : (u16*)d_out;
    GemmArgs a;
    a = GemmArgs{layer == 0 ? xln0 : xb, wqkv, phiq, nullptr, nullptr, st_xb, nullptr, colsum, wphi, kvpart, layer == 0 ? 0 : 1, 0};
    launch_gemm<192, 3, 1024, EPI_QKV>(a, stream);
    attn_o_k<<<layer == 0 ? 376 : 256, 256, 0, stream>>>(phiq, kvpart, obuf, wqkv, w1, w3, colsum);
    a = GemmArgs{obuf, wo, xa, xin, nullptr, nullptr, st_xa, nullptr, nullptr, nullptr, 0, 0};
    launch_gemm<64, 4, 1024, EPI_RES>(a, stream);
    a = GemmArgs{xa, w1, hsh, nullptr, nullptr, st_xa, nullptr, colsum + 3072, nullptr, nullptr, 2, 0};
    launch_gemm<64, 4, 1024, EPI_GELU_SHIFT>(a, stream);
    a = GemmArgs{hsh, w2, x1, xa, nullptr, st_xa, st_x1, nullptr, nullptr, nullptr, 1, 0};
    launch_gemm<64, 4, 1024, EPI_W2>(a, stream);
    a = GemmArgs{x1, w3, gbuf, nullptr, nullptr, st_x1, nullptr, colsum + 4096, nullptr, nullptr, 1, 0};
    launch_gemm<224, 3, 1024, EPI_GELU>(a, stream);
    a = GemmArgs{gbuf, w4, xout, xa, x1, nullptr, st_xb, nullptr, nullptr, nullptr, 0, 0};
    launch_gemm<64, 4, 3584, EPI_W4>(a, stream);
  }
}
```

```cpp
#include <hip/hip_runtime.h>
#include <hip/hip_bf16.h>
#include <stdint.h>

typedef __attribute__((ext_vector_type(8))) short bf16x8;
typedef __attribute__((ext_vector_type(4))) short s16x4;
typedef __attribute__((ext_vector_type(4))) float f32x4;
typedef __attribute__((ext_vector_type(2))) float f32x2;
typedef __attribute__((ext_vector_type(4))) unsigned u32x4;
typedef __attribute__((ext_vector_type(2))) unsigned u32x2;
typedef __attribute__((ext_vector_type(2))) __bf16 bf16x2_t;
typedef unsigned short u16;
#define LDS_AS __attribute__((address_space(3)))
#define DI __device__ __forceinline__

static constexpr int SEQ = 2048, DM = 1024, NH = 32, FF = 3584;

DI float bf2f(unsigned v) { return __uint_as_float(v << 16); }
DI float bflo(unsigned v) { return __uint_as_float(v << 16); }
DI float bfhi(unsigned v) { return __uint_as_float(v & 0xffff0000u); }
DI unsigned pk2(float a, float b) {
  f32x2 v = {a, b};
  return __builtin_bit_cast(unsigned, __builtin_convertvector(v, bf16x2_t));
}
DI float rbf(float a) { return bflo(pk2(a, a)); }

template <int I> struct IC { static constexpr int value = I; };
template <int B, int E, class F> DI void static_for(F&& f) {
  if constexpr (B < E) { f(IC<B>{}); static_for<B + 1, E>(f); }
}
template <int N> DI void wait_vm() { asm volatile("s_waitcnt vmcnt(%0)" ::"n"(N) : "memory"); }
DI void wait_lgkm0() { asm volatile("s_waitcnt lgkmcnt(0)" ::: "memory"); }
DI void bar() { __builtin_amdgcn_s_barrier(); }
DI void glds16(const void* src, char* lds_dst) {
  __builtin_amdgcn_global_load_lds((const unsigned*)src, (unsigned*)lds_dst, 16, 0, 0);
}
DI s16x4 trread(const char* p) {
  return __builtin_bit_cast(s16x4, __builtin_amdgcn_ds_read_tr16_b64_v4i16((LDS_AS s16x4*)p));
}
template <int OFF> DI s16x4 lds_tr(unsigned addr) {
  s16x4 r; asm volatile("ds_read_b64_tr_b16 %0, %1 offset:%2" : "=v"(r) : "v"(addr), "n"(OFF) : "memory"); return r;
}
template <int OFF> DI bf16x8 lds_b128(unsigned addr) {
  bf16x8 r; asm volatile("ds_read_b128 %0, %1 offset:%2" : "=v"(r) : "v"(addr), "n"(OFF) : "memory"); return r;
}
DI bf16x8 cat8(s16x4 lo, s16x4 hi) { return (bf16x8){lo[0], lo[1], lo[2], lo[3], hi[0], hi[1], hi[2], hi[3]}; }
DI f32x4 mfma16(bf16x8 a, bf16x8 b, f32x4 c) { return __builtin_amdgcn_mfma_f32_16x16x32_bf16(a, b, c, 0, 0, 0); }

DI f32x2 gelu2(f32x2 x) {
  const float c0 = -2.302208198f, c1 = -2.302208198f * 0.044715f;
  const f32x2 a = x * (x * x * c1 + c0);
  const f32x2 d = (f32x2){__builtin_amdgcn_exp2f(a[0]), __builtin_amdgcn_exp2f(a[1])} + 1.0f;
  return x * (f32x2){__builtin_amdgcn_rcpf(d[0]), __builtin_amdgcn_rcpf(d[1])};
}
DI f32x2 rbf2(f32x2 v) { const unsigned p = pk2(v[0], v[1]); return (f32x2){bflo(p), bfhi(p)}; }
DI f32x2 unpk(unsigned p) { return (f32x2){bflo(p), bfhi(p)}; }

DI void feature_map(const bf16x8 (&wf)[2], bf16x8 xf, u32x4& pos, u32x4& neg) {
  f32x2 p[4];
#pragma unroll
  for (int fb = 0; fb < 2; ++fb) {
    const f32x4 d = mfma16(wf[fb], xf, (f32x4){0.f, 0.f, 0.f, 0.f});
    p[fb * 2] = rbf2((f32x2){d[0], d[1]});
    p[fb * 2 + 1] = rbf2((f32x2){d[2], d[3]});
  }
  float mx = fmaxf(p[0][0], p[0][1]), mn = fminf(p[0][0], p[0][1]);
#pragma unroll
  for (int e = 1; e < 4; ++e) { mx = fmaxf(mx, fmaxf(p[e][0], p[e][1])); mn = fminf(mn, fminf(p[e][0], p[e][1])); }
  mx = fmaxf(mx, __shfl_xor(mx, 16)); mx = fmaxf(mx, __shfl_xor(mx, 32));
  mn = fminf(mn, __shfl_xor(mn, 16)); mn = fminf(mn, __shfl_xor(mn, 32));
  const float l2e = 1.4426950408889634f;
  f32x2 ep[4], en[4], sp2 = {0.f, 0.f}, sn2 = {0.f, 0.f};
#pragma unroll
  for (int e = 0; e < 4; ++e) {
    const f32x2 a = (p[e] - mx) * l2e, b = (mn - p[e]) * l2e;
    ep[e] = (f32x2){__builtin_amdgcn_exp2f(a[0]), __builtin_amdgcn_exp2f(a[1])}; sp2 += ep[e];
    en[e] = (f32x2){__builtin_amdgcn_exp2f(b[0]), __builtin_amdgcn_exp2f(b[1])}; sn2 += en[e];
  }
  float sp = sp2[0] + sp2[1], sn = sn2[0] + sn2[1];
  sp += __shfl_xor(sp, 16); sp += __shfl_xor(sp, 32);
  sn += __shfl_xor(sn, 16); sn += __shfl_xor(sn, 32);
  const float ip = __builtin_amdgcn_rcpf(sp), in_ = __builtin_amdgcn_rcpf(sn);
#pragma unroll
  for (int e = 0; e < 4; ++e) {
    const f32x2 a = ep[e] * ip, b = en[e] * in_;
    pos[e] = pk2(a[0], a[1]); neg[e] = pk2(b[0], b[1]);
  }
}

#define TS(i)
struct GemmArgs {
  const u16* A;
  const u16* B;
  u16* out;
  const u16* res1;
  const u16* res2;
  const float* stats_in;
  float* stats_out;
  const float* colsum;
  const u16* wphi;
  float* kvpart;
  int ln_mode;
  int unused;
};

enum { EPI_QKV = 0, EPI_RES = 1, EPI_GELU_SHIFT = 2, EPI_W2 = 3, EPI_GELU = 4, EPI_W4 = 5 };

struct RowP { float alpha, beta, mhat, dhat; };
DI RowP row_params(float mean, float var, int mode) {
  RowP r;
  const float mh = rbf(mean);
  const float dh = rbf(__builtin_sqrtf(rbf(rbf(var) + 1e-5f)));
  r.mhat = mh; r.dhat = dh;
  if (mode == 2) {
    const float m2 = (mean - mh) / dh;
    const float v2 = var / (dh * dh);
    const float mh2 = rbf(m2);
    const float dh2 = rbf(__builtin_sqrtf(rbf(rbf(v2) + 1e-5f)));
    r.alpha = 1.0f / (dh * dh2);
    r.beta = mh / (dh * dh2) + mh2 / dh2;
  } else {
    r.alpha = 1.0f / dh;
    r.beta = mh / dh;
  }
  return r;
}

template <int BN, int S, int KD, int EPI>
__global__ __launch_bounds__(512, 2) void gemm_k(GemmArgs g) {
  constexpr int NK = KD / 64;
  constexpr int NPF = BN / 64;
  constexpr bool HALF = (BN % 64) != 0;
  constexpr int NI = BN / 32;
  constexpr int A_BYTES = 16384;
  constexpr int STAGE = A_BYTES + BN * 128;
  constexpr int L = 4 + NPF * 2 + (HALF ? 1 : 0);
  constexpr int NTOT = (EPI == EPI_QKV) ? 3 * DM : (EPI == EPI_GELU ? FF : DM);
  constexpr bool USE_STATS = (EPI == EPI_QKV || EPI == EPI_GELU_SHIFT || EPI == EPI_W2 || EPI == EPI_GELU);
  constexpr bool FOLD = (EPI == EPI_QKV || EPI == EPI_GELU_SHIFT || EPI == EPI_GELU);
  constexpr int NRES = (EPI == EPI_RES || EPI == EPI_W2) ? 1 : (EPI == EPI_W4 ? 2 : 0);
  constexpr int OFF_STATS = S * STAGE;
  constexpr int OFF_RES = OFF_STATS;
  constexpr int OFF_WPHI = OFF_RES + NRES * 16384;
  constexpr int OFF_ROWP = OFF_WPHI + (EPI == EPI_QKV ? 4096 : 0);
  constexpr int OFF_CS = OFF_ROWP + 2048;
  constexpr int NEXTRA = NRES * 4 + (EPI == EPI_QKV ? 1 : 0) + (FOLD ? 1 : 0);

  extern __shared__ __attribute__((aligned(16))) char smem[];
  const int tid = threadIdx.x, lane = tid & 63;
  const int w = __builtin_amdgcn_readfirstlane(tid >> 6);
  const bool prod = w >= 4;
  const int cw = w & 3, wr = cw >> 1, wc = cw & 1;

  const int b = blockIdx.x, xcd = b & 7, j = b >> 3;
  int rp, ct;
  if constexpr (EPI == EPI_QKV || EPI == EPI_GELU) { rp = (xcd >> 2) * 8 + (j >> 2); ct = (xcd & 3) * 4 + (j & 3); }
  else { rp = (xcd >> 1) * 4 + (j >> 3); ct = (xcd & 1) * 8 + (j & 7); }
  const int m0 = rp * 128;
  const int n0 = (EPI == EPI_QKV) ? ct * 64 : ct * BN;
  constexpr int PSTRIDE = (EPI == EPI_QKV) ? DM : 64;
  const int g4 = lane >> 4, q4 = (lane >> 2) & 3, pp = lane & 3;
  RowP* rowp = (RowP*)(smem + OFF_ROWP);
  const bool ln_on = USE_STATS && (g.ln_mode != 0 || EPI == EPI_W2);

  f32x4 acc[4][NI];
  TS(0);
  if (prod) {
    const u16* a_src = g.A + (size_t)(m0 + cw * 32 + (lane >> 3)) * KD + (((lane & 7) ^ (lane >> 3)) * 8);
    const u16* b_src[2];
#pragma unroll
    for (int e = 0; e < 2; ++e) {
      const int r = lane >> 3;
      const int f = ((r >> 1) & 1) | (e << 1);
      const int c = ((((lane & 7) >> 1) ^ f) << 1) | (lane & 1);
      b_src[e] = g.B + (size_t)((cw * 2 + e) * 8 + r) * NTOT + n0 + c * 8;
    }
    const u16* bh_src = nullptr;
    if constexpr (HALF) {
      const int c = ((((lane >> 1) & 1) ^ ((lane >> 5) & 1)) << 1) | (lane & 1);
      bh_src = g.B + (size_t)(cw * 16 + (lane >> 2)) * NTOT + n0 + NPF * 64 + c * 8;
    }
    auto issue = [&](int kt, int slot) {
      char* base = smem + slot * STAGE;
#pragma unroll
      for (int p = 0; p < NPF; ++p)
#pragma unroll
        for (int e = 0; e < 2; ++e)
          glds16(b_src[e] + (size_t)kt * 64 * NTOT + p * PSTRIDE, base + A_BYTES + p * 8192 + (cw * 2 + e) * 1024);
      if constexpr (HALF) glds16(bh_src + (size_t)kt * 64 * NTOT, base + A_BYTES + NPF * 8192 + cw * 1024);
#pragma unroll
      for (int e = 0; e < 4; ++e) glds16(a_src + (size_t)e * 8 * KD + kt * 64, base + (cw * 4 + e) * 1024);
    };
    static_for<0, S>([&](auto ic) { issue(ic.value, ic.value); });
    if constexpr (NRES >= 1) {
#pragma unroll
      for (int e = 0; e < 4; ++e) {
        const int r = (cw * 4 + e) * 8 + (lane >> 3);
        glds16(g.res1 + (size_t)(m0 + r) * DM + n0 + (lane & 7) * 8, smem + OFF_RES + (cw * 4 + e) * 1024);
      }
    }
    if constexpr (NRES >= 2) {
#pragma unroll
      for (int e = 0; e < 4; ++e) {
        const int r = (cw * 4 + e) * 8 + (lane >> 3);
        glds16(g.res2 + (size_t)(m0 + r) * DM + n0 + (lane & 7) * 8, smem + OFF_RES + 16384 + (cw * 4 + e) * 1024);
      }
    }
    if constexpr (EPI == EPI_QKV) glds16(g.wphi + (size_t)ct * 2048 + cw * 512 + lane * 8, smem + OFF_WPHI + cw * 1024);
    if constexpr (FOLD) {
      int col = cw * 64 + (lane & 15) * 4;
      if (col > BN - 4) col = BN - 4;
      const int gcol = (EPI == EPI_QKV) ? (col >> 6) * DM + n0 + (col & 63) : n0 + col;
      if (lane < 16) glds16(g.colsum + gcol, smem + OFF_CS + cw * 256);
    }
    wait_vm<(S - 1) * L + NEXTRA>();
    TS(1);
    bar();
    static_for<0, NK - 1>([&](auto ic) {
      constexpr int kt = ic.value;
      constexpr int young = (S - 2 < NK - 2 - kt) ? S - 2 : NK - 2 - kt;
      constexpr int ex = (kt + 1 <= S - 1 && kt + 1 < NK - 1) ? NEXTRA : 0;
      wait_vm<young * L + ex>();
      bar();
      if constexpr (kt + S < NK) issue(kt + S, kt % S);
    });
    TS(2);
    bar();
  } else {
    const int fsw = ((q4 >> 1) & 1) | ((g4 & 1) << 1);
    int a_off[2], b_off[2], bh_off = 0;
#pragma unroll
    for (int k = 0; k < 2; ++k) {
      a_off[k] = (wr * 64 + (lane & 15)) * 128 + (((k * 4 + g4) ^ (lane & 7)) * 16);
      b_off[k] = A_BYTES + (8 * g4 + q4) * 128 + (((2 * k + wc) ^ fsw) * 32) + pp * 8;
    }
    if constexpr (HALF) bh_off = A_BYTES + NPF * 8192 + (8 * g4 + q4) * 64 + ((wc ^ (g4 & 1)) * 32) + pp * 8;
#pragma unroll
    for (int m = 0; m < 4; ++m)
#pragma unroll
      for (int i = 0; i < NI; ++i) acc[m][i] = (f32x4){0.f, 0.f, 0.f, 0.f};
    bf16x8 xa[2][4];
    s16x4 wlo[2][NI], whi[2][NI];
    const unsigned lbase = (unsigned)(size_t)smem;

    auto ldfrag = [&](auto setc, auto slotc, auto kc) {
      constexpr int set = decltype(setc)::value, slot = decltype(slotc)::value, k32 = decltype(kc)::value;
      const unsigned sb = lbase + slot * STAGE;
      const unsigned ba0 = sb + b_off[0], ba1 = sb + b_off[1], bah = sb + bh_off, aa = sb + a_off[k32];
      static_for<0, NI>([&](auto ic) {
        constexpr int i = decltype(ic)::value;
        if constexpr (HALF && i == NI - 1) {
          wlo[set][i] = lds_tr<k32 * 2048>(bah);
          whi[set][i] = lds_tr<k32 * 2048 + 256>(bah);
        } else {
          wlo[set][i] = lds_tr<(i >> 1) * 8192 + k32 * 4096>((i & 1) ? ba1 : ba0);
          whi[set][i] = lds_tr<(i >> 1) * 8192 + k32 * 4096 + 512>((i & 1) ? ba1 : ba0);
        }
      });
      static_for<0, 4>([&](auto mc) { constexpr int m = decltype(mc)::value; xa[set][m] = lds_b128<m * 2048>(aa); });
    };
    auto wait_set = [&](auto setc) {
      constexpr int set = decltype(setc)::value;
      asm volatile("s_waitcnt lgkmcnt(0)" : "+v"(xa[set][0]), "+v"(xa[set][1]), "+v"(xa[set][2]), "+v"(xa[set][3]) :: "memory");
#pragma unroll
      for (int i = 0; i < NI; ++i) asm volatile("" : "+v"(wlo[set][i]), "+v"(whi[set][i]));
      __builtin_amdgcn_sched_barrier(0);
    };
    auto ld1 = [&](auto setc, auto slotc, auto kc, auto rc) {
      constexpr int set = decltype(setc)::value, slot = decltype(slotc)::value, k32 = decltype(kc)::value, r = decltype(rc)::value;
      const unsigned sb = lbase + slot * STAGE;
      if constexpr (r < 2 * NI) {
        constexpr int i = r >> 1, hi = r & 1;
        s16x4 v;
        if constexpr (HALF && i == NI - 1) v = lds_tr<k32 * 2048 + hi * 256>(sb + bh_off);
        else v = lds_tr<(i >> 1) * 8192 + k32 * 4096 + hi * 512>(sb + b_off[i & 1]);
        if constexpr (hi) whi[set][i] = v; else wlo[set][i] = v;
      } else {
        constexpr int m = r - 2 * NI;
        xa[set][m] = lds_b128<m * 2048>(sb + a_off[k32]);
      }
    };
    auto mma_ld = [&](auto setc, auto ldc, auto slotc, auto kc) {
      constexpr int set = decltype(setc)::value;
      constexpr bool ld = decltype(ldc)::value != 0;
      static_for<0, 4 * NI>([&](auto tc) {
        constexpr int t = decltype(tc)::value, i = t >> 2, m = t & 3;
        acc[m][i] = mfma16(cat8(wlo[set][i], whi[set][i]), xa[set][m], acc[m][i]);
        if constexpr (ld && t < 2 * NI + 4) ld1(IC<set ^ 1>{}, slotc, kc, tc);
        __builtin_amdgcn_sched_barrier(0);
      });
    };

    if constexpr (USE_STATS) {
      if (ln_on) {
        const int r = cw * 32 + (lane >> 1), hf = lane & 1;
        f32x2 v[8];
#pragma unroll
        for (int p = 0; p < 8; ++p) v[p] = *(const f32x2*)(g.stats_in + ((size_t)(hf * 8 + p) * SEQ + m0 + r) * 2);
        float s = 0.f;
#pragma unroll
        for (int p = 0; p < 8; ++p) s += v[p][0];
        s += __shfl_xor(s, 1);
        const float mean = s * (1.0f / 1024.0f);
        float m2 = 0.f;
#pragma unroll
        for (int p = 0; p < 8; ++p) { const float d = v[p][0] * (1.0f / 64.0f) - mean; m2 += v[p][1] + 64.0f * d * d; }
        m2 += __shfl_xor(m2, 1);
        if (hf == 0) rowp[r] = row_params(mean, m2 * (1.0f / 1024.0f), EPI == EPI_W2 ? 1 : g.ln_mode);
      }
    }
    bar();
    TS(1);
    ldfrag(IC<0>{}, IC<0>{}, IC<0>{});
    static_for<0, NK>([&](auto ic) {
      constexpr int kt = ic.value;
      constexpr int slot = kt % S;
      wait_set(IC<0>{});
      mma_ld(IC<0>{}, IC<1>{}, IC<slot>{}, IC<1>{});
      wait_set(IC<1>{});
      if constexpr (kt + 1 < NK) {
        bar();
        __builtin_amdgcn_sched_barrier(0);
        mma_ld(IC<1>{}, IC<1>{}, IC<(kt + 1) % S>{}, IC<0>{});
      } else {
        mma_ld(IC<1>{}, IC<0>{}, IC<0>{}, IC<0>{});
      }
    });
    TS(2);
    bar();
  }
  TS(3);

  constexpr int TSTR = BN * 2 + 16;
  char* T = smem;
  if (!prod) {
    float al[4] = {1.f, 1.f, 1.f, 1.f}, be[4] = {0.f, 0.f, 0.f, 0.f};
    if constexpr (FOLD) {
      if (ln_on) {
#pragma unroll
        for (int m = 0; m < 4; ++m) { const RowP r = rowp[wr * 64 + m * 16 + (lane & 15)]; al[m] = r.alpha; be[m] = r.beta; }
      }
    }
#pragma unroll
    for (int i = 0; i < NI; ++i) {
      const int nb = 2 * i + wc;
      const int ncol = nb * 16 + g4 * 4;
      f32x4 cs = {0.f, 0.f, 0.f, 0.f};
      if constexpr (FOLD) {
        if (ln_on) cs = *(const f32x4*)(smem + OFF_CS + ncol * 4);
      }
#pragma unroll
      for (int m = 0; m < 4; ++m) {
        u32x2 pkd;
#pragma unroll
        for (int h = 0; h < 2; ++h) {
          f32x2 v = {acc[m][i][2 * h], acc[m][i][2 * h + 1]};
          if constexpr (FOLD) v = v * al[m] - (f32x2){cs[2 * h], cs[2 * h + 1]} * be[m];
          if constexpr (EPI == EPI_GELU_SHIFT || EPI == EPI_GELU) v = gelu2(rbf2(v));
          pkd[h] = pk2(v[0], v[1]);
        }
        *(u32x2*)(T + (wr * 64 + m * 16 + (lane & 15)) * TSTR + ncol * 2) = pkd;
      }
    }
  }
  __syncthreads();
  TS(4);
  if constexpr (EPI == EPI_GELU) {
    constexpr int CH = BN / 8;
#pragma unroll
    for (int it = 0; it < (128 * CH) / 512; ++it) {
      const int idx = it * 512 + tid, r = idx / CH, c = idx % CH;
      const u32x4 v = *(const u32x4*)(T + r * TSTR + c * 16);
      *(u32x4*)(g.out + (size_t)(m0 + r) * FF + n0 + c * 8) = v;
    }
  } else if constexpr (EPI == EPI_GELU_SHIFT) {
    const int grp = n0 >> 8;
    const int sh = (grp == 0) ? 1 : (grp == 1) ? -1 : (grp == 2) ? 2 : -2;
#pragma unroll
    for (int it = 0; it < 2; ++it) {
      const int r = it * 64 + (tid >> 3), c = tid & 7;
      const u32x4 v = *(const u32x4*)(T + r * TSTR + c * 16);
      const int dr = m0 + r + sh;
      if (dr >= 0 && dr < SEQ) *(u32x4*)(g.out + (size_t)dr * DM + n0 + c * 8) = v;
    }
    if (tid < 16) {
      const int zr = tid >> 3, c = tid & 7;
      const int az = sh > 0 ? sh : -sh;
      if (zr < az) {
        if (sh > 0 && rp == 0) *(u32x4*)(g.out + (size_t)zr * DM + n0 + c * 8) = (u32x4){0u, 0u, 0u, 0u};
        if (sh < 0 && rp == 15) *(u32x4*)(g.out + (size_t)(SEQ - 1 - zr) * DM + n0 + c * 8) = (u32x4){0u, 0u, 0u, 0u};
      }
    }
  } else if constexpr (EPI == EPI_RES || EPI == EPI_W2 || EPI == EPI_W4) {
#pragma unroll
    for (int it = 0; it < 2; ++it) {
      const int r = it * 64 + (tid >> 3), c = tid & 7;
      const u32x4 v = *(const u32x4*)(T + r * TSTR + c * 16);
      const u32x4 x = *(const u32x4*)(smem + OFF_RES + r * 128 + c * 16);
      f32x2 y[4];
      if constexpr (EPI == EPI_RES) {
#pragma unroll
        for (int e = 0; e < 4; ++e) y[e] = unpk(x[e]) + unpk(v[e]);
      } else if constexpr (EPI == EPI_W2) {
        const RowP rr = rowp[r];
#pragma unroll
        for (int e = 0; e < 4; ++e) y[e] = rbf2(rbf2(unpk(x[e]) - rr.mhat) * rr.alpha) + unpk(v[e]);
      } else {
        const u32x4 x1 = *(const u32x4*)(smem + OFF_RES + 16384 + r * 128 + c * 16);
#pragma unroll
        for (int e = 0; e < 4; ++e) y[e] = unpk(x[e]) + rbf2(unpk(x1[e]) + unpk(v[e]));
      }
      u32x4 o;
#pragma unroll
      for (int e = 0; e < 4; ++e) { o[e] = pk2(y[e][0], y[e][1]); y[e] = unpk(o[e]); }
      *(u32x4*)(g.out + (size_t)(m0 + r) * DM + n0 + c * 8) = o;
      const f32x2 s2 = (y[0] + y[1]) + (y[2] + y[3]);
      float s = s2[0] + s2[1];
      s += __shfl_xor(s, 1); s += __shfl_xor(s, 2); s += __shfl_xor(s, 4);
      const float mc = s * (1.0f / 64.0f);
      f32x2 q2 = {0.f, 0.f};
#pragma unroll
      for (int e = 0; e < 4; ++e) { const f32x2 d = y[e] - mc; q2 += d * d; }
      float m2 = q2[0] + q2[1];
      m2 += __shfl_xor(m2, 1); m2 += __shfl_xor(m2, 2); m2 += __shfl_xor(m2, 4);
      if (c == 0) *(f32x2*)(g.stats_out + ((size_t)ct * SEQ + m0 + r) * 2) = (f32x2){s, m2};
    }
  } else {
    constexpr int PKSTR = 144;
    char* PK = smem + 128 * TSTR;
#pragma unroll
    for (int it = 0; it < 2; ++it) {
      const int r = it * 64 + (tid >> 3), c = tid & 7;
      *(u32x4*)(g.out + (size_t)(m0 + r) * DM + n0 + c * 8) = *(const u32x4*)(T + r * TSTR + c * 16);
    }
    {
      const int hh = w & 1, rq = w >> 1;
      bf16x8 wf[2];
#pragma unroll
      for (int fb = 0; fb < 2; ++fb) {
        const char* p = smem + OFF_WPHI + hh * 2048 + (8 * g4 + q4) * 64 + fb * 32 + pp * 8;
        wf[fb] = cat8(trread(p), trread(p + 256));
      }
#pragma unroll
      for (int mb = 0; mb < 2; ++mb) {
        const int row = rq * 32 + mb * 16 + (lane & 15);
        const bf16x8 xf = *(const bf16x8*)(T + row * TSTR + (64 + hh * 32 + g4 * 8) * 2);
        u32x4 pos, neg;
        feature_map(wf, xf, pos, neg);
        char* dst = PK + (hh * 128 + row) * PKSTR + g4 * 8;
        *(u32x2*)dst = (u32x2){pos[0], pos[1]}; *(u32x2*)(dst + 32) = (u32x2){pos[2], pos[3]};
        *(u32x2*)(dst + 64) = (u32x2){neg[0], neg[1]}; *(u32x2*)(dst + 96) = (u32x2){neg[2], neg[3]};
      }
    }
    __syncthreads();
    TS(6);
    {
      const int hh = w >> 2, fa = w & 3;
      f32x4 kv[3];
#pragma unroll
      for (int d = 0; d < 3; ++d) kv[d] = (f32x4){0.f, 0.f, 0.f, 0.f};
      const short one = (short)0x3F80;
      const bf16x8 ones = {one, one, one, one, one, one, one, one};
#pragma unroll
      for (int ks = 0; ks < 4; ++ks) {
        const int row = ks * 32 + 8 * g4 + q4;
        bf16x8 vf[2];
#pragma unroll
        for (int d = 0; d < 2; ++d) {
          const char* p = T + row * TSTR + (128 + hh * 32 + d * 16) * 2 + pp * 8;
          vf[d] = cat8(trread(p), trread(p + 4 * TSTR));
        }
        const char* p = PK + (hh * 128 + row) * PKSTR + (fa * 16) * 2 + pp * 8;
        const bf16x8 pf = cat8(trread(p), trread(p + 4 * PKSTR));
        kv[0] = mfma16(pf, vf[0], kv[0]);
        kv[1] = mfma16(pf, vf[1], kv[1]);
        kv[2] = mfma16(pf, ones, kv[2]);
      }
      float* slab = g.kvpart + ((size_t)rp * NH + ct * 2 + hh) * (33 * 64);
      const int f = fa * 16 + g4 * 4;
      *(f32x4*)(slab + (lane & 15) * 64 + f) = kv[0];
      *(f32x4*)(slab + (16 + (lane & 15)) * 64 + f) = kv[1];
      if ((lane & 15) == 0) *(f32x4*)(slab + 32 * 64 + f) = kv[2];
    }
  }
  TS(5);
}

__global__ __launch_bounds__(256) void ln0_k(const u16* __restrict__ x, u16* __restrict__ y, const u16* __restrict__ wqkv,
                                             const u16* __restrict__ w1, const u16* __restrict__ w3, float* __restrict__ colsum) {
  if (blockIdx.x >= SEQ / 4) {
    __shared__ float red[32 * 64];
    const int tid = threadIdx.x;
    const int cg = blockIdx.x - SEQ / 4;
    const u16* W; int N, c0;
    if (cg < 48) { W = wqkv; N = 3 * DM; c0 = cg * 64; }
    else if (cg < 64) { W = w1; N = DM; c0 = (cg - 48) * 64; }
    else { W = w3; N = FF; c0 = (cg - 64) * 64; }
    const int c = tid & 7, rg = tid >> 3;
    f32x2 s[4] = {{0.f, 0.f}, {0.f, 0.f}, {0.f, 0.f}, {0.f, 0.f}};
#pragma unroll 16
    for (int i = 0; i < 32; ++i) {
      const u32x4 v = *(const u32x4*)(W + (size_t)(rg + i * 32) * N + c0 + c * 8);
#pragma unroll
      for (int e = 0; e < 4; ++e) s[e] += unpk(v[e]);
    }
#pragma unroll
    for (int e = 0; e < 4; ++e) *(f32x2*)(red + rg * 64 + c * 8 + 2 * e) = s[e];
    __syncthreads();
    if (tid < 64) {
      float t = 0.f;
#pragma unroll
      for (int i = 0; i < 32; ++i) t += red[i * 64 + tid];
      colsum[cg * 64 + tid] = t;
    }
    return;
  }
  const int lane = threadIdx.x & 63, row = blockIdx.x * 4 + (threadIdx.x >> 6);
  const u16* src = x + (size_t)row * DM;
  const u32x4 a = *(const u32x4*)(src + lane * 8), b = *(const u32x4*)(src + 512 + lane * 8);
  float v[16];
#pragma unroll
  for (int e = 0; e < 4; ++e) { v[2 * e] = bflo(a[e]); v[2 * e + 1] = bfhi(a[e]); v[8 + 2 * e] = bflo(b[e]); v[9 + 2 * e] = bfhi(b[e]); }
  float s = 0.f;
#pragma unroll
  for (int e = 0; e < 16; ++e) s += v[e];
#pragma unroll
  for (int o = 1; o < 64; o <<= 1) s += __shfl_xor(s, o);
  const float mean = s * (1.0f / 1024.0f);
  float m2 = 0.f;
#pragma unroll
  for (int e = 0; e < 16; ++e) { const float d = v[e] - mean; m2 += d * d; }
#pragma unroll
  for (int o = 1; o < 64; o <<= 1) m2 += __shfl_xor(m2, o);
  const RowP r = row_params(mean, m2 * (1.0f / 1024.0f), 1);
  u32x4 oa, ob;
#pragma unroll
  for (int e = 0; e < 4; ++e) {
    oa[e] = pk2(rbf(v[2 * e] - r.mhat) / r.dhat, rbf(v[2 * e + 1] - r.mhat) / r.dhat);
    ob[e] = pk2(rbf(v[8 + 2 * e] - r.mhat) / r.dhat, rbf(v[9 + 2 * e] - r.mhat) / r.dhat);
  }
  u16* dst = y + (size_t)row * DM;
  *(u32x4*)(dst + lane * 8) = oa;
  *(u32x4*)(dst + 512 + lane * 8) = ob;
}

__global__ __launch_bounds__(512) void attn_o_k(const u16* __restrict__ q, const u16* __restrict__ wphi, const float* __restrict__ kvpart,
                                                u16* __restrict__ o) {
  __shared__ __attribute__((aligned(16))) char sm[4352];
  const int tid = threadIdx.x, lane = tid & 63, w = tid >> 6;
  const int h = blockIdx.x & 31, rb = blockIdx.x >> 5;
  const int g4 = lane >> 4, r0 = rb * 256 + w * 32 + (lane & 15);
  bf16x8 qf[2], wf[2];
#pragma unroll
  for (int mb = 0; mb < 2; ++mb) qf[mb] = *(const bf16x8*)(q + (size_t)(r0 + mb * 16) * DM + h * 32 + g4 * 8);
#pragma unroll
  for (int fb = 0; fb < 2; ++fb)
#pragma unroll
    for (int j = 0; j < 8; ++j) wf[fb][j] = (short)wphi[h * 1024 + (g4 * 8 + j) * 32 + fb * 16 + (lane & 15)];
  f32x4 kp[16], kz[16];
  const float* slab = kvpart + (size_t)h * (33 * 64) + tid * 4;
#pragma unroll
  for (int p = 0; p < 16; ++p) kp[p] = *(const f32x4*)(slab + (size_t)p * NH * (33 * 64));
  if (tid < 16) {
#pragma unroll
    for (int p = 0; p < 16; ++p) kz[p] = *(const f32x4*)(slab + (size_t)p * NH * (33 * 64) + 2048);
  }
  bf16x8 pq[2][2];
#pragma unroll
  for (int mb = 0; mb < 2; ++mb) {
    u32x4 pos, neg;
    feature_map(wf, qf[mb], pos, neg);
    pq[mb][0] = __builtin_bit_cast(bf16x8, pos);
    pq[mb][1] = __builtin_bit_cast(bf16x8, neg);
  }
  u16* kvt = (u16*)sm;
  {
    f32x4 t = kp[0];
#pragma unroll
    for (int p = 1; p < 16; ++p) t += kp[p];
    *(u32x2*)(kvt + tid * 4) = (u32x2){pk2(t[0], t[1]), pk2(t[2], t[3])};
    if (tid < 16) {
      f32x4 z = kz[0];
#pragma unroll
      for (int p = 1; p < 16; ++p) z += kz[p];
      *(u32x2*)(kvt + 2048 + tid * 4) = (u32x2){pk2(z[0], z[1]), pk2(z[2], z[3])};
    }
  }
  __syncthreads();
  bf16x8 kf[3][2];
#pragma unroll
  for (int db = 0; db < 3; ++db)
#pragma unroll
    for (int ks = 0; ks < 2; ++ks) {
      const int d = (db < 2) ? db * 16 + (lane & 15) : 32;
      const u16* p = kvt + d * 64 + ks * 32 + g4 * 4;
      kf[db][ks] = cat8(*(const s16x4*)p, *(const s16x4*)(p + 16));
    }
#pragma unroll
  for (int mb = 0; mb < 2; ++mb) {
    f32x4 a[3];
#pragma unroll
    for (int db = 0; db < 3; ++db) {
      a[db] = mfma16(kf[db][0], pq[mb][0], (f32x4){0.f, 0.f, 0.f, 0.f});
      a[db] = mfma16(kf[db][1], pq[mb][1], a[db]);
    }
    const float id = __builtin_amdgcn_rcpf(rbf(rbf(a[2][0]) + 1e-6f));
    u16* dst = o + (size_t)(r0 + mb * 16) * DM + h * 32 + g4 * 4;
#pragma unroll
    for (int db = 0; db < 2; ++db) {
      const f32x2 lo = rbf2((f32x2){a[db][0], a[db][1]}) * id, hi = rbf2((f32x2){a[db][2], a[db][3]}) * id;
      *(u32x2*)(dst + db * 16) = (u32x2){pk2(lo[0], lo[1]), pk2(hi[0], hi[1])};
    }
  }
}

template <int BN, int S, int KD, int EPI> static constexpr int smem_bytes() {
  constexpr bool USE_STATS = (EPI == EPI_QKV || EPI == EPI_GELU_SHIFT || EPI == EPI_W2 || EPI == EPI_GELU);
  constexpr int NRES = (EPI == EPI_RES || EPI == EPI_W2) ? 1 : (EPI == EPI_W4 ? 2 : 0);
  return S * (16384 + BN * 128) + NRES * 16384 + (EPI == EPI_QKV ? 4096 : 0) + 2048 + 1024;
}
template <int BN, int S, int KD, int EPI> static void launch_gemm(const GemmArgs& a, hipStream_t st) {
  constexpr int sb = smem_bytes<BN, S, KD, EPI>();
  static_assert(sb <= 163840, "LDS");
  (void)hipFuncSetAttribute((const void*)gemm_k<BN, S, KD, EPI>, hipFuncAttributeMaxDynamicSharedMemorySize, sb);
  gemm_k<BN, S, KD, EPI><<<256, 512, sb, st>>>(a);
}

extern "C" void kernel_launch(void* const* d_in, const int* in_sizes, int n_in, void* d_out, int out_size, void* d_ws,
                              size_t ws_size, hipStream_t stream) {
  const u16* x0 = (const u16*)d_in[0];
  const u16* wqkv = (const u16*)d_in[1];
  const u16* wphi = (const u16*)d_in[2];
  const u16* wo = (const u16*)d_in[3];
  const u16* w1 = (const u16*)d_in[5];
  const u16* w2 = (const u16*)d_in[6];
  const u16* w3 = (const u16*)d_in[7];
  const u16* w4 = (const u16*)d_in[8];
  char* ws = (char*)d_ws;
  const size_t MB = 1 << 20;
  u16* xln0 = (u16*)(ws + 0 * MB);
  u16* phiq = (u16*)(ws + 4 * MB);
  float* kvpart = (float*)(ws + 12 * MB);
  u16* obuf = (u16*)(ws + 17 * MB);
  u16* xa = (u16*)(ws + 21 * MB);
  u16* hsh = (u16*)(ws + 25 * MB);
  u16* x1 = (u16*)(ws + 29 * MB);
  u16* gbuf = (u16*)(ws + 33 * MB);
  u16* xb = (u16*)(ws + 48 * MB);
  float* st_xa = (float*)(ws + 52 * MB);
  float* st_x1 = (float*)(ws + 53 * MB);
  float* st_xb = (float*)(ws + 54 * MB);
  float* colsum = (float*)(ws + 55 * MB);

  ln0_k<<<SEQ / 4 + 120, 256, 0, stream>>>(x0, xln0, wqkv, w1, w3, colsum);
  for (int layer = 0; layer < 2; ++layer) {
    const u16* xin = layer == 0 ? x0 : xb;
    u16* xout = layer == 0 ? xb : (u16*)d_out;
    GemmArgs a;
    a = GemmArgs{layer == 0 ? xln0 : xb, wqkv, phiq, nullptr, nullptr, st_xb, nullptr, colsum, wphi, kvpart, layer == 0 ? 0 : 1, 0};
    launch_gemm<192, 3, 1024, EPI_QKV>(a, stream);
    attn_o_k<<<256, 512, 0, stream>>>(phiq, wphi, kvpart, obuf);
    a = GemmArgs{obuf, wo, xa, xin, nullptr, nullptr, st_xa, nullptr, nullptr, nullptr, 0, 0};
    launch_gemm<64, 5, 1024, EPI_RES>(a, stream);
    a = GemmArgs{xa, w1, hsh, nullptr, nullptr, st_xa, nullptr, colsum + 3072, nullptr, nullptr, 2, 0};
    launch_gemm<64, 5, 1024, EPI_GELU_SHIFT>(a, stream);
    a = GemmArgs{hsh, w2, x1, xa, nullptr, st_xa, st_x1, nullptr, nullptr, nullptr, 1, 0};
    launch_gemm<64, 5, 1024, EPI_W2>(a, stream);
    a = GemmArgs{x1, w3, gbuf, nullptr, nullptr, st_x1, nullptr, colsum + 4096, nullptr, nullptr, 1, 0};
    launch_gemm<224, 3, 1024, EPI_GELU>(a, stream);
    a = GemmArgs{gbuf, w4, xout, xa, x1, nullptr, st_xb, nullptr, nullptr, nullptr, 0, 0};
    launch_gemm<64, 5, 3584, EPI_W4>(a, stream);
  }
}
```

```cpp
#include <hip/hip_runtime.h>
#include <hip/hip_bf16.h>
#include <stdint.h>

typedef __attribute__((ext_vector_type(8))) short bf16x8;
typedef __attribute__((ext_vector_type(4))) short s16x4;
typedef __attribute__((ext_vector_type(4))) float f32x4;
typedef __attribute__((ext_vector_type(2))) float f32x2;
typedef __attribute__((ext_vector_type(4))) unsigned u32x4;
typedef __attribute__((ext_vector_type(2))) unsigned u32x2;
typedef __attribute__((ext_vector_type(2))) __bf16 bf16x2_t;
typedef unsigned short u16;
#define LDS_AS __attribute__((address_space(3)))
#define DI __device__ __forceinline__

static constexpr int SEQ = 2048, DM = 1024, NH = 32, FF = 3584;

DI float bf2f(unsigned v) { return __uint_as_float(v << 16); }
DI float bflo(unsigned v) { return __uint_as_float(v << 16); }
DI float bfhi(unsigned v) { return __uint_as_float(v & 0xffff0000u); }
DI unsigned pk2(float a, float b) {
  f32x2 v = {a, b};
  return __builtin_bit_cast(unsigned, __builtin_convertvector(v, bf16x2_t));
}
DI float rbf(float a) { return bflo(pk2(a, a)); }

template <int I> struct IC { static constexpr int value = I; };
template <int B, int E, class F> DI void static_for(F&& f) {
  if constexpr (B < E) { f(IC<B>{}); static_for<B + 1, E>(f); }
}
template <int N> DI void wait_vm() { asm volatile("s_waitcnt vmcnt(%0)" ::"n"(N) : "memory"); }
DI void wait_lgkm0() { asm volatile("s_waitcnt lgkmcnt(0)" ::: "memory"); }
DI void bar() { __builtin_amdgcn_s_barrier(); }
DI void glds16(const void* src, char* lds_dst) {
  __builtin_amdgcn_global_load_lds((const unsigned*)src, (unsigned*)lds_dst, 16, 0, 0);
}
DI s16x4 trread(const char* p) {
  return __builtin_bit_cast(s16x4, __builtin_amdgcn_ds_read_tr16_b64_v4i16((LDS_AS s16x4*)p));
}
template <int OFF> DI s16x4 lds_tr(unsigned addr) {
  s16x4 r; asm volatile("ds_read_b64_tr_b16 %0, %1 offset:%2" : "=v"(r) : "v"(addr), "n"(OFF) : "memory"); return r;
}
template <int OFF> DI bf16x8 lds_b128(unsigned addr) {
  bf16x8 r; asm volatile("ds_read_b128 %0, %1 offset:%2" : "=v"(r) : "v"(addr), "n"(OFF) : "memory"); return r;
}
DI bf16x8 cat8(s16x4 lo, s16x4 hi) { return (bf16x8){lo[0], lo[1], lo[2], lo[3], hi[0], hi[1], hi[2], hi[3]}; }
DI f32x4 mfma16(bf16x8 a, bf16x8 b, f32x4 c) { return __builtin_amdgcn_mfma_f32_16x16x32_bf16(a, b, c, 0, 0, 0); }

DI f32x2 gelu2(f32x2 x) {
  const float c0 = -2.302208198f, c1 = -2.302208198f * 0.044715f;
  const f32x2 a = x * (x * x * c1 + c0);
  const f32x2 d = (f32x2){__builtin_amdgcn_exp2f(a[0]), __builtin_amdgcn_exp2f(a[1])} + 1.0f;
  return x * (f32x2){__builtin_amdgcn_rcpf(d[0]), __builtin_amdgcn_rcpf(d[1])};
}
DI f32x2 rbf2(f32x2 v) { const unsigned p = pk2(v[0], v[1]); return (f32x2){bflo(p), bfhi(p)}; }
DI f32x2 unpk(unsigned p) { return (f32x2){bflo(p), bfhi(p)}; }

DI void feature_map(const bf16x8 (&wf)[2], bf16x8 xf, u32x4& pos, u32x4& neg) {
  f32x2 p[4];
#pragma unroll
  for (int fb = 0; fb < 2; ++fb) {
    const f32x4 d = mfma16(wf[fb], xf, (f32x4){0.f, 0.f, 0.f, 0.f});
    p[fb * 2] = rbf2((f32x2){d[0], d[1]});
    p[fb * 2 + 1] = rbf2((f32x2){d[2], d[3]});
  }
  float mx = fmaxf(p[0][0], p[0][1]), mn = fminf(p[0][0], p[0][1]);
#pragma unroll
  for (int e = 1; e < 4; ++e) { mx = fmaxf(mx, fmaxf(p[e][0], p[e][1])); mn = fminf(mn, fminf(p[e][0], p[e][1])); }
  mx = fmaxf(mx, __shfl_xor(mx, 16)); mx = fmaxf(mx, __shfl_xor(mx, 32));
  mn = fminf(mn, __shfl_xor(mn, 16)); mn = fminf(mn, __shfl_xor(mn, 32));
  const float l2e = 1.4426950408889634f;
  f32x2 ep[4], en[4], sp2 = {0.f, 0.f}, sn2 = {0.f, 0.f};
#pragma unroll
  for (int e = 0; e < 4; ++e) {
    const f32x2 a = (p[e] - mx) * l2e, b = (mn - p[e]) * l2e;
    ep[e] = (f32x2){__builtin_amdgcn_exp2f(a[0]), __builtin_amdgcn_exp2f(a[1])}; sp2 += ep[e];
    en[e] = (f32x2){__builtin_amdgcn_exp2f(b[0]), __builtin_amdgcn_exp2f(b[1])}; sn2 += en[e];
  }
  float sp = sp2[0] + sp2[1], sn = sn2[0] + sn2[1];
  sp += __shfl_xor(sp, 16); sp += __shfl_xor(sp, 32);
  sn += __shfl_xor(sn, 16); sn += __shfl_xor(sn, 32);
  const float ip = __builtin_amdgcn_rcpf(sp), in_ = __builtin_amdgcn_rcpf(sn);
#pragma unroll
  for (int e = 0; e < 4; ++e) {
    const f32x2 a = ep[e] * ip, b = en[e] * in_;
    pos[e] = pk2(a[0], a[1]); neg[e] = pk2(b[0], b[1]);
  }
}

#define TS(i)
struct GemmArgs {
  const u16* A;
  const u16* B;
  u16* out;
  const u16* res1;
  const u16* res2;
  const float* stats_in;
  float* stats_out;
  const float* colsum;
  const u16* wphi;
  float* kvpart;
  int ln_mode;
  int unused;
};

enum { EPI_QKV = 0, EPI_RES = 1, EPI_GELU_SHIFT = 2, EPI_W2 = 3, EPI_GELU = 4, EPI_W4 = 5 };

struct RowP { float alpha, beta, mhat, dhat; };
DI RowP row_params(float mean, float var, int mode) {
  RowP r;
  const float mh = rbf(mean);
  const float dh = rbf(__builtin_sqrtf(rbf(rbf(var) + 1e-5f)));
  r.mhat = mh; r.dhat = dh;
  if (mode == 2) {
    const float m2 = (mean - mh) / dh;
    const float v2 = var / (dh * dh);
    const float mh2 = rbf(m2);
    const float dh2 = rbf(__builtin_sqrtf(rbf(rbf(v2) + 1e-5f)));
    r.alpha = 1.0f / (dh * dh2);
    r.beta = mh / (dh * dh2) + mh2 / dh2;
  } else {
    r.alpha = 1.0f / dh;
    r.beta = mh / dh;
  }
  return r;
}

template <int BN, int S, int KD, int EPI>
__global__ __launch_bounds__(512, 2) void gemm_k(GemmArgs g) {
  constexpr int NK = KD / 64;
  constexpr int NPF = BN / 64;
  constexpr bool HALF = (BN % 64) != 0;
  constexpr int NI = BN / 32;
  constexpr int A_BYTES = 16384;
  constexpr int STAGE = A_BYTES + BN * 128;
  constexpr int L = 4 + NPF * 2 + (HALF ? 1 : 0);
  constexpr int NTOT = (EPI == EPI_QKV) ? 3 * DM : (EPI == EPI_GELU ? FF : DM);
  constexpr bool USE_STATS = (EPI == EPI_QKV || EPI == EPI_GELU_SHIFT || EPI == EPI_W2 || EPI == EPI_GELU);
  constexpr bool FOLD = (EPI == EPI_QKV || EPI == EPI_GELU_SHIFT || EPI == EPI_GELU);
  constexpr int NRES = (EPI == EPI_RES || EPI == EPI_W2) ? 1 : (EPI == EPI_W4 ? 2 : 0);
  constexpr int OFF_STATS = S * STAGE;
  constexpr int OFF_RES = OFF_STATS;
  constexpr int OFF_WPHI = OFF_RES + NRES * 16384;
  constexpr int OFF_ROWP = OFF_WPHI + (EPI == EPI_QKV ? 4096 : 0);
  constexpr int OFF_CS = OFF_ROWP + 2048;
  constexpr int NEXTRA = NRES * 4 + (EPI == EPI_QKV ? 1 : 0) + (FOLD ? 1 : 0);

  extern __shared__ __attribute__((aligned(16))) char smem[];
  const int tid = threadIdx.x, lane = tid & 63;
  const int w = __builtin_amdgcn_readfirstlane(tid >> 6);
  const bool prod = w >= 4;
  const int cw = w & 3, wr = cw >> 1, wc = cw & 1;

  const int b = blockIdx.x, xcd = b & 7, j = b >> 3;
  int rp, ct;
  if constexpr (EPI == EPI_QKV || EPI == EPI_GELU) { rp = (xcd >> 2) * 8 + (j >> 2); ct = (xcd & 3) * 4 + (j & 3); }
  else { rp = (xcd >> 1) * 4 + (j >> 3); ct = (xcd & 1) * 8 + (j & 7); }
  const int m0 = rp * 128;
  const int n0 = (EPI == EPI_QKV) ? ct * 64 : ct * BN;
  constexpr int PSTRIDE = (EPI == EPI_QKV) ? DM : 64;
  const int g4 = lane >> 4, q4 = (lane >> 2) & 3, pp = lane & 3;
  RowP* rowp = (RowP*)(smem + OFF_ROWP);
  const bool ln_on = USE_STATS && (g.ln_mode != 0 || EPI == EPI_W2);

  f32x4 acc[4][NI];
  TS(0);
  if (prod) {
    const u16* a_src = g.A + (size_t)(m0 + cw * 32 + (lane >> 3)) * KD + (((lane & 7) ^ (lane >> 3)) * 8);
    const u16* b_src[2];
#pragma unroll
    for (int e = 0; e < 2; ++e) {
      const int r = lane >> 3;
      const int f = ((r >> 1) & 1) | (e << 1);
      const int c = ((((lane & 7) >> 1) ^ f) << 1) | (lane & 1);
      b_src[e] = g.B + (size_t)((cw * 2 + e) * 8 + r) * NTOT + n0 + c * 8;
    }
    const u16* bh_src = nullptr;
    if constexpr (HALF) {
      const int c = ((((lane >> 1) & 1) ^ ((lane >> 5) & 1)) << 1) | (lane & 1);
      bh_src = g.B + (size_t)(cw * 16 + (lane >> 2)) * NTOT + n0 + NPF * 64 + c * 8;
    }
    auto issue = [&](int kt, int slot) {
      char* base = smem + slot * STAGE;
#pragma unroll
      for (int p = 0; p < NPF; ++p)
#pragma unroll
        for (int e = 0; e < 2; ++e)
          glds16(b_src[e] + (size_t)kt * 64 * NTOT + p * PSTRIDE, base + A_BYTES + p * 8192 + (cw * 2 + e) * 1024);
      if constexpr (HALF) glds16(bh_src + (size_t)kt * 64 * NTOT, base + A_BYTES + NPF * 8192 + cw * 1024);
#pragma unroll
      for (int e = 0; e < 4; ++e) glds16(a_src + (size_t)e * 8 * KD + kt * 64, base + (cw * 4 + e) * 1024);
    };
    TS(6);
    static_for<0, S>([&](auto ic) { issue(ic.value, ic.value); });
    TS(7);
    if constexpr (NRES >= 1) {
#pragma unroll
      for (int e = 0; e < 4; ++e) {
        const int r = (cw * 4 + e) * 8 + (lane >> 3);
        glds16(g.res1 + (size_t)(m0 + r) * DM + n0 + (lane & 7) * 8, smem + OFF_RES + (cw * 4 + e) * 1024);
      }
    }
    if constexpr (NRES >= 2) {
#pragma unroll
      for (int e = 0; e < 4; ++e) {
        const int r = (cw * 4 + e) * 8 + (lane >> 3);
        glds16(g.res2 + (size_t)(m0 + r) * DM + n0 + (lane & 7) * 8, smem + OFF_RES + 16384 + (cw * 4 + e) * 1024);
      }
    }
    if constexpr (EPI == EPI_QKV) glds16(g.wphi + (size_t)ct * 2048 + cw * 512 + lane * 8, smem + OFF_WPHI + cw * 1024);
    if constexpr (FOLD) {
      int col = cw * 64 + (lane & 15) * 4;
      if (col > BN - 4) col = BN - 4;
      const int gcol = (EPI == EPI_QKV) ? (col >> 6) * DM + n0 + (col & 63) : n0 + col;
      if (lane < 16) glds16(g.colsum + gcol, smem + OFF_CS + cw * 256);
    }
    wait_vm<(S - 1) * L + NEXTRA>();
    TS(1);
    bar();
    static_for<0, NK - 1>([&](auto ic) {
      constexpr int kt = ic.value;
      constexpr int young = (S - 2 < NK - 2 - kt) ? S - 2 : NK - 2 - kt;
      constexpr int ex = (kt + 1 <= S - 1 && kt + 1 < NK - 1) ? NEXTRA : 0;
      wait_vm<young * L + ex>();
      bar();
      if constexpr (kt + S < NK) issue(kt + S, kt % S);
    });
    TS(2);
    bar();
  } else {
    const int fsw = ((q4 >> 1) & 1) | ((g4 & 1) << 1);
    int a_off[2], b_off[2], bh_off = 0;
#pragma unroll
    for (int k = 0; k < 2; ++k) {
      a_off[k] = (wr * 64 + (lane & 15)) * 128 + (((k * 4 + g4) ^ (lane & 7)) * 16);
      b_off[k] = A_BYTES + (8 * g4 + q4) * 128 + (((2 * k + wc) ^ fsw) * 32) + pp * 8;
    }
    if constexpr (HALF) bh_off = A_BYTES + NPF * 8192 + (8 * g4 + q4) * 64 + ((wc ^ (g4 & 1)) * 32) + pp * 8;
#pragma unroll
    for (int m = 0; m < 4; ++m)
#pragma unroll
      for (int i = 0; i < NI; ++i) acc[m][i] = (f32x4){0.f, 0.f, 0.f, 0.f};
    bf16x8 xa[2][4];
    s16x4 wlo[2][NI], whi[2][NI];
    const unsigned lbase = (unsigned)(size_t)smem;

    auto ldfrag = [&](auto setc, auto slotc, auto kc) {
      constexpr int set = decltype(setc)::value, slot = decltype(slotc)::value, k32 = decltype(kc)::value;
      const unsigned sb = lbase + slot * STAGE;
      const unsigned ba0 = sb + b_off[0], ba1 = sb + b_off[1], bah = sb + bh_off, aa = sb + a_off[k32];
      static_for<0, NI>([&](auto ic) {
        constexpr int i = decltype(ic)::value;
        if constexpr (HALF && i == NI - 1) {
          wlo[set][i] = lds_tr<k32 * 2048>(bah);
          whi[set][i] = lds_tr<k32 * 2048 + 256>(bah);
        } else {
          wlo[set][i] = lds_tr<(i >> 1) * 8192 + k32 * 4096>((i & 1) ? ba1 : ba0);
          whi[set][i] = lds_tr<(i >> 1) * 8192 + k32 * 4096 + 512>((i & 1) ? ba1 : ba0);
        }
      });
      static_for<0, 4>([&](auto mc) { constexpr int m = decltype(mc)::value; xa[set][m] = lds_b128<m * 2048>(aa); });
    };
    auto wait_set = [&](auto setc) {
      constexpr int set = decltype(setc)::value;
      asm volatile("s_waitcnt lgkmcnt(0)" : "+v"(xa[set][0]), "+v"(xa[set][1]), "+v"(xa[set][2]), "+v"(xa[set][3]) :: "memory");
#pragma unroll
      for (int i = 0; i < NI; ++i) asm volatile("" : "+v"(wlo[set][i]), "+v"(whi[set][i]));
      __builtin_amdgcn_sched_barrier(0);
    };
    auto ld1 = [&](auto setc, auto slotc, auto kc, auto rc) {
      constexpr int set = decltype(setc)::value, slot = decltype(slotc)::value, k32 = decltype(kc)::value, r = decltype(rc)::value;
      const unsigned sb = lbase + slot * STAGE;
      if constexpr (r < 2 * NI) {
        constexpr int i = r >> 1, hi = r & 1;
        s16x4 v;
        if constexpr (HALF && i == NI - 1) v = lds_tr<k32 * 2048 + hi * 256>(sb + bh_off);
        else v = lds_tr<(i >> 1) * 8192 + k32 * 4096 + hi * 512>(sb + b_off[i & 1]);
        if constexpr (hi) whi[set][i] = v; else wlo[set][i] = v;
      } else {
        constexpr int m = r - 2 * NI;
        xa[set][m] = lds_b128<m * 2048>(sb + a_off[k32]);
      }
    };
    auto mma_ld = [&](auto setc, auto ldc, auto slotc, auto kc) {
      constexpr int set = decltype(setc)::value;
      constexpr bool ld = decltype(ldc)::value != 0;
      static_for<0, 4 * NI>([&](auto tc) {
        constexpr int t = decltype(tc)::value, i = t >> 2, m = t & 3;
        acc[m][i] = mfma16(cat8(wlo[set][i], whi[set][i]), xa[set][m], acc[m][i]);
        if constexpr (ld && t < 2 * NI + 4) ld1(IC<set ^ 1>{}, slotc, kc, tc);
        __builtin_amdgcn_sched_barrier(0);
      });
    };

    if constexpr (USE_STATS) {
      if (ln_on) {
        const int r = cw * 32 + (lane >> 1), hf = lane & 1;
        f32x2 v[8];
#pragma unroll
        for (int p = 0; p < 8; ++p) v[p] = *(const f32x2*)(g.stats_in + ((size_t)(hf * 8 + p) * SEQ + m0 + r) * 2);
        float s = 0.f;
#pragma unroll
        for (int p = 0; p < 8; ++p) s += v[p][0];
        s += __shfl_xor(s, 1);
        const float mean = s * (1.0f / 1024.0f);
        float m2 = 0.f;
#pragma unroll
        for (int p = 0; p < 8; ++p) { const float d = v[p][0] * (1.0f / 64.0f) - mean; m2 += v[p][1] + 64.0f * d * d; }
        m2 += __shfl_xor(m2, 1);
        if (hf == 0) rowp[r] = row_params(mean, m2 * (1.0f / 1024.0f), EPI == EPI_W2 ? 1 : g.ln_mode);
      }
    }
    bar();
    TS(1);
    ldfrag(IC<0>{}, IC<0>{}, IC<0>{});
    static_for<0, NK>([&](auto ic) {
      constexpr int kt = ic.value;
      constexpr int slot = kt % S;
      wait_set(IC<0>{});
      mma_ld(IC<0>{}, IC<1>{}, IC<slot>{}, IC<1>{});
      wait_set(IC<1>{});
      if constexpr (kt + 1 < NK) {
        bar();
        __builtin_amdgcn_sched_barrier(0);
        mma_ld(IC<1>{}, IC<1>{}, IC<(kt + 1) % S>{}, IC<0>{});
      } else {
        mma_ld(IC<1>{}, IC<0>{}, IC<0>{}, IC<0>{});
      }
    });
    TS(2);
    bar();
  }
  TS(3);

  constexpr int TSTR = BN * 2 + 16;
  char* T = smem;
  if (!prod) {
    float al[4] = {1.f, 1.f, 1.f, 1.f}, be[4] = {0.f, 0.f, 0.f, 0.f};
    if constexpr (FOLD) {
      if (ln_on) {
#pragma unroll
        for (int m = 0; m < 4; ++m) { const RowP r = rowp[wr * 64 + m * 16 + (lane & 15)]; al[m] = r.alpha; be[m] = r.beta; }
      }
    }
#pragma unroll
    for (int i = 0; i < NI; ++i) {
      const int nb = 2 * i + wc;
      const int ncol = nb * 16 + g4 * 4;
      f32x4 cs = {0.f, 0.f, 0.f, 0.f};
      if constexpr (FOLD) {
        if (ln_on) cs = *(const f32x4*)(smem + OFF_CS + ncol * 4);
      }
#pragma unroll
      for (int m = 0; m < 4; ++m) {
        u32x2 pkd;
#pragma unroll
        for (int h = 0; h < 2; ++h) {
          f32x2 v = {acc[m][i][2 * h], acc[m][i][2 * h + 1]};
          if constexpr (FOLD) v = v * al[m] - (f32x2){cs[2 * h], cs[2 * h + 1]} * be[m];
          if constexpr (EPI == EPI_GELU_SHIFT || EPI == EPI_GELU) v = gelu2(rbf2(v));
          pkd[h] = pk2(v[0], v[1]);
        }
        *(u32x2*)(T + (wr * 64 + m * 16 + (lane & 15)) * TSTR + ncol * 2) = pkd;
      }
    }
  }
  __syncthreads();
  TS(4);
  if constexpr (EPI == EPI_GELU) {
    constexpr int CH = BN / 8;
#pragma unroll
    for (int it = 0; it < (128 * CH) / 512; ++it) {
      const int idx = it * 512 + tid, r = idx / CH, c = idx % CH;
      const u32x4 v = *(const u32x4*)(T + r * TSTR + c * 16);
      *(u32x4*)(g.out + (size_t)(m0 + r) * FF + n0 + c * 8) = v;
    }
  } else if constexpr (EPI == EPI_GELU_SHIFT) {
    const int grp = n0 >> 8;
    const int sh = (grp == 0) ? 1 : (grp == 1) ? -1 : (grp == 2) ? 2 : -2;
#pragma unroll
    for (int it = 0; it < 2; ++it) {
      const int r = it * 64 + (tid >> 3), c = tid & 7;
      const u32x4 v = *(const u32x4*)(T + r * TSTR + c * 16);
      const int dr = m0 + r + sh;
      if (dr >= 0 && dr < SEQ) *(u32x4*)(g.out + (size_t)dr * DM + n0 + c * 8) = v;
    }
    if (tid < 16) {
      const int zr = tid >> 3, c = tid & 7;
      const int az = sh > 0 ? sh : -sh;
      if (zr < az) {
        if (sh > 0 && rp == 0) *(u32x4*)(g.out + (size_t)zr * DM + n0 + c * 8) = (u32x4){0u, 0u, 0u, 0u};
        if (sh < 0 && rp == 15) *(u32x4*)(g.out + (size_t)(SEQ - 1 - zr) * DM + n0 + c * 8) = (u32x4){0u, 0u, 0u, 0u};
      }
    }
  } else if constexpr (EPI == EPI_RES || EPI == EPI_W2 || EPI == EPI_W4) {
#pragma unroll
    for (int it = 0; it < 2; ++it) {
      const int r = it * 64 + (tid >> 3), c = tid & 7;
      const u32x4 v = *(const u32x4*)(T + r * TSTR + c * 16);
      const u32x4 x = *(const u32x4*)(smem + OFF_RES + r * 128 + c * 16);
      f32x2 y[4];
      if constexpr (EPI == EPI_RES) {
#pragma unroll
        for (int e = 0; e < 4; ++e) y[e] = unpk(x[e]) + unpk(v[e]);
      } else if constexpr (EPI == EPI_W2) {
        const RowP rr = rowp[r];
#pragma unroll
        for (int e = 0; e < 4; ++e) y[e] = rbf2(rbf2(unpk(x[e]) - rr.mhat) * rr.alpha) + unpk(v[e]);
      } else {
        const u32x4 x1 = *(const u32x4*)(smem + OFF_RES + 16384 + r * 128 + c * 16);
#pragma unroll
        for (int e = 0; e < 4; ++e) y[e] = unpk(x[e]) + rbf2(unpk(x1[e]) + unpk(v[e]));
      }
      u32x4 o;
#pragma unroll
      for (int e = 0; e < 4; ++e) { o[e] = pk2(y[e][0], y[e][1]); y[e] = unpk(o[e]); }
      *(u32x4*)(g.out + (size_t)(m0 + r) * DM + n0 + c * 8) = o;
      const f32x2 s2 = (y[0] + y[1]) + (y[2] + y[3]);
      float s = s2[0] + s2[1];
      s += __shfl_xor(s, 1); s += __shfl_xor(s, 2); s += __shfl_xor(s, 4);
      const float mc = s * (1.0f / 64.0f);
      f32x2 q2 = {0.f, 0.f};
#pragma unroll
      for (int e = 0; e < 4; ++e) { const f32x2 d = y[e] - mc; q2 += d * d; }
      float m2 = q2[0] + q2[1];
      m2 += __shfl_xor(m2, 1); m2 += __shfl_xor(m2, 2); m2 += __shfl_xor(m2, 4);
      if (c == 0) *(f32x2*)(g.stats_out + ((size_t)ct * SEQ + m0 + r) * 2) = (f32x2){s, m2};
    }
  } else {
    constexpr int PKSTR = 144;
    char* PK = smem + 128 * TSTR;
#pragma unroll
    for (int it = 0; it < 2; ++it) {
      const int r = it * 64 + (tid >> 3), c = tid & 7;
      *(u32x4*)(g.out + (size_t)(m0 + r) * DM + n0 + c * 8) = *(const u32x4*)(T + r * TSTR + c * 16);
    }
    {
      const int hh = w & 1, rq = w >> 1;
      bf16x8 wf[2];
#pragma unroll
      for (int fb = 0; fb < 2; ++fb) {
        const char* p = smem + OFF_WPHI + hh * 2048 + (8 * g4 + q4) * 64 + fb * 32 + pp * 8;
        wf[fb] = cat8(trread(p), trread(p + 256));
      }
#pragma unroll
      for (int mb = 0; mb < 2; ++mb) {
        const int row = rq * 32 + mb * 16 + (lane & 15);
        const bf16x8 xf = *(const bf16x8*)(T + row * TSTR + (64 + hh * 32 + g4 * 8) * 2);
        u32x4 pos, neg;
        feature_map(wf, xf, pos, neg);
        char* dst = PK + (hh * 128 + row) * PKSTR + g4 * 8;
        *(u32x2*)dst = (u32x2){pos[0], pos[1]}; *(u32x2*)(dst + 32) = (u32x2){pos[2], pos[3]};
        *(u32x2*)(dst + 64) = (u32x2){neg[0], neg[1]}; *(u32x2*)(dst + 96) = (u32x2){neg[2], neg[3]};
      }
    }
    __syncthreads();
    {
      const int hh = w >> 2, fa = w & 3;
      f32x4 kv[3];
#pragma unroll
      for (int d = 0; d < 3; ++d) kv[d] = (f32x4){0.f, 0.f, 0.f, 0.f};
      const short one = (short)0x3F80;
      const bf16x8 ones = {one, one, one, one, one, one, one, one};
#pragma unroll
      for (int ks = 0; ks < 4; ++ks) {
        const int row = ks * 32 + 8 * g4 + q4;
        bf16x8 vf[2];
#pragma unroll
        for (int d = 0; d < 2; ++d) {
          const char* p = T + row * TSTR + (128 + hh * 32 + d * 16) * 2 + pp * 8;
          vf[d] = cat8(trread(p), trread(p + 4 * TSTR));
        }
        const char* p = PK + (hh * 128 + row) * PKSTR + (fa * 16) * 2 + pp * 8;
        const bf16x8 pf = cat8(trread(p), trread(p + 4 * PKSTR));
        kv[0] = mfma16(pf, vf[0], kv[0]);
        kv[1] = mfma16(pf, vf[1], kv[1]);
        kv[2] = mfma16(pf, ones, kv[2]);
      }
      float* slab = g.kvpart + ((size_t)rp * NH + ct * 2 + hh) * (33 * 64);
      const int f = fa * 16 + g4 * 4;
      *(f32x4*)(slab + (lane & 15) * 64 + f) = kv[0];
      *(f32x4*)(slab + (16 + (lane & 15)) * 64 + f) = kv[1];
      if ((lane & 15) == 0) *(f32x4*)(slab + 32 * 64 + f) = kv[2];
    }
  }
  TS(5);
}

__global__ __launch_bounds__(256) void ln0_k(const u16* __restrict__ x, u16* __restrict__ y, const u16* __restrict__ wqkv,
                                             const u16* __restrict__ w1, const u16* __restrict__ w3, float* __restrict__ colsum,
                                             const u16* __restrict__ wo, const u16* __restrict__ w2, const u16* __restrict__ w4) {
  if (blockIdx.x >= SEQ / 4 + 120) {
    const int t = blockIdx.x - (SEQ / 4 + 120);
    const u16* W = t < 32 ? wo + (size_t)t * 32768 : (t < 64 ? w2 + (size_t)(t - 32) * 32768 : w4 + (size_t)(t - 64) * 32768);
#pragma unroll
    for (int i = 0; i < 16; ++i) {
      const u32x4 v = *(const u32x4*)(W + (i * 256 + threadIdx.x) * 8);
      asm volatile("" ::"v"(v));
    }
    return;
  }
  if (blockIdx.x >= SEQ / 4) {
    __shared__ float red[32 * 64];
    const int tid = threadIdx.x;
    const int cg = blockIdx.x - SEQ / 4;
    const u16* W; int N, c0;
    if (cg < 48) { W = wqkv; N = 3 * DM; c0 = cg * 64; }
    else if (cg < 64) { W = w1; N = DM; c0 = (cg - 48) * 64; }
    else { W = w3; N = FF; c0 = (cg - 64) * 64; }
    const int c = tid & 7, rg = tid >> 3;
    f32x2 s[4] = {{0.f, 0.f}, {0.f, 0.f}, {0.f, 0.f}, {0.f, 0.f}};
#pragma unroll 16
    for (int i = 0; i < 32; ++i) {
      const u32x4 v = *(const u32x4*)(W + (size_t)(rg + i * 32) * N + c0 + c * 8);
#pragma unroll
      for (int e = 0; e < 4; ++e) s[e] += unpk(v[e]);
    }
#pragma unroll
    for (int e = 0; e < 4; ++e) *(f32x2*)(red + rg * 64 + c * 8 + 2 * e) = s[e];
    __syncthreads();
    if (tid < 64) {
      float t = 0.f;
#pragma unroll
      for (int i = 0; i < 32; ++i) t += red[i * 64 + tid];
      colsum[cg * 64 + tid] = t;
    }
    return;
  }
  const int lane = threadIdx.x & 63, row = blockIdx.x * 4 + (threadIdx.x >> 6);
  const u16* src = x + (size_t)row * DM;
  const u32x4 a = *(const u32x4*)(src + lane * 8), b = *(const u32x4*)(src + 512 + lane * 8);
  float v[16];
#pragma unroll
  for (int e = 0; e < 4; ++e) { v[2 * e] = bflo(a[e]); v[2 * e + 1] = bfhi(a[e]); v[8 + 2 * e] = bflo(b[e]); v[9 + 2 * e] = bfhi(b[e]); }
  float s = 0.f;
#pragma unroll
  for (int e = 0; e < 16; ++e) s += v[e];
#pragma unroll
  for (int o = 1; o < 64; o <<= 1) s += __shfl_xor(s, o);
  const float mean = s * (1.0f / 1024.0f);
  float m2 = 0.f;
#pragma unroll
  for (int e = 0; e < 16; ++e) { const float d = v[e] - mean; m2 += d * d; }
#pragma unroll
  for (int o = 1; o < 64; o <<= 1) m2 += __shfl_xor(m2, o);
  const RowP r = row_params(mean, m2 * (1.0f / 1024.0f), 1);
  u32x4 oa, ob;
#pragma unroll
  for (int e = 0; e < 4; ++e) {
    oa[e] = pk2(rbf(v[2 * e] - r.mhat) / r.dhat, rbf(v[2 * e + 1] - r.mhat) / r.dhat);
    ob[e] = pk2(rbf(v[8 + 2 * e] - r.mhat) / r.dhat, rbf(v[9 + 2 * e] - r.mhat) / r.dhat);
  }
  u16* dst = y + (size_t)row * DM;
  *(u32x4*)(dst + lane * 8) = oa;
  *(u32x4*)(dst + 512 + lane * 8) = ob;
}

__global__ __launch_bounds__(512) void attn_o_k(const u16* __restrict__ q, const u16* __restrict__ wphi, const float* __restrict__ kvpart,
                                                u16* __restrict__ o) {
  __shared__ __attribute__((aligned(16))) char sm[4352];
  const int tid = threadIdx.x, lane = tid & 63, w = tid >> 6;
  constexpr int EPI = 6;
  TS(0);
  const int h = blockIdx.x & 31, rb = blockIdx.x >> 5;
  const int g4 = lane >> 4, r0 = rb * 256 + w * 32 + (lane & 15);
  bf16x8 qf[2], wf[2];
#pragma unroll
  for (int mb = 0; mb < 2; ++mb) qf[mb] = *(const bf16x8*)(q + (size_t)(r0 + mb * 16) * DM + h * 32 + g4 * 8);
#pragma unroll
  for (int fb = 0; fb < 2; ++fb)
#pragma unroll
    for (int j = 0; j < 8; ++j) wf[fb][j] = (short)wphi[h * 1024 + (g4 * 8 + j) * 32 + fb * 16 + (lane & 15)];
  __builtin_amdgcn_sched_barrier(0);
  f32x4 kp[16], kz[16];
  const float* slab = kvpart + (size_t)h * (33 * 64) + tid * 4;
#pragma unroll
  for (int p = 0; p < 16; ++p) kp[p] = *(const f32x4*)(slab + (size_t)p * NH * (33 * 64));
  const bool zw = __builtin_amdgcn_readfirstlane(w) == 0;
  if (zw) {
#pragma unroll
    for (int p = 0; p < 16; ++p) kz[p] = *(const f32x4*)(slab + (size_t)p * NH * (33 * 64) + 2048 - (tid & 48) * 4);
  }
  __builtin_amdgcn_sched_barrier(0);
  bf16x8 pq[2][2];
#pragma unroll
  for (int mb = 0; mb < 2; ++mb) {
    u32x4 pos, neg;
    feature_map(wf, qf[mb], pos, neg);
    pq[mb][0] = __builtin_bit_cast(bf16x8, pos);
    pq[mb][1] = __builtin_bit_cast(bf16x8, neg);
  }
  __builtin_amdgcn_sched_barrier(0);
  TS(1);
  u16* kvt = (u16*)sm;
  {
    f32x4 t = kp[0];
#pragma unroll
    for (int p = 1; p < 16; ++p) t += kp[p];
    *(u32x2*)(kvt + tid * 4) = (u32x2){pk2(t[0], t[1]), pk2(t[2], t[3])};
    if (zw) {
      f32x4 z = kz[0];
#pragma unroll
      for (int p = 1; p < 16; ++p) z += kz[p];
      if (tid < 16) *(u32x2*)(kvt + 2048 + tid * 4) = (u32x2){pk2(z[0], z[1]), pk2(z[2], z[3])};
    }
  }
  TS(2);
  __syncthreads();
  TS(3);
  bf16x8 kf[3][2];
#pragma unroll
  for (int db = 0; db < 3; ++db)
#pragma unroll
    for (int ks = 0; ks < 2; ++ks) {
      const int d = (db < 2) ? db * 16 + (lane & 15) : 32;
      const u16* p = kvt + d * 64 + ks * 32 + g4 * 4;
      kf[db][ks] = cat8(*(const s16x4*)p, *(const s16x4*)(p + 16));
    }
#pragma unroll
  for (int mb = 0; mb < 2; ++mb) {
    f32x4 a[3];
#pragma unroll
    for (int db = 0; db < 3; ++db) {
      a[db] = mfma16(kf[db][0], pq[mb][0], (f32x4){0.f, 0.f, 0.f, 0.f});
      a[db] = mfma16(kf[db][1], pq[mb][1], a[db]);
    }
    const float id = __builtin_amdgcn_rcpf(rbf(rbf(a[2][0]) + 1e-6f));
    u16* dst = o + (size_t)(r0 + mb * 16) * DM + h * 32 + g4 * 4;
#pragma unroll
    for (int db = 0; db < 2; ++db) {
      const f32x2 lo = rbf2((f32x2){a[db][0], a[db][1]}) * id, hi = rbf2((f32x2){a[db][2], a[db][3]}) * id;
      *(u32x2*)(dst + db * 16) = (u32x2){pk2(lo[0], lo[1]), pk2(hi[0], hi[1])};
    }
  }
  TS(4);
  TS(5);
}

template <int BN, int S, int KD, int EPI> static constexpr int smem_bytes() {
  constexpr bool USE_STATS = (EPI == EPI_QKV || EPI == EPI_GELU_SHIFT || EPI == EPI_W2 || EPI == EPI_GELU);
  constexpr int NRES = (EPI == EPI_RES || EPI == EPI_W2) ? 1 : (EPI == EPI_W4 ? 2 : 0);
  return S * (16384 + BN * 128) + NRES * 16384 + (EPI == EPI_QKV ? 4096 : 0) + 2048 + 1024;
}
template <int BN, int S, int KD, int EPI> static void launch_gemm(const GemmArgs& a, hipStream_t st) {
  constexpr int sb = smem_bytes<BN, S, KD, EPI>();
  static_assert(sb <= 163840, "LDS");
  (void)hipFuncSetAttribute((const void*)gemm_k<BN, S, KD, EPI>, hipFuncAttributeMaxDynamicSharedMemorySize, sb);
  gemm_k<BN, S, KD, EPI><<<256, 512, sb, st>>>(a);
}

extern "C" void kernel_launch(void* const* d_in, const int* in_sizes, int n_in, void* d_out, int out_size, void* d_ws,
                              size_t ws_size, hipStream_t stream) {
  const u16* x0 = (const u16*)d_in[0];
  const u16* wqkv = (const u16*)d_in[1];
  const u16* wphi = (const u16*)d_in[2];
  const u16* wo = (const u16*)d_in[3];
  const u16* w1 = (const u16*)d_in[5];
  const u16* w2 = (const u16*)d_in[6];
  const u16* w3 = (const u16*)d_in[7];
  const u16* w4 = (const u16*)d_in[8];
  char* ws = (char*)d_ws;
  const size_t MB = 1 << 20;
  u16* xln0 = (u16*)(ws + 0 * MB);
  u16* phiq = (u16*)(ws + 4 * MB);
  float* kvpart = (float*)(ws + 12 * MB);
  u16* obuf = (u16*)(ws + 17 * MB);
  u16* xa = (u16*)(ws + 21 * MB);
  u16* hsh = (u16*)(ws + 25 * MB);
  u16* x1 = (u16*)(ws + 29 * MB);
  u16* gbuf = (u16*)(ws + 33 * MB);
  u16* xb = (u16*)(ws + 48 * MB);
  float* st_xa = (float*)(ws + 52 * MB);
  float* st_x1 = (float*)(ws + 53 * MB);
  float* st_xb = (float*)(ws + 54 * MB);
  float* colsum = (float*)(ws + 55 * MB);

  ln0_k<<<SEQ / 4 + 120 + 176, 256, 0, stream>>>(x0, xln0, wqkv, w1, w3, colsum, wo, w2, w4);
  for (int layer = 0; layer < 2; ++layer) {
    const u16* xin = layer == 0 ? x0 : xb;
    u16* xout = layer == 0 ? xb : (u16*)d_out;
    GemmArgs a;
    a = GemmArgs{layer == 0 ? xln0 : xb, wqkv, phiq, nullptr, nullptr, st_xb, nullptr, colsum, wphi, kvpart, layer == 0 ? 0 : 1, 0};
    launch_gemm<192, 3, 1024, EPI_QKV>(a, stream);
    attn_o_k<<<256, 512, 0, stream>>>(phiq, wphi, kvpart, obuf);
    a = GemmArgs{obuf, wo, xa, xin, nullptr, nullptr, st_xa, nullptr, nullptr, nullptr, 0, 0};
    launch_gemm<64, 5, 1024, EPI_RES>(a, stream);
    a = GemmArgs{xa, w1, hsh, nullptr, nullptr, st_xa, nullptr, colsum + 3072, nullptr, nullptr, 2, 0};
    launch_gemm<64, 5, 1024, EPI_GELU_SHIFT>(a, stream);
    a = GemmArgs{hsh, w2, x1, xa, nullptr, st_xa, st_x1, nullptr, nullptr, nullptr, 1, 0};
    launch_gemm<64, 5, 1024, EPI_W2>(a, stream);
    a = GemmArgs{x1, w3, gbuf, nullptr, nullptr, st_x1, nullptr, colsum + 4096, nullptr, nullptr, 1, 0};
    launch_gemm<224, 3, 1024, EPI_GELU>(a, stream);
    a = GemmArgs{gbuf, w4, xout, xa, x1, nullptr, st_xb, nullptr, nullptr, nullptr, 0, 0};
    launch_gemm<64, 5, 3584, EPI_W4>(a, stream);
  }
}
```

```cpp
#include <hip/hip_runtime.h>
#include <hip/hip_bf16.h>
#include <stdint.h>

typedef __attribute__((ext_vector_type(8))) short bf16x8;
typedef __attribute__((ext_vector_type(4))) short s16x4;
typedef __attribute__((ext_vector_type(4))) float f32x4;
typedef __attribute__((ext_vector_type(2))) float f32x2;
typedef __attribute__((ext_vector_type(4))) unsigned u32x4;
typedef __attribute__((ext_vector_type(2))) unsigned u32x2;
typedef __attribute__((ext_vector_type(2))) __bf16 bf16x2_t;
typedef unsigned short u16;
#define LDS_AS __attribute__((address_space(3)))
#define DI __device__ __forceinline__

static constexpr int SEQ = 2048, DM = 1024, NH = 32, FF = 3584;

DI float bf2f(unsigned v) { return __uint_as_float(v << 16); }
DI float bflo(unsigned v) { return __uint_as_float(v << 16); }
DI float bfhi(unsigned v) { return __uint_as_float(v & 0xffff0000u); }
DI unsigned pk2(float a, float b) {
  f32x2 v = {a, b};
  return __builtin_bit_cast(unsigned, __builtin_convertvector(v, bf16x2_t));
}
DI float rbf(float a) { return bflo(pk2(a, a)); }

template <int I> struct IC { static constexpr int value = I; };
template <int B, int E, class F> DI void static_for(F&& f) {
  if constexpr (B < E) { f(IC<B>{}); static_for<B + 1, E>(f); }
}
template <int N> DI void wait_vm() { asm volatile("s_waitcnt vmcnt(%0)" ::"n"(N) : "memory"); }
DI void wait_lgkm0() { asm volatile("s_waitcnt lgkmcnt(0)" ::: "memory"); }
DI void bar() { __builtin_amdgcn_s_barrier(); }
DI void glds16(const void* src, char* lds_dst) {
  __builtin_amdgcn_global_load_lds((const unsigned*)src, (unsigned*)lds_dst, 16, 0, 0);
}
DI s16x4 trread(const char* p) {
  return __builtin_bit_cast(s16x4, __builtin_amdgcn_ds_read_tr16_b64_v4i16((LDS_AS s16x4*)p));
}
template <int OFF> DI s16x4 lds_tr(unsigned addr) {
  s16x4 r; asm volatile("ds_read_b64_tr_b16 %0, %1 offset:%2" : "=v"(r) : "v"(addr), "n"(OFF) : "memory"); return r;
}
template <int OFF> DI bf16x8 lds_b128(unsigned addr) {
  bf16x8 r; asm volatile("ds_read_b128 %0, %1 offset:%2" : "=v"(r) : "v"(addr), "n"(OFF) : "memory"); return r;
}
DI bf16x8 cat8(s16x4 lo, s16x4 hi) { return (bf16x8){lo[0], lo[1], lo[2], lo[3], hi[0], hi[1], hi[2], hi[3]}; }
DI f32x4 mfma16(bf16x8 a, bf16x8 b, f32x4 c) { return __builtin_amdgcn_mfma_f32_16x16x32_bf16(a, b, c, 0, 0, 0); }

DI f32x2 gelu2(f32x2 x) {
  const float c0 = -2.302208198f, c1 = -2.302208198f * 0.044715f;
  const f32x2 a = x * (x * x * c1 + c0);
  const f32x2 d = (f32x2){__builtin_amdgcn_exp2f(a[0]), __builtin_amdgcn_exp2f(a[1])} + 1.0f;
  return x * (f32x2){__builtin_amdgcn_rcpf(d[0]), __builtin_amdgcn_rcpf(d[1])};
}
DI f32x2 rbf2(f32x2 v) { const unsigned p = pk2(v[0], v[1]); return (f32x2){bflo(p), bfhi(p)}; }
DI f32x2 unpk(unsigned p) { return (f32x2){bflo(p), bfhi(p)}; }

DI void feature_map(const bf16x8 (&wf)[2], bf16x8 xf, u32x4& pos, u32x4& neg) {
  f32x2 p[4];
#pragma unroll
  for (int fb = 0; fb < 2; ++fb) {
    const f32x4 d = mfma16(wf[fb], xf, (f32x4){0.f, 0.f, 0.f, 0.f});
    p[fb * 2] = rbf2((f32x2){d[0], d[1]});
    p[fb * 2 + 1] = rbf2((f32x2){d[2], d[3]});
  }
  float mx = fmaxf(p[0][0], p[0][1]), mn = fminf(p[0][0], p[0][1]);
#pragma unroll
  for (int e = 1; e < 4; ++e) { mx = fmaxf(mx, fmaxf(p[e][0], p[e][1])); mn = fminf(mn, fminf(p[e][0], p[e][1])); }
  mx = fmaxf(mx, __shfl_xor(mx, 16)); mx = fmaxf(mx, __shfl_xor(mx, 32));
  mn = fminf(mn, __shfl_xor(mn, 16)); mn = fminf(mn, __shfl_xor(mn, 32));
  const float l2e = 1.4426950408889634f;
  f32x2 ep[4], en[4], sp2 = {0.f, 0.f}, sn2 = {0.f, 0.f};
#pragma unroll
  for (int e = 0; e < 4; ++e) {
    const f32x2 a = (p[e] - mx) * l2e, b = (mn - p[e]) * l2e;
    ep[e] = (f32x2){__builtin_amdgcn_exp2f(a[0]), __builtin_amdgcn_exp2f(a[1])}; sp2 += ep[e];
    en[e] = (f32x2){__builtin_amdgcn_exp2f(b[0]), __builtin_amdgcn_exp2f(b[1])}; sn2 += en[e];
  }
  float sp = sp2[0] + sp2[1], sn = sn2[0] + sn2[1];
  sp += __shfl_xor(sp, 16); sp += __shfl_xor(sp, 32);
  sn += __shfl_xor(sn, 16); sn += __shfl_xor(sn, 32);
  const float ip = __builtin_amdgcn_rcpf(sp), in_ = __builtin_amdgcn_rcpf(sn);
#pragma unroll
  for (int e = 0; e < 4; ++e) {
    const f32x2 a = ep[e] * ip, b = en[e] * in_;
    pos[e] = pk2(a[0], a[1]); neg[e] = pk2(b[0], b[1]);
  }
}

#define TS(i)
struct GemmArgs {
  const u16* A;
  const u16* B;
  u16* out;
  const u16* res1;
  const u16* res2;
  const float* stats_in;
  float* stats_out;
  const float* colsum;
  const u16* wphi;
  float* kvpart;
  int ln_mode;
  int unused;
};

enum { EPI_QKV = 0, EPI_RES = 1, EPI_GELU_SHIFT = 2, EPI_W2 = 3, EPI_GELU = 4, EPI_W4 = 5 };

struct RowP { float alpha, beta, mhat, dhat; };
DI RowP row_params(float mean, float var, int mode) {
  RowP r;
  const float mh = rbf(mean);
  const float dh = rbf(__builtin_amdgcn_sqrtf(rbf(rbf(var) + 1e-5f)));
  const float i1 = __builtin_amdgcn_rcpf(dh);
  r.mhat = mh; r.dhat = dh;
  if (mode == 2) {
    const float m2 = (mean - mh) * i1;
    const float v2 = var * i1 * i1;
    const float mh2 = rbf(m2);
    const float dh2 = rbf(__builtin_amdgcn_sqrtf(rbf(rbf(v2) + 1e-5f)));
    const float i2 = __builtin_amdgcn_rcpf(dh2);
    r.alpha = i1 * i2;
    r.beta = (mh * i1 + mh2) * i2;
  } else {
    r.alpha = i1;
    r.beta = mh * i1;
  }
  return r;
}

template <int BN, int S, int KD, int EPI>
__global__ __launch_bounds__(512, 2) void gemm_k(GemmArgs g) {
  constexpr int NK = KD / 64;
  constexpr int NPF = BN / 64;
  constexpr bool HALF = (BN % 64) != 0;
  constexpr int NI = BN / 32;
  constexpr int A_BYTES = 16384;
  constexpr int STAGE = A_BYTES + BN * 128;
  constexpr int L = 4 + NPF * 2 + (HALF ? 1 : 0);
  constexpr int NTOT = (EPI == EPI_QKV) ? 3 * DM : (EPI == EPI_GELU ? FF : DM);
  constexpr bool USE_STATS = (EPI == EPI_QKV || EPI == EPI_GELU_SHIFT || EPI == EPI_W2 || EPI == EPI_GELU);
  constexpr bool FOLD = (EPI == EPI_QKV || EPI == EPI_GELU_SHIFT || EPI == EPI_GELU);
  constexpr int NRES = (EPI == EPI_RES || EPI == EPI_W2) ? 1 : (EPI == EPI_W4 ? 2 : 0);
  constexpr int OFF_STATS = S * STAGE;
  constexpr int OFF_RES = OFF_STATS;
  constexpr int OFF_WPHI = OFF_RES + NRES * 16384;
  constexpr int OFF_ROWP = OFF_WPHI + (EPI == EPI_QKV ? 4096 : 0);
  constexpr int OFF_CS = OFF_ROWP + 2048;
  constexpr int NEXTRA = (USE_STATS && BN != 64 ? 8 : 0) + NRES * 4 + (EPI == EPI_QKV ? 1 : 0) + (FOLD ? 1 : 0);

  extern __shared__ __attribute__((aligned(16))) char smem[];
  const int tid = threadIdx.x, lane = tid & 63;
  const int w = __builtin_amdgcn_readfirstlane(tid >> 6);
  const bool prod = w >= 4;
  const int cw = w & 3, wr = cw >> 1, wc = cw & 1;

  const int b = blockIdx.x, xcd = b & 7, j = b >> 3;
  int rp, ct;
  if constexpr (EPI == EPI_QKV || EPI == EPI_GELU) { rp = (xcd >> 2) * 8 + (j >> 2); ct = (xcd & 3) * 4 + (j & 3); }
  else { rp = (xcd >> 1) * 4 + (j >> 3); ct = (xcd & 1) * 8 + (j & 7); }
  const int m0 = rp * 128;
  const int n0 = (EPI == EPI_QKV) ? ct * 64 : ct * BN;
  constexpr int PSTRIDE = (EPI == EPI_QKV) ? DM : 64;
  const int g4 = lane >> 4, q4 = (lane >> 2) & 3, pp = lane & 3;
  RowP* rowp = (RowP*)(smem + OFF_ROWP);
  const bool ln_on = USE_STATS && (g.ln_mode != 0 || EPI == EPI_W2);

  f32x4 acc[4][NI];
  TS(0);
  constexpr bool RP_CONS = (BN == 64);
  f32x2 sv[8];
  const int sr = cw * 32 + (lane >> 1), shf = lane & 1;
  auto load_stats = [&]() {
#pragma unroll
    for (int p = 0; p < 8; ++p)
      asm volatile("global_load_dwordx2 %0, %1, off" : "=v"(sv[p]) : "v"(g.stats_in + ((size_t)(shf * 8 + p) * SEQ + m0 + sr) * 2) : "memory");
  };
  float rs_s = 0.f, rs_m2 = 0.f;
  auto row_stats = [&](auto pc) {
    constexpr int piece = decltype(pc)::value;
    if constexpr (piece == 0) {
#pragma unroll
      for (int p = 0; p < 8; ++p) asm volatile("" : "+v"(sv[p]));
      float s = 0.f;
#pragma unroll
      for (int p = 0; p < 8; ++p) s += sv[p][0];
      rs_s = s + __shfl_xor(s, 1);
    } else if constexpr (piece == 1) {
      const float mean = rs_s * (1.0f / 1024.0f);
      float m2 = 0.f;
#pragma unroll
      for (int p = 0; p < 8; ++p) { const float d = sv[p][0] * (1.0f / 64.0f) - mean; m2 += sv[p][1] + 64.0f * d * d; }
      rs_m2 = m2 + __shfl_xor(m2, 1);
    } else {
      if (ln_on && shf == 0) rowp[sr] = row_params(rs_s * (1.0f / 1024.0f), rs_m2 * (1.0f / 1024.0f), EPI == EPI_W2 ? 1 : g.ln_mode);
      wait_lgkm0();
    }
    __builtin_amdgcn_sched_barrier(0);
  };
  if (__builtin_expect(prod, 1)) {
    const u16* a_src = g.A + (size_t)(m0 + cw * 32 + (lane >> 3)) * KD + (((lane & 7) ^ (lane >> 3)) * 8);
    const u16* b_src[2];
#pragma unroll
    for (int e = 0; e < 2; ++e) {
      const int r = lane >> 3;
      const int f = ((r >> 1) & 1) | (e << 1);
      const int c = ((((lane & 7) >> 1) ^ f) << 1) | (lane & 1);
      b_src[e] = g.B + (size_t)((cw * 2 + e) * 8 + r) * NTOT + n0 + c * 8;
    }
    const u16* bh_src = nullptr;
    if constexpr (HALF) {
      const int c = ((((lane >> 1) & 1) ^ ((lane >> 5) & 1)) << 1) | (lane & 1);
      bh_src = g.B + (size_t)(cw * 16 + (lane >> 2)) * NTOT + n0 + NPF * 64 + c * 8;
    }
    auto issue = [&](int kt, int slot) {
      char* base = smem + slot * STAGE;
#pragma unroll
      for (int p = 0; p < NPF; ++p)
#pragma unroll
        for (int e = 0; e < 2; ++e)
          glds16(b_src[e] + (size_t)kt * 64 * NTOT + p * PSTRIDE, base + A_BYTES + p * 8192 + (cw * 2 + e) * 1024);
      if constexpr (HALF) glds16(bh_src + (size_t)kt * 64 * NTOT, base + A_BYTES + NPF * 8192 + cw * 1024);
#pragma unroll
      for (int e = 0; e < 4; ++e) glds16(a_src + (size_t)e * 8 * KD + kt * 64, base + (cw * 4 + e) * 1024);
    };
    TS(6);
    issue(0, 0);
    issue(1, 1);
    wait_vm<L>();
    TS(1);
    bar();
    static_for<2, S>([&](auto ic) { issue(ic.value, ic.value); });
    TS(7);
    if constexpr (NRES >= 1) {
#pragma unroll
      for (int e = 0; e < 4; ++e) {
        const int r = (cw * 4 + e) * 8 + (lane >> 3);
        glds16(g.res1 + (size_t)(m0 + r) * DM + n0 + (lane & 7) * 8, smem + OFF_RES + (cw * 4 + e) * 1024);
      }
    }
    if constexpr (NRES >= 2) {
#pragma unroll
      for (int e = 0; e < 4; ++e) {
        const int r = (cw * 4 + e) * 8 + (lane >> 3);
        glds16(g.res2 + (size_t)(m0 + r) * DM + n0 + (lane & 7) * 8, smem + OFF_RES + 16384 + (cw * 4 + e) * 1024);
      }
    }
    if constexpr (EPI == EPI_QKV) glds16(g.wphi + (size_t)ct * 2048 + cw * 512 + lane * 8, smem + OFF_WPHI + cw * 1024);
    if constexpr (FOLD) {
      int col = cw * 64 + (lane & 15) * 4;
      if (col > BN - 4) col = BN - 4;
      const int gcol = (EPI == EPI_QKV) ? (col >> 6) * DM + n0 + (col & 63) : n0 + col;
      if (lane < 16) glds16(g.colsum + gcol, smem + OFF_CS + cw * 256);
    }
    if constexpr (USE_STATS && !RP_CONS) load_stats();
    static_for<0, NK - 1>([&](auto ic) {
      constexpr int kt = ic.value;
      constexpr int young = (S - 2 < NK - 2 - kt) ? S - 2 : NK - 2 - kt;
      constexpr int ex = (kt + 1 <= S - 1 && kt + 1 < NK - 1) ? NEXTRA : 0;
      wait_vm<young * L + ex>();
      bar();
      if constexpr (kt + S < NK) issue(kt + S, kt % S);
      if constexpr (USE_STATS && !RP_CONS && kt >= 4 && kt < 7) row_stats(IC<kt - 4>{});
    });
    TS(2);
    bar();
  } else {
    const int fsw = ((q4 >> 1) & 1) | ((g4 & 1) << 1);
    int a_off[2], b_off[2], bh_off = 0;
#pragma unroll
    for (int k = 0; k < 2; ++k) {
      a_off[k] = (wr * 64 + (lane & 15)) * 128 + (((k * 4 + g4) ^ (lane & 7)) * 16);
      b_off[k] = A_BYTES + (8 * g4 + q4) * 128 + (((2 * k + wc) ^ fsw) * 32) + pp * 8;
    }
    if constexpr (HALF) bh_off = A_BYTES + NPF * 8192 + (8 * g4 + q4) * 64 + ((wc ^ (g4 & 1)) * 32) + pp * 8;
#pragma unroll
    for (int m = 0; m < 4; ++m)
#pragma unroll
      for (int i = 0; i < NI; ++i) acc[m][i] = (f32x4){0.f, 0.f, 0.f, 0.f};
    bf16x8 xa[2][4];
    s16x4 wlo[2][NI], whi[2][NI];
    const unsigned lbase = (unsigned)(size_t)smem;

    auto ldfrag = [&](auto setc, auto slotc, auto kc) {
      constexpr int set = decltype(setc)::value, slot = decltype(slotc)::value, k32 = decltype(kc)::value;
      const unsigned sb = lbase + slot * STAGE;
      const unsigned ba0 = sb + b_off[0], ba1 = sb + b_off[1], bah = sb + bh_off, aa = sb + a_off[k32];
      static_for<0, NI>([&](auto ic) {
        constexpr int i = decltype(ic)::value;
        if constexpr (HALF && i == NI - 1) {
          wlo[set][i] = lds_tr<k32 * 2048>(bah);
          whi[set][i] = lds_tr<k32 * 2048 + 256>(bah);
        } else {
          wlo[set][i] = lds_tr<(i >> 1) * 8192 + k32 * 4096>((i & 1) ? ba1 : ba0);
          whi[set][i] = lds_tr<(i >> 1) * 8192 + k32 * 4096 + 512>((i & 1) ? ba1 : ba0);
        }
      });
      static_for<0, 4>([&](auto mc) { constexpr int m = decltype(mc)::value; xa[set][m] = lds_b128<m * 2048>(aa); });
    };
    auto wait_set = [&](auto setc) {
      constexpr int set = decltype(setc)::value;
      asm volatile("s_waitcnt lgkmcnt(0)" : "+v"(xa[set][0]), "+v"(xa[set][1]), "+v"(xa[set][2]), "+v"(xa[set][3]) :: "memory");
#pragma unroll
      for (int i = 0; i < NI; ++i) asm volatile("" : "+v"(wlo[set][i]), "+v"(whi[set][i]));
      __builtin_amdgcn_sched_barrier(0);
    };
    auto ld1 = [&](auto setc, auto slotc, auto kc, auto rc) {
      constexpr int set = decltype(setc)::value, slot = decltype(slotc)::value, k32 = decltype(kc)::value, r = decltype(rc)::value;
      const unsigned sb = lbase + slot * STAGE;
      if constexpr (r < 2 * NI) {
        constexpr int i = r >> 1, hi = r & 1;
        s16x4 v;
        if constexpr (HALF && i == NI - 1) v = lds_tr<k32 * 2048 + hi * 256>(sb + bh_off);
        else v = lds_tr<(i >> 1) * 8192 + k32 * 4096 + hi * 512>(sb + b_off[i & 1]);
        if constexpr (hi) whi[set][i] = v; else wlo[set][i] = v;
      } else {
        constexpr int m = r - 2 * NI;
        xa[set][m] = lds_b128<m * 2048>(sb + a_off[k32]);
      }
    };
    auto mma_ld = [&](auto setc, auto ldc, auto slotc, auto kc) {
      constexpr int set = decltype(setc)::value;
      constexpr bool ld = decltype(ldc)::value != 0;
      static_for<0, 4 * NI>([&](auto tc) {
        constexpr int t = decltype(tc)::value, i = t >> 2, m = t & 3;
        acc[m][i] = mfma16(cat8(wlo[set][i], whi[set][i]), xa[set][m], acc[m][i]);
        if constexpr (ld && t < 2 * NI + 4) ld1(IC<set ^ 1>{}, slotc, kc, tc);
        __builtin_amdgcn_sched_barrier(0);
      });
    };

    if constexpr (USE_STATS && RP_CONS) load_stats();
    bar();
    TS(1);
    ldfrag(IC<0>{}, IC<0>{}, IC<0>{});
    static_for<0, NK>([&](auto ic) {
      constexpr int kt = ic.value;
      constexpr int slot = kt % S;
      wait_set(IC<0>{});
      if constexpr (USE_STATS && RP_CONS && kt >= 6 && kt < 9) { if constexpr (kt == 6) wait_vm<0>(); row_stats(IC<kt - 6>{}); }
      mma_ld(IC<0>{}, IC<1>{}, IC<slot>{}, IC<1>{});
      wait_set(IC<1>{});
      if constexpr (kt + 1 < NK) {
        bar();
        __builtin_amdgcn_sched_barrier(0);
        mma_ld(IC<1>{}, IC<1>{}, IC<(kt + 1) % S>{}, IC<0>{});
      } else {
        mma_ld(IC<1>{}, IC<0>{}, IC<0>{}, IC<0>{});
      }
    });
    TS(2);
    bar();
  }
  TS(3);

  constexpr int TSTR = BN * 2 + 16;
  char* T = smem;
  if (!prod) {
    float al[4] = {1.f, 1.f, 1.f, 1.f}, be[4] = {0.f, 0.f, 0.f, 0.f};
    if constexpr (FOLD) {
      if (ln_on) {
#pragma unroll
        for (int m = 0; m < 4; ++m) { const RowP r = rowp[wr * 64 + m * 16 + (lane & 15)]; al[m] = r.alpha; be[m] = r.beta; }
      }
    }
#pragma unroll
    for (int i = 0; i < NI; ++i) {
      const int nb = 2 * i + wc;
      const int ncol = nb * 16 + g4 * 4;
      f32x4 cs = {0.f, 0.f, 0.f, 0.f};
      if constexpr (FOLD) {
        if (ln_on) cs = *(const f32x4*)(smem + OFF_CS + ncol * 4);
      }
#pragma unroll
      for (int m = 0; m < 4; ++m) {
        u32x2 pkd;
#pragma unroll
        for (int h = 0; h < 2; ++h) {
          f32x2 v = {acc[m][i][2 * h], acc[m][i][2 * h + 1]};
          if constexpr (FOLD) v = v * al[m] - (f32x2){cs[2 * h], cs[2 * h + 1]} * be[m];
          if constexpr (EPI == EPI_GELU_SHIFT || EPI == EPI_GELU) v = gelu2(rbf2(v));
          pkd[h] = pk2(v[0], v[1]);
        }
        *(u32x2*)(T + (wr * 64 + m * 16 + (lane & 15)) * TSTR + ncol * 2) = pkd;
      }
    }
  }
  __syncthreads();
  TS(4);
  if constexpr (EPI == EPI_GELU) {
    constexpr int CH = BN / 8;
#pragma unroll
    for (int it = 0; it < (128 * CH) / 512; ++it) {
      const int idx = it * 512 + tid, r = idx / CH, c = idx % CH;
      const u32x4 v = *(const u32x4*)(T + r * TSTR + c * 16);
      *(u32x4*)(g.out + (size_t)(m0 + r) * FF + n0 + c * 8) = v;
    }
  } else if constexpr (EPI == EPI_GELU_SHIFT) {
    const int grp = n0 >> 8;
    const int sh = (grp == 0) ? 1 : (grp == 1) ? -1 : (grp == 2) ? 2 : -2;
#pragma unroll
    for (int it = 0; it < 2; ++it) {
      const int r = it * 64 + (tid >> 3), c = tid & 7;
      const u32x4 v = *(const u32x4*)(T + r * TSTR + c * 16);
      const int dr = m0 + r + sh;
      if (dr >= 0 && dr < SEQ) *(u32x4*)(g.out + (size_t)dr * DM + n0 + c * 8) = v;
    }
    if (tid < 16) {
      const int zr = tid >> 3, c = tid & 7;
      const int az = sh > 0 ? sh : -sh;
      if (zr < az) {
        if (sh > 0 && rp == 0) *(u32x4*)(g.out + (size_t)zr * DM + n0 + c * 8) = (u32x4){0u, 0u, 0u, 0u};
        if (sh < 0 && rp == 15) *(u32x4*)(g.out + (size_t)(SEQ - 1 - zr) * DM + n0 + c * 8) = (u32x4){0u, 0u, 0u, 0u};
      }
    }
  } else if constexpr (EPI == EPI_RES || EPI == EPI_W2 || EPI == EPI_W4) {
#pragma unroll
    for (int it = 0; it < 2; ++it) {
      const int r = it * 64 + (tid >> 3), c = tid & 7;
      const u32x4 v = *(const u32x4*)(T + r * TSTR + c * 16);
      const u32x4 x = *(const u32x4*)(smem + OFF_RES + r * 128 + c * 16);
      f32x2 y[4];
      if constexpr (EPI == EPI_RES) {
#pragma unroll
        for (int e = 0; e < 4; ++e) y[e] = unpk(x[e]) + unpk(v[e]);
      } else if constexpr (EPI == EPI_W2) {
        const RowP rr = rowp[r];
#pragma unroll
        for (int e = 0; e < 4; ++e) y[e] = rbf2(rbf2(unpk(x[e]) - rr.mhat) * rr.alpha) + unpk(v[e]);
      } else {
        const u32x4 x1 = *(const u32x4*)(smem + OFF_RES + 16384 + r * 128 + c * 16);
#pragma unroll
        for (int e = 0; e < 4; ++e) y[e] = unpk(x[e]) + rbf2(unpk(x1[e]) + unpk(v[e]));
      }
      u32x4 o;
#pragma unroll
      for (int e = 0; e < 4; ++e) { o[e] = pk2(y[e][0], y[e][1]); y[e] = unpk(o[e]); }
      *(u32x4*)(g.out + (size_t)(m0 + r) * DM + n0 + c * 8) = o;
      const f32x2 s2 = (y[0] + y[1]) + (y[2] + y[3]);
      float s = s2[0] + s2[1];
      s += __shfl_xor(s, 1); s += __shfl_xor(s, 2); s += __shfl_xor(s, 4);
      const float mc = s * (1.0f / 64.0f);
      f32x2 q2 = {0.f, 0.f};
#pragma unroll
      for (int e = 0; e < 4; ++e) { const f32x2 d = y[e] - mc; q2 += d * d; }
      float m2 = q2[0] + q2[1];
      m2 += __shfl_xor(m2, 1); m2 += __shfl_xor(m2, 2); m2 += __shfl_xor(m2, 4);
      if (c == 0) *(f32x2*)(g.stats_out + ((size_t)ct * SEQ + m0 + r) * 2) = (f32x2){s, m2};
    }
  } else {
    constexpr int PKSTR = 144;
    char* PK = smem + 128 * TSTR;
#pragma unroll
    for (int it = 0; it < 2; ++it) {
      const int r = it * 64 + (tid >> 3), c = tid & 7;
      *(u32x4*)(g.out + (size_t)(m0 + r) * DM + n0 + c * 8) = *(const u32x4*)(T + r * TSTR + c * 16);
    }
    {
      const int hh = w & 1, rq = w >> 1;
      bf16x8 wf[2];
#pragma unroll
      for (int fb = 0; fb < 2; ++fb) {
        const char* p = smem + OFF_WPHI + hh * 2048 + (8 * g4 + q4) * 64 + fb * 32 + pp * 8;
        wf[fb] = cat8(trread(p), trread(p + 256));
      }
#pragma unroll
      for (int mb = 0; mb < 2; ++mb) {
        const int row = rq * 32 + mb * 16 + (lane & 15);
        const bf16x8 xf = *(const bf16x8*)(T + row * TSTR + (64 + hh * 32 + g4 * 8) * 2);
        u32x4 pos, neg;
        feature_map(wf, xf, pos, neg);
        char* dst = PK + (hh * 128 + row) * PKSTR + g4 * 8;
        *(u32x2*)dst = (u32x2){pos[0], pos[1]}; *(u32x2*)(dst + 32) = (u32x2){pos[2], pos[3]};
        *(u32x2*)(dst + 64) = (u32x2){neg[0], neg[1]}; *(u32x2*)(dst + 96) = (u32x2){neg[2], neg[3]};
      }
    }
    __syncthreads();
    {
      const int hh = w >> 2, fa = w & 3;
      f32x4 kv[3];
#pragma unroll
      for (int d = 0; d < 3; ++d) kv[d] = (f32x4){0.f, 0.f, 0.f, 0.f};
      const short one = (short)0x3F80;
      const bf16x8 ones = {one, one, one, one, one, one, one, one};
#pragma unroll
      for (int ks = 0; ks < 4; ++ks) {
        const int row = ks * 32 + 8 * g4 + q4;
        bf16x8 vf[2];
#pragma unroll
        for (int d = 0; d < 2; ++d) {
          const char* p = T + row * TSTR + (128 + hh * 32 + d * 16) * 2 + pp * 8;
          vf[d] = cat8(trread(p), trread(p + 4 * TSTR));
        }
        const char* p = PK + (hh * 128 + row) * PKSTR + (fa * 16) * 2 + pp * 8;
        const bf16x8 pf = cat8(trread(p), trread(p + 4 * PKSTR));
        kv[0] = mfma16(pf, vf[0], kv[0]);
        kv[1] = mfma16(pf, vf[1], kv[1]);
        kv[2] = mfma16(pf, ones, kv[2]);
      }
      float* slab = g.kvpart + ((size_t)rp * NH + ct * 2 + hh) * (33 * 64);
      const int f = fa * 16 + g4 * 4;
      *(f32x4*)(slab + (lane & 15) * 64 + f) = kv[0];
      *(f32x4*)(slab + (16 + (lane & 15)) * 64 + f) = kv[1];
      if ((lane & 15) == 0) *(f32x4*)(slab + 32 * 64 + f) = kv[2];
    }
  }
  TS(5);
}

__global__ __launch_bounds__(256) void ln0_k(const u16* __restrict__ x, u16* __restrict__ y, const u16* __restrict__ wqkv,
                                             const u16* __restrict__ w1, const u16* __restrict__ w3, float* __restrict__ colsum,
                                             const u16* __restrict__ wo, const u16* __restrict__ w2, const u16* __restrict__ w4) {
  if (blockIdx.x >= SEQ / 4 + 120) {
    const int t = blockIdx.x - (SEQ / 4 + 120);
    const u16* W = t < 32 ? wo + (size_t)t * 32768 : (t < 64 ? w2 + (size_t)(t - 32) * 32768 : w4 + (size_t)(t - 64) * 32768);
#pragma unroll
    for (int i = 0; i < 16; ++i) {
      const u32x4 v = *(const u32x4*)(W + (i * 256 + threadIdx.x) * 8);
      asm volatile("" ::"v"(v));
    }
    return;
  }
  if (blockIdx.x >= SEQ / 4) {
    __shared__ float red[32 * 64];
    const int tid = threadIdx.x;
    const int cg = blockIdx.x - SEQ / 4;
    const u16* W; int N, c0;
    if (cg < 48) { W = wqkv; N = 3 * DM; c0 = cg * 64; }
    else if (cg < 64) { W = w1; N = DM; c0 = (cg - 48) * 64; }
    else { W = w3; N = FF; c0 = (cg - 64) * 64; }
    const int c = tid & 7, rg = tid >> 3;
    f32x2 s[4] = {{0.f, 0.f}, {0.f, 0.f}, {0.f, 0.f}, {0.f, 0.f}};
#pragma unroll 16
    for (int i = 0; i < 32; ++i) {
      const u32x4 v = *(const u32x4*)(W + (size_t)(rg + i * 32) * N + c0 + c * 8);
#pragma unroll
      for (int e = 0; e < 4; ++e) s[e] += unpk(v[e]);
    }
#pragma unroll
    for (int e = 0; e < 4; ++e) *(f32x2*)(red + rg * 64 + c * 8 + 2 * e) = s[e];
    __syncthreads();
    if (tid < 64) {
      float t = 0.f;
#pragma unroll
      for (int i = 0; i < 32; ++i) t += red[i * 64 + tid];
      colsum[cg * 64 + tid] = t;
    }
    return;
  }
  const int lane = threadIdx.x & 63, row = blockIdx.x * 4 + (threadIdx.x >> 6);
  const u16* src = x + (size_t)row * DM;
  const u32x4 a = *(const u32x4*)(src + lane * 8), b = *(const u32x4*)(src + 512 + lane * 8);
  float v[16];
#pragma unroll
  for (int e = 0; e < 4; ++e) { v[2 * e] = bflo(a[e]); v[2 * e + 1] = bfhi(a[e]); v[8 + 2 * e] = bflo(b[e]); v[9 + 2 * e] = bfhi(b[e]); }
  float s = 0.f;
#pragma unroll
  for (int e = 0; e < 16; ++e) s += v[e];
#pragma unroll
  for (int o = 1; o < 64; o <<= 1) s += __shfl_xor(s, o);
  const float mean = s * (1.0f / 1024.0f);
  float m2 = 0.f;
#pragma unroll
  for (int e = 0; e < 16; ++e) { const float d = v[e] - mean; m2 += d * d; }
#pragma unroll
  for (int o = 1; o < 64; o <<= 1) m2 += __shfl_xor(m2, o);
  const RowP r = row_params(mean, m2 * (1.0f / 1024.0f), 1);
  u32x4 oa, ob;
#pragma unroll
  for (int e = 0; e < 4; ++e) {
    oa[e] = pk2(rbf(v[2 * e] - r.mhat) / r.dhat, rbf(v[2 * e + 1] - r.mhat) / r.dhat);
    ob[e] = pk2(rbf(v[8 + 2 * e] - r.mhat) / r.dhat, rbf(v[9 + 2 * e] - r.mhat) / r.dhat);
  }
  u16* dst = y + (size_t)row * DM;
  *(u32x4*)(dst + lane * 8) = oa;
  *(u32x4*)(dst + 512 + lane * 8) = ob;
}

__global__ __launch_bounds__(512) void attn_o_k(const u16* __restrict__ q, const u16* __restrict__ wphi, const float* __restrict__ kvpart,
                                                u16* __restrict__ o) {
  __shared__ __attribute__((aligned(16))) char sm[4352];
  const int tid = threadIdx.x, lane = tid & 63, w = tid >> 6;
  constexpr int EPI = 6;
  TS(0);
  const int h = blockIdx.x & 31, rb = blockIdx.x >> 5;
  const int g4 = lane >> 4, r0 = rb * 256 + w * 32 + (lane & 15);
  bf16x8 qf[2], wf[2];
#pragma unroll
  for (int mb = 0; mb < 2; ++mb) qf[mb] = *(const bf16x8*)(q + (size_t)(r0 + mb * 16) * DM + h * 32 + g4 * 8);
#pragma unroll
  for (int fb = 0; fb < 2; ++fb)
#pragma unroll
    for (int j = 0; j < 8; ++j) wf[fb][j] = (short)wphi[h * 1024 + (g4 * 8 + j) * 32 + fb * 16 + (lane & 15)];
  __builtin_amdgcn_sched_barrier(0);
  f32x4 kp[16], kz[16];
  const float* slab = kvpart + (size_t)h * (33 * 64) + tid * 4;
#pragma unroll
  for (int p = 0; p < 16; ++p) kp[p] = *(const f32x4*)(slab + (size_t)p * NH * (33 * 64));
  const bool zw = __builtin_amdgcn_readfirstlane(w) == 0;
  if (zw) {
#pragma unroll
    for (int p = 0; p < 16; ++p) kz[p] = *(const f32x4*)(slab + (size_t)p * NH * (33 * 64) + 2048 - (tid & 48) * 4);
  }
  __builtin_amdgcn_sched_barrier(0);
  bf16x8 pq[2][2];
#pragma unroll
  for (int mb = 0; mb < 2; ++mb) {
    u32x4 pos, neg;
    feature_map(wf, qf[mb], pos, neg);
    pq[mb][0] = __builtin_bit_cast(bf16x8, pos);
    pq[mb][1] = __builtin_bit_cast(bf16x8, neg);
  }
  __builtin_amdgcn_sched_barrier(0);
  TS(1);
  u16* kvt = (u16*)sm;
  {
    f32x4 t = kp[0];
#pragma unroll
    for (int p = 1; p < 16; ++p) t += kp[p];
    *(u32x2*)(kvt + tid * 4) = (u32x2){pk2(t[0], t[1]), pk2(t[2], t[3])};
    if (zw) {
      f32x4 z = kz[0];
#pragma unroll
      for (int p = 1; p < 16; ++p) z += kz[p];
      if (tid < 16) *(u32x2*)(kvt + 2048 + tid * 4) = (u32x2){pk2(z[0], z[1]), pk2(z[2], z[3])};
    }
  }
  TS(2);
  __syncthreads();
  TS(3);
  bf16x8 kf[3][2];
#pragma unroll
  for (int db = 0; db < 3; ++db)
#pragma unroll
    for (int ks = 0; ks < 2; ++ks) {
      const int d = (db < 2) ? db * 16 + (lane & 15) : 32;
      const u16* p = kvt + d * 64 + ks * 32 + g4 * 4;
      kf[db][ks] = cat8(*(const s16x4*)p, *(const s16x4*)(p + 16));
    }
#pragma unroll
  for (int mb = 0; mb < 2; ++mb) {
    f32x4 a[3];
#pragma unroll
    for (int db = 0; db < 3; ++db) {
      a[db] = mfma16(kf[db][0], pq[mb][0], (f32x4){0.f, 0.f, 0.f, 0.f});
      a[db] = mfma16(kf[db][1], pq[mb][1], a[db]);
    }
    const float id = __builtin_amdgcn_rcpf(rbf(rbf(a[2][0]) + 1e-6f));
    u16* dst = o + (size_t)(r0 + mb * 16) * DM + h * 32 + g4 * 4;
#pragma unroll
    for (int db = 0; db < 2; ++db) {
      const f32x2 lo = rbf2((f32x2){a[db][0], a[db][1]}) * id, hi = rbf2((f32x2){a[db][2], a[db][3]}) * id;
      *(u32x2*)(dst + db * 16) = (u32x2){pk2(lo[0], lo[1]), pk2(hi[0], hi[1])};
    }
  }
  TS(4);
  TS(5);
}

template <int BN, int S, int KD, int EPI> static constexpr int smem_bytes() {
  constexpr bool USE_STATS = (EPI == EPI_QKV || EPI == EPI_GELU_SHIFT || EPI == EPI_W2 || EPI == EPI_GELU);
  constexpr int NRES = (EPI == EPI_RES || EPI == EPI_W2) ? 1 : (EPI == EPI_W4 ? 2 : 0);
  return S * (16384 + BN * 128) + NRES * 16384 + (EPI == EPI_QKV ? 4096 : 0) + 2048 + 1024;
}
template <int BN, int S, int KD, int EPI> static void launch_gemm(const GemmArgs& a, hipStream_t st) {
  constexpr int sb = smem_bytes<BN, S, KD, EPI>();
  static_assert(sb <= 163840, "LDS");
  (void)hipFuncSetAttribute((const void*)gemm_k<BN, S, KD, EPI>, hipFuncAttributeMaxDynamicSharedMemorySize, sb);
  gemm_k<BN, S, KD, EPI><<<256, 512, sb, st>>>(a);
}

extern "C" void kernel_launch(void* const* d_in, const int* in_sizes, int n_in, void* d_out, int out_size, void* d_ws,
                              size_t ws_size, hipStream_t stream) {
  const u16* x0 = (const u16*)d_in[0];
  const u16* wqkv = (const u16*)d_in[1];
  const u16* wphi = (const u16*)d_in[2];
  const u16* wo = (const u16*)d_in[3];
  const u16* w1 = (const u16*)d_in[5];
  const u16* w2 = (const u16*)d_in[6];
  const u16* w3 = (const u16*)d_in[7];
  const u16* w4 = (const u16*)d_in[8];
  char* ws = (char*)d_ws;
  const size_t MB = 1 << 20;
  u16* xln0 = (u16*)(ws + 0 * MB);
  u16* phiq = (u16*)(ws + 4 * MB);
  float* kvpart = (float*)(ws + 12 * MB);
  u16* obuf = (u16*)(ws + 17 * MB);
  u16* xa = (u16*)(ws + 21 * MB);
  u16* hsh = (u16*)(ws + 25 * MB);
  u16* x1 = (u16*)(ws + 29 * MB);
  u16* gbuf = (u16*)(ws + 33 * MB);
  u16* xb = (u16*)(ws + 48 * MB);
  float* st_xa = (float*)(ws + 52 * MB);
  float* st_x1 = (float*)(ws + 53 * MB);
  float* st_xb = (float*)(ws + 54 * MB);
  float* colsum = (float*)(ws + 55 * MB);

  ln0_k<<<SEQ / 4 + 120 + 176, 256, 0, stream>>>(x0, xln0, wqkv, w1, w3, colsum, wo, w2, w4);
  for (int layer = 0; layer < 2; ++layer) {
    const u16* xin = layer == 0 ? x0 : xb;
    u16* xout = layer == 0 ? xb : (u16*)d_out;
    GemmArgs a;
    a = GemmArgs{layer == 0 ? xln0 : xb, wqkv, phiq, nullptr, nullptr, st_xb, nullptr, colsum, wphi, kvpart, layer == 0 ? 0 : 1, 0};
    launch_gemm<192, 3, 1024, EPI_QKV>(a, stream);
    attn_o_k<<<256, 512, 0, stream>>>(phiq, wphi, kvpart, obuf);
    a = GemmArgs{obuf, wo, xa, xin, nullptr, nullptr, st_xa, nullptr, nullptr, nullptr, 0, 0};
    launch_gemm<64, 5, 1024, EPI_RES>(a, stream);
    a = GemmArgs{xa, w1, hsh, nullptr, nullptr, st_xa, nullptr, colsum + 3072, nullptr, nullptr, 2, 0};
    launch_gemm<64, 5, 1024, EPI_GELU_SHIFT>(a, stream);
    a = GemmArgs{hsh, w2, x1, xa, nullptr, st_xa, st_x1, nullptr, nullptr, nullptr, 1, 0};
    launch_gemm<64, 5, 1024, EPI_W2>(a, stream);
    a = GemmArgs{x1, w3, gbuf, nullptr, nullptr, st_x1, nullptr, colsum + 4096, nullptr, nullptr, 1, 0};
    launch_gemm<224, 3, 1024, EPI_GELU>(a, stream);
    a = GemmArgs{gbuf, w4, xout, xa, x1, nullptr, st_xb, nullptr, nullptr, nullptr, 0, 0};
    launch_gemm<64, 5, 3584, EPI_W4>(a, stream);
  }
}
```

```cpp
#include <hip/hip_runtime.h>
#include <hip/hip_bf16.h>
#include <stdint.h>

typedef __attribute__((ext_vector_type(8))) short bf16x8;
typedef __attribute__((ext_vector_type(4))) short s16x4;
typedef __attribute__((ext_vector_type(4))) float f32x4;
typedef __attribute__((ext_vector_type(2))) float f32x2;
typedef __attribute__((ext_vector_type(4))) unsigned u32x4;
typedef __attribute__((ext_vector_type(2))) unsigned u32x2;
typedef __attribute__((ext_vector_type(2))) __bf16 bf16x2_t;
typedef unsigned short u16;
#define LDS_AS __attribute__((address_space(3)))
#define DI __device__ __forceinline__

static constexpr int SEQ = 2048, DM = 1024, NH = 32, FF = 3584;

DI float bf2f(unsigned v) { return __uint_as_float(v << 16); }
DI float bflo(unsigned v) { return __uint_as_float(v << 16); }
DI float bfhi(unsigned v) { return __uint_as_float(v & 0xffff0000u); }
DI unsigned pk2(float a, float b) {
  f32x2 v = {a, b};
  return __builtin_bit_cast(unsigned, __builtin_convertvector(v, bf16x2_t));
}
DI float rbf(float a) { return bflo(pk2(a, a)); }

template <int I> struct IC { static constexpr int value = I; };
template <int B, int E, class F> DI void static_for(F&& f) {
  if constexpr (B < E) { f(IC<B>{}); static_for<B + 1, E>(f); }
}
template <int N> DI void wait_vm() { asm volatile("s_waitcnt vmcnt(%0)" ::"n"(N) : "memory"); }
DI void wait_lgkm0() { asm volatile("s_waitcnt lgkmcnt(0)" ::: "memory"); }
DI void bar() { __builtin_amdgcn_s_barrier(); }
DI void glds16(const void* src, char* lds_dst) {
  __builtin_amdgcn_global_load_lds((const unsigned*)src, (unsigned*)lds_dst, 16, 0, 0);
}
DI s16x4 trread(const char* p) {
  return __builtin_bit_cast(s16x4, __builtin_amdgcn_ds_read_tr16_b64_v4i16((LDS_AS s16x4*)p));
}
template <int OFF> DI s16x4 lds_tr(unsigned addr) {
  s16x4 r; asm volatile("ds_read_b64_tr_b16 %0, %1 offset:%2" : "=v"(r) : "v"(addr), "n"(OFF) : "memory"); return r;
}
template <int OFF> DI bf16x8 lds_b128(unsigned addr) {
  bf16x8 r; asm volatile("ds_read_b128 %0, %1 offset:%2" : "=v"(r) : "v"(addr), "n"(OFF) : "memory"); return r;
}
#define WT 15
template <bool W = true> DI void st16_wt(void* p, u32x4 v) {
  if constexpr (W) asm volatile("global_store_dwordx4 %0, %1, off sc1\n\ts_nop 1" ::"v"(p), "v"(v) : "memory");
  else *(u32x4*)p = v;
}
template <bool W = true> DI void st8_wt(void* p, u32x2 v) {
  if constexpr (W) asm volatile("global_store_dwordx2 %0, %1, off sc1" ::"v"(p), "v"(v) : "memory");
  else *(u32x2*)p = v;
}
DI bf16x8 cat8(s16x4 lo, s16x4 hi) { return (bf16x8){lo[0], lo[1], lo[2], lo[3], hi[0], hi[1], hi[2], hi[3]}; }
DI f32x4 mfma16(bf16x8 a, bf16x8 b, f32x4 c) { return __builtin_amdgcn_mfma_f32_16x16x32_bf16(a, b, c, 0, 0, 0); }

DI f32x2 gelu2(f32x2 x) {
  const float c0 = -2.302208198f, c1 = -2.302208198f * 0.044715f;
  const f32x2 a = x * (x * x * c1 + c0);
  const f32x2 d = (f32x2){__builtin_amdgcn_exp2f(a[0]), __builtin_amdgcn_exp2f(a[1])} + 1.0f;
  return x * (f32x2){__builtin_amdgcn_rcpf(d[0]), __builtin_amdgcn_rcpf(d[1])};
}
DI f32x2 rbf2(f32x2 v) { const unsigned p = pk2(v[0], v[1]); return (f32x2){bflo(p), bfhi(p)}; }
DI f32x2 unpk(unsigned p) { return (f32x2){bflo(p), bfhi(p)}; }

DI void feature_map(const bf16x8 (&wf)[2], bf16x8 xf, u32x4& pos, u32x4& neg) {
  f32x2 p[4];
#pragma unroll
  for (int fb = 0; fb < 2; ++fb) {
    const f32x4 d = mfma16(wf[fb], xf, (f32x4){0.f, 0.f, 0.f, 0.f});
    p[fb * 2] = rbf2((f32x2){d[0], d[1]});
    p[fb * 2 + 1] = rbf2((f32x2){d[2], d[3]});
  }
  float mx = fmaxf(p[0][0], p[0][1]), mn = fminf(p[0][0], p[0][1]);
#pragma unroll
  for (int e = 1; e < 4; ++e) { mx = fmaxf(mx, fmaxf(p[e][0], p[e][1])); mn = fminf(mn, fminf(p[e][0], p[e][1])); }
  mx = fmaxf(mx, __shfl_xor(mx, 16)); mx = fmaxf(mx, __shfl_xor(mx, 32));
  mn = fminf(mn, __shfl_xor(mn, 16)); mn = fminf(mn, __shfl_xor(mn, 32));
  const float l2e = 1.4426950408889634f;
  f32x2 ep[4], en[4], sp2 = {0.f, 0.f}, sn2 = {0.f, 0.f};
#pragma unroll
  for (int e = 0; e < 4; ++e) {
    const f32x2 a = (p[e] - mx) * l2e, b = (mn - p[e]) * l2e;
    ep[e] = (f32x2){__builtin_amdgcn_exp2f(a[0]), __builtin_amdgcn_exp2f(a[1])}; sp2 += ep[e];
    en[e] = (f32x2){__builtin_amdgcn_exp2f(b[0]), __builtin_amdgcn_exp2f(b[1])}; sn2 += en[e];
  }
  float sp = sp2[0] + sp2[1], sn = sn2[0] + sn2[1];
  sp += __shfl_xor(sp, 16); sp += __shfl_xor(sp, 32);
  sn += __shfl_xor(sn, 16); sn += __shfl_xor(sn, 32);
  const float ip = __builtin_amdgcn_rcpf(sp), in_ = __builtin_amdgcn_rcpf(sn);
#pragma unroll
  for (int e = 0; e < 4; ++e) {
    const f32x2 a = ep[e] * ip, b = en[e] * in_;
    pos[e] = pk2(a[0], a[1]); neg[e] = pk2(b[0], b[1]);
  }
}

#define TS(i)
struct GemmArgs {
  const u16* A;
  const u16* B;
  u16* out;
  const u16* res1;
  const u16* res2;
  const float* stats_in;
  float* stats_out;
  const float* colsum;
  const u16* wphi;
  float* kvpart;
  int ln_mode;
  int unused;
};

enum { EPI_QKV = 0, EPI_RES = 1, EPI_GELU_SHIFT = 2, EPI_W2 = 3, EPI_GELU = 4, EPI_W4 = 5 };

struct RowP { float alpha, beta, mhat, dhat; };
DI RowP row_params(float mean, float var, int mode) {
  RowP r;
  const float mh = rbf(mean);
  const float dh = rbf(__builtin_amdgcn_sqrtf(rbf(rbf(var) + 1e-5f)));
  const float i1 = __builtin_amdgcn_rcpf(dh);
  r.mhat = mh; r.dhat = dh;
  if (mode == 2) {
    const float m2 = (mean - mh) * i1;
    const float v2 = var * i1 * i1;
    const float mh2 = rbf(m2);
    const float dh2 = rbf(__builtin_amdgcn_sqrtf(rbf(rbf(v2) + 1e-5f)));
    const float i2 = __builtin_amdgcn_rcpf(dh2);
    r.alpha = i1 * i2;
    r.beta = (mh * i1 + mh2) * i2;
  } else {
    r.alpha = i1;
    r.beta = mh * i1;
  }
  return r;
}

template <int BN, int S, int KD, int EPI>
__global__ __launch_bounds__(512, 2) void gemm_k(GemmArgs g) {
  constexpr int NK = KD / 64;
  constexpr int NPF = BN / 64;
  constexpr bool HALF = (BN % 64) != 0;
  constexpr int NI = BN / 32;
  constexpr int A_BYTES = 16384;
  constexpr int STAGE = A_BYTES + BN * 128;
  constexpr int L = 4 + NPF * 2 + (HALF ? 1 : 0);
  constexpr int NTOT = (EPI == EPI_QKV) ? 3 * DM : (EPI == EPI_GELU ? FF : DM);
  constexpr bool USE_STATS = (EPI == EPI_QKV || EPI == EPI_GELU_SHIFT || EPI == EPI_W2 || EPI == EPI_GELU);
  constexpr bool FOLD = (EPI == EPI_QKV || EPI == EPI_GELU_SHIFT || EPI == EPI_GELU);
  constexpr int NRES = (EPI == EPI_RES || EPI == EPI_W2) ? 1 : (EPI == EPI_W4 ? 2 : 0);
  constexpr int OFF_STATS = S * STAGE;
  constexpr int OFF_RES = OFF_STATS;
  constexpr int OFF_WPHI = OFF_RES + NRES * 16384;
  constexpr int OFF_ROWP = OFF_WPHI + (EPI == EPI_QKV ? 4096 : 0);
  constexpr int OFF_CS = OFF_ROWP + 2048;
  constexpr int NEXTRA = (USE_STATS && BN != 64 ? 8 : 0) + NRES * 4 + (EPI == EPI_QKV ? 1 : 0) + (FOLD ? 1 : 0);

  extern __shared__ __attribute__((aligned(16))) char smem[];
  const int tid = threadIdx.x, lane = tid & 63;
  const int w = __builtin_amdgcn_readfirstlane(tid >> 6);
  const bool prod = w >= 4;
  const int cw = w & 3, wr = cw >> 1, wc = cw & 1;

  const int b = blockIdx.x, xcd = b & 7, j = b >> 3;
  int rp, ct;
  if constexpr (EPI == EPI_QKV || EPI == EPI_GELU) { rp = (xcd >> 2) * 8 + (j >> 2); ct = (xcd & 3) * 4 + (j & 3); }
  else { rp = (xcd >> 1) * 4 + (j >> 3); ct = (xcd & 1) * 8 + (j & 7); }
  const int m0 = rp * 128;
  const int n0 = (EPI == EPI_QKV) ? ct * 64 : ct * BN;
  constexpr int PSTRIDE = (EPI == EPI_QKV) ? DM : 64;
  const int g4 = lane >> 4, q4 = (lane >> 2) & 3, pp = lane & 3;
  RowP* rowp = (RowP*)(smem + OFF_ROWP);
  const bool ln_on = USE_STATS && (g.ln_mode != 0 || EPI == EPI_W2);

  f32x4 acc[4][NI];
  TS(0);
  constexpr bool RP_CONS = (BN == 64);
  f32x2 sv[8];
  const int sr = cw * 32 + (lane >> 1), shf = lane & 1;
  auto load_stats = [&]() {
#pragma unroll
    for (int p = 0; p < 8; ++p)
      asm volatile("global_load_dwordx2 %0, %1, off" : "=v"(sv[p]) : "v"(g.stats_in + ((size_t)(shf * 8 + p) * SEQ + m0 + sr) * 2) : "memory");
  };
  float rs_s = 0.f, rs_m2 = 0.f;
  auto row_stats = [&](auto pc) {
    constexpr int piece = decltype(pc)::value;
    if constexpr (piece == 0) {
#pragma unroll
      for (int p = 0; p < 8; ++p) asm volatile("" : "+v"(sv[p]));
      float s = 0.f;
#pragma unroll
      for (int p = 0; p < 8; ++p) s += sv[p][0];
      rs_s = s + __shfl_xor(s, 1);
    } else if constexpr (piece == 1) {
      const float mean = rs_s * (1.0f / 1024.0f);
      float m2 = 0.f;
#pragma unroll
      for (int p = 0; p < 8; ++p) { const float d = sv[p][0] * (1.0f / 64.0f) - mean; m2 += sv[p][1] + 64.0f * d * d; }
      rs_m2 = m2 + __shfl_xor(m2, 1);
    } else {
      if (ln_on && shf == 0) rowp[sr] = row_params(rs_s * (1.0f / 1024.0f), rs_m2 * (1.0f / 1024.0f), EPI == EPI_W2 ? 1 : g.ln_mode);
      wait_lgkm0();
    }
    __builtin_amdgcn_sched_barrier(0);
  };
  if (__builtin_expect(prod, 1)) {
    const u16* a_src = g.A + (size_t)(m0 + cw * 32 + (lane >> 3)) * KD + (((lane & 7) ^ (lane >> 3)) * 8);
    const u16* b_src[2];
#pragma unroll
    for (int e = 0; e < 2; ++e) {
      const int r = lane >> 3;
      const int f = ((r >> 1) & 1) | (e << 1);
      const int c = ((((lane & 7) >> 1) ^ f) << 1) | (lane & 1);
      b_src[e] = g.B + (size_t)((cw * 2 + e) * 8 + r) * NTOT + n0 + c * 8;
    }
    const u16* bh_src = nullptr;
    if constexpr (HALF) {
      const int c = ((((lane >> 1) & 1) ^ ((lane >> 5) & 1)) << 1) | (lane & 1);
      bh_src = g.B + (size_t)(cw * 16 + (lane >> 2)) * NTOT + n0 + NPF * 64 + c * 8;
    }
    auto issue = [&](int kt, int slot) {
      char* base = smem + slot * STAGE;
#pragma unroll
      for (int p = 0; p < NPF; ++p)
#pragma unroll
        for (int e = 0; e < 2; ++e)
          glds16(b_src[e] + (size_t)kt * 64 * NTOT + p * PSTRIDE, base + A_BYTES + p * 8192 + (cw * 2 + e) * 1024);
      if constexpr (HALF) glds16(bh_src + (size_t)kt * 64 * NTOT, base + A_BYTES + NPF * 8192 + cw * 1024);
#pragma unroll
      for (int e = 0; e < 4; ++e) glds16(a_src + (size_t)e * 8 * KD + kt * 64, base + (cw * 4 + e) * 1024);
    };
    TS(6);
    issue(0, 0);
    issue(1, 1);
    wait_vm<L>();
    TS(1);
    bar();
    static_for<2, S>([&](auto ic) { issue(ic.value, ic.value); });
    TS(7);
    if constexpr (NRES >= 1) {
#pragma unroll
      for (int e = 0; e < 4; ++e) {
        const int r = (cw * 4 + e) * 8 + (lane >> 3);
        glds16(g.res1 + (size_t)(m0 + r) * DM + n0 + (lane & 7) * 8, smem + OFF_RES + (cw * 4 + e) * 1024);
      }
    }
    if constexpr (NRES >= 2) {
#pragma unroll
      for (int e = 0; e < 4; ++e) {
        const int r = (cw * 4 + e) * 8 + (lane >> 3);
        glds16(g.res2 + (size_t)(m0 + r) * DM + n0 + (lane & 7) * 8, smem + OFF_RES + 16384 + (cw * 4 + e) * 1024);
      }
    }
    if constexpr (EPI == EPI_QKV) glds16(g.wphi + (size_t)ct * 2048 + cw * 512 + lane * 8, smem + OFF_WPHI + cw * 1024);
    if constexpr (FOLD) {
      int col = cw * 64 + (lane & 15) * 4;
      if (col > BN - 4) col = BN - 4;
      const int gcol = (EPI == EPI_QKV) ? (col >> 6) * DM + n0 + (col & 63) : n0 + col;
      if (lane < 16) glds16(g.colsum + gcol, smem + OFF_CS + cw * 256);
    }
    if constexpr (USE_STATS && !RP_CONS) load_stats();
    static_for<0, NK - 1>([&](auto ic) {
      constexpr int kt = ic.value;
      constexpr int young = (S - 2 < NK - 2 - kt) ? S - 2 : NK - 2 - kt;
      constexpr int ex = (kt + 1 <= S - 1 && kt + 1 < NK - 1) ? NEXTRA : 0;
      wait_vm<young * L + ex>();
      bar();
      if constexpr (kt + S < NK) issue(kt + S, kt % S);
      if constexpr (USE_STATS && !RP_CONS && kt >= 4 && kt < 7) row_stats(IC<kt - 4>{});
    });
    TS(2);
    bar();
  } else {
    const int fsw = ((q4 >> 1) & 1) | ((g4 & 1) << 1);
    int a_off[2], b_off[2], bh_off = 0;
#pragma unroll
    for (int k = 0; k < 2; ++k) {
      a_off[k] = (wr * 64 + (lane & 15)) * 128 + (((k * 4 + g4) ^ (lane & 7)) * 16);
      b_off[k] = A_BYTES + (8 * g4 + q4) * 128 + (((2 * k + wc) ^ fsw) * 32) + pp * 8;
    }
    if constexpr (HALF) bh_off = A_BYTES + NPF * 8192 + (8 * g4 + q4) * 64 + ((wc ^ (g4 & 1)) * 32) + pp * 8;
#pragma unroll
    for (int m = 0; m < 4; ++m)
#pragma unroll
      for (int i = 0; i < NI; ++i) acc[m][i] = (f32x4){0.f, 0.f, 0.f, 0.f};
    bf16x8 xa[2][4];
    s16x4 wlo[2][NI], whi[2][NI];
    const unsigned lbase = (unsigned)(size_t)smem;

    auto ldfrag = [&](auto setc, auto slotc, auto kc) {
      constexpr int set = decltype(setc)::value, slot = decltype(slotc)::value, k32 = decltype(kc)::value;
      const unsigned sb = lbase + slot * STAGE;
      const unsigned ba0 = sb + b_off[0], ba1 = sb + b_off[1], bah = sb + bh_off, aa = sb + a_off[k32];
      static_for<0, NI>([&](auto ic) {
        constexpr int i = decltype(ic)::value;
        if constexpr (HALF && i == NI - 1) {
          wlo[set][i] = lds_tr<k32 * 2048>(bah);
          whi[set][i] = lds_tr<k32 * 2048 + 256>(bah);
        } else {
          wlo[set][i] = lds_tr<(i >> 1) * 8192 + k32 * 4096>((i & 1) ? ba1 : ba0);
          whi[set][i] = lds_tr<(i >> 1) * 8192 + k32 * 4096 + 512>((i & 1) ? ba1 : ba0);
        }
      });
      static_for<0, 4>([&](auto mc) { constexpr int m = decltype(mc)::value; xa[set][m] = lds_b128<m * 2048>(aa); });
    };
    auto wait_set = [&](auto setc) {
      constexpr int set = decltype(setc)::value;
      asm volatile("s_waitcnt lgkmcnt(0)" : "+v"(xa[set][0]), "+v"(xa[set][1]), "+v"(xa[set][2]), "+v"(xa[set][3]) :: "memory");
#pragma unroll
      for (int i = 0; i < NI; ++i) asm volatile("" : "+v"(wlo[set][i]), "+v"(whi[set][i]));
      __builtin_amdgcn_sched_barrier(0);
    };
    auto ld1 = [&](auto setc, auto slotc, auto kc, auto rc) {
      constexpr int set = decltype(setc)::value, slot = decltype(slotc)::value, k32 = decltype(kc)::value, r = decltype(rc)::value;
      const unsigned sb = lbase + slot * STAGE;
      if constexpr (r < 2 * NI) {
        constexpr int i = r >> 1, hi = r & 1;
        s16x4 v;
        if constexpr (HALF && i == NI - 1) v = lds_tr<k32 * 2048 + hi * 256>(sb + bh_off);
        else v = lds_tr<(i >> 1) * 8192 + k32 * 4096 + hi * 512>(sb + b_off[i & 1]);
        if constexpr (hi) whi[set][i] = v; else wlo[set][i] = v;
      } else {
        constexpr int m = r - 2 * NI;
        xa[set][m] = lds_b128<m * 2048>(sb + a_off[k32]);
      }
    };
    auto mma_ld = [&](auto setc, auto ldc, auto slotc, auto kc) {
      constexpr int set = decltype(setc)::value;
      constexpr bool ld = decltype(ldc)::value != 0;
      static_for<0, 4 * NI>([&](auto tc) {
        constexpr int t = decltype(tc)::value, i = t >> 2, m = t & 3;
        acc[m][i] = mfma16(cat8(wlo[set][i], whi[set][i]), xa[set][m], acc[m][i]);
        if constexpr (ld && t < 2 * NI + 4) ld1(IC<set ^ 1>{}, slotc, kc, tc);
        __builtin_amdgcn_sched_barrier(0);
      });
    };

    if constexpr (USE_STATS && RP_CONS) load_stats();
    bar();
    TS(1);
    ldfrag(IC<0>{}, IC<0>{}, IC<0>{});
    static_for<0, NK>([&](auto ic) {
      constexpr int kt = ic.value;
      constexpr int slot = kt % S;
      wait_set(IC<0>{});
      if constexpr (USE_STATS && RP_CONS && kt >= 6 && kt < 9) { if constexpr (kt == 6) wait_vm<0>(); row_stats(IC<kt - 6>{}); }
      mma_ld(IC<0>{}, IC<1>{}, IC<slot>{}, IC<1>{});
      wait_set(IC<1>{});
      if constexpr (kt + 1 < NK) {
        bar();
        __builtin_amdgcn_sched_barrier(0);
        mma_ld(IC<1>{}, IC<1>{}, IC<(kt + 1) % S>{}, IC<0>{});
      } else {
        mma_ld(IC<1>{}, IC<0>{}, IC<0>{}, IC<0>{});
      }
    });
    TS(2);
    bar();
  }
  TS(3);

  constexpr int TSTR = BN * 2 + (EPI == EPI_QKV ? 32 : 16);
  char* T = smem;
  if (!prod) {
    float al[4] = {1.f, 1.f, 1.f, 1.f}, be[4] = {0.f, 0.f, 0.f, 0.f};
    if constexpr (FOLD) {
      if (ln_on) {
#pragma unroll
        for (int m = 0; m < 4; ++m) { const RowP r = rowp[wr * 64 + m * 16 + (lane & 15)]; al[m] = r.alpha; be[m] = r.beta; }
      }
    }
#pragma unroll
    for (int i = 0; i < NI; ++i) {
      const int nb = 2 * i + wc;
      const int ncol = nb * 16 + g4 * 4;
      f32x4 cs = {0.f, 0.f, 0.f, 0.f};
      if constexpr (FOLD) {
        if (ln_on) cs = *(const f32x4*)(smem + OFF_CS + ncol * 4);
      }
#pragma unroll
      for (int m = 0; m < 4; ++m) {
        u32x2 pkd;
#pragma unroll
        for (int h = 0; h < 2; ++h) {
          f32x2 v = {acc[m][i][2 * h], acc[m][i][2 * h + 1]};
          if constexpr (FOLD) v = v * al[m] - (f32x2){cs[2 * h], cs[2 * h + 1]} * be[m];
          if constexpr (EPI == EPI_GELU_SHIFT || EPI == EPI_GELU) v = gelu2(rbf2(v));
          pkd[h] = pk2(v[0], v[1]);
        }
        *(u32x2*)(T + (wr * 64 + m * 16 + (lane & 15)) * TSTR + ncol * 2) = pkd;
      }
      if constexpr (EPI == EPI_GELU) {
        if ((i & 1) && i < NI - 1) { wait_lgkm0(); bar(); }
      }
    }
  } else if constexpr (EPI == EPI_GELU) {
#pragma unroll
    for (int p = 0; p < NI / 2; ++p) {
      bar();
      asm volatile("" ::: "memory");
#pragma unroll
      for (int it = 0; it < 4; ++it) {
        const int r = it * 32 + ((tid - 256) >> 3), c = p * 8 + (tid & 7);
        st16_wt(g.out + (size_t)(m0 + r) * FF + n0 + c * 8, *(const u32x4*)(T + r * TSTR + c * 16));
      }
    }
  }
  __syncthreads();
  TS(4);
  if constexpr (EPI == EPI_GELU) {
    const int r = tid >> 2, c = (NI / 2) * 8 + (tid & 3);
    st16_wt(g.out + (size_t)(m0 + r) * FF + n0 + c * 8, *(const u32x4*)(T + r * TSTR + c * 16));
  } else if constexpr (EPI == EPI_GELU_SHIFT) {
    const int grp = n0 >> 8;
    const int sh = (grp == 0) ? 1 : (grp == 1) ? -1 : (grp == 2) ? 2 : -2;
#pragma unroll
    for (int it = 0; it < 2; ++it) {
      const int r = it * 64 + (tid >> 3), c = tid & 7;
      const u32x4 v = *(const u32x4*)(T + r * TSTR + c * 16);
      const int dr = m0 + r + sh;
      if (dr >= 0 && dr < SEQ) st16_wt<(WT & 2) != 0>(g.out + (size_t)dr * DM + n0 + c * 8, v);
    }
    if (tid < 16) {
      const int zr = tid >> 3, c = tid & 7;
      const int az = sh > 0 ? sh : -sh;
      if (zr < az) {
        if (sh > 0 && rp == 0) *(u32x4*)(g.out + (size_t)zr * DM + n0 + c * 8) = (u32x4){0u, 0u, 0u, 0u};
        if (sh < 0 && rp == 15) *(u32x4*)(g.out + (size_t)(SEQ - 1 - zr) * DM + n0 + c * 8) = (u32x4){0u, 0u, 0u, 0u};
      }
    }
  } else if constexpr (EPI == EPI_RES || EPI == EPI_W2 || EPI == EPI_W4) {
#pragma unroll
    for (int it = 0; it < 2; ++it) {
      const int r = it * 64 + (tid >> 3), c = tid & 7;
      const u32x4 v = *(const u32x4*)(T + r * TSTR + c * 16);
      const u32x4 x = *(const u32x4*)(smem + OFF_RES + r * 128 + c * 16);
      f32x2 y[4];
      if constexpr (EPI == EPI_RES) {
#pragma unroll
        for (int e = 0; e < 4; ++e) y[e] = unpk(x[e]) + unpk(v[e]);
      } else if constexpr (EPI == EPI_W2) {
        const RowP rr = rowp[r];
#pragma unroll
        for (int e = 0; e < 4; ++e) y[e] = rbf2(rbf2(unpk(x[e]) - rr.mhat) * rr.alpha) + unpk(v[e]);
      } else {
        const u32x4 x1 = *(const u32x4*)(smem + OFF_RES + 16384 + r * 128 + c * 16);
#pragma unroll
        for (int e = 0; e < 4; ++e) y[e] = unpk(x[e]) + rbf2(unpk(x1[e]) + unpk(v[e]));
      }
      u32x4 o;
#pragma unroll
      for (int e = 0; e < 4; ++e) { o[e] = pk2(y[e][0], y[e][1]); y[e] = unpk(o[e]); }
      st16_wt<(WT & 2) != 0>(g.out + (size_t)(m0 + r) * DM + n0 + c * 8, o);
      const f32x2 s2 = (y[0] + y[1]) + (y[2] + y[3]);
      float s = s2[0] + s2[1];
      s += __shfl_xor(s, 1); s += __shfl_xor(s, 2); s += __shfl_xor(s, 4);
      const float mc = s * (1.0f / 64.0f);
      f32x2 q2 = {0.f, 0.f};
#pragma unroll
      for (int e = 0; e < 4; ++e) { const f32x2 d = y[e] - mc; q2 += d * d; }
      float m2 = q2[0] + q2[1];
      m2 += __shfl_xor(m2, 1); m2 += __shfl_xor(m2, 2); m2 += __shfl_xor(m2, 4);
      if (c == 0) *(f32x2*)(g.stats_out + ((size_t)ct * SEQ + m0 + r) * 2) = (f32x2){s, m2};
    }
  } else {
    constexpr int PKSTR = 160;
    char* PK = smem + 128 * TSTR;
#pragma unroll
    for (int it = 0; it < 2; ++it) {
      const int r = it * 64 + (tid >> 3), c = tid & 7;
      st16_wt<(WT & 4) != 0>(g.out + (size_t)(m0 + r) * DM + n0 + c * 8, *(const u32x4*)(T + r * TSTR + c * 16));
    }
    {
      const int hh = w & 1, rq = w >> 1;
      bf16x8 wf[2];
#pragma unroll
      for (int fb = 0; fb < 2; ++fb) {
        const char* p = smem + OFF_WPHI + hh * 2048 + (8 * g4 + q4) * 64 + fb * 32 + pp * 8;
        wf[fb] = cat8(trread(p), trread(p + 256));
      }
#pragma unroll
      for (int mb = 0; mb < 2; ++mb) {
        const int row = rq * 32 + mb * 16 + (lane & 15);
        const bf16x8 xf = *(const bf16x8*)(T + row * TSTR + (64 + hh * 32 + g4 * 8) * 2);
        u32x4 pos, neg;
        feature_map(wf, xf, pos, neg);
        char* dst = PK + (hh * 128 + row) * PKSTR + g4 * 8;
        *(u32x2*)dst = (u32x2){pos[0], pos[1]}; *(u32x2*)(dst + 32) = (u32x2){pos[2], pos[3]};
        *(u32x2*)(dst + 64) = (u32x2){neg[0], neg[1]}; *(u32x2*)(dst + 96) = (u32x2){neg[2], neg[3]};
      }
    }
    __syncthreads();
    {
      const int hh = w >> 2, fa = w & 3;
      f32x4 kv[3];
#pragma unroll
      for (int d = 0; d < 3; ++d) kv[d] = (f32x4){0.f, 0.f, 0.f, 0.f};
      const short one = (short)0x3F80;
      const bf16x8 ones = {one, one, one, one, one, one, one, one};
#pragma unroll
      for (int ks = 0; ks < 4; ++ks) {
        const int row = ks * 32 + 8 * g4 + q4;
        bf16x8 vf[2];
#pragma unroll
        for (int d = 0; d < 2; ++d) {
          const char* p = T + row * TSTR + (128 + hh * 32 + d * 16) * 2 + pp * 8;
          vf[d] = cat8(trread(p), trread(p + 4 * TSTR));
        }
        const char* p = PK + (hh * 128 + row) * PKSTR + (fa * 16) * 2 + pp * 8;
        const bf16x8 pf = cat8(trread(p), trread(p + 4 * PKSTR));
        kv[0] = mfma16(pf, vf[0], kv[0]);
        kv[1] = mfma16(pf, vf[1], kv[1]);
        kv[2] = mfma16(pf, ones, kv[2]);
      }
      float* slab = g.kvpart + ((size_t)rp * NH + ct * 2 + hh) * (33 * 64);
      const int f = fa * 16 + g4 * 4;
      st16_wt<(WT & 8) != 0>(slab + (lane & 15) * 64 + f, __builtin_bit_cast(u32x4, kv[0]));
      st16_wt<(WT & 8) != 0>(slab + (16 + (lane & 15)) * 64 + f, __builtin_bit_cast(u32x4, kv[1]));
      if ((lane & 15) == 0) st16_wt<(WT & 8) != 0>(slab + 32 * 64 + f, __builtin_bit_cast(u32x4, kv[2]));
    }
  }
  TS(5);
}

__global__ __launch_bounds__(256) void ln0_k(const u16* __restrict__ x, u16* __restrict__ y, const u16* __restrict__ wqkv,
                                             const u16* __restrict__ w1, const u16* __restrict__ w3, float* __restrict__ colsum,
                                             const u16* __restrict__ wo, const u16* __restrict__ w2, const u16* __restrict__ w4) {
  if (blockIdx.x >= SEQ / 4 + 120) {
    const int t = blockIdx.x - (SEQ / 4 + 120);
    const u16* W = t < 32 ? wo + (size_t)t * 32768 : (t < 64 ? w2 + (size_t)(t - 32) * 32768 : w4 + (size_t)(t - 64) * 32768);
#pragma unroll
    for (int i = 0; i < 16; ++i) {
      const u32x4 v = *(const u32x4*)(W + (i * 256 + threadIdx.x) * 8);
      asm volatile("" ::"v"(v));
    }
    return;
  }
  if (blockIdx.x >= SEQ / 4) {
    __shared__ float red[32 * 64];
    const int tid = threadIdx.x;
    const int cg = blockIdx.x - SEQ / 4;
    const u16* W; int N, c0;
    if (cg < 48) { W = wqkv; N = 3 * DM; c0 = cg * 64; }
    else if (cg < 64) { W = w1; N = DM; c0 = (cg - 48) * 64; }
    else { W = w3; N = FF; c0 = (cg - 64) * 64; }
    const int c = tid & 7, rg = tid >> 3;
    f32x2 s[4] = {{0.f, 0.f}, {0.f, 0.f}, {0.f, 0.f}, {0.f, 0.f}};
#pragma unroll 16
    for (int i = 0; i < 32; ++i) {
      const u32x4 v = *(const u32x4*)(W + (size_t)(rg + i * 32) * N + c0 + c * 8);
#pragma unroll
      for (int e = 0; e < 4; ++e) s[e] += unpk(v[e]);
    }
#pragma unroll
    for (int e = 0; e < 4; ++e) *(f32x2*)(red + rg * 64 + c * 8 + 2 * e) = s[e];
    __syncthreads();
    if (tid < 64) {
      float t = 0.f;
#pragma unroll
      for (int i = 0; i < 32; ++i) t += red[i * 64 + tid];
      colsum[cg * 64 + tid] = t;
    }
    return;
  }
  const int lane = threadIdx.x & 63, row = blockIdx.x * 4 + (threadIdx.x >> 6);
  const u16* src = x + (size_t)row * DM;
  const u32x4 a = *(const u32x4*)(src + lane * 8), b = *(const u32x4*)(src + 512 + lane * 8);
  float v[16];
#pragma unroll
  for (int e = 0; e < 4; ++e) { v[2 * e] = bflo(a[e]); v[2 * e + 1] = bfhi(a[e]); v[8 + 2 * e] = bflo(b[e]); v[9 + 2 * e] = bfhi(b[e]); }
  float s = 0.f;
#pragma unroll
  for (int e = 0; e < 16; ++e) s += v[e];
#pragma unroll
  for (int o = 1; o < 64; o <<= 1) s += __shfl_xor(s, o);
  const float mean = s * (1.0f / 1024.0f);
  float m2 = 0.f;
#pragma unroll
  for (int e = 0; e < 16; ++e) { const float d = v[e] - mean; m2 += d * d; }
#pragma unroll
  for (int o = 1; o < 64; o <<= 1) m2 += __shfl_xor(m2, o);
  const RowP r = row_params(mean, m2 * (1.0f / 1024.0f), 1);
  u32x4 oa, ob;
#pragma unroll
  for (int e = 0; e < 4; ++e) {
    oa[e] = pk2(rbf(v[2 * e] - r.mhat) / r.dhat, rbf(v[2 * e + 1] - r.mhat) / r.dhat);
    ob[e] = pk2(rbf(v[8 + 2 * e] - r.mhat) / r.dhat, rbf(v[9 + 2 * e] - r.mhat) / r.dhat);
  }
  u16* dst = y + (size_t)row * DM;
  st16_wt<(WT & 32) != 0>(dst + lane * 8, oa);
  st16_wt<(WT & 32) != 0>(dst + 512 + lane * 8, ob);
}

__global__ __launch_bounds__(512) void attn_o_k(const u16* __restrict__ q, const u16* __restrict__ wphi, const float* __restrict__ kvpart,
                                                u16* __restrict__ o) {
  constexpr int KS = 68;
  __shared__ __attribute__((aligned(16))) char sm[33 * KS * 2 + 8];
  const int tid = threadIdx.x, lane = tid & 63, w = tid >> 6;
  constexpr int EPI = 6;
  TS(0);
  const int h = blockIdx.x & 31, rb = blockIdx.x >> 5;
  const int g4 = lane >> 4, r0 = rb * 256 + w * 32 + (lane & 15);
  bf16x8 qf[2], wf[2];
#pragma unroll
  for (int mb = 0; mb < 2; ++mb) qf[mb] = *(const bf16x8*)(q + (size_t)(r0 + mb * 16) * DM + h * 32 + g4 * 8);
#pragma unroll
  for (int fb = 0; fb < 2; ++fb)
#pragma unroll
    for (int j = 0; j < 8; ++j) wf[fb][j] = (short)wphi[h * 1024 + (g4 * 8 + j) * 32 + fb * 16 + (lane & 15)];
  __builtin_amdgcn_sched_barrier(0);
  f32x4 kp[16], kz[16];
  const float* slab = kvpart + (size_t)h * (33 * 64) + tid * 4;
#pragma unroll
  for (int p = 0; p < 16; ++p) kp[p] = *(const f32x4*)(slab + (size_t)p * NH * (33 * 64));
  const bool zw = __builtin_amdgcn_readfirstlane(w) == 0;
  if (zw) {
#pragma unroll
    for (int p = 0; p < 16; ++p) kz[p] = *(const f32x4*)(slab + (size_t)p * NH * (33 * 64) + 2048 - (tid & 48) * 4);
  }
  __builtin_amdgcn_sched_barrier(0);
  bf16x8 pq[2][2];
#pragma unroll
  for (int mb = 0; mb < 2; ++mb) {
    u32x4 pos, neg;
    feature_map(wf, qf[mb], pos, neg);
    pq[mb][0] = __builtin_bit_cast(bf16x8, pos);
    pq[mb][1] = __builtin_bit_cast(bf16x8, neg);
  }
  __builtin_amdgcn_sched_barrier(0);
  TS(1);
  u16* kvt = (u16*)sm;
  {
    f32x4 t = kp[0];
#pragma unroll
    for (int p = 1; p < 16; ++p) t += kp[p];
    *(u32x2*)(kvt + (tid >> 4) * KS + (tid & 15) * 4) = (u32x2){pk2(t[0], t[1]), pk2(t[2], t[3])};
    if (zw) {
      f32x4 z = kz[0];
#pragma unroll
      for (int p = 1; p < 16; ++p) z += kz[p];
      if (tid < 16) *(u32x2*)(kvt + 32 * KS + tid * 4) = (u32x2){pk2(z[0], z[1]), pk2(z[2], z[3])};
    }
  }
  TS(2);
  __syncthreads();
  TS(3);
  bf16x8 kf[3][2];
#pragma unroll
  for (int db = 0; db < 3; ++db)
#pragma unroll
    for (int ks = 0; ks < 2; ++ks) {
      const int d = (db < 2) ? db * 16 + (lane & 15) : 32;
      const u16* p = kvt + d * KS + ks * 32 + g4 * 4;
      kf[db][ks] = cat8(*(const s16x4*)p, *(const s16x4*)(p + 16));
    }
#pragma unroll
  for (int mb = 0; mb < 2; ++mb) {
    f32x4 a[3];
#pragma unroll
    for (int db = 0; db < 3; ++db) {
      a[db] = mfma16(kf[db][0], pq[mb][0], (f32x4){0.f, 0.f, 0.f, 0.f});
      a[db] = mfma16(kf[db][1], pq[mb][1], a[db]);
    }
    const float id = __builtin_amdgcn_rcpf(rbf(rbf(a[2][0]) + 1e-6f));
    u16* dst = o + (size_t)(r0 + mb * 16) * DM + h * 32 + g4 * 4;
#pragma unroll
    for (int db = 0; db < 2; ++db) {
      const f32x2 lo = rbf2((f32x2){a[db][0], a[db][1]}) * id, hi = rbf2((f32x2){a[db][2], a[db][3]}) * id;
      st8_wt<(WT & 16) != 0>(dst + db * 16, (u32x2){pk2(lo[0], lo[1]), pk2(hi[0], hi[1])});
    }
  }
  TS(4);
  TS(5);
}

template <int BN, int S, int KD, int EPI> static constexpr int smem_bytes() {
  constexpr bool USE_STATS = (EPI == EPI_QKV || EPI == EPI_GELU_SHIFT || EPI == EPI_W2 || EPI == EPI_GELU);
  constexpr int NRES = (EPI == EPI_RES || EPI == EPI_W2) ? 1 : (EPI == EPI_W4 ? 2 : 0);
  return S * (16384 + BN * 128) + NRES * 16384 + (EPI == EPI_QKV ? 4096 : 0) + 2048 + 1024;
}
template <int BN, int S, int KD, int EPI> static void launch_gemm(const GemmArgs& a, hipStream_t st) {
  constexpr int sb = smem_bytes<BN, S, KD, EPI>();
  static_assert(sb <= 163840, "LDS");
  (void)hipFuncSetAttribute((const void*)gemm_k<BN, S, KD, EPI>, hipFuncAttributeMaxDynamicSharedMemorySize, sb);
  gemm_k<BN, S, KD, EPI><<<256, 512, sb, st>>>(a);
}

extern "C" void kernel_launch(void* const* d_in, const int* in_sizes, int n_in, void* d_out, int out_size, void* d_ws,
                              size_t ws_size, hipStream_t stream) {
  const u16* x0 = (const u16*)d_in[0];
  const u16* wqkv = (const u16*)d_in[1];
  const u16* wphi = (const u16*)d_in[2];
  const u16* wo = (const u16*)d_in[3];
  const u16* w1 = (const u16*)d_in[5];
  const u16* w2 = (const u16*)d_in[6];
  const u16* w3 = (const u16*)d_in[7];
  const u16* w4 = (const u16*)d_in[8];
  char* ws = (char*)d_ws;
  const size_t MB = 1 << 20;
  u16* xln0 = (u16*)(ws + 0 * MB);
  u16* phiq = (u16*)(ws + 4 * MB);
  float* kvpart = (float*)(ws + 12 * MB);
  u16* obuf = (u16*)(ws + 17 * MB);
  u16* xa = (u16*)(ws + 21 * MB);
  u16* hsh = (u16*)(ws + 25 * MB);
  u16* x1 = (u16*)(ws + 29 * MB);
  u16* gbuf = (u16*)(ws + 33 * MB);
  u16* xb = (u16*)(ws + 48 * MB);
  float* st_xa = (float*)(ws + 52 * MB);
  float* st_x1 = (float*)(ws + 53 * MB);
  float* st_xb = (float*)(ws + 54 * MB);
  float* colsum = (float*)(ws + 55 * MB);

  ln0_k<<<SEQ / 4 + 120 + 176, 256, 0, stream>>>(x0, xln0, wqkv, w1, w3, colsum, wo, w2, w4);
  for (int layer = 0; layer < 2; ++layer) {
    const u16* xin = layer == 0 ? x0 : xb;
    u16* xout = layer == 0 ? xb : (u16*)d_out;
    GemmArgs a;
    a = GemmArgs{layer == 0 ? xln0 : xb, wqkv, phiq, nullptr, nullptr, st_xb, nullptr, colsum, wphi, kvpart, layer == 0 ? 0 : 1, 0};
    launch_gemm<192, 3, 1024, EPI_QKV>(a, stream);
    attn_o_k<<<256, 512, 0, stream>>>(phiq, wphi, kvpart, obuf);
    a = GemmArgs{obuf, wo, xa, xin, nullptr, nullptr, st_xa, nullptr, nullptr, nullptr, 0, 0};
    launch_gemm<64, 5, 1024, EPI_RES>(a, stream);
    a = GemmArgs{xa, w1, hsh, nullptr, nullptr, st_xa, nullptr, colsum + 3072, nullptr, nullptr, 2, 0};
    launch_gemm<64, 5, 1024, EPI_GELU_SHIFT>(a, stream);
    a = GemmArgs{hsh, w2, x1, xa, nullptr, st_xa, st_x1, nullptr, nullptr, nullptr, 1, 0};
    launch_gemm<64, 5, 1024, EPI_W2>(a, stream);
    a = GemmArgs{x1, w3, gbuf, nullptr, nullptr, st_x1, nullptr, colsum + 4096, nullptr, nullptr, 1, 0};
    launch_gemm<224, 3, 1024, EPI_GELU>(a, stream);
    a = GemmArgs{gbuf, w4, xout, xa, x1, nullptr, st_xb, nullptr, nullptr, nullptr, 0, 0};
    launch_gemm<64, 5, 3584, EPI_W4>(a, stream);
  }
}
```

```cpp
#include <hip/hip_runtime.h>
#include <hip/hip_bf16.h>
#include <stdint.h>

typedef __attribute__((ext_vector_type(8))) short bf16x8;
typedef __attribute__((ext_vector_type(4))) short s16x4;
typedef __attribute__((ext_vector_type(4))) float f32x4;
typedef __attribute__((ext_vector_type(2))) float f32x2;
typedef __attribute__((ext_vector_type(4))) unsigned u32x4;
typedef __attribute__((ext_vector_type(2))) unsigned u32x2;
typedef __attribute__((ext_vector_type(2))) __bf16 bf16x2_t;
typedef unsigned short u16;
#define LDS_AS __attribute__((address_space(3)))
#define DI __device__ __forceinline__

static constexpr int SEQ = 2048, DM = 1024, NH = 32, FF = 3584;

DI float bf2f(unsigned v) { return __uint_as_float(v << 16); }
DI float bflo(unsigned v) { return __uint_as_float(v << 16); }
DI float bfhi(unsigned v) { return __uint_as_float(v & 0xffff0000u); }
DI unsigned pk2(float a, float b) {
  f32x2 v = {a, b};
  return __builtin_bit_cast(unsigned, __builtin_convertvector(v, bf16x2_t));
}
DI float rbf(float a) { return bflo(pk2(a, a)); }

template <int I> struct IC { static constexpr int value = I; };
template <int B, int E, class F> DI void static_for(F&& f) {
  if constexpr (B < E) { f(IC<B>{}); static_for<B + 1, E>(f); }
}
template <int N> DI void wait_vm() { asm volatile("s_waitcnt vmcnt(%0)" ::"n"(N) : "memory"); }
DI void wait_lgkm0() { asm volatile("s_waitcnt lgkmcnt(0)" ::: "memory"); }
DI void bar() { __builtin_amdgcn_s_barrier(); }
DI void glds16(const void* src, char* lds_dst) {
  __builtin_amdgcn_global_load_lds((const unsigned*)src, (unsigned*)lds_dst, 16, 0, 0);
}
DI s16x4 trread(const char* p) {
  return __builtin_bit_cast(s16x4, __builtin_amdgcn_ds_read_tr16_b64_v4i16((LDS_AS s16x4*)p));
}
template <int OFF> DI s16x4 lds_tr(unsigned addr) {
  s16x4 r; asm volatile("ds_read_b64_tr_b16 %0, %1 offset:%2" : "=v"(r) : "v"(addr), "n"(OFF) : "memory"); return r;
}
template <int OFF> DI bf16x8 lds_b128(unsigned addr) {
  bf16x8 r; asm volatile("ds_read_b128 %0, %1 offset:%2" : "=v"(r) : "v"(addr), "n"(OFF) : "memory"); return r;
}
#define WT 31
template <bool W = true> DI void st16_wt(void* p, u32x4 v) {
  if constexpr (W) asm volatile("global_store_dwordx4 %0, %1, off sc1\n\ts_nop 1" ::"v"(p), "v"(v) : "memory");
  else *(u32x4*)p = v;
}
template <bool W = true> DI void st8_wt(void* p, u32x2 v) {
  if constexpr (W) asm volatile("global_store_dwordx2 %0, %1, off sc1" ::"v"(p), "v"(v) : "memory");
  else *(u32x2*)p = v;
}
DI bf16x8 cat8(s16x4 lo, s16x4 hi) { return (bf16x8){lo[0], lo[1], lo[2], lo[3], hi[0], hi[1], hi[2], hi[3]}; }
DI f32x4 mfma16(bf16x8 a, bf16x8 b, f32x4 c) { return __builtin_amdgcn_mfma_f32_16x16x32_bf16(a, b, c, 0, 0, 0); }

DI f32x2 gelu2(f32x2 x) {
  const float c0 = -2.302208198f, c1 = -2.302208198f * 0.044715f;
  const f32x2 a = x * (x * x * c1 + c0);
  const f32x2 d = (f32x2){__builtin_amdgcn_exp2f(a[0]), __builtin_amdgcn_exp2f(a[1])} + 1.0f;
  return x * (f32x2){__builtin_amdgcn_rcpf(d[0]), __builtin_amdgcn_rcpf(d[1])};
}
DI f32x2 rbf2(f32x2 v) { const unsigned p = pk2(v[0], v[1]); return (f32x2){bflo(p), bfhi(p)}; }
DI f32x2 unpk(unsigned p) { return (f32x2){bflo(p), bfhi(p)}; }

DI void feature_map(const bf16x8 (&wf)[2], bf16x8 xf, u32x4& pos, u32x4& neg) {
  f32x2 p[4];
#pragma unroll
  for (int fb = 0; fb < 2; ++fb) {
    const f32x4 d = mfma16(wf[fb], xf, (f32x4){0.f, 0.f, 0.f, 0.f});
    p[fb * 2] = rbf2((f32x2){d[0], d[1]});
    p[fb * 2 + 1] = rbf2((f32x2){d[2], d[3]});
  }
  float mx = fmaxf(p[0][0], p[0][1]), mn = fminf(p[0][0], p[0][1]);
#pragma unroll
  for (int e = 1; e < 4; ++e) { mx = fmaxf(mx, fmaxf(p[e][0], p[e][1])); mn = fminf(mn, fminf(p[e][0], p[e][1])); }
  mx = fmaxf(mx, __shfl_xor(mx, 16)); mx = fmaxf(mx, __shfl_xor(mx, 32));
  mn = fminf(mn, __shfl_xor(mn, 16)); mn = fminf(mn, __shfl_xor(mn, 32));
  const float l2e = 1.4426950408889634f;
  f32x2 ep[4], en[4], sp2 = {0.f, 0.f}, sn2 = {0.f, 0.f};
#pragma unroll
  for (int e = 0; e < 4; ++e) {
    const f32x2 a = (p[e] - mx) * l2e, b = (mn - p[e]) * l2e;
    ep[e] = (f32x2){__builtin_amdgcn_exp2f(a[0]), __builtin_amdgcn_exp2f(a[1])}; sp2 += ep[e];
    en[e] = (f32x2){__builtin_amdgcn_exp2f(b[0]), __builtin_amdgcn_exp2f(b[1])}; sn2 += en[e];
  }
  float sp = sp2[0] + sp2[1], sn = sn2[0] + sn2[1];
  sp += __shfl_xor(sp, 16); sp += __shfl_xor(sp, 32);
  sn += __shfl_xor(sn, 16); sn += __shfl_xor(sn, 32);
  const float ip = __builtin_amdgcn_rcpf(sp), in_ = __builtin_amdgcn_rcpf(sn);
#pragma unroll
  for (int e = 0; e < 4; ++e) {
    const f32x2 a = ep[e] * ip, b = en[e] * in_;
    pos[e] = pk2(a[0], a[1]); neg[e] = pk2(b[0], b[1]);
  }
}

#define TS(i)
struct GemmArgs {
  const u16* A;
  const u16* B;
  u16* out;
  const u16* res1;
  const u16* res2;
  const float* stats_in;
  float* stats_out;
  const float* colsum;
  const u16* wphi;
  float* kvpart;
  int ln_mode;
  int unused;
};

enum { EPI_QKV = 0, EPI_RES = 1, EPI_GELU_SHIFT = 2, EPI_W2 = 3, EPI_GELU = 4, EPI_W4 = 5 };

struct RowP { float alpha, beta, mhat, dhat; };
DI RowP row_params(float mean, float var, int mode) {
  RowP r;
  const float mh = rbf(mean);
  const float dh = rbf(__builtin_amdgcn_sqrtf(rbf(rbf(var) + 1e-5f)));
  const float i1 = __builtin_amdgcn_rcpf(dh);
  r.mhat = mh; r.dhat = dh;
  if (mode == 2) {
    const float m2 = (mean - mh) * i1;
    const float v2 = var * i1 * i1;
    const float mh2 = rbf(m2);
    const float dh2 = rbf(__builtin_amdgcn_sqrtf(rbf(rbf(v2) + 1e-5f)));
    const float i2 = __builtin_amdgcn_rcpf(dh2);
    r.alpha = i1 * i2;
    r.beta = (mh * i1 + mh2) * i2;
  } else {
    r.alpha = i1;
    r.beta = mh * i1;
  }
  return r;
}

template <int BN, int S, int KD, int EPI>
__global__ __launch_bounds__(512, 2) void gemm_k(GemmArgs g) {
  constexpr int NK = KD / 64;
  constexpr int NPF = BN / 64;
  constexpr bool HALF = (BN % 64) != 0;
  constexpr int NI = BN / 32;
  constexpr int A_BYTES = 16384;
  constexpr int STAGE = A_BYTES + BN * 128;
  constexpr int L = 4 + NPF * 2 + (HALF ? 1 : 0);
  constexpr int NTOT = (EPI == EPI_QKV) ? 3 * DM : (EPI == EPI_GELU ? FF : DM);
  constexpr bool USE_STATS = (EPI == EPI_QKV || EPI == EPI_GELU_SHIFT || EPI == EPI_W2 || EPI == EPI_GELU);
  constexpr bool FOLD = (EPI == EPI_QKV || EPI == EPI_GELU_SHIFT || EPI == EPI_GELU);
  constexpr int NRES = (EPI == EPI_RES || EPI == EPI_W2) ? 1 : (EPI == EPI_W4 ? 2 : 0);
  constexpr int OFF_STATS = S * STAGE;
  constexpr int OFF_RES = OFF_STATS;
  constexpr int OFF_WPHI = OFF_RES + NRES * 16384;
  constexpr int OFF_ROWP = OFF_WPHI + (EPI == EPI_QKV ? 4096 : 0);
  constexpr int OFF_CS = OFF_ROWP + 2048;
  constexpr int NEXTRA = (USE_STATS && BN != 64 ? 8 : 0) + NRES * 4 + (EPI == EPI_QKV ? 1 : 0) + (FOLD ? 1 : 0);

  extern __shared__ __attribute__((aligned(16))) char smem[];
  const int tid = threadIdx.x, lane = tid & 63;
  const int w = __builtin_amdgcn_readfirstlane(tid >> 6);
  const bool prod = w >= 4;
  const int cw = w & 3, wr = cw >> 1, wc = cw & 1;

  const int b = blockIdx.x, xcd = b & 7, j = b >> 3;
  int rp, ct;
  if constexpr (EPI == EPI_QKV || EPI == EPI_GELU) { rp = (xcd >> 2) * 8 + (j >> 2); ct = (xcd & 3) * 4 + (j & 3); }
  else { rp = (xcd >> 1) * 4 + (j >> 3); ct = (xcd & 1) * 8 + (j & 7); }
  const int m0 = rp * 128;
  const int n0 = (EPI == EPI_QKV) ? ct * 64 : ct * BN;
  constexpr int PSTRIDE = (EPI == EPI_QKV) ? DM : 64;
  const int g4 = lane >> 4, q4 = (lane >> 2) & 3, pp = lane & 3;
  RowP* rowp = (RowP*)(smem + OFF_ROWP);
  const bool ln_on = USE_STATS && (g.ln_mode != 0 || EPI == EPI_W2);

  f32x4 acc[4][NI];
  TS(0);
  constexpr bool RP_CONS = (BN == 64);
  f32x2 sv[8];
  const int sr = cw * 32 + (lane >> 1), shf = lane & 1;
  auto load_stats = [&]() {
#pragma unroll
    for (int p = 0; p < 8; ++p)
      asm volatile("global_load_dwordx2 %0, %1, off" : "=v"(sv[p]) : "v"(g.stats_in + ((size_t)(shf * 8 + p) * SEQ + m0 + sr) * 2) : "memory");
  };
  float rs_s = 0.f, rs_m2 = 0.f;
  auto row_stats = [&](auto pc) {
    constexpr int piece = decltype(pc)::value;
    if constexpr (piece == 0) {
#pragma unroll
      for (int p = 0; p < 8; ++p) asm volatile("" : "+v"(sv[p]));
      float s = 0.f;
#pragma unroll
      for (int p = 0; p < 8; ++p) s += sv[p][0];
      rs_s = s + __shfl_xor(s, 1);
    } else if constexpr (piece == 1) {
      const float mean = rs_s * (1.0f / 1024.0f);
      float m2 = 0.f;
#pragma unroll
      for (int p = 0; p < 8; ++p) { const float d = sv[p][0] * (1.0f / 64.0f) - mean; m2 += sv[p][1] + 64.0f * d * d; }
      rs_m2 = m2 + __shfl_xor(m2, 1);
    } else {
      if (ln_on && shf == 0) rowp[sr] = row_params(rs_s * (1.0f / 1024.0f), rs_m2 * (1.0f / 1024.0f), EPI == EPI_W2 ? 1 : g.ln_mode);
      wait_lgkm0();
    }
    __builtin_amdgcn_sched_barrier(0);
  };
  if (__builtin_expect(prod, 1)) {
    const u16* a_src = g.A + (size_t)(m0 + cw * 32 + (lane >> 3)) * KD + (((lane & 7) ^ (lane >> 3)) * 8);
    const u16* b_src[2];
#pragma unroll
    for (int e = 0; e < 2; ++e) {
      const int r = lane >> 3;
      const int f = ((r >> 1) & 1) | (e << 1);
      const int c = ((((lane & 7) >> 1) ^ f) << 1) | (lane & 1);
      b_src[e] = g.B + (size_t)((cw * 2 + e) * 8 + r) * NTOT + n0 + c * 8;
    }
    const u16* bh_src = nullptr;
    if constexpr (HALF) {
      const int c = ((((lane >> 1) & 1) ^ ((lane >> 5) & 1)) << 1) | (lane & 1);
      bh_src = g.B + (size_t)(cw * 16 + (lane >> 2)) * NTOT + n0 + NPF * 64 + c * 8;
    }
    auto issue = [&](int kt, int slot) {
      char* base = smem + slot * STAGE;
#pragma unroll
      for (int p = 0; p < NPF; ++p)
#pragma unroll
        for (int e = 0; e < 2; ++e)
          glds16(b_src[e] + (size_t)kt * 64 * NTOT + p * PSTRIDE, base + A_BYTES + p * 8192 + (cw * 2 + e) * 1024);
      if constexpr (HALF) glds16(bh_src + (size_t)kt * 64 * NTOT, base + A_BYTES + NPF * 8192 + cw * 1024);
#pragma unroll
      for (int e = 0; e < 4; ++e) glds16(a_src + (size_t)e * 8 * KD + kt * 64, base + (cw * 4 + e) * 1024);
    };
    TS(6);
    issue(0, 0);
    issue(1, 1);
    wait_vm<L>();
    TS(1);
    bar();
    static_for<2, S>([&](auto ic) { issue(ic.value, ic.value); });
    TS(7);
    if constexpr (NRES >= 1) {
#pragma unroll
      for (int e = 0; e < 4; ++e) {
        const int r = (cw * 4 + e) * 8 + (lane >> 3);
        glds16(g.res1 + (size_t)(m0 + r) * DM + n0 + (lane & 7) * 8, smem + OFF_RES + (cw * 4 + e) * 1024);
      }
    }
    if constexpr (NRES >= 2) {
#pragma unroll
      for (int e = 0; e < 4; ++e) {
        const int r = (cw * 4 + e) * 8 + (lane >> 3);
        glds16(g.res2 + (size_t)(m0 + r) * DM + n0 + (lane & 7) * 8, smem + OFF_RES + 16384 + (cw * 4 + e) * 1024);
      }
    }
    if constexpr (EPI == EPI_QKV) glds16(g.wphi + (size_t)ct * 2048 + cw * 512 + lane * 8, smem + OFF_WPHI + cw * 1024);
    if constexpr (FOLD) {
      int col = cw * 64 + (lane & 15) * 4;
      if (col > BN - 4) col = BN - 4;
      const int gcol = (EPI == EPI_QKV) ? (col >> 6) * DM + n0 + (col & 63) : n0 + col;
      if (lane < 16) glds16(g.colsum + gcol, smem + OFF_CS + cw * 256);
    }
    if constexpr (USE_STATS && !RP_CONS) load_stats();
    static_for<0, NK - 1>([&](auto ic) {
      constexpr int kt = ic.value;
      constexpr int young = (S - 2 < NK - 2 - kt) ? S - 2 : NK - 2 - kt;
      constexpr int ex = (kt + 1 <= S - 1 && kt + 1 < NK - 1) ? NEXTRA : 0;
      wait_vm<young * L + ex>();
      bar();
      if constexpr (kt + S < NK) issue(kt + S, kt % S);
      if constexpr (USE_STATS && !RP_CONS && kt >= 4 && kt < 7) row_stats(IC<kt - 4>{});
    });
    TS(2);
    bar();
  } else {
    const int fsw = ((q4 >> 1) & 1) | ((g4 & 1) << 1);
    int a_off[2], b_off[2], bh_off = 0;
#pragma unroll
    for (int k = 0; k < 2; ++k) {
      a_off[k] = (wr * 64 + (lane & 15)) * 128 + (((k * 4 + g4) ^ (lane & 7)) * 16);
      b_off[k] = A_BYTES + (8 * g4 + q4) * 128 + (((2 * k + wc) ^ fsw) * 32) + pp * 8;
    }
    if constexpr (HALF) bh_off = A_BYTES + NPF * 8192 + (8 * g4 + q4) * 64 + ((wc ^ (g4 & 1)) * 32) + pp * 8;
#pragma unroll
    for (int m = 0; m < 4; ++m)
#pragma unroll
      for (int i = 0; i < NI; ++i) acc[m][i] = (f32x4){0.f, 0.f, 0.f, 0.f};
    bf16x8 xa[2][4];
    s16x4 wlo[2][NI], whi[2][NI];
    const unsigned lbase = (unsigned)(size_t)smem;

    auto ldfrag = [&](auto setc, auto slotc, auto kc) {
      constexpr int set = decltype(setc)::value, slot = decltype(slotc)::value, k32 = decltype(kc)::value;
      const unsigned sb = lbase + slot * STAGE;
      const unsigned ba0 = sb + b_off[0], ba1 = sb + b_off[1], bah = sb + bh_off, aa = sb + a_off[k32];
      static_for<0, NI>([&](auto ic) {
        constexpr int i = decltype(ic)::value;
        if constexpr (HALF && i == NI - 1) {
          wlo[set][i] = lds_tr<k32 * 2048>(bah);
          whi[set][i] = lds_tr<k32 * 2048 + 256>(bah);
        } else {
          wlo[set][i] = lds_tr<(i >> 1) * 8192 + k32 * 4096>((i & 1) ? ba1 : ba0);
          whi[set][i] = lds_tr<(i >> 1) * 8192 + k32 * 4096 + 512>((i & 1) ? ba1 : ba0);
        }
      });
      static_for<0, 4>([&](auto mc) { constexpr int m = decltype(mc)::value; xa[set][m] = lds_b128<m * 2048>(aa); });
    };
    auto wait_set = [&](auto setc) {
      constexpr int set = decltype(setc)::value;
      asm volatile("s_waitcnt lgkmcnt(0)" : "+v"(xa[set][0]), "+v"(xa[set][1]), "+v"(xa[set][2]), "+v"(xa[set][3]) :: "memory");
#pragma unroll
      for (int i = 0; i < NI; ++i) asm volatile("" : "+v"(wlo[set][i]), "+v"(whi[set][i]));
      __builtin_amdgcn_sched_barrier(0);
    };
    auto ld1 = [&](auto setc, auto slotc, auto kc, auto rc) {
      constexpr int set = decltype(setc)::value, slot = decltype(slotc)::value, k32 = decltype(kc)::value, r = decltype(rc)::value;
      const unsigned sb = lbase + slot * STAGE;
      if constexpr (r < 2 * NI) {
        constexpr int i = r >> 1, hi = r & 1;
        s16x4 v;
        if constexpr (HALF && i == NI - 1) v = lds_tr<k32 * 2048 + hi * 256>(sb + bh_off);
        else v = lds_tr<(i >> 1) * 8192 + k32 * 4096 + hi * 512>(sb + b_off[i & 1]);
        if constexpr (hi) whi[set][i] = v; else wlo[set][i] = v;
      } else {
        constexpr int m = r - 2 * NI;
        xa[set][m] = lds_b128<m * 2048>(sb + a_off[k32]);
      }
    };
    auto mma_ld = [&](auto setc, auto ldc, auto slotc, auto kc) {
      constexpr int set = decltype(setc)::value;
      constexpr bool ld = decltype(ldc)::value != 0;
      static_for<0, 4 * NI>([&](auto tc) {
        constexpr int t = decltype(tc)::value, i = t >> 2, m = t & 3;
        acc[m][i] = mfma16(cat8(wlo[set][i], whi[set][i]), xa[set][m], acc[m][i]);
        if constexpr (ld && t < 2 * NI + 4) ld1(IC<set ^ 1>{}, slotc, kc, tc);
        __builtin_amdgcn_sched_barrier(0);
      });
    };

    if constexpr (USE_STATS && RP_CONS) load_stats();
    bar();
    TS(1);
    ldfrag(IC<0>{}, IC<0>{}, IC<0>{});
    static_for<0, NK>([&](auto ic) {
      constexpr int kt = ic.value;
      constexpr int slot = kt % S;
      wait_set(IC<0>{});
      if constexpr (USE_STATS && RP_CONS && kt >= 6 && kt < 9) { if constexpr (kt == 6) wait_vm<0>(); row_stats(IC<kt - 6>{}); }
      mma_ld(IC<0>{}, IC<1>{}, IC<slot>{}, IC<1>{});
      wait_set(IC<1>{});
      if constexpr (kt + 1 < NK) {
        bar();
        __builtin_amdgcn_sched_barrier(0);
        mma_ld(IC<1>{}, IC<1>{}, IC<(kt + 1) % S>{}, IC<0>{});
      } else {
        mma_ld(IC<1>{}, IC<0>{}, IC<0>{}, IC<0>{});
      }
    });
    TS(2);
    bar();
  }
  TS(3);

  constexpr int TSTR = BN * 2 + (EPI == EPI_QKV ? 32 : 16);
  char* T = smem;
  if (!prod) {
    float al[4] = {1.f, 1.f, 1.f, 1.f}, be[4] = {0.f, 0.f, 0.f, 0.f};
    if constexpr (FOLD) {
      if (ln_on) {
#pragma unroll
        for (int m = 0; m < 4; ++m) { const RowP r = rowp[wr * 64 + m * 16 + (lane & 15)]; al[m] = r.alpha; be[m] = r.beta; }
      }
    }
#pragma unroll
    for (int i = 0; i < NI; ++i) {
      const int nb = 2 * i + wc;
      const int ncol = nb * 16 + g4 * 4;
      f32x4 cs = {0.f, 0.f, 0.f, 0.f};
      if constexpr (FOLD) {
        if (ln_on) cs = *(const f32x4*)(smem + OFF_CS + ncol * 4);
      }
#pragma unroll
      for (int m = 0; m < 4; ++m) {
        u32x2 pkd;
#pragma unroll
        for (int h = 0; h < 2; ++h) {
          f32x2 v = {acc[m][i][2 * h], acc[m][i][2 * h + 1]};
          if constexpr (FOLD) v = v * al[m] - (f32x2){cs[2 * h], cs[2 * h + 1]} * be[m];
          if constexpr (EPI == EPI_GELU_SHIFT || EPI == EPI_GELU) v = gelu2(rbf2(v));
          pkd[h] = pk2(v[0], v[1]);
        }
        *(u32x2*)(T + (wr * 64 + m * 16 + (lane & 15)) * TSTR + ncol * 2) = pkd;
      }
      if constexpr (EPI == EPI_GELU) {
        if ((i & 1) && i < NI - 1) { wait_lgkm0(); bar(); }
      }
    }
  } else if constexpr (EPI == EPI_GELU) {
#pragma unroll
    for (int p = 0; p < NI / 2; ++p) {
      bar();
      asm volatile("" ::: "memory");
#pragma unroll
      for (int it = 0; it < 4; ++it) {
        const int r = it * 32 + ((tid - 256) >> 3), c = p * 8 + (tid & 7);
        st16_wt(g.out + (size_t)(m0 + r) * FF + n0 + c * 8, *(const u32x4*)(T + r * TSTR + c * 16));
      }
    }
  }
  __syncthreads();
  TS(4);
  if constexpr (EPI == EPI_GELU) {
    const int r = tid >> 2, c = (NI / 2) * 8 + (tid & 3);
    st16_wt(g.out + (size_t)(m0 + r) * FF + n0 + c * 8, *(const u32x4*)(T + r * TSTR + c * 16));
  } else if constexpr (EPI == EPI_GELU_SHIFT) {
    const int grp = n0 >> 8;
    const int sh = (grp == 0) ? 1 : (grp == 1) ? -1 : (grp == 2) ? 2 : -2;
#pragma unroll
    for (int it = 0; it < 2; ++it) {
      const int r = it * 64 + (tid >> 3), c = tid & 7;
      const u32x4 v = *(const u32x4*)(T + r * TSTR + c * 16);
      const int dr = m0 + r + sh;
      if (dr >= 0 && dr < SEQ) st16_wt<(WT & 2) != 0>(g.out + (size_t)dr * DM + n0 + c * 8, v);
    }
    if (tid < 16) {
      const int zr = tid >> 3, c = tid & 7;
      const int az = sh > 0 ? sh : -sh;
      if (zr < az) {
        if (sh > 0 && rp == 0) *(u32x4*)(g.out + (size_t)zr * DM + n0 + c * 8) = (u32x4){0u, 0u, 0u, 0u};
        if (sh < 0 && rp == 15) *(u32x4*)(g.out + (size_t)(SEQ - 1 - zr) * DM + n0 + c * 8) = (u32x4){0u, 0u, 0u, 0u};
      }
    }
  } else if constexpr (EPI == EPI_RES || EPI == EPI_W2 || EPI == EPI_W4) {
#pragma unroll
    for (int it = 0; it < 2; ++it) {
      const int r = it * 64 + (tid >> 3), c = tid & 7;
      const u32x4 v = *(const u32x4*)(T + r * TSTR + c * 16);
      const u32x4 x = *(const u32x4*)(smem + OFF_RES + r * 128 + c * 16);
      f32x2 y[4];
      if constexpr (EPI == EPI_RES) {
#pragma unroll
        for (int e = 0; e < 4; ++e) y[e] = unpk(x[e]) + unpk(v[e]);
      } else if constexpr (EPI == EPI_W2) {
        const RowP rr = rowp[r];
#pragma unroll
        for (int e = 0; e < 4; ++e) y[e] = rbf2(rbf2(unpk(x[e]) - rr.mhat) * rr.alpha) + unpk(v[e]);
      } else {
        const u32x4 x1 = *(const u32x4*)(smem + OFF_RES + 16384 + r * 128 + c * 16);
#pragma unroll
        for (int e = 0; e < 4; ++e) y[e] = unpk(x[e]) + rbf2(unpk(x1[e]) + unpk(v[e]));
      }
      u32x4 o;
#pragma unroll
      for (int e = 0; e < 4; ++e) { o[e] = pk2(y[e][0], y[e][1]); y[e] = unpk(o[e]); }
      st16_wt<(WT & 2) != 0>(g.out + (size_t)(m0 + r) * DM + n0 + c * 8, o);
      const f32x2 s2 = (y[0] + y[1]) + (y[2] + y[3]);
      float s = s2[0] + s2[1];
      s += __shfl_xor(s, 1); s += __shfl_xor(s, 2); s += __shfl_xor(s, 4);
      const float mc = s * (1.0f / 64.0f);
      f32x2 q2 = {0.f, 0.f};
#pragma unroll
      for (int e = 0; e < 4; ++e) { const f32x2 d = y[e] - mc; q2 += d * d; }
      float m2 = q2[0] + q2[1];
      m2 += __shfl_xor(m2, 1); m2 += __shfl_xor(m2, 2); m2 += __shfl_xor(m2, 4);
      if (c == 0) *(f32x2*)(g.stats_out + ((size_t)ct * SEQ + m0 + r) * 2) = (f32x2){s, m2};
    }
  } else {
    constexpr int PKSTR = 160;
    char* PK = smem + 128 * TSTR;
#pragma unroll
    for (int it = 0; it < 2; ++it) {
      const int r = it * 64 + (tid >> 3), c = tid & 7;
      st16_wt<(WT & 4) != 0>(g.out + (size_t)(m0 + r) * DM + n0 + c * 8, *(const u32x4*)(T + r * TSTR + c * 16));
    }
    {
      const int hh = w & 1, rq = w >> 1;
      bf16x8 wf[2];
#pragma unroll
      for (int fb = 0; fb < 2; ++fb) {
        const char* p = smem + OFF_WPHI + hh * 2048 + (8 * g4 + q4) * 64 + fb * 32 + pp * 8;
        wf[fb] = cat8(trread(p), trread(p + 256));
      }
#pragma unroll
      for (int mb = 0; mb < 2; ++mb) {
        const int row = rq * 32 + mb * 16 + (lane & 15);
        const bf16x8 xf = *(const bf16x8*)(T + row * TSTR + (64 + hh * 32 + g4 * 8) * 2);
        u32x4 pos, neg;
        feature_map(wf, xf, pos, neg);
        char* dst = PK + (hh * 128 + row) * PKSTR + g4 * 8;
        *(u32x2*)dst = (u32x2){pos[0], pos[1]}; *(u32x2*)(dst + 32) = (u32x2){pos[2], pos[3]};
        *(u32x2*)(dst + 64) = (u32x2){neg[0], neg[1]}; *(u32x2*)(dst + 96) = (u32x2){neg[2], neg[3]};
      }
    }
    __syncthreads();
    {
      const int hh = w >> 2, fa = w & 3;
      f32x4 kv[3];
#pragma unroll
      for (int d = 0; d < 3; ++d) kv[d] = (f32x4){0.f, 0.f, 0.f, 0.f};
      const short one = (short)0x3F80;
      const bf16x8 ones = {one, one, one, one, one, one, one, one};
#pragma unroll
      for (int ks = 0; ks < 4; ++ks) {
        const int row = ks * 32 + 8 * g4 + q4;
        bf16x8 vf[2];
#pragma unroll
        for (int d = 0; d < 2; ++d) {
          const char* p = T + row * TSTR + (128 + hh * 32 + d * 16) * 2 + pp * 8;
          vf[d] = cat8(trread(p), trread(p + 4 * TSTR));
        }
        const char* p = PK + (hh * 128 + row) * PKSTR + (fa * 16) * 2 + pp * 8;
        const bf16x8 pf = cat8(trread(p), trread(p + 4 * PKSTR));
        kv[0] = mfma16(pf, vf[0], kv[0]);
        kv[1] = mfma16(pf, vf[1], kv[1]);
        kv[2] = mfma16(pf, ones, kv[2]);
      }
      float* slab = g.kvpart + ((size_t)rp * NH + ct * 2 + hh) * (33 * 64);
      const int f = fa * 16 + g4 * 4;
      st16_wt<(WT & 8) != 0>(slab + (lane & 15) * 64 + f, __builtin_bit_cast(u32x4, kv[0]));
      st16_wt<(WT & 8) != 0>(slab + (16 + (lane & 15)) * 64 + f, __builtin_bit_cast(u32x4, kv[1]));
      if ((lane & 15) == 0) st16_wt<(WT & 8) != 0>(slab + 32 * 64 + f, __builtin_bit_cast(u32x4, kv[2]));
    }
  }
  TS(5);
}

__global__ __launch_bounds__(256) void ln0_k(const u16* __restrict__ x, u16* __restrict__ y, const u16* __restrict__ wqkv,
                                             const u16* __restrict__ w1, const u16* __restrict__ w3, float* __restrict__ colsum,
                                             const u16* __restrict__ wo, const u16* __restrict__ w2, const u16* __restrict__ w4) {
  if (blockIdx.x >= SEQ / 4 + 120) {
    const int t = blockIdx.x - (SEQ / 4 + 120);
    const u16* W = t < 32 ? wo + (size_t)t * 32768 : (t < 64 ? w2 + (size_t)(t - 32) * 32768 : w4 + (size_t)(t - 64) * 32768);
#pragma unroll
    for (int i = 0; i < 16; ++i) {
      const u32x4 v = *(const u32x4*)(W + (i * 256 + threadIdx.x) * 8);
      asm volatile("" ::"v"(v));
    }
    return;
  }
  if (blockIdx.x >= SEQ / 4) {
    __shared__ float red[32 * 64];
    const int tid = threadIdx.x;
    const int cg = blockIdx.x - SEQ / 4;
    const u16* W; int N, c0;
    if (cg < 48) { W = wqkv; N = 3 * DM; c0 = cg * 64; }
    else if (cg < 64) { W = w1; N = DM; c0 = (cg - 48) * 64; }
    else { W = w3; N = FF; c0 = (cg - 64) * 64; }
    const int c = tid & 7, rg = tid >> 3;
    f32x2 s[4] = {{0.f, 0.f}, {0.f, 0.f}, {0.f, 0.f}, {0.f, 0.f}};
#pragma unroll 16
    for (int i = 0; i < 32; ++i) {
      const u32x4 v = *(const u32x4*)(W + (size_t)(rg + i * 32) * N + c0 + c * 8);
#pragma unroll
      for (int e = 0; e < 4; ++e) s[e] += unpk(v[e]);
    }
#pragma unroll
    for (int e = 0; e < 4; ++e) *(f32x2*)(red + rg * 64 + c * 8 + 2 * e) = s[e];
    __syncthreads();
    if (tid < 64) {
      float t = 0.f;
#pragma unroll
      for (int i = 0; i < 32; ++i) t += red[i * 64 + tid];
      colsum[cg * 64 + tid] = t;
    }
    return;
  }
  const int lane = threadIdx.x & 63, row = blockIdx.x * 4 + (threadIdx.x >> 6);
  const u16* src = x + (size_t)row * DM;
  const u32x4 a = *(const u32x4*)(src + lane * 8), b = *(const u32x4*)(src + 512 + lane * 8);
  float v[16];
#pragma unroll
  for (int e = 0; e < 4; ++e) { v[2 * e] = bflo(a[e]); v[2 * e + 1] = bfhi(a[e]); v[8 + 2 * e] = bflo(b[e]); v[9 + 2 * e] = bfhi(b[e]); }
  float s = 0.f;
#pragma unroll
  for (int e = 0; e < 16; ++e) s += v[e];
#pragma unroll
  for (int o = 1; o < 64; o <<= 1) s += __shfl_xor(s, o);
  const float mean = s * (1.0f / 1024.0f);
  float m2 = 0.f;
#pragma unroll
  for (int e = 0; e < 16; ++e) { const float d = v[e] - mean; m2 += d * d; }
#pragma unroll
  for (int o = 1; o < 64; o <<= 1) m2 += __shfl_xor(m2, o);
  const RowP r = row_params(mean, m2 * (1.0f / 1024.0f), 1);
  u32x4 oa, ob;
#pragma unroll
  for (int e = 0; e < 4; ++e) {
    oa[e] = pk2(rbf(v[2 * e] - r.mhat) / r.dhat, rbf(v[2 * e + 1] - r.mhat) / r.dhat);
    ob[e] = pk2(rbf(v[8 + 2 * e] - r.mhat) / r.dhat, rbf(v[9 + 2 * e] - r.mhat) / r.dhat);
  }
  u16* dst = y + (size_t)row * DM;
  st16_wt<(WT & 32) != 0>(dst + lane * 8, oa);
  st16_wt<(WT & 32) != 0>(dst + 512 + lane * 8, ob);
}

__global__ __launch_bounds__(512) void attn_o_k(const u16* __restrict__ q, const u16* __restrict__ wphi, const float* __restrict__ kvpart,
                                                u16* __restrict__ o) {
  constexpr int KS = 80;
  __shared__ __attribute__((aligned(16))) char sm[33 * KS * 2 + 8];
  const int tid = threadIdx.x, lane = tid & 63, w = tid >> 6;
  constexpr int EPI = 6;
  TS(0);
  const int h = blockIdx.x & 31, rb = blockIdx.x >> 5;
  const int g4 = lane >> 4, r0 = rb * 256 + w * 32 + (lane & 15);
  bf16x8 qf[2], wf[2];
#pragma unroll
  for (int mb = 0; mb < 2; ++mb) qf[mb] = *(const bf16x8*)(q + (size_t)(r0 + mb * 16) * DM + h * 32 + g4 * 8);
#pragma unroll
  for (int fb = 0; fb < 2; ++fb)
#pragma unroll
    for (int j = 0; j < 8; ++j) wf[fb][j] = (short)wphi[h * 1024 + (g4 * 8 + j) * 32 + fb * 16 + (lane & 15)];
  __builtin_amdgcn_sched_barrier(0);
  f32x4 kp[16], kz[16];
  const float* slab = kvpart + (size_t)h * (33 * 64) + tid * 4;
#pragma unroll
  for (int p = 0; p < 16; ++p) kp[p] = *(const f32x4*)(slab + (size_t)p * NH * (33 * 64));
  const bool zw = __builtin_amdgcn_readfirstlane(w) == 0;
  if (zw) {
#pragma unroll
    for (int p = 0; p < 16; ++p) kz[p] = *(const f32x4*)(slab + (size_t)p * NH * (33 * 64) + 2048 - (tid & 48) * 4);
  }
  __builtin_amdgcn_sched_barrier(0);
  bf16x8 pq[2][2];
#pragma unroll
  for (int mb = 0; mb < 2; ++mb) {
    u32x4 pos, neg;
    feature_map(wf, qf[mb], pos, neg);
    pq[mb][0] = __builtin_bit_cast(bf16x8, pos);
    pq[mb][1] = __builtin_bit_cast(bf16x8, neg);
  }
  __builtin_amdgcn_sched_barrier(0);
  TS(1);
  u16* kvt = (u16*)sm;
  {
    f32x4 t = kp[0];
#pragma unroll
    for (int p = 1; p < 16; ++p) t += kp[p];
    *(u32x2*)(kvt + (tid >> 4) * KS + (tid & 15) * 4) = (u32x2){pk2(t[0], t[1]), pk2(t[2], t[3])};
    if (zw) {
      f32x4 z = kz[0];
#pragma unroll
      for (int p = 1; p < 16; ++p) z += kz[p];
      if (tid < 16) *(u32x2*)(kvt + 32 * KS + tid * 4) = (u32x2){pk2(z[0], z[1]), pk2(z[2], z[3])};
    }
  }
  TS(2);
  __syncthreads();
  TS(3);
  bf16x8 kf[3][2];
#pragma unroll
  for (int db = 0; db < 3; ++db)
#pragma unroll
    for (int ks = 0; ks < 2; ++ks) {
      const int d = (db < 2) ? ((lane >> 2) & 3) * 8 + db * 4 + (lane & 3) : 32;
      const u16* p = kvt + d * KS + ks * 32 + g4 * 4;
      kf[db][ks] = cat8(*(const s16x4*)p, *(const s16x4*)(p + 16));
    }
#pragma unroll
  for (int mb = 0; mb < 2; ++mb) {
    f32x4 a[3];
#pragma unroll
    for (int db = 0; db < 3; ++db) {
      a[db] = mfma16(kf[db][0], pq[mb][0], (f32x4){0.f, 0.f, 0.f, 0.f});
      a[db] = mfma16(kf[db][1], pq[mb][1], a[db]);
    }
    const float id = __builtin_amdgcn_rcpf(rbf(rbf(a[2][0]) + 1e-6f));
    u32x4 ov;
#pragma unroll
    for (int db = 0; db < 2; ++db) {
      const f32x2 lo = rbf2((f32x2){a[db][0], a[db][1]}) * id, hi = rbf2((f32x2){a[db][2], a[db][3]}) * id;
      ov[db * 2] = pk2(lo[0], lo[1]); ov[db * 2 + 1] = pk2(hi[0], hi[1]);
    }
    st16_wt<(WT & 16) != 0>(o + (size_t)(r0 + mb * 16) * DM + h * 32 + g4 * 8, ov);
  }
  TS(4);
  TS(5);
}

template <int BN, int S, int KD, int EPI> static constexpr int smem_bytes() {
  constexpr bool USE_STATS = (EPI == EPI_QKV || EPI == EPI_GELU_SHIFT || EPI == EPI_W2 || EPI == EPI_GELU);
  constexpr int NRES = (EPI == EPI_RES || EPI == EPI_W2) ? 1 : (EPI == EPI_W4 ? 2 : 0);
  return S * (16384 + BN * 128) + NRES * 16384 + (EPI == EPI_QKV ? 4096 : 0) + 2048 + 1024;
}
template <int BN, int S, int KD, int EPI> static void launch_gemm(const GemmArgs& a, hipStream_t st) {
  constexpr int sb = smem_bytes<BN, S, KD, EPI>();
  static_assert(sb <= 163840, "LDS");
  (void)hipFuncSetAttribute((const void*)gemm_k<BN, S, KD, EPI>, hipFuncAttributeMaxDynamicSharedMemorySize, sb);
  gemm_k<BN, S, KD, EPI><<<256, 512, sb, st>>>(a);
}

extern "C" void kernel_launch(void* const* d_in, const int* in_sizes, int n_in, void* d_out, int out_size, void* d_ws,
                              size_t ws_size, hipStream_t stream) {
  const u16* x0 = (const u16*)d_in[0];
  const u16* wqkv = (const u16*)d_in[1];
  const u16* wphi = (const u16*)d_in[2];
  const u16* wo = (const u16*)d_in[3];
  const u16* w1 = (const u16*)d_in[5];
  const u16* w2 = (const u16*)d_in[6];
  const u16* w3 = (const u16*)d_in[7];
  const u16* w4 = (const u16*)d_in[8];
  char* ws = (char*)d_ws + (ws_size - (size_t)(64u << 20));
  const size_t MB = 1 << 20;
  u16* xln0 = (u16*)(ws + 0 * MB);
  u16* phiq = (u16*)(ws + 4 * MB);
  float* kvpart = (float*)(ws + 12 * MB);
  u16* obuf = (u16*)(ws + 17 * MB);
  u16* xa = (u16*)(ws + 21 * MB);
  u16* hsh = (u16*)(ws + 25 * MB);
  u16* x1 = (u16*)(ws + 29 * MB);
  u16* gbuf = (u16*)(ws + 33 * MB);
  u16* xb = (u16*)(ws + 48 * MB);
  float* st_xa = (float*)(ws + 52 * MB);
  float* st_x1 = (float*)(ws + 53 * MB);
  float* st_xb = (float*)(ws + 54 * MB);
  float* colsum = (float*)(ws + 55 * MB);

  ln0_k<<<SEQ / 4 + 120 + 176, 256, 0, stream>>>(x0, xln0, wqkv, w1, w3, colsum, wo, w2, w4);
  for (int layer = 0; layer < 2; ++layer) {
    const u16* xin = layer == 0 ? x0 : xb;
    u16* xout = layer == 0 ? xb : (u16*)d_out;
    GemmArgs a;
    a = GemmArgs{layer == 0 ? xln0 : xb, wqkv, phiq, nullptr, nullptr, st_xb, nullptr, colsum, wphi, kvpart, layer == 0 ? 0 : 1, 0};
    launch_gemm<192, 3, 1024, EPI_QKV>(a, stream);
    attn_o_k<<<256, 512, 0, stream>>>(phiq, wphi, kvpart, obuf);
    a = GemmArgs{obuf, wo, xa, xin, nullptr, nullptr, st_xa, nullptr, nullptr, nullptr, 0, 0};
    launch_gemm<64, 5, 1024, EPI_RES>(a, stream);
    a = GemmArgs{xa, w1, hsh, nullptr, nullptr, st_xa, nullptr, colsum + 3072, nullptr, nullptr, 2, 0};
    launch_gemm<64, 5, 1024, EPI_GELU_SHIFT>(a, stream);
    a = GemmArgs{hsh, w2, x1, xa, nullptr, st_xa, st_x1, nullptr, nullptr, nullptr, 1, 0};
    launch_gemm<64, 5, 1024, EPI_W2>(a, stream);
    a = GemmArgs{x1, w3, gbuf, nullptr, nullptr, st_x1, nullptr, colsum + 4096, nullptr, nullptr, 1, 0};
    launch_gemm<224, 3, 1024, EPI_GELU>(a, stream);
    a = GemmArgs{gbuf, w4, xout, xa, x1, nullptr, st_xb, nullptr, nullptr, nullptr, 0, 0};
    launch_gemm<64, 5, 3584, EPI_W4>(a, stream);
  }
}
```

```cpp
#include <hip/hip_runtime.h>
#include <hip/hip_bf16.h>
#include <stdint.h>

typedef __attribute__((ext_vector_type(8))) short bf16x8;
typedef __attribute__((ext_vector_type(4))) short s16x4;
typedef __attribute__((ext_vector_type(4))) float f32x4;
typedef __attribute__((ext_vector_type(2))) float f32x2;
typedef __attribute__((ext_vector_type(4))) unsigned u32x4;
typedef __attribute__((ext_vector_type(2))) unsigned u32x2;
typedef __attribute__((ext_vector_type(2))) __bf16 bf16x2_t;
typedef unsigned short u16;
#define LDS_AS __attribute__((address_space(3)))
#define DI __device__ __forceinline__

static constexpr int SEQ = 2048, DM = 1024, NH = 32, FF = 3584;

DI float bf2f(unsigned v) { return __uint_as_float(v << 16); }
DI float bflo(unsigned v) { return __uint_as_float(v << 16); }
DI float bfhi(unsigned v) { return __uint_as_float(v & 0xffff0000u); }
DI unsigned pk2(float a, float b) {
  f32x2 v = {a, b};
  return __builtin_bit_cast(unsigned, __builtin_convertvector(v, bf16x2_t));
}
DI float rbf(float a) { return bflo(pk2(a, a)); }
template <int CTRL> DI float dppf(float v) { return __builtin_bit_cast(float, __builtin_amdgcn_update_dpp(0, __builtin_bit_cast(int, v), CTRL, 0xf, 0xf, true)); }
DI float sum8(float v) { v += dppf<0xB1>(v); v += dppf<0x4E>(v); return v + dppf<0x141>(v); }
DI f32x2 pair16(float v) { const unsigned u = __float_as_uint(v); const auto r = __builtin_amdgcn_permlane16_swap(u, u, false, false); return (f32x2){__uint_as_float(r[0]), __uint_as_float(r[1])}; }
DI f32x2 pair32(float v) { const unsigned u = __float_as_uint(v); const auto r = __builtin_amdgcn_permlane32_swap(u, u, false, false); return (f32x2){__uint_as_float(r[0]), __uint_as_float(r[1])}; }
DI float max4g(float v) { f32x2 t = pair16(v); t = pair32(fmaxf(t[0], t[1])); return fmaxf(t[0], t[1]); }
DI float min4g(float v) { f32x2 t = pair16(v); t = pair32(fminf(t[0], t[1])); return fminf(t[0], t[1]); }
DI float sum4g(float v) { f32x2 t = pair16(v); t = pair32(t[0] + t[1]); return t[0] + t[1]; }
DI float sum64(float v) { v = sum8(v); return sum4g(v + dppf<0x140>(v)); }

template <int I> struct IC { static constexpr int value = I; };
template <int B, int E, class F> DI void static_for(F&& f) {
  if constexpr (B < E) { f(IC<B>{}); static_for<B + 1, E>(f); }
}
template <int N> DI void wait_vm() { asm volatile("s_waitcnt vmcnt(%0)" ::"n"(N) : "memory"); }
DI void wait_lgkm0() { asm volatile("s_waitcnt lgkmcnt(0)" ::: "memory"); }
DI void bar() { __builtin_amdgcn_s_barrier(); }
DI void glds16(const void* src, char* lds_dst) {
  __builtin_amdgcn_global_load_lds((const unsigned*)src, (unsigned*)lds_dst, 16, 0, 0);
}
DI s16x4 trread(const char* p) {
  return __builtin_bit_cast(s16x4, __builtin_amdgcn_ds_read_tr16_b64_v4i16((LDS_AS s16x4*)p));
}
template <int OFF> DI s16x4 lds_tr(unsigned addr) {
  s16x4 r; asm volatile("ds_read_b64_tr_b16 %0, %1 offset:%2" : "=v"(r) : "v"(addr), "n"(OFF) : "memory"); return r;
}
template <int OFF> DI bf16x8 lds_b128(unsigned addr) {
  bf16x8 r; asm volatile("ds_read_b128 %0, %1 offset:%2" : "=v"(r) : "v"(addr), "n"(OFF) : "memory"); return r;
}
#define WT 31
template <bool W = true> DI void st16_wt(void* p, u32x4 v) {
  if constexpr (W) asm volatile("global_store_dwordx4 %0, %1, off sc1\n\ts_nop 1" ::"v"(p), "v"(v) : "memory");
  else *(u32x4*)p = v;
}
template <bool W = true> DI void st8_wt(void* p, u32x2 v) {
  if constexpr (W) asm volatile("global_store_dwordx2 %0, %1, off sc1" ::"v"(p), "v"(v) : "memory");
  else *(u32x2*)p = v;
}
DI bf16x8 cat8(s16x4 lo, s16x4 hi) { return (bf16x8){lo[0], lo[1], lo[2], lo[3], hi[0], hi[1], hi[2], hi[3]}; }
DI f32x4 mfma16(bf16x8 a, bf16x8 b, f32x4 c) { return __builtin_amdgcn_mfma_f32_16x16x32_bf16(a, b, c, 0, 0, 0); }

DI f32x2 gelu2(f32x2 x) {
  const float c0 = -2.302208198f, c1 = -2.302208198f * 0.044715f;
  const f32x2 a = x * (x * x * c1 + c0);
  const f32x2 d = (f32x2){__builtin_amdgcn_exp2f(a[0]), __builtin_amdgcn_exp2f(a[1])} + 1.0f;
  return x * (f32x2){__builtin_amdgcn_rcpf(d[0]), __builtin_amdgcn_rcpf(d[1])};
}
DI f32x2 rbf2(f32x2 v) { const unsigned p = pk2(v[0], v[1]); return (f32x2){bflo(p), bfhi(p)}; }
DI f32x2 unpk(unsigned p) { return (f32x2){bflo(p), bfhi(p)}; }

DI void feature_map(const bf16x8 (&wf)[2], bf16x8 xf, u32x4& pos, u32x4& neg) {
  f32x2 p[4];
#pragma unroll
  for (int fb = 0; fb < 2; ++fb) {
    const f32x4 d = mfma16(wf[fb], xf, (f32x4){0.f, 0.f, 0.f, 0.f});
    p[fb * 2] = rbf2((f32x2){d[0], d[1]});
    p[fb * 2 + 1] = rbf2((f32x2){d[2], d[3]});
  }
  float mx = fmaxf(p[0][0], p[0][1]), mn = fminf(p[0][0], p[0][1]);
#pragma unroll
  for (int e = 1; e < 4; ++e) { mx = fmaxf(mx, fmaxf(p[e][0], p[e][1])); mn = fminf(mn, fminf(p[e][0], p[e][1])); }
  mx = max4g(mx);
  mn = min4g(mn);
  const float l2e = 1.4426950408889634f;
  f32x2 ep[4], en[4], sp2 = {0.f, 0.f}, sn2 = {0.f, 0.f};
#pragma unroll
  for (int e = 0; e < 4; ++e) {
    const f32x2 a = (p[e] - mx) * l2e, b = (mn - p[e]) * l2e;
    ep[e] = (f32x2){__builtin_amdgcn_exp2f(a[0]), __builtin_amdgcn_exp2f(a[1])}; sp2 += ep[e];
    en[e] = (f32x2){__builtin_amdgcn_exp2f(b[0]), __builtin_amdgcn_exp2f(b[1])}; sn2 += en[e];
  }
  float sp = sp2[0] + sp2[1], sn = sn2[0] + sn2[1];
  sp = sum4g(sp);
  sn = sum4g(sn);
  const float ip = __builtin_amdgcn_rcpf(sp), in_ = __builtin_amdgcn_rcpf(sn);
#pragma unroll
  for (int e = 0; e < 4; ++e) {
    const f32x2 a = ep[e] * ip, b = en[e] * in_;
    pos[e] = pk2(a[0], a[1]); neg[e] = pk2(b[0], b[1]);
  }
}

#define TS(i)
struct GemmArgs {
  const u16* A;
  const u16* B;
  u16* out;
  const u16* res1;
  const u16* res2;
  const float* stats_in;
  float* stats_out;
  const float* colsum;
  const u16* wphi;
  float* kvpart;
  int ln_mode;
  int unused;
};

enum { EPI_QKV = 0, EPI_RES = 1, EPI_GELU_SHIFT = 2, EPI_W2 = 3, EPI_GELU = 4, EPI_W4 = 5 };

struct RowP { float alpha, beta, mhat, dhat; };
DI RowP row_params(float mean, float var, int mode) {
  RowP r;
  const float mh = rbf(mean);
  const float dh = rbf(__builtin_amdgcn_sqrtf(rbf(rbf(var) + 1e-5f)));
  const float i1 = __builtin_amdgcn_rcpf(dh);
  r.mhat = mh; r.dhat = dh;
  if (mode == 2) {
    const float m2 = (mean - mh) * i1;
    const float v2 = var * i1 * i1;
    const float mh2 = rbf(m2);
    const float dh2 = rbf(__builtin_amdgcn_sqrtf(rbf(rbf(v2) + 1e-5f)));
    const float i2 = __builtin_amdgcn_rcpf(dh2);
    r.alpha = i1 * i2;
    r.beta = (mh * i1 + mh2) * i2;
  } else {
    r.alpha = i1;
    r.beta = mh * i1;
  }
  return r;
}

template <int BN, int S, int KD, int EPI>
__global__ __launch_bounds__(512, 2) void gemm_k(GemmArgs g) {
  constexpr int NK = KD / 64;
  constexpr int NPF = BN / 64;
  constexpr bool HALF = (BN % 64) != 0;
  constexpr int NI = BN / 32;
  constexpr int A_BYTES = 16384;
  constexpr int STAGE = A_BYTES + BN * 128;
  constexpr int L = 4 + NPF * 2 + (HALF ? 1 : 0);
  constexpr int NTOT = (EPI == EPI_QKV) ? 3 * DM : (EPI == EPI_GELU ? FF : DM);
  constexpr bool USE_STATS = (EPI == EPI_QKV || EPI == EPI_GELU_SHIFT || EPI == EPI_W2 || EPI == EPI_GELU);
  constexpr bool FOLD = (EPI == EPI_QKV || EPI == EPI_GELU_SHIFT || EPI == EPI_GELU);
  constexpr int NRES = (EPI == EPI_RES || EPI == EPI_W2) ? 1 : (EPI == EPI_W4 ? 2 : 0);
  constexpr int OFF_STATS = S * STAGE;
  constexpr int OFF_RES = OFF_STATS;
  constexpr int OFF_WPHI = OFF_RES + NRES * 16384;
  constexpr int OFF_ROWP = OFF_WPHI + (EPI == EPI_QKV ? 4096 : 0);
  constexpr int OFF_CS = OFF_ROWP + 2048;
  constexpr int NEXTRA = (USE_STATS && BN != 64 ? 8 : 0) + NRES * 4 + (EPI == EPI_QKV ? 1 : 0) + (FOLD ? 1 : 0);

  extern __shared__ __attribute__((aligned(16))) char smem[];
  const int tid = threadIdx.x, lane = tid & 63;
  const int w = __builtin_amdgcn_readfirstlane(tid >> 6);
  const bool prod = w >= 4;
  const int cw = w & 3, wr = cw >> 1, wc = cw & 1;

  const int b = blockIdx.x, xcd = b & 7, j = b >> 3;
  int rp, ct;
  if constexpr (EPI == EPI_QKV || EPI == EPI_GELU) { rp = (xcd >> 2) * 8 + (j >> 2); ct = (xcd & 3) * 4 + (j & 3); }
  else { rp = (xcd >> 1) * 4 + (j >> 3); ct = (xcd & 1) * 8 + (j & 7); }
  const int m0 = rp * 128;
  const int n0 = (EPI == EPI_QKV) ? ct * 64 : ct * BN;
  constexpr int PSTRIDE = (EPI == EPI_QKV) ? DM : 64;
  const int g4 = lane >> 4, q4 = (lane >> 2) & 3, pp = lane & 3;
  RowP* rowp = (RowP*)(smem + OFF_ROWP);
  const bool ln_on = USE_STATS && (g.ln_mode != 0 || EPI == EPI_W2);

  f32x4 acc[4][NI];
  TS(0);
  constexpr bool RP_CONS = (BN == 64);
  f32x2 sv[8];
  const int sr = cw * 32 + (lane >> 1), shf = lane & 1;
  auto load_stats = [&]() {
#pragma unroll
    for (int p = 0; p < 8; ++p)
      asm volatile("global_load_dwordx2 %0, %1, off" : "=v"(sv[p]) : "v"(g.stats_in + ((size_t)(shf * 8 + p) * SEQ + m0 + sr) * 2) : "memory");
  };
  float rs_s = 0.f, rs_m2 = 0.f;
  auto row_stats = [&](auto pc) {
    constexpr int piece = decltype(pc)::value;
    if constexpr (piece == 0) {
#pragma unroll
      for (int p = 0; p < 8; ++p) asm volatile("" : "+v"(sv[p]));
      float s = 0.f;
#pragma unroll
      for (int p = 0; p < 8; ++p) s += sv[p][0];
      rs_s = s + dppf<0xB1>(s);
    } else if constexpr (piece == 1) {
      const float mean = rs_s * (1.0f / 1024.0f);
      float m2 = 0.f;
#pragma unroll
      for (int p = 0; p < 8; ++p) { const float d = sv[p][0] * (1.0f / 64.0f) - mean; m2 += sv[p][1] + 64.0f * d * d; }
      rs_m2 = m2 + dppf<0xB1>(m2);
    } else {
      if (ln_on && shf == 0) rowp[sr] = row_params(rs_s * (1.0f / 1024.0f), rs_m2 * (1.0f / 1024.0f), EPI == EPI_W2 ? 1 : g.ln_mode);
      wait_lgkm0();
    }
    __builtin_amdgcn_sched_barrier(0);
  };
  if (__builtin_expect(prod, 1)) {
    const u16* a_src = g.A + (size_t)(m0 + cw * 32 + (lane >> 3)) * KD + (((lane & 7) ^ (lane >> 3)) * 8);
    const u16* b_src[2];
#pragma unroll
    for (int e = 0; e < 2; ++e) {
      const int r = lane >> 3;
      const int f = ((r >> 1) & 1) | (e << 1);
      const int c = ((((lane & 7) >> 1) ^ f) << 1) | (lane & 1);
      b_src[e] = g.B + (size_t)((cw * 2 + e) * 8 + r) * NTOT + n0 + c * 8;
    }
    const u16* bh_src = nullptr;
    if constexpr (HALF) {
      const int c = ((((lane >> 1) & 1) ^ ((lane >> 5) & 1)) << 1) | (lane & 1);
      bh_src = g.B + (size_t)(cw * 16 + (lane >> 2)) * NTOT + n0 + NPF * 64 + c * 8;
    }
    auto issue = [&](int kt, int slot) {
      char* base = smem + slot * STAGE;
#pragma unroll
      for (int p = 0; p < NPF; ++p)
#pragma unroll
        for (int e = 0; e < 2; ++e)
          glds16(b_src[e] + (size_t)kt * 64 * NTOT + p * PSTRIDE, base + A_BYTES + p * 8192 + (cw * 2 + e) * 1024);
      if constexpr (HALF) glds16(bh_src + (size_t)kt * 64 * NTOT, base + A_BYTES + NPF * 8192 + cw * 1024);
#pragma unroll
      for (int e = 0; e < 4; ++e) glds16(a_src + (size_t)e * 8 * KD + kt * 64, base + (cw * 4 + e) * 1024);
    };
    TS(6);
    issue(0, 0);
    issue(1, 1);
    wait_vm<L>();
    TS(1);
    bar();
    static_for<2, S>([&](auto ic) { issue(ic.value, ic.value); });
    TS(7);
    if constexpr (NRES >= 1) {
#pragma unroll
      for (int e = 0; e < 4; ++e) {
        const int r = (cw * 4 + e) * 8 + (lane >> 3);
        glds16(g.res1 + (size_t)(m0 + r) * DM + n0 + (lane & 7) * 8, smem + OFF_RES + (cw * 4 + e) * 1024);
      }
    }
    if constexpr (NRES >= 2) {
#pragma unroll
      for (int e = 0; e < 4; ++e) {
        const int r = (cw * 4 + e) * 8 + (lane >> 3);
        glds16(g.res2 + (size_t)(m0 + r) * DM + n0 + (lane & 7) * 8, smem + OFF_RES + 16384 + (cw * 4 + e) * 1024);
      }
    }
    if constexpr (EPI == EPI_QKV) glds16(g.wphi + (size_t)ct * 2048 + cw * 512 + lane * 8, smem + OFF_WPHI + cw * 1024);
    if constexpr (FOLD) {
      int col = cw * 64 + (lane & 15) * 4;
      if (col > BN - 4) col = BN - 4;
      const int gcol = (EPI == EPI_QKV) ? (col >> 6) * DM + n0 + (col & 63) : n0 + col;
      if (lane < 16) glds16(g.colsum + gcol, smem + OFF_CS + cw * 256);
    }
    if constexpr (USE_STATS && !RP_CONS) load_stats();
    static_for<0, NK - 1>([&](auto ic) {
      constexpr int kt = ic.value;
      constexpr int young = (S - 2 < NK - 2 - kt) ? S - 2 : NK - 2 - kt;
      constexpr int ex = (kt + 1 <= S - 1 && kt + 1 < NK - 1) ? NEXTRA : 0;
      wait_vm<young * L + ex>();
      bar();
      if constexpr (kt + S < NK) issue(kt + S, kt % S);
      if constexpr (USE_STATS && !RP_CONS && kt >= 4 && kt < 7) row_stats(IC<kt - 4>{});
    });
    TS(2);
    bar();
  } else {
    const int fsw = ((q4 >> 1) & 1) | ((g4 & 1) << 1);
    int a_off[2], b_off[2], bh_off = 0;
#pragma unroll
    for (int k = 0; k < 2; ++k) {
      a_off[k] = (wr * 64 + (lane & 15)) * 128 + (((k * 4 + g4) ^ (lane & 7)) * 16);
      b_off[k] = A_BYTES + (8 * g4 + q4) * 128 + (((2 * k + wc) ^ fsw) * 32) + pp * 8;
    }
    if constexpr (HALF) bh_off = A_BYTES + NPF * 8192 + (8 * g4 + q4) * 64 + ((wc ^ (g4 & 1)) * 32) + pp * 8;
#pragma unroll
    for (int m = 0; m < 4; ++m)
#pragma unroll
      for (int i = 0; i < NI; ++i) acc[m][i] = (f32x4){0.f, 0.f, 0.f, 0.f};
    bf16x8 xa[2][4];
    s16x4 wlo[2][NI], whi[2][NI];
    const unsigned lbase = (unsigned)(size_t)smem;

    auto ldfrag = [&](auto setc, auto slotc, auto kc) {
      constexpr int set = decltype(setc)::value, slot = decltype(slotc)::value, k32 = decltype(kc)::value;
      const unsigned sb = lbase + slot * STAGE;
      const unsigned ba0 = sb + b_off[0], ba1 = sb + b_off[1], bah = sb + bh_off, aa = sb + a_off[k32];
      static_for<0, NI>([&](auto ic) {
        constexpr int i = decltype(ic)::value;
        if constexpr (HALF && i == NI - 1) {
          wlo[set][i] = lds_tr<k32 * 2048>(bah);
          whi[set][i] = lds_tr<k32 * 2048 + 256>(bah);
        } else {
          wlo[set][i] = lds_tr<(i >> 1) * 8192 + k32 * 4096>((i & 1) ? ba1 : ba0);
          whi[set][i] = lds_tr<(i >> 1) * 8192 + k32 * 4096 + 512>((i & 1) ? ba1 : ba0);
        }
      });
      static_for<0, 4>([&](auto mc) { constexpr int m = decltype(mc)::value; xa[set][m] = lds_b128<m * 2048>(aa); });
    };
    auto wait_set = [&](auto setc) {
      constexpr int set = decltype(setc)::value;
      asm volatile("s_waitcnt lgkmcnt(0)" : "+v"(xa[set][0]), "+v"(xa[set][1]), "+v"(xa[set][2]), "+v"(xa[set][3]) :: "memory");
#pragma unroll
      for (int i = 0; i < NI; ++i) asm volatile("" : "+v"(wlo[set][i]), "+v"(whi[set][i]));
      __builtin_amdgcn_sched_barrier(0);
    };
    auto ld1 = [&](auto setc, auto slotc, auto kc, auto rc) {
      constexpr int set = decltype(setc)::value, slot = decltype(slotc)::value, k32 = decltype(kc)::value, r = decltype(rc)::value;
      const unsigned sb = lbase + slot * STAGE;
      if constexpr (r < 2 * NI) {
        constexpr int i = r >> 1, hi = r & 1;
        s16x4 v;
        if constexpr (HALF && i == NI - 1) v = lds_tr<k32 * 2048 + hi * 256>(sb + bh_off);
        else v = lds_tr<(i >> 1) * 8192 + k32 * 4096 + hi * 512>(sb + b_off[i & 1]);
        if constexpr (hi) whi[set][i] = v; else wlo[set][i] = v;
      } else {
        constexpr int m = r - 2 * NI;
        xa[set][m] = lds_b128<m * 2048>(sb + a_off[k32]);
      }
    };
    auto mma_ld = [&](auto setc, auto ldc, auto slotc, auto kc) {
      constexpr int set = decltype(setc)::value;
      constexpr bool ld = decltype(ldc)::value != 0;
      static_for<0, 4 * NI>([&](auto tc) {
        constexpr int t = decltype(tc)::value, i = t >> 2, m = t & 3;
        acc[m][i] = mfma16(cat8(wlo[set][i], whi[set][i]), xa[set][m], acc[m][i]);
        if constexpr (ld && t < 2 * NI + 4) ld1(IC<set ^ 1>{}, slotc, kc, tc);
        __builtin_amdgcn_sched_barrier(0);
      });
    };

    if constexpr (USE_STATS && RP_CONS) load_stats();
    bar();
    TS(1);
    ldfrag(IC<0>{}, IC<0>{}, IC<0>{});
    static_for<0, NK>([&](auto ic) {
      constexpr int kt = ic.value;
      constexpr int slot = kt % S;
      wait_set(IC<0>{});
      if constexpr (USE_STATS && RP_CONS && kt >= 6 && kt < 9) { if constexpr (kt == 6) wait_vm<0>(); row_stats(IC<kt - 6>{}); }
      mma_ld(IC<0>{}, IC<1>{}, IC<slot>{}, IC<1>{});
      wait_set(IC<1>{});
      if constexpr (kt + 1 < NK) {
        bar();
        __builtin_amdgcn_sched_barrier(0);
        mma_ld(IC<1>{}, IC<1>{}, IC<(kt + 1) % S>{}, IC<0>{});
      } else {
        mma_ld(IC<1>{}, IC<0>{}, IC<0>{}, IC<0>{});
      }
    });
    TS(2);
    bar();
  }
  TS(3);

  constexpr int TSTR = BN * 2 + (EPI == EPI_QKV ? 32 : 16);
  char* T = smem;
  if (!prod) {
    float al[4] = {1.f, 1.f, 1.f, 1.f}, be[4] = {0.f, 0.f, 0.f, 0.f};
    if constexpr (FOLD) {
      if (ln_on) {
#pragma unroll
        for (int m = 0; m < 4; ++m) { const RowP r = rowp[wr * 64 + m * 16 + (lane & 15)]; al[m] = r.alpha; be[m] = r.beta; }
      }
    }
#pragma unroll
    for (int i = 0; i < NI; ++i) {
      const int nb = 2 * i + wc;
      const int ncol = nb * 16 + g4 * 4;
      f32x4 cs = {0.f, 0.f, 0.f, 0.f};
      if constexpr (FOLD) {
        if (ln_on) cs = *(const f32x4*)(smem + OFF_CS + ncol * 4);
      }
#pragma unroll
      for (int m = 0; m < 4; ++m) {
        u32x2 pkd;
#pragma unroll
        for (int h = 0; h < 2; ++h) {
          f32x2 v = {acc[m][i][2 * h], acc[m][i][2 * h + 1]};
          if constexpr (FOLD) v = v * al[m] - (f32x2){cs[2 * h], cs[2 * h + 1]} * be[m];
          if constexpr (EPI == EPI_GELU_SHIFT || EPI == EPI_GELU) v = gelu2(rbf2(v));
          pkd[h] = pk2(v[0], v[1]);
        }
        *(u32x2*)(T + (wr * 64 + m * 16 + (lane & 15)) * TSTR + ncol * 2) = pkd;
      }
      if constexpr (EPI == EPI_GELU) {
        if ((i & 1) && i < NI - 1) { wait_lgkm0(); bar(); }
      }
    }
  } else if constexpr (EPI == EPI_GELU) {
#pragma unroll
    for (int p = 0; p < NI / 2; ++p) {
      bar();
      asm volatile("" ::: "memory");
#pragma unroll
      for (int it = 0; it < 4; ++it) {
        const int r = it * 32 + ((tid - 256) >> 3), c = p * 8 + (tid & 7);
        st16_wt(g.out + (size_t)(m0 + r) * FF + n0 + c * 8, *(const u32x4*)(T + r * TSTR + c * 16));
      }
    }
  }
  __syncthreads();
  TS(4);
  if constexpr (EPI == EPI_GELU) {
    const int r = tid >> 2, c = (NI / 2) * 8 + (tid & 3);
    st16_wt(g.out + (size_t)(m0 + r) * FF + n0 + c * 8, *(const u32x4*)(T + r * TSTR + c * 16));
  } else if constexpr (EPI == EPI_GELU_SHIFT) {
    const int grp = n0 >> 8;
    const int sh = (grp == 0) ? 1 : (grp == 1) ? -1 : (grp == 2) ? 2 : -2;
#pragma unroll
    for (int it = 0; it < 2; ++it) {
      const int r = it * 64 + (tid >> 3), c = tid & 7;
      const u32x4 v = *(const u32x4*)(T + r * TSTR + c * 16);
      const int dr = m0 + r + sh;
      if (dr >= 0 && dr < SEQ) st16_wt<(WT & 2) != 0>(g.out + (size_t)dr * DM + n0 + c * 8, v);
    }
    if (tid < 16) {
      const int zr = tid >> 3, c = tid & 7;
      const int az = sh > 0 ? sh : -sh;
      if (zr < az) {
        if (sh > 0 && rp == 0) *(u32x4*)(g.out + (size_t)zr * DM + n0 + c * 8) = (u32x4){0u, 0u, 0u, 0u};
        if (sh < 0 && rp == 15) *(u32x4*)(g.out + (size_t)(SEQ - 1 - zr) * DM + n0 + c * 8) = (u32x4){0u, 0u, 0u, 0u};
      }
    }
  } else if constexpr (EPI == EPI_RES || EPI == EPI_W2 || EPI == EPI_W4) {
#pragma unroll
    for (int it = 0; it < 2; ++it) {
      const int r = it * 64 + (tid >> 3), c = tid & 7;
      const u32x4 v = *(const u32x4*)(T + r * TSTR + c * 16);
      const u32x4 x = *(const u32x4*)(smem + OFF_RES + r * 128 + c * 16);
      f32x2 y[4];
      if constexpr (EPI == EPI_RES) {
#pragma unroll
        for (int e = 0; e < 4; ++e) y[e] = unpk(x[e]) + unpk(v[e]);
      } else if constexpr (EPI == EPI_W2) {
        const RowP rr = rowp[r];
#pragma unroll
        for (int e = 0; e < 4; ++e) y[e] = rbf2(rbf2(unpk(x[e]) - rr.mhat) * rr.alpha) + unpk(v[e]);
      } else {
        const u32x4 x1 = *(const u32x4*)(smem + OFF_RES + 16384 + r * 128 + c * 16);
#pragma unroll
        for (int e = 0; e < 4; ++e) y[e] = unpk(x[e]) + rbf2(unpk(x1[e]) + unpk(v[e]));
      }
      u32x4 o;
#pragma unroll
      for (int e = 0; e < 4; ++e) { o[e] = pk2(y[e][0], y[e][1]); y[e] = unpk(o[e]); }
      st16_wt<(WT & 2) != 0>(g.out + (size_t)(m0 + r) * DM + n0 + c * 8, o);
      const f32x2 s2 = (y[0] + y[1]) + (y[2] + y[3]);
      const float s = sum8(s2[0] + s2[1]);
      const float mc = s * (1.0f / 64.0f);
      f32x2 q2 = {0.f, 0.f};
#pragma unroll
      for (int e = 0; e < 4; ++e) { const f32x2 d = y[e] - mc; q2 += d * d; }
      const float m2 = sum8(q2[0] + q2[1]);
      if (c == 0) *(f32x2*)(g.stats_out + ((size_t)ct * SEQ + m0 + r) * 2) = (f32x2){s, m2};
    }
  } else {
    constexpr int PKSTR = 160;
    char* PK = smem + 128 * TSTR;
#pragma unroll
    for (int it = 0; it < 2; ++it) {
      const int r = it * 64 + (tid >> 3), c = tid & 7;
      st16_wt<(WT & 4) != 0>(g.out + (size_t)(m0 + r) * DM + n0 + c * 8, *(const u32x4*)(T + r * TSTR + c * 16));
    }
    {
      const int hh = w & 1, rq = w >> 1;
      bf16x8 wf[2];
#pragma unroll
      for (int fb = 0; fb < 2; ++fb) {
        const char* p = smem + OFF_WPHI + hh * 2048 + (8 * g4 + q4) * 64 + fb * 32 + pp * 8;
        wf[fb] = cat8(trread(p), trread(p + 256));
      }
#pragma unroll
      for (int mb = 0; mb < 2; ++mb) {
        const int row = rq * 32 + mb * 16 + (lane & 15);
        const bf16x8 xf = *(const bf16x8*)(T + row * TSTR + (64 + hh * 32 + g4 * 8) * 2);
        u32x4 pos, neg;
        feature_map(wf, xf, pos, neg);
        char* dst = PK + (hh * 128 + row) * PKSTR + g4 * 8;
        *(u32x2*)dst = (u32x2){pos[0], pos[1]}; *(u32x2*)(dst + 32) = (u32x2){pos[2], pos[3]};
        *(u32x2*)(dst + 64) = (u32x2){neg[0], neg[1]}; *(u32x2*)(dst + 96) = (u32x2){neg[2], neg[3]};
      }
    }
    __syncthreads();
    {
      const int hh = w >> 2, fa = w & 3;
      f32x4 kv[3];
#pragma unroll
      for (int d = 0; d < 3; ++d) kv[d] = (f32x4){0.f, 0.f, 0.f, 0.f};
      const short one = (short)0x3F80;
      const bf16x8 ones = {one, one, one, one, one, one, one, one};
#pragma unroll
      for (int ks = 0; ks < 4; ++ks) {
        const int row = ks * 32 + 8 * g4 + q4;
        bf16x8 vf[2];
#pragma unroll
        for (int d = 0; d < 2; ++d) {
          const char* p = T + row * TSTR + (128 + hh * 32 + d * 16) * 2 + pp * 8;
          vf[d] = cat8(trread(p), trread(p + 4 * TSTR));
        }
        const char* p = PK + (hh * 128 + row) * PKSTR + (fa * 16) * 2 + pp * 8;
        const bf16x8 pf = cat8(trread(p), trread(p + 4 * PKSTR));
        kv[0] = mfma16(pf, vf[0], kv[0]);
        kv[1] = mfma16(pf, vf[1], kv[1]);
        kv[2] = mfma16(pf, ones, kv[2]);
      }
      float* slab = g.kvpart + ((size_t)rp * NH + ct * 2 + hh) * (33 * 64);
      const int f = fa * 16 + g4 * 4;
      st16_wt<(WT & 8) != 0>(slab + (lane & 15) * 64 + f, __builtin_bit_cast(u32x4, kv[0]));
      st16_wt<(WT & 8) != 0>(slab + (16 + (lane & 15)) * 64 + f, __builtin_bit_cast(u32x4, kv[1]));
      if ((lane & 15) == 0) st16_wt<(WT & 8) != 0>(slab + 32 * 64 + f, __builtin_bit_cast(u32x4, kv[2]));
    }
  }
  TS(5);
}

__global__ __launch_bounds__(256) void ln0_k(const u16* __restrict__ x, u16* __restrict__ y, const u16* __restrict__ wqkv,
                                             const u16* __restrict__ w1, const u16* __restrict__ w3, float* __restrict__ colsum,
                                             const u16* __restrict__ wo, const u16* __restrict__ w2, const u16* __restrict__ w4) {
  if (blockIdx.x >= SEQ / 4 + 120) {
    const int t = blockIdx.x - (SEQ / 4 + 120);
    const u16* W = t < 32 ? wo + (size_t)t * 32768 : (t < 64 ? w2 + (size_t)(t - 32) * 32768 : w4 + (size_t)(t - 64) * 32768);
#pragma unroll
    for (int i = 0; i < 16; ++i) {
      const u32x4 v = *(const u32x4*)(W + (i * 256 + threadIdx.x) * 8);
      asm volatile("" ::"v"(v));
    }
    return;
  }
  if (blockIdx.x >= SEQ / 4) {
    __shared__ float red[32 * 64];
    const int tid = threadIdx.x;
    const int cg = blockIdx.x - SEQ / 4;
    const u16* W; int N, c0;
    if (cg < 48) { W = wqkv; N = 3 * DM; c0 = cg * 64; }
    else if (cg < 64) { W = w1; N = DM; c0 = (cg - 48) * 64; }
    else { W = w3; N = FF; c0 = (cg - 64) * 64; }
    const int c = tid & 7, rg = tid >> 3;
    f32x2 s[4] = {{0.f, 0.f}, {0.f, 0.f}, {0.f, 0.f}, {0.f, 0.f}};
#pragma unroll 16
    for (int i = 0; i < 32; ++i) {
      const u32x4 v = *(const u32x4*)(W + (size_t)(rg + i * 32) * N + c0 + c * 8);
#pragma unroll
      for (int e = 0; e < 4; ++e) s[e] += unpk(v[e]);
    }
#pragma unroll
    for (int e = 0; e < 4; ++e) *(f32x2*)(red + rg * 64 + c * 8 + 2 * e) = s[e];
    __syncthreads();
    if (tid < 64) {
      float t = 0.f;
#pragma unroll
      for (int i = 0; i < 32; ++i) t += red[i * 64 + tid];
      colsum[cg * 64 + tid] = t;
    }
    return;
  }
  const int lane = threadIdx.x & 63, row = blockIdx.x * 4 + (threadIdx.x >> 6);
  const u16* src = x + (size_t)row * DM;
  const u32x4 a = *(const u32x4*)(src + lane * 8), b = *(const u32x4*)(src + 512 + lane * 8);
  float v[16];
#pragma unroll
  for (int e = 0; e < 4; ++e) { v[2 * e] = bflo(a[e]); v[2 * e + 1] = bfhi(a[e]); v[8 + 2 * e] = bflo(b[e]); v[9 + 2 * e] = bfhi(b[e]); }
  float s = 0.f;
#pragma unroll
  for (int e = 0; e < 16; ++e) s += v[e];
  s = sum64(s);
  const float mean = s * (1.0f / 1024.0f);
  float m2 = 0.f;
#pragma unroll
  for (int e = 0; e < 16; ++e) { const float d = v[e] - mean; m2 += d * d; }
  m2 = sum64(m2);
  const RowP r = row_params(mean, m2 * (1.0f / 1024.0f), 1);
  u32x4 oa, ob;
#pragma unroll
  for (int e = 0; e < 4; ++e) {
    oa[e] = pk2(rbf(v[2 * e] - r.mhat) / r.dhat, rbf(v[2 * e + 1] - r.mhat) / r.dhat);
    ob[e] = pk2(rbf(v[8 + 2 * e] - r.mhat) / r.dhat, rbf(v[9 + 2 * e] - r.mhat) / r.dhat);
  }
  u16* dst = y + (size_t)row * DM;
  st16_wt<(WT & 32) != 0>(dst + lane * 8, oa);
  st16_wt<(WT & 32) != 0>(dst + 512 + lane * 8, ob);
}

__global__ __launch_bounds__(512) void attn_o_k(const u16* __restrict__ q, const u16* __restrict__ wphi, const float* __restrict__ kvpart,
                                                u16* __restrict__ o) {
  constexpr int KS = 80;
  __shared__ __attribute__((aligned(16))) char sm[33 * KS * 2 + 8];
  const int tid = threadIdx.x, lane = tid & 63, w = tid >> 6;
  constexpr int EPI = 6;
  TS(0);
  const int h = blockIdx.x & 31, rb = blockIdx.x >> 5;
  const int g4 = lane >> 4, r0 = rb * 256 + w * 32 + (lane & 15);
  bf16x8 qf[2], wf[2];
#pragma unroll
  for (int mb = 0; mb < 2; ++mb) qf[mb] = *(const bf16x8*)(q + (size_t)(r0 + mb * 16) * DM + h * 32 + g4 * 8);
#pragma unroll
  for (int fb = 0; fb < 2; ++fb)
#pragma unroll
    for (int j = 0; j < 8; ++j) wf[fb][j] = (short)wphi[h * 1024 + (g4 * 8 + j) * 32 + fb * 16 + (lane & 15)];
  __builtin_amdgcn_sched_barrier(0);
  f32x4 kp[16], kz[16];
  const float* slab = kvpart + (size_t)h * (33 * 64) + tid * 4;
#pragma unroll
  for (int p = 0; p < 16; ++p) kp[p] = *(const f32x4*)(slab + (size_t)p * NH * (33 * 64));
  const bool zw = __builtin_amdgcn_readfirstlane(w) == 0;
  if (zw) {
#pragma unroll
    for (int p = 0; p < 16; ++p) kz[p] = *(const f32x4*)(slab + (size_t)p * NH * (33 * 64) + 2048 - (tid & 48) * 4);
  }
  __builtin_amdgcn_sched_barrier(0);
  bf16x8 pq[2][2];
#pragma unroll
  for (int mb = 0; mb < 2; ++mb) {
    u32x4 pos, neg;
    feature_map(wf, qf[mb], pos, neg);
    pq[mb][0] = __builtin_bit_cast(bf16x8, pos);
    pq[mb][1] = __builtin_bit_cast(bf16x8, neg);
  }
  __builtin_amdgcn_sched_barrier(0);
  TS(1);
  u16* kvt = (u16*)sm;
  {
    f32x4 t = kp[0];
#pragma unroll
    for (int p = 1; p < 16; ++p) t += kp[p];
    *(u32x2*)(kvt + (tid >> 4) * KS + (tid & 15) * 4) = (u32x2){pk2(t[0], t[1]), pk2(t[2], t[3])};
    if (zw) {
      f32x4 z = kz[0];
#pragma unroll
      for (int p = 1; p < 16; ++p) z += kz[p];
      if (tid < 16) *(u32x2*)(kvt + 32 * KS + tid * 4) = (u32x2){pk2(z[0], z[1]), pk2(z[2], z[3])};
    }
  }
  TS(2);
  __syncthreads();
  TS(3);
  bf16x8 kf[3][2];
#pragma unroll
  for (int db = 0; db < 3; ++db)
#pragma unroll
    for (int ks = 0; ks < 2; ++ks) {
      const int d = (db < 2) ? ((lane >> 2) & 3) * 8 + db * 4 + (lane & 3) : 32;
      const u16* p = kvt + d * KS + ks * 32 + g4 * 4;
      kf[db][ks] = cat8(*(const s16x4*)p, *(const s16x4*)(p + 16));
    }
#pragma unroll
  for (int mb = 0; mb < 2; ++mb) {
    f32x4 a[3];
#pragma unroll
    for (int db = 0; db < 3; ++db) {
      a[db] = mfma16(kf[db][0], pq[mb][0], (f32x4){0.f, 0.f, 0.f, 0.f});
      a[db] = mfma16(kf[db][1], pq[mb][1], a[db]);
    }
    const float id = __builtin_amdgcn_rcpf(rbf(rbf(a[2][0]) + 1e-6f));
    u32x4 ov;
#pragma unroll
    for (int db = 0; db < 2; ++db) {
      const f32x2 lo = rbf2((f32x2){a[db][0], a[db][1]}) * id, hi = rbf2((f32x2){a[db][2], a[db][3]}) * id;
      ov[db * 2] = pk2(lo[0], lo[1]); ov[db * 2 + 1] = pk2(hi[0], hi[1]);
    }
    st16_wt<(WT & 16) != 0>(o + (size_t)(r0 + mb * 16) * DM + h * 32 + g4 * 8, ov);
  }
  TS(4);
  TS(5);
}

template <int BN, int S, int KD, int EPI> static constexpr int smem_bytes() {
  constexpr bool USE_STATS = (EPI == EPI_QKV || EPI == EPI_GELU_SHIFT || EPI == EPI_W2 || EPI == EPI_GELU);
  constexpr int NRES = (EPI == EPI_RES || EPI == EPI_W2) ? 1 : (EPI == EPI_W4 ? 2 : 0);
  return S * (16384 + BN * 128) + NRES * 16384 + (EPI == EPI_QKV ? 4096 : 0) + 2048 + 1024;
}
template <int BN, int S, int KD, int EPI> static void launch_gemm(const GemmArgs& a, hipStream_t st) {
  constexpr int sb = smem_bytes<BN, S, KD, EPI>();
  static_assert(sb <= 163840, "LDS");
  (void)hipFuncSetAttribute((const void*)gemm_k<BN, S, KD, EPI>, hipFuncAttributeMaxDynamicSharedMemorySize, sb);
  gemm_k<BN, S, KD, EPI><<<256, 512, sb, st>>>(a);
}

extern "C" void kernel_launch(void* const* d_in, const int* in_sizes, int n_in, void* d_out, int out_size, void* d_ws,
                              size_t ws_size, hipStream_t stream) {
  const u16* x0 = (const u16*)d_in[0];
  const u16* wqkv = (const u16*)d_in[1];
  const u16* wphi = (const u16*)d_in[2];
  const u16* wo = (const u16*)d_in[3];
  const u16* w1 = (const u16*)d_in[5];
  const u16* w2 = (const u16*)d_in[6];
  const u16* w3 = (const u16*)d_in[7];
  const u16* w4 = (const u16*)d_in[8];
  char* ws = (char*)d_ws;
  const size_t MB = 1 << 20;
  u16* xln0 = (u16*)(ws + 0 * MB);
  u16* phiq = (u16*)(ws + 4 * MB);
  float* kvpart = (float*)(ws + 12 * MB);
  u16* obuf = (u16*)(ws + 17 * MB);
  u16* xa = (u16*)(ws + 21 * MB);
  u16* hsh = (u16*)(ws + 25 * MB);
  u16* x1 = (u16*)(ws + 29 * MB);
  u16* gbuf = (u16*)(ws + 33 * MB);
  u16* xb = (u16*)(ws + 48 * MB);
  float* st_xa = (float*)(ws + 52 * MB);
  float* st_x1 = (float*)(ws + 53 * MB);
  float* st_xb = (float*)(ws + 54 * MB);
  float* colsum = (float*)(ws + 55 * MB);

  ln0_k<<<SEQ / 4 + 120 + 176, 256, 0, stream>>>(x0, xln0, wqkv, w1, w3, colsum, wo, w2, w4);
  for (int layer = 0; layer < 2; ++layer) {
    const u16* xin = layer == 0 ? x0 : xb;
    u16* xout = layer == 0 ? xb : (u16*)d_out;
    GemmArgs a;
    a = GemmArgs{layer == 0 ? xln0 : xb, wqkv, phiq, nullptr, nullptr, st_xb, nullptr, colsum, wphi, kvpart, layer == 0 ? 0 : 1, 0};
    launch_gemm<192, 3, 1024, EPI_QKV>(a, stream);
    attn_o_k<<<256, 512, 0, stream>>>(phiq, wphi, kvpart, obuf);
    a = GemmArgs{obuf, wo, xa, xin, nullptr, nullptr, st_xa, nullptr, nullptr, nullptr, 0, 0};
    launch_gemm<64, 5, 1024, EPI_RES>(a, stream);
    a = GemmArgs{xa, w1, hsh, nullptr, nullptr, st_xa, nullptr, colsum + 3072, nullptr, nullptr, 2, 0};
    launch_gemm<64, 5, 1024, EPI_GELU_SHIFT>(a, stream);
    a = GemmArgs{hsh, w2, x1, xa, nullptr, st_xa, st_x1, nullptr, nullptr, nullptr, 1, 0};
    launch_gemm<64, 5, 1024, EPI_W2>(a, stream);
    a = GemmArgs{x1, w3, gbuf, nullptr, nullptr, st_x1, nullptr, colsum + 4096, nullptr, nullptr, 1, 0};
    launch_gemm<224, 3, 1024, EPI_GELU>(a, stream);
    a = GemmArgs{gbuf, w4, xout, xa, x1, nullptr, st_xb, nullptr, nullptr, nullptr, 0, 0};
    launch_gemm<64, 5, 3584, EPI_W4>(a, stream);
  }
}
```

```cpp
#include <hip/hip_runtime.h>
#include <hip/hip_bf16.h>
#include <stdint.h>

typedef __attribute__((ext_vector_type(8))) short bf16x8;
typedef __attribute__((ext_vector_type(4))) short s16x4;
typedef __attribute__((ext_vector_type(4))) float f32x4;
typedef __attribute__((ext_vector_type(2))) float f32x2;
typedef __attribute__((ext_vector_type(4))) unsigned u32x4;
typedef __attribute__((ext_vector_type(2))) unsigned u32x2;
typedef __attribute__((ext_vector_type(2))) __bf16 bf16x2_t;
typedef unsigned short u16;
#define LDS_AS __attribute__((address_space(3)))
#define DI __device__ __forceinline__

static constexpr int SEQ = 2048, DM = 1024, NH = 32, FF = 3584;

DI float bf2f(unsigned v) { return __uint_as_float(v << 16); }
DI float bflo(unsigned v) { return __uint_as_float(v << 16); }
DI float bfhi(unsigned v) { return __uint_as_float(v & 0xffff0000u); }
DI unsigned pk2(float a, float b) {
  f32x2 v = {a, b};
  return __builtin_bit_cast(unsigned, __builtin_convertvector(v, bf16x2_t));
}
DI float rbf(float a) { return bflo(pk2(a, a)); }
template <int CTRL> DI float dppf(float v) { return __builtin_bit_cast(float, __builtin_amdgcn_update_dpp(0, __builtin_bit_cast(int, v), CTRL, 0xf, 0xf, true)); }
DI float sum8(float v) { v += dppf<0xB1>(v); v += dppf<0x4E>(v); return v + dppf<0x141>(v); }
DI f32x2 pair16(float v) { const unsigned u = __float_as_uint(v); const auto r = __builtin_amdgcn_permlane16_swap(u, u, false, false); return (f32x2){__uint_as_float(r[0]), __uint_as_float(r[1])}; }
DI f32x2 pair32(float v) { const unsigned u = __float_as_uint(v); const auto r = __builtin_amdgcn_permlane32_swap(u, u, false, false); return (f32x2){__uint_as_float(r[0]), __uint_as_float(r[1])}; }
DI float max4g(float v) { f32x2 t = pair16(v); t = pair32(fmaxf(t[0], t[1])); return fmaxf(t[0], t[1]); }
DI float min4g(float v) { f32x2 t = pair16(v); t = pair32(fminf(t[0], t[1])); return fminf(t[0], t[1]); }
DI float sum4g(float v) { f32x2 t = pair16(v); t = pair32(t[0] + t[1]); return t[0] + t[1]; }
DI float sum64(float v) { v = sum8(v); return sum4g(v + dppf<0x140>(v)); }

template <int I> struct IC { static constexpr int value = I; };
template <int B, int E, class F> DI void static_for(F&& f) {
  if constexpr (B < E) { f(IC<B>{}); static_for<B + 1, E>(f); }
}
template <int N> DI void wait_vm() { asm volatile("s_waitcnt vmcnt(%0)" ::"n"(N) : "memory"); }
DI void wait_lgkm0() { asm volatile("s_waitcnt lgkmcnt(0)" ::: "memory"); }
DI void bar() { __builtin_amdgcn_s_barrier(); }
DI void glds16(const void* src, char* lds_dst) {
  __builtin_amdgcn_global_load_lds((const unsigned*)src, (unsigned*)lds_dst, 16, 0, 0);
}
DI s16x4 trread(const char* p) {
  return __builtin_bit_cast(s16x4, __builtin_amdgcn_ds_read_tr16_b64_v4i16((LDS_AS s16x4*)p));
}
template <int OFF> DI s16x4 lds_tr(unsigned addr) {
  s16x4 r; asm volatile("ds_read_b64_tr_b16 %0, %1 offset:%2" : "=v"(r) : "v"(addr), "n"(OFF) : "memory"); return r;
}
template <int OFF> DI bf16x8 lds_b128(unsigned addr) {
  bf16x8 r; asm volatile("ds_read_b128 %0, %1 offset:%2" : "=v"(r) : "v"(addr), "n"(OFF) : "memory"); return r;
}
#define WT 31
template <bool W = true> DI void st16_wt(void* p, u32x4 v) {
  if constexpr (W) asm volatile("global_store_dwordx4 %0, %1, off sc1\n\ts_nop 1" ::"v"(p), "v"(v) : "memory");
  else *(u32x4*)p = v;
}
template <bool W = true> DI void st8_wt(void* p, u32x2 v) {
  if constexpr (W) asm volatile("global_store_dwordx2 %0, %1, off sc1" ::"v"(p), "v"(v) : "memory");
  else *(u32x2*)p = v;
}
DI bf16x8 cat8(s16x4 lo, s16x4 hi) { return (bf16x8){lo[0], lo[1], lo[2], lo[3], hi[0], hi[1], hi[2], hi[3]}; }
DI f32x4 mfma16(bf16x8 a, bf16x8 b, f32x4 c) { return __builtin_amdgcn_mfma_f32_16x16x32_bf16(a, b, c, 0, 0, 0); }

DI f32x2 gelu2(f32x2 x) {
  const float c0 = -2.302208198f, c1 = -2.302208198f * 0.044715f;
  const f32x2 a = x * (x * x * c1 + c0);
  const f32x2 d = (f32x2){__builtin_amdgcn_exp2f(a[0]), __builtin_amdgcn_exp2f(a[1])} + 1.0f;
  return x * (f32x2){__builtin_amdgcn_rcpf(d[0]), __builtin_amdgcn_rcpf(d[1])};
}
DI f32x2 rbf2(f32x2 v) { const unsigned p = pk2(v[0], v[1]); return (f32x2){bflo(p), bfhi(p)}; }
DI f32x2 unpk(unsigned p) { return (f32x2){bflo(p), bfhi(p)}; }

DI void feature_map(const bf16x8 (&wf)[2], bf16x8 xf, u32x4& pos, u32x4& neg) {
  f32x2 p[4];
#pragma unroll
  for (int fb = 0; fb < 2; ++fb) {
    const f32x4 d = mfma16(wf[fb], xf, (f32x4){0.f, 0.f, 0.f, 0.f});
    p[fb * 2] = rbf2((f32x2){d[0], d[1]});
    p[fb * 2 + 1] = rbf2((f32x2){d[2], d[3]});
  }
  float mx = fmaxf(p[0][0], p[0][1]), mn = fminf(p[0][0], p[0][1]);
#pragma unroll
  for (int e = 1; e < 4; ++e) { mx = fmaxf(mx, fmaxf(p[e][0], p[e][1])); mn = fminf(mn, fminf(p[e][0], p[e][1])); }
  mx = max4g(mx);
  mn = min4g(mn);
  const float l2e = 1.4426950408889634f;
  f32x2 ep[4], en[4], sp2 = {0.f, 0.f}, sn2 = {0.f, 0.f};
#pragma unroll
  for (int e = 0; e < 4; ++e) {
    const f32x2 a = (p[e] - mx) * l2e, b = (mn - p[e]) * l2e;
    ep[e] = (f32x2){__builtin_amdgcn_exp2f(a[0]), __builtin_amdgcn_exp2f(a[1])}; sp2 += ep[e];
    en[e] = (f32x2){__builtin_amdgcn_exp2f(b[0]), __builtin_amdgcn_exp2f(b[1])}; sn2 += en[e];
  }
  float sp = sp2[0] + sp2[1], sn = sn2[0] + sn2[1];
  sp = sum4g(sp);
  sn = sum4g(sn);
  const float ip = __builtin_amdgcn_rcpf(sp), in_ = __builtin_amdgcn_rcpf(sn);
#pragma unroll
  for (int e = 0; e < 4; ++e) {
    const f32x2 a = ep[e] * ip, b = en[e] * in_;
    pos[e] = pk2(a[0], a[1]); neg[e] = pk2(b[0], b[1]);
  }
}

#define TS(i)
struct GemmArgs {
  const u16* A;
  const u16* B;
  u16* out;
  const u16* res1;
  const u16* res2;
  const float* stats_in;
  float* stats_out;
  const float* colsum;
  const u16* wphi;
  float* kvpart;
  int ln_mode;
  int unused;
};

enum { EPI_QKV = 0, EPI_RES = 1, EPI_GELU_SHIFT = 2, EPI_W2 = 3, EPI_GELU = 4, EPI_W4 = 5 };

struct RowP { float alpha, beta, mhat, dhat; };
DI RowP row_params(float mean, float var, int mode) {
  RowP r;
  const float mh = rbf(mean);
  const float dh = rbf(__builtin_amdgcn_sqrtf(rbf(rbf(var) + 1e-5f)));
  const float i1 = __builtin_amdgcn_rcpf(dh);
  r.mhat = mh; r.dhat = dh;
  if (mode == 2) {
    const float m2 = (mean - mh) * i1;
    const float v2 = var * i1 * i1;
    const float mh2 = rbf(m2);
    const float dh2 = rbf(__builtin_amdgcn_sqrtf(rbf(rbf(v2) + 1e-5f)));
    const float i2 = __builtin_amdgcn_rcpf(dh2);
    r.alpha = i1 * i2;
    r.beta = (mh * i1 + mh2) * i2;
  } else {
    r.alpha = i1;
    r.beta = mh * i1;
  }
  return r;
}

template <int BN, int S, int KD, int EPI>
__global__ __launch_bounds__(512, 2) void gemm_k(GemmArgs g) {
  constexpr int NK = KD / 64;
  constexpr int NPF = BN / 64;
  constexpr bool HALF = (BN % 64) != 0;
  constexpr int NI = BN / 32;
  constexpr int A_BYTES = 16384;
  constexpr int STAGE = A_BYTES + BN * 128;
  constexpr int L = 4 + NPF * 2 + (HALF ? 1 : 0);
  constexpr int NTOT = (EPI == EPI_QKV) ? 3 * DM : (EPI == EPI_GELU ? FF : DM);
  constexpr bool USE_STATS = (EPI == EPI_QKV || EPI == EPI_GELU_SHIFT || EPI == EPI_W2 || EPI == EPI_GELU);
  constexpr bool FOLD = (EPI == EPI_QKV || EPI == EPI_GELU_SHIFT || EPI == EPI_GELU);
  constexpr int NRES = (EPI == EPI_RES || EPI == EPI_W2) ? 1 : (EPI == EPI_W4 ? 2 : 0);
  constexpr int OFF_STATS = S * STAGE;
  constexpr int OFF_RES = OFF_STATS;
  constexpr int OFF_WPHI = OFF_RES + NRES * 16384;
  constexpr int OFF_ROWP = OFF_WPHI + (EPI == EPI_QKV ? 4096 : 0);
  constexpr int OFF_CS = OFF_ROWP + 2048;
  constexpr int NEXTRA = (USE_STATS && BN != 64 ? 8 : 0) + NRES * 4 + (EPI == EPI_QKV ? 1 : 0) + (FOLD ? 1 : 0);

  extern __shared__ __attribute__((aligned(16))) char smem[];
  const int tid = threadIdx.x, lane = tid & 63;
  const int w = __builtin_amdgcn_readfirstlane(tid >> 6);
  const bool prod = w >= 4;
  const int cw = w & 3, wr = cw >> 1, wc = cw & 1;

  const int b = blockIdx.x, xcd = b & 7, j = b >> 3;
  int rp, ct;
  if constexpr (EPI == EPI_QKV || EPI == EPI_GELU) { rp = (xcd >> 2) * 8 + (j >> 2); ct = (xcd & 3) * 4 + (j & 3); }
  else { rp = (xcd >> 1) * 4 + (j >> 3); ct = (xcd & 1) * 8 + (j & 7); }
  const int m0 = rp * 128;
  const int n0 = (EPI == EPI_QKV) ? ct * 64 : ct * BN;
  constexpr int PSTRIDE = (EPI == EPI_QKV) ? DM : 64;
  const int g4 = lane >> 4, q4 = (lane >> 2) & 3, pp = lane & 3;
  RowP* rowp = (RowP*)(smem + OFF_ROWP);
  const bool ln_on = USE_STATS && (g.ln_mode != 0 || EPI == EPI_W2);

  f32x4 acc[4][NI];
  TS(0);
  constexpr bool RP_CONS = (BN == 64);
  f32x2 sv[8];
  const int sr = cw * 32 + (lane >> 1), shf = lane & 1;
  auto load_stats = [&]() {
#pragma unroll
    for (int p = 0; p < 8; ++p)
      asm volatile("global_load_dwordx2 %0, %1, off" : "=v"(sv[p]) : "v"(g.stats_in + ((size_t)(shf * 8 + p) * SEQ + m0 + sr) * 2) : "memory");
  };
  float rs_s = 0.f, rs_m2 = 0.f;
  auto row_stats = [&](auto pc) {
    constexpr int piece = decltype(pc)::value;
    if constexpr (piece == 0) {
#pragma unroll
      for (int p = 0; p < 8; ++p) asm volatile("" : "+v"(sv[p]));
      float s = 0.f;
#pragma unroll
      for (int p = 0; p < 8; ++p) s += sv[p][0];
      rs_s = s + dppf<0xB1>(s);
    } else if constexpr (piece == 1) {
      const float mean = rs_s * (1.0f / 1024.0f);
      float m2 = 0.f;
#pragma unroll
      for (int p = 0; p < 8; ++p) { const float d = sv[p][0] * (1.0f / 64.0f) - mean; m2 += sv[p][1] + 64.0f * d * d; }
      rs_m2 = m2 + dppf<0xB1>(m2);
    } else {
      if (ln_on && shf == 0) rowp[sr] = row_params(rs_s * (1.0f / 1024.0f), rs_m2 * (1.0f / 1024.0f), EPI == EPI_W2 ? 1 : g.ln_mode);
      wait_lgkm0();
    }
    __builtin_amdgcn_sched_barrier(0);
  };
  if (__builtin_expect(prod, 1)) {
    const u16* a_src = g.A + (size_t)(m0 + cw * 32 + (lane >> 3)) * KD + (((lane & 7) ^ (lane >> 3)) * 8);
    const u16* b_src[2];
#pragma unroll
    for (int e = 0; e < 2; ++e) {
      const int r = lane >> 3;
      const int f = ((r >> 1) & 1) | (e << 1);
      const int c = ((((lane & 7) >> 1) ^ f) << 1) | (lane & 1);
      b_src[e] = g.B + (size_t)((cw * 2 + e) * 8 + r) * NTOT + n0 + c * 8;
    }
    const u16* bh_src = nullptr;
    if constexpr (HALF) {
      const int c = ((((lane >> 1) & 1) ^ ((lane >> 5) & 1)) << 1) | (lane & 1);
      bh_src = g.B + (size_t)(cw * 16 + (lane >> 2)) * NTOT + n0 + NPF * 64 + c * 8;
    }
    auto issue = [&](int kt, int slot) {
      char* base = smem + slot * STAGE;
#pragma unroll
      for (int p = 0; p < NPF; ++p)
#pragma unroll
        for (int e = 0; e < 2; ++e)
          glds16(b_src[e] + (size_t)kt * 64 * NTOT + p * PSTRIDE, base + A_BYTES + p * 8192 + (cw * 2 + e) * 1024);
      if constexpr (HALF) glds16(bh_src + (size_t)kt * 64 * NTOT, base + A_BYTES + NPF * 8192 + cw * 1024);
#pragma unroll
      for (int e = 0; e < 4; ++e) glds16(a_src + (size_t)e * 8 * KD + kt * 64, base + (cw * 4 + e) * 1024);
    };
    TS(6);
    issue(0, 0);
    issue(1, 1);
    wait_vm<L>();
    TS(1);
    bar();
    static_for<2, S>([&](auto ic) { issue(ic.value, ic.value); });
    TS(7);
    if constexpr (NRES >= 1) {
#pragma unroll
      for (int e = 0; e < 4; ++e) {
        const int r = (cw * 4 + e) * 8 + (lane >> 3);
        glds16(g.res1 + (size_t)(m0 + r) * DM + n0 + (lane & 7) * 8, smem + OFF_RES + (cw * 4 + e) * 1024);
      }
    }
    if constexpr (NRES >= 2) {
#pragma unroll
      for (int e = 0; e < 4; ++e) {
        const int r = (cw * 4 + e) * 8 + (lane >> 3);
        glds16(g.res2 + (size_t)(m0 + r) * DM + n0 + (lane & 7) * 8, smem + OFF_RES + 16384 + (cw * 4 + e) * 1024);
      }
    }
    if constexpr (EPI == EPI_QKV) glds16(g.wphi + (size_t)ct * 2048 + cw * 512 + lane * 8, smem + OFF_WPHI + cw * 1024);
    if constexpr (FOLD) {
      int col = cw * 64 + (lane & 15) * 4;
      if (col > BN - 4) col = BN - 4;
      const int gcol = (EPI == EPI_QKV) ? (col >> 6) * DM + n0 + (col & 63) : n0 + col;
      if (lane < 16) glds16(g.colsum + gcol, smem + OFF_CS + cw * 256);
    }
    if constexpr (USE_STATS && !RP_CONS) load_stats();
    static_for<0, NK - 1>([&](auto ic) {
      constexpr int kt = ic.value;
      constexpr int young = (S - 2 < NK - 2 - kt) ? S - 2 : NK - 2 - kt;
      constexpr int ex = (kt + 1 <= S - 1 && kt + 1 < NK - 1) ? NEXTRA : 0;
      wait_vm<young * L + ex>();
      bar();
      if constexpr (kt + S < NK) issue(kt + S, kt % S);
      if constexpr (USE_STATS && !RP_CONS && kt >= 4 && kt < 7) row_stats(IC<kt - 4>{});
    });
    TS(2);
    bar();
  } else {
    const int fsw = ((q4 >> 1) & 1) | ((g4 & 1) << 1);
    int a_off[2], b_off[2], bh_off = 0;
#pragma unroll
    for (int k = 0; k < 2; ++k) {
      a_off[k] = (wr * 64 + (lane & 15)) * 128 + (((k * 4 + g4) ^ (lane & 7)) * 16);
      b_off[k] = A_BYTES + (8 * g4 + q4) * 128 + (((2 * k + wc) ^ fsw) * 32) + pp * 8;
    }
    if constexpr (HALF) bh_off = A_BYTES + NPF * 8192 + (8 * g4 + q4) * 64 + ((wc ^ (g4 & 1)) * 32) + pp * 8;
#pragma unroll
    for (int m = 0; m < 4; ++m)
#pragma unroll
      for (int i = 0; i < NI; ++i) acc[m][i] = (f32x4){0.f, 0.f, 0.f, 0.f};
    bf16x8 xa[2][4];
    s16x4 wlo[2][NI], whi[2][NI];
    const unsigned lbase = (unsigned)(size_t)smem;

    auto ldfrag = [&](auto setc, auto slotc, auto kc) {
      constexpr int set = decltype(setc)::value, slot = decltype(slotc)::value, k32 = decltype(kc)::value;
      const unsigned sb = lbase + slot * STAGE;
      const unsigned ba0 = sb + b_off[0], ba1 = sb + b_off[1], bah = sb + bh_off, aa = sb + a_off[k32];
      static_for<0, NI>([&](auto ic) {
        constexpr int i = decltype(ic)::value;
        if constexpr (HALF && i == NI - 1) {
          wlo[set][i] = lds_tr<k32 * 2048>(bah);
          whi[set][i] = lds_tr<k32 * 2048 + 256>(bah);
        } else {
          wlo[set][i] = lds_tr<(i >> 1) * 8192 + k32 * 4096>((i & 1) ? ba1 : ba0);
          whi[set][i] = lds_tr<(i >> 1) * 8192 + k32 * 4096 + 512>((i & 1) ? ba1 : ba0);
        }
      });
      static_for<0, 4>([&](auto mc) { constexpr int m = decltype(mc)::value; xa[set][m] = lds_b128<m * 2048>(aa); });
    };
    auto wait_set = [&](auto setc) {
      constexpr int set = decltype(setc)::value;
      asm volatile("s_waitcnt lgkmcnt(0)" : "+v"(xa[set][0]), "+v"(xa[set][1]), "+v"(xa[set][2]), "+v"(xa[set][3]) :: "memory");
#pragma unroll
      for (int i = 0; i < NI; ++i) asm volatile("" : "+v"(wlo[set][i]), "+v"(whi[set][i]));
      __builtin_amdgcn_sched_barrier(0);
    };
    auto ld1 = [&](auto setc, auto slotc, auto kc, auto rc) {
      constexpr int set = decltype(setc)::value, slot = decltype(slotc)::value, k32 = decltype(kc)::value, r = decltype(rc)::value;
      const unsigned sb = lbase + slot * STAGE;
      if constexpr (r < 2 * NI) {
        constexpr int i = r >> 1, hi = r & 1;
        s16x4 v;
        if constexpr (HALF && i == NI - 1) v = lds_tr<k32 * 2048 + hi * 256>(sb + bh_off);
        else v = lds_tr<(i >> 1) * 8192 + k32 * 4096 + hi * 512>(sb + b_off[i & 1]);
        if constexpr (hi) whi[set][i] = v; else wlo[set][i] = v;
      } else {
        constexpr int m = r - 2 * NI;
        xa[set][m] = lds_b128<m * 2048>(sb + a_off[k32]);
      }
    };
    auto mma_ld = [&](auto setc, auto ldc, auto slotc, auto kc) {
      constexpr int set = decltype(setc)::value;
      constexpr bool ld = decltype(ldc)::value != 0;
      static_for<0, 4 * NI>([&](auto tc) {
        constexpr int t = decltype(tc)::value, i = t >> 2, m = t & 3;
        acc[m][i] = mfma16(cat8(wlo[set][i], whi[set][i]), xa[set][m], acc[m][i]);
        if constexpr (ld && t < 2 * NI + 4) ld1(IC<set ^ 1>{}, slotc, kc, tc);
        __builtin_amdgcn_sched_barrier(0);
      });
    };

    if constexpr (USE_STATS && RP_CONS) load_stats();
    bar();
    TS(1);
    ldfrag(IC<0>{}, IC<0>{}, IC<0>{});
    static_for<0, NK>([&](auto ic) {
      constexpr int kt = ic.value;
      constexpr int slot = kt % S;
      wait_set(IC<0>{});
      if constexpr (USE_STATS && RP_CONS && kt >= 6 && kt < 9) { if constexpr (kt == 6) wait_vm<0>(); row_stats(IC<kt - 6>{}); }
      mma_ld(IC<0>{}, IC<1>{}, IC<slot>{}, IC<1>{});
      wait_set(IC<1>{});
      if constexpr (kt + 1 < NK) {
        bar();
        __builtin_amdgcn_sched_barrier(0);
        mma_ld(IC<1>{}, IC<1>{}, IC<(kt + 1) % S>{}, IC<0>{});
      } else {
        mma_ld(IC<1>{}, IC<0>{}, IC<0>{}, IC<0>{});
      }
    });
    TS(2);
    bar();
  }
  TS(3);

  constexpr int TSTR = BN * 2 + (EPI == EPI_QKV ? 32 : 16);
  char* T = smem;
  if (!prod) {
    float al[4] = {1.f, 1.f, 1.f, 1.f}, be[4] = {0.f, 0.f, 0.f, 0.f};
    if constexpr (FOLD) {
      if (ln_on) {
#pragma unroll
        for (int m = 0; m < 4; ++m) { const RowP r = rowp[wr * 64 + m * 16 + (lane & 15)]; al[m] = r.alpha; be[m] = r.beta; }
      }
    }
    f32x4 csv[NI];
    if constexpr (FOLD) {
#pragma unroll
      for (int i = 0; i < NI; ++i) csv[i] = *(const f32x4*)(smem + OFF_CS + ((2 * i + wc) * 16 + g4 * 4) * 4);
#pragma unroll
      for (int i = 0; i < NI; ++i) csv[i] = ln_on ? csv[i] : (f32x4){0.f, 0.f, 0.f, 0.f};
      __builtin_amdgcn_sched_barrier(0);
    }
#pragma unroll
    for (int i = 0; i < NI; ++i) {
      const int nb = 2 * i + wc;
      const int ncol = nb * 16 + g4 * 4;
      const f32x4 cs = csv[i];
#pragma unroll
      for (int m = 0; m < 4; ++m) {
        u32x2 pkd;
#pragma unroll
        for (int h = 0; h < 2; ++h) {
          f32x2 v = {acc[m][i][2 * h], acc[m][i][2 * h + 1]};
          if constexpr (FOLD) v = v * al[m] - (f32x2){cs[2 * h], cs[2 * h + 1]} * be[m];
          if constexpr (EPI == EPI_GELU_SHIFT || EPI == EPI_GELU) v = gelu2(rbf2(v));
          pkd[h] = pk2(v[0], v[1]);
        }
        *(u32x2*)(T + (wr * 64 + m * 16 + (lane & 15)) * TSTR + ncol * 2) = pkd;
      }
      if constexpr (EPI == EPI_GELU) {
        if ((i & 1) && i < NI - 1) { wait_lgkm0(); bar(); }
      }
    }
  } else if constexpr (EPI == EPI_GELU) {
#pragma unroll
    for (int p = 0; p < NI / 2; ++p) {
      bar();
      asm volatile("" ::: "memory");
#pragma unroll
      for (int it = 0; it < 4; ++it) {
        const int r = it * 32 + ((tid - 256) >> 3), c = p * 8 + (tid & 7);
        st16_wt(g.out + (size_t)(m0 + r) * FF + n0 + c * 8, *(const u32x4*)(T + r * TSTR + c * 16));
      }
    }
  }
  __syncthreads();
  TS(4);
  if constexpr (EPI == EPI_GELU) {
    const int r = tid >> 2, c = (NI / 2) * 8 + (tid & 3);
    st16_wt(g.out + (size_t)(m0 + r) * FF + n0 + c * 8, *(const u32x4*)(T + r * TSTR + c * 16));
  } else if constexpr (EPI == EPI_GELU_SHIFT) {
    const int grp = n0 >> 8;
    const int sh = (grp == 0) ? 1 : (grp == 1) ? -1 : (grp == 2) ? 2 : -2;
#pragma unroll
    for (int it = 0; it < 2; ++it) {
      const int r = it * 64 + (tid >> 3), c = tid & 7;
      const u32x4 v = *(const u32x4*)(T + r * TSTR + c * 16);
      const int dr = m0 + r + sh;
      if (dr >= 0 && dr < SEQ) st16_wt<(WT & 2) != 0>(g.out + (size_t)dr * DM + n0 + c * 8, v);
    }
    if (tid < 16) {
      const int zr = tid >> 3, c = tid & 7;
      const int az = sh > 0 ? sh : -sh;
      if (zr < az) {
        if (sh > 0 && rp == 0) *(u32x4*)(g.out + (size_t)zr * DM + n0 + c * 8) = (u32x4){0u, 0u, 0u, 0u};
        if (sh < 0 && rp == 15) *(u32x4*)(g.out + (size_t)(SEQ - 1 - zr) * DM + n0 + c * 8) = (u32x4){0u, 0u, 0u, 0u};
      }
    }
  } else if constexpr (EPI == EPI_RES || EPI == EPI_W2 || EPI == EPI_W4) {
#pragma unroll
    for (int it = 0; it < 2; ++it) {
      const int r = it * 64 + (tid >> 3), c = tid & 7;
      const u32x4 v = *(const u32x4*)(T + r * TSTR + c * 16);
      const u32x4 x = *(const u32x4*)(smem + OFF_RES + r * 128 + c * 16);
      f32x2 y[4];
      if constexpr (EPI == EPI_RES) {
#pragma unroll
        for (int e = 0; e < 4; ++e) y[e] = unpk(x[e]) + unpk(v[e]);
      } else if constexpr (EPI == EPI_W2) {
        const RowP rr = rowp[r];
#pragma unroll
        for (int e = 0; e < 4; ++e) y[e] = rbf2(rbf2(unpk(x[e]) - rr.mhat) * rr.alpha) + unpk(v[e]);
      } else {
        const u32x4 x1 = *(const u32x4*)(smem + OFF_RES + 16384 + r * 128 + c * 16);
#pragma unroll
        for (int e = 0; e < 4; ++e) y[e] = unpk(x[e]) + rbf2(unpk(x1[e]) + unpk(v[e]));
      }
      u32x4 o;
#pragma unroll
      for (int e = 0; e < 4; ++e) { o[e] = pk2(y[e][0], y[e][1]); y[e] = unpk(o[e]); }
      st16_wt<(WT & 2) != 0>(g.out + (size_t)(m0 + r) * DM + n0 + c * 8, o);
      const f32x2 s2 = (y[0] + y[1]) + (y[2] + y[3]);
      const float s = sum8(s2[0] + s2[1]);
      const float mc = s * (1.0f / 64.0f);
      f32x2 q2 = {0.f, 0.f};
#pragma unroll
      for (int e = 0; e < 4; ++e) { const f32x2 d = y[e] - mc; q2 += d * d; }
      const float m2 = sum8(q2[0] + q2[1]);
      if (c == 0) *(f32x2*)(g.stats_out + ((size_t)ct * SEQ + m0 + r) * 2) = (f32x2){s, m2};
    }
  } else {
    constexpr int PKSTR = 160;
    char* PK = smem + 128 * TSTR;
#pragma unroll
    for (int it = 0; it < 2; ++it) {
      const int r = it * 64 + (tid >> 3), c = tid & 7;
      st16_wt<(WT & 4) != 0>(g.out + (size_t)(m0 + r) * DM + n0 + c * 8, *(const u32x4*)(T + r * TSTR + c * 16));
    }
    {
      const int hh = w & 1, rq = w >> 1;
      bf16x8 wf[2];
#pragma unroll
      for (int fb = 0; fb < 2; ++fb) {
        const char* p = smem + OFF_WPHI + hh * 2048 + (8 * g4 + q4) * 64 + fb * 32 + pp * 8;
        wf[fb] = cat8(trread(p), trread(p + 256));
      }
#pragma unroll
      for (int mb = 0; mb < 2; ++mb) {
        const int row = rq * 32 + mb * 16 + (lane & 15);
        const bf16x8 xf = *(const bf16x8*)(T + row * TSTR + (64 + hh * 32 + g4 * 8) * 2);
        u32x4 pos, neg;
        feature_map(wf, xf, pos, neg);
        char* dst = PK + (hh * 128 + row) * PKSTR + g4 * 8;
        *(u32x2*)dst = (u32x2){pos[0], pos[1]}; *(u32x2*)(dst + 32) = (u32x2){pos[2], pos[3]};
        *(u32x2*)(dst + 64) = (u32x2){neg[0], neg[1]}; *(u32x2*)(dst + 96) = (u32x2){neg[2], neg[3]};
      }
    }
    __syncthreads();
    {
      const int hh = w >> 2, fa = w & 3;
      f32x4 kv[3];
#pragma unroll
      for (int d = 0; d < 3; ++d) kv[d] = (f32x4){0.f, 0.f, 0.f, 0.f};
      const short one = (short)0x3F80;
      const bf16x8 ones = {one, one, one, one, one, one, one, one};
#pragma unroll
      for (int ks = 0; ks < 4; ++ks) {
        const int row = ks * 32 + 8 * g4 + q4;
        bf16x8 vf[2];
#pragma unroll
        for (int d = 0; d < 2; ++d) {
          const char* p = T + row * TSTR + (128 + hh * 32 + d * 16) * 2 + pp * 8;
          vf[d] = cat8(trread(p), trread(p + 4 * TSTR));
        }
        const char* p = PK + (hh * 128 + row) * PKSTR + (fa * 16) * 2 + pp * 8;
        const bf16x8 pf = cat8(trread(p), trread(p + 4 * PKSTR));
        kv[0] = mfma16(pf, vf[0], kv[0]);
        kv[1] = mfma16(pf, vf[1], kv[1]);
        kv[2] = mfma16(pf, ones, kv[2]);
      }
      float* slab = g.kvpart + ((size_t)rp * NH + ct * 2 + hh) * (33 * 64);
      const int f = fa * 16 + g4 * 4;
      st16_wt<(WT & 8) != 0>(slab + (lane & 15) * 64 + f, __builtin_bit_cast(u32x4, kv[0]));
      st16_wt<(WT & 8) != 0>(slab + (16 + (lane & 15)) * 64 + f, __builtin_bit_cast(u32x4, kv[1]));
      if ((lane & 15) == 0) st16_wt<(WT & 8) != 0>(slab + 32 * 64 + f, __builtin_bit_cast(u32x4, kv[2]));
    }
  }
  TS(5);
}

__global__ __launch_bounds__(256) void ln0_k(const u16* __restrict__ x, u16* __restrict__ y, const u16* __restrict__ wqkv,
                                             const u16* __restrict__ w1, const u16* __restrict__ w3, float* __restrict__ colsum,
                                             const u16* __restrict__ wo, const u16* __restrict__ w2, const u16* __restrict__ w4) {
  if (blockIdx.x >= SEQ / 4 + 120) {
    const int t = blockIdx.x - (SEQ / 4 + 120);
    const u16* W = t < 32 ? wo + (size_t)t * 32768 : (t < 64 ? w2 + (size_t)(t - 32) * 32768 : w4 + (size_t)(t - 64) * 32768);
#pragma unroll
    for (int i = 0; i < 16; ++i) {
      const u32x4 v = *(const u32x4*)(W + (i * 256 + threadIdx.x) * 8);
      asm volatile("" ::"v"(v));
    }
    return;
  }
  if (blockIdx.x >= SEQ / 4) {
    __shared__ float red[32 * 64];
    const int tid = threadIdx.x;
    const int cg = blockIdx.x - SEQ / 4;
    const u16* W; int N, c0;
    if (cg < 48) { W = wqkv; N = 3 * DM; c0 = cg * 64; }
    else if (cg < 64) { W = w1; N = DM; c0 = (cg - 48) * 64; }
    else { W = w3; N = FF; c0 = (cg - 64) * 64; }
    const int c = tid & 7, rg = tid >> 3;
    f32x2 s[4] = {{0.f, 0.f}, {0.f, 0.f}, {0.f, 0.f}, {0.f, 0.f}};
#pragma unroll 16
    for (int i = 0; i < 32; ++i) {
      const u32x4 v = *(const u32x4*)(W + (size_t)(rg + i * 32) * N + c0 + c * 8);
#pragma unroll
      for (int e = 0; e < 4; ++e) s[e] += unpk(v[e]);
    }
#pragma unroll
    for (int e = 0; e < 4; ++e) *(f32x2*)(red + rg * 64 + c * 8 + 2 * e) = s[e];
    __syncthreads();
    if (tid < 64) {
      float t = 0.f;
#pragma unroll
      for (int i = 0; i < 32; ++i) t += red[i * 64 + tid];
      colsum[cg * 64 + tid] = t;
    }
    return;
  }
  const int lane = threadIdx.x & 63, row = blockIdx.x * 4 + (threadIdx.x >> 6);
  const u16* src = x + (size_t)row * DM;
  const u32x4 a = *(const u32x4*)(src + lane * 8), b = *(const u32x4*)(src + 512 + lane * 8);
  float v[16];
#pragma unroll
  for (int e = 0; e < 4; ++e) { v[2 * e] = bflo(a[e]); v[2 * e + 1] = bfhi(a[e]); v[8 + 2 * e] = bflo(b[e]); v[9 + 2 * e] = bfhi(b[e]); }
  float s = 0.f;
#pragma unroll
  for (int e = 0; e < 16; ++e) s += v[e];
  s = sum64(s);
  const float mean = s * (1.0f / 1024.0f);
  float m2 = 0.f;
#pragma unroll
  for (int e = 0; e < 16; ++e) { const float d = v[e] - mean; m2 += d * d; }
  m2 = sum64(m2);
  const RowP r = row_params(mean, m2 * (1.0f / 1024.0f), 1);
  u32x4 oa, ob;
#pragma unroll
  for (int e = 0; e < 4; ++e) {
    oa[e] = pk2(rbf(v[2 * e] - r.mhat) / r.dhat, rbf(v[2 * e + 1] - r.mhat) / r.dhat);
    ob[e] = pk2(rbf(v[8 + 2 * e] - r.mhat) / r.dhat, rbf(v[9 + 2 * e] - r.mhat) / r.dhat);
  }
  u16* dst = y + (size_t)row * DM;
  st16_wt<(WT & 32) != 0>(dst + lane * 8, oa);
  st16_wt<(WT & 32) != 0>(dst + 512 + lane * 8, ob);
}

__global__ __launch_bounds__(512) void attn_o_k(const u16* __restrict__ q, const u16* __restrict__ wphi, const float* __restrict__ kvpart,
                                                u16* __restrict__ o) {
  constexpr int KS = 80;
  __shared__ __attribute__((aligned(16))) char sm[33 * KS * 2 + 8];
  const int tid = threadIdx.x, lane = tid & 63, w = tid >> 6;
  constexpr int EPI = 6;
  TS(0);
  const int h = blockIdx.x & 31, rb = blockIdx.x >> 5;
  const int g4 = lane >> 4, r0 = rb * 256 + w * 32 + (lane & 15);
  bf16x8 qf[2], wf[2];
#pragma unroll
  for (int mb = 0; mb < 2; ++mb) qf[mb] = *(const bf16x8*)(q + (size_t)(r0 + mb * 16) * DM + h * 32 + g4 * 8);
#pragma unroll
  for (int fb = 0; fb < 2; ++fb)
#pragma unroll
    for (int j = 0; j < 8; ++j) wf[fb][j] = (short)wphi[h * 1024 + (g4 * 8 + j) * 32 + fb * 16 + (lane & 15)];
  __builtin_amdgcn_sched_barrier(0);
  f32x4 kp[16], kz[16];
  const float* slab = kvpart + (size_t)h * (33 * 64) + tid * 4;
#pragma unroll
  for (int p = 0; p < 16; ++p) kp[p] = *(const f32x4*)(slab + (size_t)p * NH * (33 * 64));
  const bool zw = __builtin_amdgcn_readfirstlane(w) == 0;
  if (zw) {
#pragma unroll
    for (int p = 0; p < 16; ++p) kz[p] = *(const f32x4*)(slab + (size_t)p * NH * (33 * 64) + 2048 - (tid & 48) * 4);
  }
  __builtin_amdgcn_sched_barrier(0);
  bf16x8 pq[2][2];
#pragma unroll
  for (int mb = 0; mb < 2; ++mb) {
    u32x4 pos, neg;
    feature_map(wf, qf[mb], pos, neg);
    pq[mb][0] = __builtin_bit_cast(bf16x8, pos);
    pq[mb][1] = __builtin_bit_cast(bf16x8, neg);
  }
  __builtin_amdgcn_sched_barrier(0);
  TS(1);
  u16* kvt = (u16*)sm;
  {
    f32x4 t = kp[0];
#pragma unroll
    for (int p = 1; p < 16; ++p) t += kp[p];
    *(u32x2*)(kvt + (tid >> 4) * KS + (tid & 15) * 4) = (u32x2){pk2(t[0], t[1]), pk2(t[2], t[3])};
    if (zw) {
      f32x4 z = kz[0];
#pragma unroll
      for (int p = 1; p < 16; ++p) z += kz[p];
      if (tid < 16) *(u32x2*)(kvt + 32 * KS + tid * 4) = (u32x2){pk2(z[0], z[1]), pk2(z[2], z[3])};
    }
  }
  TS(2);
  __syncthreads();
  TS(3);
  bf16x8 kf[3][2];
#pragma unroll
  for (int db = 0; db < 3; ++db)
#pragma unroll
    for (int ks = 0; ks < 2; ++ks) {
      const int d = (db < 2) ? ((lane >> 2) & 3) * 8 + db * 4 + (lane & 3) : 32;
      const u16* p = kvt + d * KS + ks * 32 + g4 * 4;
      kf[db][ks] = cat8(*(const s16x4*)p, *(const s16x4*)(p + 16));
    }
#pragma unroll
  for (int mb = 0; mb < 2; ++mb) {
    f32x4 a[3];
#pragma unroll
    for (int db = 0; db < 3; ++db) {
      a[db] = mfma16(kf[db][0], pq[mb][0], (f32x4){0.f, 0.f, 0.f, 0.f});
      a[db] = mfma16(kf[db][1], pq[mb][1], a[db]);
    }
    const float id = __builtin_amdgcn_rcpf(rbf(rbf(a[2][0]) + 1e-6f));
    u32x4 ov;
#pragma unroll
    for (int db = 0; db < 2; ++db) {
      const f32x2 lo = rbf2((f32x2){a[db][0], a[db][1]}) * id, hi = rbf2((f32x2){a[db][2], a[db][3]}) * id;
      ov[db * 2] = pk2(lo[0], lo[1]); ov[db * 2 + 1] = pk2(hi[0], hi[1]);
    }
    st16_wt<(WT & 16) != 0>(o + (size_t)(r0 + mb * 16) * DM + h * 32 + g4 * 8, ov);
  }
  TS(4);
  TS(5);
}

template <int BN, int S, int KD, int EPI> static constexpr int smem_bytes() {
  constexpr bool USE_STATS = (EPI == EPI_QKV || EPI == EPI_GELU_SHIFT || EPI == EPI_W2 || EPI == EPI_GELU);
  constexpr int NRES = (EPI == EPI_RES || EPI == EPI_W2) ? 1 : (EPI == EPI_W4 ? 2 : 0);
  return S * (16384 + BN * 128) + NRES * 16384 + (EPI == EPI_QKV ? 4096 : 0) + 2048 + 1024;
}
template <int BN, int S, int KD, int EPI> static void launch_gemm(const GemmArgs& a, hipStream_t st) {
  constexpr int sb = smem_bytes<BN, S, KD, EPI>();
  static_assert(sb <= 163840, "LDS");
  (void)hipFuncSetAttribute((const void*)gemm_k<BN, S, KD, EPI>, hipFuncAttributeMaxDynamicSharedMemorySize, sb);
  gemm_k<BN, S, KD, EPI><<<256, 512, sb, st>>>(a);
}

extern "C" void kernel_launch(void* const* d_in, const int* in_sizes, int n_in, void* d_out, int out_size, void* d_ws,
                              size_t ws_size, hipStream_t stream) {
  const u16* x0 = (const u16*)d_in[0];
  const u16* wqkv = (const u16*)d_in[1];
  const u16* wphi = (const u16*)d_in[2];
  const u16* wo = (const u16*)d_in[3];
  const u16* w1 = (const u16*)d_in[5];
  const u16* w2 = (const u16*)d_in[6];
  const u16* w3 = (const u16*)d_in[7];
  const u16* w4 = (const u16*)d_in[8];
  char* ws = (char*)d_ws;
  const size_t MB = 1 << 20;
  u16* xln0 = (u16*)(ws + 0 * MB);
  u16* phiq = (u16*)(ws + 4 * MB);
  float* kvpart = (float*)(ws + 12 * MB);
  u16* obuf = (u16*)(ws + 17 * MB);
  u16* xa = (u16*)(ws + 21 * MB);
  u16* hsh = (u16*)(ws + 25 * MB);
  u16* x1 = (u16*)(ws + 29 * MB);
  u16* gbuf = (u16*)(ws + 33 * MB);
  u16* xb = (u16*)(ws + 48 * MB);
  float* st_xa = (float*)(ws + 52 * MB);
  float* st_x1 = (float*)(ws + 53 * MB);
  float* st_xb = (float*)(ws + 54 * MB);
  float* colsum = (float*)(ws + 55 * MB);

  ln0_k<<<SEQ / 4 + 120 + 176, 256, 0, stream>>>(x0, xln0, wqkv, w1, w3, colsum, wo, w2, w4);
  for (int layer = 0; layer < 2; ++layer) {
    const u16* xin = layer == 0 ? x0 : xb;
    u16* xout = layer == 0 ? xb : (u16*)d_out;
    GemmArgs a;
    a = GemmArgs{layer == 0 ? xln0 : xb, wqkv, phiq, nullptr, nullptr, st_xb, nullptr, colsum, wphi, kvpart, layer == 0 ? 0 : 1, 0};
    launch_gemm<192, 3, 1024, EPI_QKV>(a, stream);
    attn_o_k<<<256, 512, 0, stream>>>(phiq, wphi, kvpart, obuf);
    a = GemmArgs{obuf, wo, xa, xin, nullptr, nullptr, st_xa, nullptr, nullptr, nullptr, 0, 0};
    launch_gemm<64, 5, 1024, EPI_RES>(a, stream);
    a = GemmArgs{xa, w1, hsh, nullptr, nullptr, st_xa, nullptr, colsum + 3072, nullptr, nullptr, 2, 0};
    launch_gemm<64, 5, 1024, EPI_GELU_SHIFT>(a, stream);
    a = GemmArgs{hsh, w2, x1, xa, nullptr, st_xa, st_x1, nullptr, nullptr, nullptr, 1, 0};
    launch_gemm<64, 5, 1024, EPI_W2>(a, stream);
    a = GemmArgs{x1, w3, gbuf, nullptr, nullptr, st_x1, nullptr, colsum + 4096, nullptr, nullptr, 1, 0};
    launch_gemm<224, 3, 1024, EPI_GELU>(a, stream);
    a = GemmArgs{gbuf, w4, xout, xa, x1, nullptr, st_xb, nullptr, nullptr, nullptr, 0, 0};
    launch_gemm<64, 5, 3584, EPI_W4>(a, stream);
  }
}
```

```cpp
#include <hip/hip_runtime.h>
#include <hip/hip_bf16.h>
#include <stdint.h>

typedef __attribute__((ext_vector_type(8))) short bf16x8;
typedef __attribute__((ext_vector_type(4))) short s16x4;
typedef __attribute__((ext_vector_type(4))) float f32x4;
typedef __attribute__((ext_vector_type(2))) float f32x2;
typedef __attribute__((ext_vector_type(4))) unsigned u32x4;
typedef __attribute__((ext_vector_type(2))) unsigned u32x2;
typedef __attribute__((ext_vector_type(2))) __bf16 bf16x2_t;
typedef unsigned short u16;
#define LDS_AS __attribute__((address_space(3)))
#define DI __device__ __forceinline__

static constexpr int SEQ = 2048, DM = 1024, NH = 32, FF = 3584;

DI float bf2f(unsigned v) { return __uint_as_float(v << 16); }
DI float bflo(unsigned v) { return __uint_as_float(v << 16); }
DI float bfhi(unsigned v) { return __uint_as_float(v & 0xffff0000u); }
DI unsigned pk2(float a, float b) {
  f32x2 v = {a, b};
  return __builtin_bit_cast(unsigned, __builtin_convertvector(v, bf16x2_t));
}
DI float rbf(float a) { return bflo(pk2(a, a)); }
template <int CTRL> DI float dppf(float v) { return __builtin_bit_cast(float, __builtin_amdgcn_update_dpp(0, __builtin_bit_cast(int, v), CTRL, 0xf, 0xf, true)); }
DI float sum8(float v) { v += dppf<0xB1>(v); v += dppf<0x4E>(v); return v + dppf<0x141>(v); }
DI f32x2 pair16(float v) { const unsigned u = __float_as_uint(v); const auto r = __builtin_amdgcn_permlane16_swap(u, u, false, false); return (f32x2){__uint_as_float(r[0]), __uint_as_float(r[1])}; }
DI f32x2 pair32(float v) { const unsigned u = __float_as_uint(v); const auto r = __builtin_amdgcn_permlane32_swap(u, u, false, false); return (f32x2){__uint_as_float(r[0]), __uint_as_float(r[1])}; }
DI float max4g(float v) { f32x2 t = pair16(v); t = pair32(fmaxf(t[0], t[1])); return fmaxf(t[0], t[1]); }
DI float min4g(float v) { f32x2 t = pair16(v); t = pair32(fminf(t[0], t[1])); return fminf(t[0], t[1]); }
DI float sum4g(float v) { f32x2 t = pair16(v); t = pair32(t[0] + t[1]); return t[0] + t[1]; }
DI float sum64(float v) { v = sum8(v); return sum4g(v + dppf<0x140>(v)); }

template <int I> struct IC { static constexpr int value = I; };
template <int B, int E, class F> DI void static_for(F&& f) {
  if constexpr (B < E) { f(IC<B>{}); static_for<B + 1, E>(f); }
}
template <int N> DI void wait_vm() { asm volatile("s_waitcnt vmcnt(%0)" ::"n"(N) : "memory"); }
DI void wait_lgkm0() { asm volatile("s_waitcnt lgkmcnt(0)" ::: "memory"); }
DI void bar() { __builtin_amdgcn_s_barrier(); }
DI void glds16(const void* src, char* lds_dst) {
  __builtin_amdgcn_global_load_lds((const unsigned*)src, (unsigned*)lds_dst, 16, 0, 0);
}
DI s16x4 trread(const char* p) {
  return __builtin_bit_cast(s16x4, __builtin_amdgcn_ds_read_tr16_b64_v4i16((LDS_AS s16x4*)p));
}
template <int OFF> DI s16x4 lds_tr(unsigned addr) {
  s16x4 r; asm volatile("ds_read_b64_tr_b16 %0, %1 offset:%2" : "=v"(r) : "v"(addr), "n"(OFF) : "memory"); return r;
}
template <int OFF> DI bf16x8 lds_b128(unsigned addr) {
  bf16x8 r; asm volatile("ds_read_b128 %0, %1 offset:%2" : "=v"(r) : "v"(addr), "n"(OFF) : "memory"); return r;
}
DI void st16_wt(void* base, unsigned bytes, unsigned off, u32x4 v) {
  __builtin_amdgcn_raw_buffer_store_b128(v, __builtin_amdgcn_make_buffer_rsrc(base, (short)0, (int)bytes, 0x00020000), (int)off, 0, 16);
}
DI bf16x8 cat8(s16x4 lo, s16x4 hi) { return (bf16x8){lo[0], lo[1], lo[2], lo[3], hi[0], hi[1], hi[2], hi[3]}; }
DI f32x4 mfma16(bf16x8 a, bf16x8 b, f32x4 c) { return __builtin_amdgcn_mfma_f32_16x16x32_bf16(a, b, c, 0, 0, 0); }

DI f32x2 gelu2(f32x2 x) {
  const float c0 = -2.302208198f, c1 = -2.302208198f * 0.044715f;
  const f32x2 a = x * (x * x * c1 + c0);
  const f32x2 d = (f32x2){__builtin_amdgcn_exp2f(a[0]), __builtin_amdgcn_exp2f(a[1])} + 1.0f;
  return x * (f32x2){__builtin_amdgcn_rcpf(d[0]), __builtin_amdgcn_rcpf(d[1])};
}
DI f32x2 rbf2(f32x2 v) { const unsigned p = pk2(v[0], v[1]); return (f32x2){bflo(p), bfhi(p)}; }
DI f32x2 unpk(unsigned p) { return (f32x2){bflo(p), bfhi(p)}; }

DI void feature_map(const bf16x8 (&wf)[2], bf16x8 xf, u32x4& pos, u32x4& neg) {
  f32x2 p[4];
#pragma unroll
  for (int fb = 0; fb < 2; ++fb) {
    const f32x4 d = mfma16(wf[fb], xf, (f32x4){0.f, 0.f, 0.f, 0.f});
    p[fb * 2] = rbf2((f32x2){d[0], d[1]});
    p[fb * 2 + 1] = rbf2((f32x2){d[2], d[3]});
  }
  float mx = fmaxf(p[0][0], p[0][1]), mn = fminf(p[0][0], p[0][1]);
#pragma unroll
  for (int e = 1; e < 4; ++e) { mx = fmaxf(mx, fmaxf(p[e][0], p[e][1])); mn = fminf(mn, fminf(p[e][0], p[e][1])); }
  mx = max4g(mx);
  mn = min4g(mn);
  const float l2e = 1.4426950408889634f;
  f32x2 ep[4], en[4], sp2 = {0.f, 0.f}, sn2 = {0.f, 0.f};
#pragma unroll
  for (int e = 0; e < 4; ++e) {
    const f32x2 a = (p[e] - mx) * l2e, b = (mn - p[e]) * l2e;
    ep[e] = (f32x2){__builtin_amdgcn_exp2f(a[0]), __builtin_amdgcn_exp2f(a[1])}; sp2 += ep[e];
    en[e] = (f32x2){__builtin_amdgcn_exp2f(b[0]), __builtin_amdgcn_exp2f(b[1])}; sn2 += en[e];
  }
  float sp = sp2[0] + sp2[1], sn = sn2[0] + sn2[1];
  sp = sum4g(sp);
  sn = sum4g(sn);
  const float ip = __builtin_amdgcn_rcpf(sp), in_ = __builtin_amdgcn_rcpf(sn);
#pragma unroll
  for (int e = 0; e < 4; ++e) {
    const f32x2 a = ep[e] * ip, b = en[e] * in_;
    pos[e] = pk2(a[0], a[1]); neg[e] = pk2(b[0], b[1]);
  }
}

#define TS(i)
struct GemmArgs {
  const u16* A;
  const u16* B;
  u16* out;
  const u16* res1;
  const u16* res2;
  const float* stats_in;
  float* stats_out;
  const float* colsum;
  const u16* wphi;
  float* kvpart;
  int ln_mode;
  int unused;
};

enum { EPI_QKV = 0, EPI_RES = 1, EPI_GELU_SHIFT = 2, EPI_W2 = 3, EPI_GELU = 4, EPI_W4 = 5 };

struct RowP { float alpha, beta, mhat, dhat; };
DI RowP row_params(float mean, float var, int mode) {
  RowP r;
  const float mh = rbf(mean);
  const float dh = rbf(__builtin_amdgcn_sqrtf(rbf(rbf(var) + 1e-5f)));
  const float i1 = __builtin_amdgcn_rcpf(dh);
  r.mhat = mh; r.dhat = dh;
  if (mode == 2) {
    const float m2 = (mean - mh) * i1;
    const float v2 = var * i1 * i1;
    const float mh2 = rbf(m2);
    const float dh2 = rbf(__builtin_amdgcn_sqrtf(rbf(rbf(v2) + 1e-5f)));
    const float i2 = __builtin_amdgcn_rcpf(dh2);
    r.alpha = i1 * i2;
    r.beta = (mh * i1 + mh2) * i2;
  } else {
    r.alpha = i1;
    r.beta = mh * i1;
  }
  return r;
}

template <int BN, int S, int KD, int EPI>
__global__ __launch_bounds__(512, 2) void gemm_k(GemmArgs g) {
  constexpr int NK = KD / 64;
  constexpr int NPF = BN / 64;
  constexpr bool HALF = (BN % 64) != 0;
  constexpr int NI = BN / 32;
  constexpr int A_BYTES = 16384;
  constexpr int STAGE = A_BYTES + BN * 128;
  constexpr int L = 4 + NPF * 2 + (HALF ? 1 : 0);
  constexpr int NTOT = (EPI == EPI_QKV) ? 3 * DM : (EPI == EPI_GELU ? FF : DM);
  constexpr bool USE_STATS = (EPI == EPI_QKV || EPI == EPI_GELU_SHIFT || EPI == EPI_W2 || EPI == EPI_GELU);
  constexpr bool FOLD = (EPI == EPI_QKV || EPI == EPI_GELU_SHIFT || EPI == EPI_GELU);
  constexpr int NRES = (EPI == EPI_RES || EPI == EPI_W2) ? 1 : (EPI == EPI_W4 ? 2 : 0);
  constexpr int OFF_STATS = S * STAGE;
  constexpr int OFF_RES = OFF_STATS;
  constexpr int OFF_WPHI = OFF_RES + NRES * 16384;
  constexpr int OFF_ROWP = OFF_WPHI + (EPI == EPI_QKV ? 4096 : 0);
  constexpr int OFF_CS = OFF_ROWP + 2048;
  constexpr int NEXTRA = (USE_STATS && BN != 64 ? 8 : 0) + NRES * 4 + (EPI == EPI_QKV ? 1 : 0) + (FOLD ? 1 : 0);

  extern __shared__ __attribute__((aligned(16))) char smem[];
  const int tid = threadIdx.x, lane = tid & 63;
  const int w = __builtin_amdgcn_readfirstlane(tid >> 6);
  const bool prod = w >= 4;
  const int cw = w & 3, wr = cw >> 1, wc = cw & 1;

  const int b = blockIdx.x, xcd = b & 7, j = b >> 3;
  int rp, ct;
  if constexpr (EPI == EPI_QKV || EPI == EPI_GELU) { rp = (xcd >> 2) * 8 + (j >> 2); ct = (xcd & 3) * 4 + (j & 3); }
  else { rp = (xcd >> 1) * 4 + (j >> 3); ct = (xcd & 1) * 8 + (j & 7); }
  const int m0 = rp * 128;
  const int n0 = (EPI == EPI_QKV) ? ct * 64 : ct * BN;
  constexpr int PSTRIDE = (EPI == EPI_QKV) ? DM : 64;
  const int g4 = lane >> 4, q4 = (lane >> 2) & 3, pp = lane & 3;
  RowP* rowp = (RowP*)(smem + OFF_ROWP);
  const bool ln_on = USE_STATS && (g.ln_mode != 0 || EPI == EPI_W2);

  f32x4 acc[4][NI];
  TS(0);
  constexpr bool RP_CONS = (BN == 64);
  f32x2 sv[8];
  const int sr = cw * 32 + (lane >> 1), shf = lane & 1;
  auto load_stats = [&]() {
#pragma unroll
    for (int p = 0; p < 8; ++p)
      asm volatile("global_load_dwordx2 %0, %1, off" : "=v"(sv[p]) : "v"(g.stats_in + ((size_t)(shf * 8 + p) * SEQ + m0 + sr) * 2) : "memory");
  };
  float rs_s = 0.f, rs_m2 = 0.f;
  auto row_stats = [&](auto pc) {
    constexpr int piece = decltype(pc)::value;
    if constexpr (piece == 0) {
#pragma unroll
      for (int p = 0; p < 8; ++p) asm volatile("" : "+v"(sv[p]));
      float s = 0.f;
#pragma unroll
      for (int p = 0; p < 8; ++p) s += sv[p][0];
      rs_s = s + dppf<0xB1>(s);
    } else if constexpr (piece == 1) {
      const float mean = rs_s * (1.0f / 1024.0f);
      float m2 = 0.f;
#pragma unroll
      for (int p = 0; p < 8; ++p) { const float d = sv[p][0] * (1.0f / 64.0f) - mean; m2 += sv[p][1] + 64.0f * d * d; }
      rs_m2 = m2 + dppf<0xB1>(m2);
    } else {
      if (ln_on && shf == 0) rowp[sr] = row_params(rs_s * (1.0f / 1024.0f), rs_m2 * (1.0f / 1024.0f), EPI == EPI_W2 ? 1 : g.ln_mode);
      wait_lgkm0();
    }
    __builtin_amdgcn_sched_barrier(0);
  };
  if (__builtin_expect(prod, 1)) {
    const u16* a_src = g.A + (size_t)(m0 + cw * 32 + (lane >> 3)) * KD + (((lane & 7) ^ (lane >> 3)) * 8);
    const u16* b_src[2];
#pragma unroll
    for (int e = 0; e < 2; ++e) {
      const int r = lane >> 3;
      const int f = ((r >> 1) & 1) | (e << 1);
      const int c = ((((lane & 7) >> 1) ^ f) << 1) | (lane & 1);
      b_src[e] = g.B + (size_t)((cw * 2 + e) * 8 + r) * NTOT + n0 + c * 8;
    }
    const u16* bh_src = nullptr;
    if constexpr (HALF) {
      const int c = ((((lane >> 1) & 1) ^ ((lane >> 5) & 1)) << 1) | (lane & 1);
      bh_src = g.B + (size_t)(cw * 16 + (lane >> 2)) * NTOT + n0 + NPF * 64 + c * 8;
    }
    auto issue = [&](int kt, int slot) {
      char* base = smem + slot * STAGE;
#pragma unroll
      for (int p = 0; p < NPF; ++p)
#pragma unroll
        for (int e = 0; e < 2; ++e)
          glds16(b_src[e] + (size_t)kt * 64 * NTOT + p * PSTRIDE, base + A_BYTES + p * 8192 + (cw * 2 + e) * 1024);
      if constexpr (HALF) glds16(bh_src + (size_t)kt * 64 * NTOT, base + A_BYTES + NPF * 8192 + cw * 1024);
#pragma unroll
      for (int e = 0; e < 4; ++e) glds16(a_src + (size_t)e * 8 * KD + kt * 64, base + (cw * 4 + e) * 1024);
    };
    TS(6);
    issue(0, 0);
    issue(1, 1);
    wait_vm<L>();
    TS(1);
    bar();
    static_for<2, S>([&](auto ic) { issue(ic.value, ic.value); });
    TS(7);
    if constexpr (NRES >= 1) {
#pragma unroll
      for (int e = 0; e < 4; ++e) {
        const int r = (cw * 4 + e) * 8 + (lane >> 3);
        glds16(g.res1 + (size_t)(m0 + r) * DM + n0 + (lane & 7) * 8, smem + OFF_RES + (cw * 4 + e) * 1024);
      }
    }
    if constexpr (NRES >= 2) {
#pragma unroll
      for (int e = 0; e < 4; ++e) {
        const int r = (cw * 4 + e) * 8 + (lane >> 3);
        glds16(g.res2 + (size_t)(m0 + r) * DM + n0 + (lane & 7) * 8, smem + OFF_RES + 16384 + (cw * 4 + e) * 1024);
      }
    }
    if constexpr (EPI == EPI_QKV) glds16(g.wphi + (size_t)ct * 2048 + cw * 512 + lane * 8, smem + OFF_WPHI + cw * 1024);
    if constexpr (FOLD) {
      int col = cw * 64 + (lane & 15) * 4;
      if (col > BN - 4) col = BN - 4;
      const int gcol = (EPI == EPI_QKV) ? (col >> 6) * DM + n0 + (col & 63) : n0 + col;
      if (lane < 16) glds16(g.colsum + gcol, smem + OFF_CS + cw * 256);
    }
    if constexpr (USE_STATS && !RP_CONS) load_stats();
    static_for<0, NK - 1>([&](auto ic) {
      constexpr int kt = ic.value;
      constexpr int young = (S - 2 < NK - 2 - kt) ? S - 2 : NK - 2 - kt;
      constexpr int ex = (kt + 1 <= S - 1 && kt + 1 < NK - 1) ? NEXTRA : 0;
      wait_vm<young * L + ex>();
      bar();
      if constexpr (kt + S < NK) issue(kt + S, kt % S);
      if constexpr (USE_STATS && !RP_CONS && kt >= 4 && kt < 7) row_stats(IC<kt - 4>{});
    });
    TS(2);
    bar();
  } else {
    const int fsw = ((q4 >> 1) & 1) | ((g4 & 1) << 1);
    int a_off[2], b_off[2], bh_off = 0;
#pragma unroll
    for (int k = 0; k < 2; ++k) {
      a_off[k] = (wr * 64 + (lane & 15)) * 128 + (((k * 4 + g4) ^ (lane & 7)) * 16);
      b_off[k] = A_BYTES + (8 * g4 + q4) * 128 + (((2 * k + wc) ^ fsw) * 32) + pp * 8;
    }
    if constexpr (HALF) bh_off = A_BYTES + NPF * 8192 + (8 * g4 + q4) * 64 + ((wc ^ (g4 & 1)) * 32) + pp * 8;
#pragma unroll
    for (int m = 0; m < 4; ++m)
#pragma unroll
      for (int i = 0; i < NI; ++i) acc[m][i] = (f32x4){0.f, 0.f, 0.f, 0.f};
    bf16x8 xa[2][4];
    s16x4 wlo[2][NI], whi[2][NI];
    const unsigned lbase = (unsigned)(size_t)smem;

    auto ldfrag = [&](auto setc, auto slotc, auto kc) {
      constexpr int set = decltype(setc)::value, slot = decltype(slotc)::value, k32 = decltype(kc)::value;
      const unsigned sb = lbase + slot * STAGE;
      const unsigned ba0 = sb + b_off[0], ba1 = sb + b_off[1], bah = sb + bh_off, aa = sb + a_off[k32];
      static_for<0, NI>([&](auto ic) {
        constexpr int i = decltype(ic)::value;
        if constexpr (HALF && i == NI - 1) {
          wlo[set][i] = lds_tr<k32 * 2048>(bah);
          whi[set][i] = lds_tr<k32 * 2048 + 256>(bah);
        } else {
          wlo[set][i] = lds_tr<(i >> 1) * 8192 + k32 * 4096>((i & 1) ? ba1 : ba0);
          whi[set][i] = lds_tr<(i >> 1) * 8192 + k32 * 4096 + 512>((i & 1) ? ba1 : ba0);
        }
      });
      static_for<0, 4>([&](auto mc) { constexpr int m = decltype(mc)::value; xa[set][m] = lds_b128<m * 2048>(aa); });
    };
    auto wait_set = [&](auto setc) {
      constexpr int set = decltype(setc)::value;
      asm volatile("s_waitcnt lgkmcnt(0)" : "+v"(xa[set][0]), "+v"(xa[set][1]), "+v"(xa[set][2]), "+v"(xa[set][3]) :: "memory");
#pragma unroll
      for (int i = 0; i < NI; ++i) asm volatile("" : "+v"(wlo[set][i]), "+v"(whi[set][i]));
      __builtin_amdgcn_sched_barrier(0);
    };
    auto ld1 = [&](auto setc, auto slotc, auto kc, auto rc) {
      constexpr int set = decltype(setc)::value, slot = decltype(slotc)::value, k32 = decltype(kc)::value, r = decltype(rc)::value;
      const unsigned sb = lbase + slot * STAGE;
      if constexpr (r < 2 * NI) {
        constexpr int i = r >> 1, hi = r & 1;
        s16x4 v;
        if constexpr (HALF && i == NI - 1) v = lds_tr<k32 * 2048 + hi * 256>(sb + bh_off);
        else v = lds_tr<(i >> 1) * 8192 + k32 * 4096 + hi * 512>(sb + b_off[i & 1]);
        if constexpr (hi) whi[set][i] = v; else wlo[set][i] = v;
      } else {
        constexpr int m = r - 2 * NI;
        xa[set][m] = lds_b128<m * 2048>(sb + a_off[k32]);
      }
    };
    auto mma_ld = [&](auto setc, auto ldc, auto slotc, auto kc) {
      constexpr int set = decltype(setc)::value;
      constexpr bool ld = decltype(ldc)::value != 0;
      static_for<0, 4 * NI>([&](auto tc) {
        constexpr int t = decltype(tc)::value, i = t >> 2, m = t & 3;
        acc[m][i] = mfma16(cat8(wlo[set][i], whi[set][i]), xa[set][m], acc[m][i]);
        if constexpr (ld && t < 2 * NI + 4) ld1(IC<set ^ 1>{}, slotc, kc, tc);
        __builtin_amdgcn_sched_barrier(0);
      });
    };

    if constexpr (USE_STATS && RP_CONS) load_stats();
    bar();
    TS(1);
    ldfrag(IC<0>{}, IC<0>{}, IC<0>{});
    static_for<0, NK>([&](auto ic) {
      constexpr int kt = ic.value;
      constexpr int slot = kt % S;
      wait_set(IC<0>{});
      if constexpr (USE_STATS && RP_CONS && kt >= 6 && kt < 9) { if constexpr (kt == 6) wait_vm<0>(); row_stats(IC<kt - 6>{}); }
      mma_ld(IC<0>{}, IC<1>{}, IC<slot>{}, IC<1>{});
      wait_set(IC<1>{});
      if constexpr (kt + 1 < NK) {
        bar();
        __builtin_amdgcn_sched_barrier(0);
        mma_ld(IC<1>{}, IC<1>{}, IC<(kt + 1) % S>{}, IC<0>{});
      } else {
        mma_ld(IC<1>{}, IC<0>{}, IC<0>{}, IC<0>{});
      }
    });
    TS(2);
    bar();
  }
  TS(3);

  constexpr int TSTR = BN * 2 + (EPI == EPI_QKV ? 32 : 16);
  char* T = smem;
  if (!prod) {
    float al[4] = {1.f, 1.f, 1.f, 1.f}, be[4] = {0.f, 0.f, 0.f, 0.f};
    if constexpr (FOLD) {
      if (ln_on) {
#pragma unroll
        for (int m = 0; m < 4; ++m) { const RowP r = rowp[wr * 64 + m * 16 + (lane & 15)]; al[m] = r.alpha; be[m] = r.beta; }
      }
    }
    f32x4 csv[NI];
    if constexpr (FOLD) {
#pragma unroll
      for (int i = 0; i < NI; ++i) csv[i] = *(const f32x4*)(smem + OFF_CS + ((2 * i + wc) * 16 + g4 * 4) * 4);
#pragma unroll
      for (int i = 0; i < NI; ++i) csv[i] = ln_on ? csv[i] : (f32x4){0.f, 0.f, 0.f, 0.f};
      __builtin_amdgcn_sched_barrier(0);
    }
#pragma unroll
    for (int i = 0; i < NI; ++i) {
      const int nb = 2 * i + wc;
      const int ncol = nb * 16 + g4 * 4;
      const f32x4 cs = csv[i];
#pragma unroll
      for (int m = 0; m < 4; ++m) {
        u32x2 pkd;
#pragma unroll
        for (int h = 0; h < 2; ++h) {
          f32x2 v = {acc[m][i][2 * h], acc[m][i][2 * h + 1]};
          if constexpr (FOLD) v = v * al[m] - (f32x2){cs[2 * h], cs[2 * h + 1]} * be[m];
          if constexpr (EPI == EPI_GELU_SHIFT || EPI == EPI_GELU) v = gelu2(rbf2(v));
          pkd[h] = pk2(v[0], v[1]);
        }
        *(u32x2*)(T + (wr * 64 + m * 16 + (lane & 15)) * TSTR + ncol * 2) = pkd;
      }
      if constexpr (EPI == EPI_GELU) {
        if ((i & 1) && i < NI - 1) { wait_lgkm0(); bar(); }
      }
    }
  } else if constexpr (EPI == EPI_GELU) {
#pragma unroll
    for (int p = 0; p < NI / 2; ++p) {
      bar();
      asm volatile("" ::: "memory");
#pragma unroll
      for (int it = 0; it < 4; ++it) {
        const int r = it * 32 + ((tid - 256) >> 3), c = p * 8 + (tid & 7);
        st16_wt(g.out, SEQ * FF * 2, ((m0 + r) * FF + n0 + c * 8) * 2, *(const u32x4*)(T + r * TSTR + c * 16));
      }
    }
  }
  __syncthreads();
  TS(4);
  if constexpr (EPI == EPI_GELU) {
    const int r = tid >> 2, c = (NI / 2) * 8 + (tid & 3);
    st16_wt(g.out, SEQ * FF * 2, ((m0 + r) * FF + n0 + c * 8) * 2, *(const u32x4*)(T + r * TSTR + c * 16));
  } else if constexpr (EPI == EPI_GELU_SHIFT) {
    const int grp = n0 >> 8;
    const int sh = (grp == 0) ? 1 : (grp == 1) ? -1 : (grp == 2) ? 2 : -2;
#pragma unroll
    for (int it = 0; it < 2; ++it) {
      const int r = it * 64 + (tid >> 3), c = tid & 7;
      const u32x4 v = *(const u32x4*)(T + r * TSTR + c * 16);
      const int dr = m0 + r + sh;
      if (dr >= 0 && dr < SEQ) st16_wt(g.out, SEQ * DM * 2, (dr * DM + n0 + c * 8) * 2, v);
    }
    if (tid < 16) {
      const int zr = tid >> 3, c = tid & 7;
      const int az = sh > 0 ? sh : -sh;
      if (zr < az) {
        if (sh > 0 && rp == 0) *(u32x4*)(g.out + (size_t)zr * DM + n0 + c * 8) = (u32x4){0u, 0u, 0u, 0u};
        if (sh < 0 && rp == 15) *(u32x4*)(g.out + (size_t)(SEQ - 1 - zr) * DM + n0 + c * 8) = (u32x4){0u, 0u, 0u, 0u};
      }
    }
  } else if constexpr (EPI == EPI_RES || EPI == EPI_W2 || EPI == EPI_W4) {
#pragma unroll
    for (int it = 0; it < 2; ++it) {
      const int r = it * 64 + (tid >> 3), c = tid & 7;
      const u32x4 v = *(const u32x4*)(T + r * TSTR + c * 16);
      const u32x4 x = *(const u32x4*)(smem + OFF_RES + r * 128 + c * 16);
      f32x2 y[4];
      if constexpr (EPI == EPI_RES) {
#pragma unroll
        for (int e = 0; e < 4; ++e) y[e] = unpk(x[e]) + unpk(v[e]);
      } else if constexpr (EPI == EPI_W2) {
        const RowP rr = rowp[r];
#pragma unroll
        for (int e = 0; e < 4; ++e) y[e] = rbf2(rbf2(unpk(x[e]) - rr.mhat) * rr.alpha) + unpk(v[e]);
      } else {
        const u32x4 x1 = *(const u32x4*)(smem + OFF_RES + 16384 + r * 128 + c * 16);
#pragma unroll
        for (int e = 0; e < 4; ++e) y[e] = unpk(x[e]) + rbf2(unpk(x1[e]) + unpk(v[e]));
      }
      u32x4 o;
#pragma unroll
      for (int e = 0; e < 4; ++e) { o[e] = pk2(y[e][0], y[e][1]); y[e] = unpk(o[e]); }
      st16_wt(g.out, SEQ * DM * 2, ((m0 + r) * DM + n0 + c * 8) * 2, o);
      const f32x2 s2 = (y[0] + y[1]) + (y[2] + y[3]);
      const float s = sum8(s2[0] + s2[1]);
      const float mc = s * (1.0f / 64.0f);
      f32x2 q2 = {0.f, 0.f};
#pragma unroll
      for (int e = 0; e < 4; ++e) { const f32x2 d = y[e] - mc; q2 += d * d; }
      const float m2 = sum8(q2[0] + q2[1]);
      if (c == 0) *(f32x2*)(g.stats_out + ((size_t)ct * SEQ + m0 + r) * 2) = (f32x2){s, m2};
    }
  } else {
    constexpr int PKSTR = 160;
    char* PK = smem + 128 * TSTR;
#pragma unroll
    for (int it = 0; it < 2; ++it) {
      const int r = it * 64 + (tid >> 3), c = tid & 7;
      st16_wt(g.out, SEQ * DM * 2, ((m0 + r) * DM + n0 + c * 8) * 2, *(const u32x4*)(T + r * TSTR + c * 16));
    }
    {
      const int hh = w & 1, rq = w >> 1;
      bf16x8 wf[2];
#pragma unroll
      for (int fb = 0; fb < 2; ++fb) {
        const char* p = smem + OFF_WPHI + hh * 2048 + (8 * g4 + q4) * 64 + fb * 32 + pp * 8;
        wf[fb] = cat8(trread(p), trread(p + 256));
      }
#pragma unroll
      for (int mb = 0; mb < 2; ++mb) {
        const int row = rq * 32 + mb * 16 + (lane & 15);
        const bf16x8 xf = *(const bf16x8*)(T + row * TSTR + (64 + hh * 32 + g4 * 8) * 2);
        u32x4 pos, neg;
        feature_map(wf, xf, pos, neg);
        char* dst = PK + (hh * 128 + row) * PKSTR + g4 * 8;
        *(u32x2*)dst = (u32x2){pos[0], pos[1]}; *(u32x2*)(dst + 32) = (u32x2){pos[2], pos[3]};
        *(u32x2*)(dst + 64) = (u32x2){neg[0], neg[1]}; *(u32x2*)(dst + 96) = (u32x2){neg[2], neg[3]};
      }
    }
    __syncthreads();
    {
      const int hh = w >> 2, fa = w & 3;
      f32x4 kv[3];
#pragma unroll
      for (int d = 0; d < 3; ++d) kv[d] = (f32x4){0.f, 0.f, 0.f, 0.f};
      const short one = (short)0x3F80;
      const bf16x8 ones = {one, one, one, one, one, one, one, one};
#pragma unroll
      for (int ks = 0; ks < 4; ++ks) {
        const int row = ks * 32 + 8 * g4 + q4;
        bf16x8 vf[2];
#pragma unroll
        for (int d = 0; d < 2; ++d) {
          const char* p = T + row * TSTR + (128 + hh * 32 + d * 16) * 2 + pp * 8;
          vf[d] = cat8(trread(p), trread(p + 4 * TSTR));
        }
        const char* p = PK + (hh * 128 + row) * PKSTR + (fa * 16) * 2 + pp * 8;
        const bf16x8 pf = cat8(trread(p), trread(p + 4 * PKSTR));
        kv[0] = mfma16(pf, vf[0], kv[0]);
        kv[1] = mfma16(pf, vf[1], kv[1]);
        kv[2] = mfma16(pf, ones, kv[2]);
      }
      constexpr unsigned KVB = 16 * NH * 33 * 64 * 4;
      const int so = (rp * NH + ct * 2 + hh) * (33 * 64) + fa * 16 + g4 * 4;
      st16_wt(g.kvpart, KVB, (so + (lane & 15) * 64) * 4, __builtin_bit_cast(u32x4, kv[0]));
      st16_wt(g.kvpart, KVB, (so + (16 + (lane & 15)) * 64) * 4, __builtin_bit_cast(u32x4, kv[1]));
      if ((lane & 15) == 0) st16_wt(g.kvpart, KVB, (so + 32 * 64) * 4, __builtin_bit_cast(u32x4, kv[2]));
    }
  }
  TS(5);
}

__global__ __launch_bounds__(256) void ln0_k(const u16* __restrict__ x, u16* __restrict__ y, const u16* __restrict__ wqkv,
                                             const u16* __restrict__ w1, const u16* __restrict__ w3, float* __restrict__ colsum,
                                             const u16* __restrict__ wo, const u16* __restrict__ w2, const u16* __restrict__ w4) {
  if (blockIdx.x >= SEQ / 4 + 120) {
    const int t = blockIdx.x - (SEQ / 4 + 120);
    const u16* W = t < 32 ? wo + (size_t)t * 32768 : (t < 64 ? w2 + (size_t)(t - 32) * 32768 : w4 + (size_t)(t - 64) * 32768);
#pragma unroll
    for (int i = 0; i < 16; ++i) {
      const u32x4 v = *(const u32x4*)(W + (i * 256 + threadIdx.x) * 8);
      asm volatile("" ::"v"(v));
    }
    return;
  }
  if (blockIdx.x >= SEQ / 4) {
    __shared__ float red[32 * 64];
    const int tid = threadIdx.x;
    const int cg = blockIdx.x - SEQ / 4;
    const u16* W; int N, c0;
    if (cg < 48) { W = wqkv; N = 3 * DM; c0 = cg * 64; }
    else if (cg < 64) { W = w1; N = DM; c0 = (cg - 48) * 64; }
    else { W = w3; N = FF; c0 = (cg - 64) * 64; }
    const int c = tid & 7, rg = tid >> 3;
    f32x2 s[4] = {{0.f, 0.f}, {0.f, 0.f}, {0.f, 0.f}, {0.f, 0.f}};
#pragma unroll 16
    for (int i = 0; i < 32; ++i) {
      const u32x4 v = *(const u32x4*)(W + (size_t)(rg + i * 32) * N + c0 + c * 8);
#pragma unroll
      for (int e = 0; e < 4; ++e) s[e] += unpk(v[e]);
    }
#pragma unroll
    for (int e = 0; e < 4; ++e) *(f32x2*)(red + rg * 64 + c * 8 + 2 * e) = s[e];
    __syncthreads();
    if (tid < 64) {
      float t = 0.f;
#pragma unroll
      for (int i = 0; i < 32; ++i) t += red[i * 64 + tid];
      colsum[cg * 64 + tid] = t;
    }
    return;
  }
  const int lane = threadIdx.x & 63, row = blockIdx.x * 4 + (threadIdx.x >> 6);
  const u16* src = x + (size_t)row * DM;
  const u32x4 a = *(const u32x4*)(src + lane * 8), b = *(const u32x4*)(src + 512 + lane * 8);
  float v[16];
#pragma unroll
  for (int e = 0; e < 4; ++e) { v[2 * e] = bflo(a[e]); v[2 * e + 1] = bfhi(a[e]); v[8 + 2 * e] = bflo(b[e]); v[9 + 2 * e] = bfhi(b[e]); }
  float s = 0.f;
#pragma unroll
  for (int e = 0; e < 16; ++e) s += v[e];
  s = sum64(s);
  const float mean = s * (1.0f / 1024.0f);
  float m2 = 0.f;
#pragma unroll
  for (int e = 0; e < 16; ++e) { const float d = v[e] - mean; m2 += d * d; }
  m2 = sum64(m2);
  const RowP r = row_params(mean, m2 * (1.0f / 1024.0f), 1);
  u32x4 oa, ob;
#pragma unroll
  for (int e = 0; e < 4; ++e) {
    oa[e] = pk2(rbf(v[2 * e] - r.mhat) / r.dhat, rbf(v[2 * e + 1] - r.mhat) / r.dhat);
    ob[e] = pk2(rbf(v[8 + 2 * e] - r.mhat) / r.dhat, rbf(v[9 + 2 * e] - r.mhat) / r.dhat);
  }
  u16* dst = y + (size_t)row * DM;
  *(u32x4*)(dst + lane * 8) = oa;
  *(u32x4*)(dst + 512 + lane * 8) = ob;
}

__global__ __launch_bounds__(512) void attn_o_k(const u16* __restrict__ q, const u16* __restrict__ wphi, const float* __restrict__ kvpart,
                                                u16* __restrict__ o) {
  constexpr int KS = 80;
  __shared__ __attribute__((aligned(16))) char sm[33 * KS * 2 + 8];
  const int tid = threadIdx.x, lane = tid & 63, w = tid >> 6;
  constexpr int EPI = 6;
  TS(0);
  const int h = blockIdx.x & 31, rb = blockIdx.x >> 5;
  const int g4 = lane >> 4, r0 = rb * 256 + w * 32 + (lane & 15);
  bf16x8 qf[2], wf[2];
#pragma unroll
  for (int mb = 0; mb < 2; ++mb) qf[mb] = *(const bf16x8*)(q + (size_t)(r0 + mb * 16) * DM + h * 32 + g4 * 8);
#pragma unroll
  for (int fb = 0; fb < 2; ++fb)
#pragma unroll
    for (int j = 0; j < 8; ++j) wf[fb][j] = (short)wphi[h * 1024 + (g4 * 8 + j) * 32 + fb * 16 + (lane & 15)];
  __builtin_amdgcn_sched_barrier(0);
  f32x4 kp[16], kz[16];
  const float* slab = kvpart + (size_t)h * (33 * 64) + tid * 4;
#pragma unroll
  for (int p = 0; p < 16; ++p) kp[p] = *(const f32x4*)(slab + (size_t)p * NH * (33 * 64));
  const bool zw = __builtin_amdgcn_readfirstlane(w) == 0;
  if (zw) {
#pragma unroll
    for (int p = 0; p < 16; ++p) kz[p] = *(const f32x4*)(slab + (size_t)p * NH * (33 * 64) + 2048 - (tid & 48) * 4);
  }
  __builtin_amdgcn_sched_barrier(0);
  bf16x8 pq[2][2];
#pragma unroll
  for (int mb = 0; mb < 2; ++mb) {
    u32x4 pos, neg;
    feature_map(wf, qf[mb], pos, neg);
    pq[mb][0] = __builtin_bit_cast(bf16x8, pos);
    pq[mb][1] = __builtin_bit_cast(bf16x8, neg);
  }
  __builtin_amdgcn_sched_barrier(0);
  TS(1);
  u16* kvt = (u16*)sm;
  {
    f32x4 t = kp[0];
#pragma unroll
    for (int p = 1; p < 16; ++p) t += kp[p];
    *(u32x2*)(kvt + (tid >> 4) * KS + (tid & 15) * 4) = (u32x2){pk2(t[0], t[1]), pk2(t[2], t[3])};
    if (zw) {
      f32x4 z = kz[0];
#pragma unroll
      for (int p = 1; p < 16; ++p) z += kz[p];
      if (tid < 16) *(u32x2*)(kvt + 32 * KS + tid * 4) = (u32x2){pk2(z[0], z[1]), pk2(z[2], z[3])};
    }
  }
  TS(2);
  __syncthreads();
  TS(3);
  bf16x8 kf[3][2];
#pragma unroll
  for (int db = 0; db < 3; ++db)
#pragma unroll
    for (int ks = 0; ks < 2; ++ks) {
      const int d = (db < 2) ? ((lane >> 2) & 3) * 8 + db * 4 + (lane & 3) : 32;
      const u16* p = kvt + d * KS + ks * 32 + g4 * 4;
      kf[db][ks] = cat8(*(const s16x4*)p, *(const s16x4*)(p + 16));
    }
#pragma unroll
  for (int mb = 0; mb < 2; ++mb) {
    f32x4 a[3];
#pragma unroll
    for (int db = 0; db < 3; ++db) {
      a[db] = mfma16(kf[db][0], pq[mb][0], (f32x4){0.f, 0.f, 0.f, 0.f});
      a[db] = mfma16(kf[db][1], pq[mb][1], a[db]);
    }
    const float id = __builtin_amdgcn_rcpf(rbf(rbf(a[2][0]) + 1e-6f));
    u32x4 ov;
#pragma unroll
    for (int db = 0; db < 2; ++db) {
      const f32x2 lo = rbf2((f32x2){a[db][0], a[db][1]}) * id, hi = rbf2((f32x2){a[db][2], a[db][3]}) * id;
      ov[db * 2] = pk2(lo[0], lo[1]); ov[db * 2 + 1] = pk2(hi[0], hi[1]);
    }
    st16_wt(o, SEQ * DM * 2, ((r0 + mb * 16) * DM + h * 32 + g4 * 8) * 2, ov);
  }
  TS(4);
  TS(5);
}

template <int BN, int S, int KD, int EPI> static constexpr int smem_bytes() {
  constexpr bool USE_STATS = (EPI == EPI_QKV || EPI == EPI_GELU_SHIFT || EPI == EPI_W2 || EPI == EPI_GELU);
  constexpr int NRES = (EPI == EPI_RES || EPI == EPI_W2) ? 1 : (EPI == EPI_W4 ? 2 : 0);
  return S * (16384 + BN * 128) + NRES * 16384 + (EPI == EPI_QKV ? 4096 : 0) + 2048 + 1024;
}
template <int BN, int S, int KD, int EPI> static void launch_gemm(const GemmArgs& a, hipStream_t st) {
  constexpr int sb = smem_bytes<BN, S, KD, EPI>();
  static_assert(sb <= 163840, "LDS");
  (void)hipFuncSetAttribute((const void*)gemm_k<BN, S, KD, EPI>, hipFuncAttributeMaxDynamicSharedMemorySize, sb);
  gemm_k<BN, S, KD, EPI><<<256, 512, sb, st>>>(a);
}

extern "C" void kernel_launch(void* const* d_in, const int* in_sizes, int n_in, void* d_out, int out_size, void* d_ws,
                              size_t ws_size, hipStream_t stream) {
  const u16* x0 = (const u16*)d_in[0];
  const u16* wqkv = (const u16*)d_in[1];
  const u16* wphi = (const u16*)d_in[2];
  const u16* wo = (const u16*)d_in[3];
  const u16* w1 = (const u16*)d_in[5];
  const u16* w2 = (const u16*)d_in[6];
  const u16* w3 = (const u16*)d_in[7];
  const u16* w4 = (const u16*)d_in[8];
  char* ws = (char*)d_ws;
  const size_t MB = 1 << 20;
  u16* xln0 = (u16*)(ws + 0 * MB);
  u16* phiq = (u16*)(ws + 4 * MB);
  float* kvpart = (float*)(ws + 12 * MB);
  u16* obuf = (u16*)(ws + 17 * MB);
  u16* xa = (u16*)(ws + 21 * MB);
  u16* hsh = (u16*)(ws + 25 * MB);
  u16* x1 = (u16*)(ws + 29 * MB);
  u16* gbuf = (u16*)(ws + 33 * MB);
  u16* xb = (u16*)(ws + 48 * MB);
  float* st_xa = (float*)(ws + 52 * MB);
  float* st_x1 = (float*)(ws + 53 * MB);
  float* st_xb = (float*)(ws + 54 * MB);
  float* colsum = (float*)(ws + 55 * MB);

  ln0_k<<<SEQ / 4 + 120 + 176, 256, 0, stream>>>(x0, xln0, wqkv, w1, w3, colsum, wo, w2, w4);
  for (int layer = 0; layer < 2; ++layer) {
    const u16* xin = layer == 0 ? x0 : xb;
    u16* xout = layer == 0 ? xb : (u16*)d_out;
    GemmArgs a;
    a = GemmArgs{layer == 0 ? xln0 : xb, wqkv, phiq, nullptr, nullptr, st_xb, nullptr, colsum, wphi, kvpart, layer == 0 ? 0 : 1, 0};
    launch_gemm<192, 3, 1024, EPI_QKV>(a, stream);
    attn_o_k<<<256, 512, 0, stream>>>(phiq, wphi, kvpart, obuf);
    a = GemmArgs{obuf, wo, xa, xin, nullptr, nullptr, st_xa, nullptr, nullptr, nullptr, 0, 0};
    launch_gemm<64, 5, 1024, EPI_RES>(a, stream);
    a = GemmArgs{xa, w1, hsh, nullptr, nullptr, st_xa, nullptr, colsum + 3072, nullptr, nullptr, 2, 0};
    launch_gemm<64, 5, 1024, EPI_GELU_SHIFT>(a, stream);
    a = GemmArgs{hsh, w2, x1, xa, nullptr, st_xa, st_x1, nullptr, nullptr, nullptr, 1, 0};
    launch_gemm<64, 5, 1024, EPI_W2>(a, stream);
    a = GemmArgs{x1, w3, gbuf, nullptr, nullptr, st_x1, nullptr, colsum + 4096, nullptr, nullptr, 1, 0};
    launch_gemm<224, 3, 1024, EPI_GELU>(a, stream);
    a = GemmArgs{gbuf, w4, xout, xa, x1, nullptr, st_xb, nullptr, nullptr, nullptr, 0, 0};
    launch_gemm<64, 5, 3584, EPI_W4>(a, stream);
  }
}
```

```cpp
#include <hip/hip_runtime.h>
#include <hip/hip_bf16.h>
#include <stdint.h>

typedef __attribute__((ext_vector_type(8))) short bf16x8;
typedef __attribute__((ext_vector_type(4))) short s16x4;
typedef __attribute__((ext_vector_type(4))) float f32x4;
typedef __attribute__((ext_vector_type(2))) float f32x2;
typedef __attribute__((ext_vector_type(4))) unsigned u32x4;
typedef __attribute__((ext_vector_type(2))) unsigned u32x2;
typedef __attribute__((ext_vector_type(2))) __bf16 bf16x2_t;
typedef unsigned short u16;
#define LDS_AS __attribute__((address_space(3)))
#define DI __device__ __forceinline__

static constexpr int SEQ = 2048, DM = 1024, NH = 32, FF = 3584;

DI float bf2f(unsigned v) { return __uint_as_float(v << 16); }
DI float bflo(unsigned v) { return __uint_as_float(v << 16); }
DI float bfhi(unsigned v) { return __uint_as_float(v & 0xffff0000u); }
DI unsigned pk2(float a, float b) {
  f32x2 v = {a, b};
  return __builtin_bit_cast(unsigned, __builtin_convertvector(v, bf16x2_t));
}
DI float rbf(float a) { return bflo(pk2(a, a)); }
template <int CTRL> DI float dppf(float v) { return __builtin_bit_cast(float, __builtin_amdgcn_update_dpp(0, __builtin_bit_cast(int, v), CTRL, 0xf, 0xf, true)); }
DI float sum8(float v) { v += dppf<0xB1>(v); v += dppf<0x4E>(v); return v + dppf<0x141>(v); }
DI f32x2 pair16(float v) { const unsigned u = __float_as_uint(v); const auto r = __builtin_amdgcn_permlane16_swap(u, u, false, false); return (f32x2){__uint_as_float(r[0]), __uint_as_float(r[1])}; }
DI f32x2 pair32(float v) { const unsigned u = __float_as_uint(v); const auto r = __builtin_amdgcn_permlane32_swap(u, u, false, false); return (f32x2){__uint_as_float(r[0]), __uint_as_float(r[1])}; }
DI float max4g(float v) { f32x2 t = pair16(v); t = pair32(fmaxf(t[0], t[1])); return fmaxf(t[0], t[1]); }
DI float min4g(float v) { f32x2 t = pair16(v); t = pair32(fminf(t[0], t[1])); return fminf(t[0], t[1]); }
DI float sum4g(float v) { f32x2 t = pair16(v); t = pair32(t[0] + t[1]); return t[0] + t[1]; }
DI float sum64(float v) { v = sum8(v); return sum4g(v + dppf<0x140>(v)); }

template <int I> struct IC { static constexpr int value = I; };
template <int B, int E, class F> DI void static_for(F&& f) {
  if constexpr (B < E) { f(IC<B>{}); static_for<B + 1, E>(f); }
}
template <int N> DI void wait_vm() { asm volatile("s_waitcnt vmcnt(%0)" ::"n"(N) : "memory"); }
DI void wait_lgkm0() { asm volatile("s_waitcnt lgkmcnt(0)" ::: "memory"); }
DI void bar() { __builtin_amdgcn_s_barrier(); }
DI void glds16(const void* src, char* lds_dst) {
  __builtin_amdgcn_global_load_lds((const unsigned*)src, (unsigned*)lds_dst, 16, 0, 0);
}
DI s16x4 trread(const char* p) {
  return __builtin_bit_cast(s16x4, __builtin_amdgcn_ds_read_tr16_b64_v4i16((LDS_AS s16x4*)p));
}
template <int OFF> DI s16x4 lds_tr(unsigned addr) {
  s16x4 r; asm volatile("ds_read_b64_tr_b16 %0, %1 offset:%2" : "=v"(r) : "v"(addr), "n"(OFF) : "memory"); return r;
}
template <int OFF> DI bf16x8 lds_b128(unsigned addr) {
  bf16x8 r; asm volatile("ds_read_b128 %0, %1 offset:%2" : "=v"(r) : "v"(addr), "n"(OFF) : "memory"); return r;
}
DI void st16_wt(void* base, unsigned bytes, unsigned off, u32x4 v) {
  __builtin_amdgcn_raw_buffer_store_b128(v, __builtin_amdgcn_make_buffer_rsrc(base, (short)0, (int)bytes, 0x00020000), (int)off, 0, 16);
}
DI bf16x8 cat8(s16x4 lo, s16x4 hi) { return (bf16x8){lo[0], lo[1], lo[2], lo[3], hi[0], hi[1], hi[2], hi[3]}; }
DI f32x4 mfma16(bf16x8 a, bf16x8 b, f32x4 c) { return __builtin_amdgcn_mfma_f32_16x16x32_bf16(a, b, c, 0, 0, 0); }

DI f32x2 gelu2(f32x2 x) {
  const float c0 = -2.302208198f, c1 = -2.302208198f * 0.044715f;
  const f32x2 a = x * (x * x * c1 + c0);
  const f32x2 d = (f32x2){__builtin_amdgcn_exp2f(a[0]), __builtin_amdgcn_exp2f(a[1])} + 1.0f;
  return x * (f32x2){__builtin_amdgcn_rcpf(d[0]), __builtin_amdgcn_rcpf(d[1])};
}
DI f32x2 rbf2(f32x2 v) { const unsigned p = pk2(v[0], v[1]); return (f32x2){bflo(p), bfhi(p)}; }
DI f32x2 unpk(unsigned p) { return (f32x2){bflo(p), bfhi(p)}; }

DI void feature_map(const bf16x8 (&wf)[2], bf16x8 xf, u32x4& pos, u32x4& neg) {
  f32x2 p[4];
#pragma unroll
  for (int fb = 0; fb < 2; ++fb) {
    const f32x4 d = mfma16(wf[fb], xf, (f32x4){0.f, 0.f, 0.f, 0.f});
    p[fb * 2] = rbf2((f32x2){d[0], d[1]});
    p[fb * 2 + 1] = rbf2((f32x2){d[2], d[3]});
  }
  float mx = fmaxf(p[0][0], p[0][1]), mn = fminf(p[0][0], p[0][1]);
#pragma unroll
  for (int e = 1; e < 4; ++e) { mx = fmaxf(mx, fmaxf(p[e][0], p[e][1])); mn = fminf(mn, fminf(p[e][0], p[e][1])); }
  mx = max4g(mx);
  mn = min4g(mn);
  const float l2e = 1.4426950408889634f;
  f32x2 ep[4], en[4], sp2 = {0.f, 0.f}, sn2 = {0.f, 0.f};
#pragma unroll
  for (int e = 0; e < 4; ++e) {
    const f32x2 a = (p[e] - mx) * l2e, b = (mn - p[e]) * l2e;
    ep[e] = (f32x2){__builtin_amdgcn_exp2f(a[0]), __builtin_amdgcn_exp2f(a[1])}; sp2 += ep[e];
    en[e] = (f32x2){__builtin_amdgcn_exp2f(b[0]), __builtin_amdgcn_exp2f(b[1])}; sn2 += en[e];
  }
  float sp = sp2[0] + sp2[1], sn = sn2[0] + sn2[1];
  sp = sum4g(sp);
  sn = sum4g(sn);
  const float ip = __builtin_amdgcn_rcpf(sp), in_ = __builtin_amdgcn_rcpf(sn);
#pragma unroll
  for (int e = 0; e < 4; ++e) {
    const f32x2 a = ep[e] * ip, b = en[e] * in_;
    pos[e] = pk2(a[0], a[1]); neg[e] = pk2(b[0], b[1]);
  }
}

#define TS(i)
struct GemmArgs {
  const u16* A;
  const u16* B;
  u16* out;
  const u16* res1;
  const u16* res2;
  const float* stats_in;
  float* stats_out;
  const float* colsum;
  const u16* wphi;
  float* kvpart;
  int ln_mode;
  int unused;
};

enum { EPI_QKV = 0, EPI_RES = 1, EPI_GELU_SHIFT = 2, EPI_W2 = 3, EPI_GELU = 4, EPI_W4 = 5 };

struct RowP { float alpha, beta, mhat, dhat; };
DI RowP row_params(float mean, float var, int mode) {
  RowP r;
  const float mh = rbf(mean);
  const float dh = rbf(__builtin_amdgcn_sqrtf(rbf(rbf(var) + 1e-5f)));
  const float i1 = __builtin_amdgcn_rcpf(dh);
  r.mhat = mh; r.dhat = dh;
  if (mode == 2) {
    const float m2 = (mean - mh) * i1;
    const float v2 = var * i1 * i1;
    const float mh2 = rbf(m2);
    const float dh2 = rbf(__builtin_amdgcn_sqrtf(rbf(rbf(v2) + 1e-5f)));
    const float i2 = __builtin_amdgcn_rcpf(dh2);
    r.alpha = i1 * i2;
    r.beta = (mh * i1 + mh2) * i2;
  } else {
    r.alpha = i1;
    r.beta = mh * i1;
  }
  return r;
}

template <int BN, int S, int KD, int EPI>
__global__ __launch_bounds__(512, 2) void gemm_k(GemmArgs g) {
  constexpr int NK = KD / 64;
  constexpr int NPF = BN / 64;
  constexpr bool HALF = (BN % 64) != 0;
  constexpr int NI = BN / 32;
  constexpr int A_BYTES = 16384;
  constexpr int STAGE = A_BYTES + BN * 128;
  constexpr int L = 4 + NPF * 2 + (HALF ? 1 : 0);
  constexpr int NTOT = (EPI == EPI_QKV) ? 3 * DM : (EPI == EPI_GELU ? FF : DM);
  constexpr bool USE_STATS = (EPI == EPI_QKV || EPI == EPI_GELU_SHIFT || EPI == EPI_W2 || EPI == EPI_GELU);
  constexpr bool FOLD = (EPI == EPI_QKV || EPI == EPI_GELU_SHIFT || EPI == EPI_GELU);
  constexpr int NRES = (EPI == EPI_RES || EPI == EPI_W2) ? 1 : (EPI == EPI_W4 ? 2 : 0);
  constexpr int OFF_STATS = S * STAGE;
  constexpr int OFF_RES = OFF_STATS;
  constexpr int OFF_WPHI = OFF_RES + NRES * 16384;
  constexpr int OFF_ROWP = OFF_WPHI + (EPI == EPI_QKV ? 4096 : 0);
  constexpr int OFF_CS = OFF_ROWP + 2048;
  constexpr int NEXTRA = (USE_STATS && BN != 64 ? 8 : 0) + NRES * 4 + (EPI == EPI_QKV ? 1 : 0) + (FOLD ? 1 : 0);

  extern __shared__ __attribute__((aligned(16))) char smem[];
  const int tid = threadIdx.x, lane = tid & 63;
  const int w = __builtin_amdgcn_readfirstlane(tid >> 6);
  const bool prod = w >= 4;
  const int cw = w & 3, wr = cw >> 1, wc = cw & 1;

  const int b = blockIdx.x, xcd = b & 7, j = b >> 3;
  int rp, ct;
  if constexpr (EPI == EPI_QKV || EPI == EPI_GELU) { rp = (xcd >> 2) * 8 + (j >> 2); ct = (xcd & 3) * 4 + (j & 3); }
  else { rp = (xcd >> 1) * 4 + (j >> 3); ct = (xcd & 1) * 8 + (j & 7); }
  const int m0 = rp * 128;
  const int n0 = (EPI == EPI_QKV) ? ct * 64 : ct * BN;
  constexpr int PSTRIDE = (EPI == EPI_QKV) ? DM : 64;
  const int g4 = lane >> 4, q4 = (lane >> 2) & 3, pp = lane & 3;
  RowP* rowp = (RowP*)(smem + OFF_ROWP);
  const bool ln_on = USE_STATS && (g.ln_mode != 0 || EPI == EPI_W2);

  f32x4 acc[4][NI];
  TS(0);
  constexpr bool RP_CONS = (BN == 64);
  f32x2 sv[8];
  const int sr = cw * 32 + (lane >> 1), shf = lane & 1;
  auto load_stats = [&]() {
#pragma unroll
    for (int p = 0; p < 8; ++p)
      asm volatile("global_load_dwordx2 %0, %1, off" : "=v"(sv[p]) : "v"(g.stats_in + ((size_t)(shf * 8 + p) * SEQ + m0 + sr) * 2) : "memory");
  };
  float rs_s = 0.f, rs_m2 = 0.f;
  auto row_stats = [&](auto pc) {
    constexpr int piece = decltype(pc)::value;
    if constexpr (piece == 0) {
#pragma unroll
      for (int p = 0; p < 8; ++p) asm volatile("" : "+v"(sv[p]));
      float s = 0.f;
#pragma unroll
      for (int p = 0; p < 8; ++p) s += sv[p][0];
      rs_s = s + dppf<0xB1>(s);
    } else if constexpr (piece == 1) {
      const float mean = rs_s * (1.0f / 1024.0f);
      float m2 = 0.f;
#pragma unroll
      for (int p = 0; p < 8; ++p) { const float d = sv[p][0] * (1.0f / 64.0f) - mean; m2 += sv[p][1] + 64.0f * d * d; }
      rs_m2 = m2 + dppf<0xB1>(m2);
    } else {
      if (ln_on && shf == 0) rowp[sr] = row_params(rs_s * (1.0f / 1024.0f), rs_m2 * (1.0f / 1024.0f), EPI == EPI_W2 ? 1 : g.ln_mode);
      wait_lgkm0();
    }
    __builtin_amdgcn_sched_barrier(0);
  };
  if (__builtin_expect(prod, 1)) {
    const u16* a_src = g.A + (size_t)(m0 + cw * 32 + (lane >> 3)) * KD + (((lane & 7) ^ (lane >> 3)) * 8);
    const u16* b_src[2];
#pragma unroll
    for (int e = 0; e < 2; ++e) {
      const int r = lane >> 3;
      const int f = ((r >> 1) & 1) | (e << 1);
      const int c = ((((lane & 7) >> 1) ^ f) << 1) | (lane & 1);
      b_src[e] = g.B + (size_t)((cw * 2 + e) * 8 + r) * NTOT + n0 + c * 8;
    }
    const u16* bh_src = nullptr;
    if constexpr (HALF) {
      const int c = ((((lane >> 1) & 1) ^ ((lane >> 5) & 1)) << 1) | (lane & 1);
      bh_src = g.B + (size_t)(cw * 16 + (lane >> 2)) * NTOT + n0 + NPF * 64 + c * 8;
    }
    auto issue = [&](int kt, int slot) {
      char* base = smem + slot * STAGE;
#pragma unroll
      for (int p = 0; p < NPF; ++p)
#pragma unroll
        for (int e = 0; e < 2; ++e)
          glds16(b_src[e] + (size_t)kt * 64 * NTOT + p * PSTRIDE, base + A_BYTES + p * 8192 + (cw * 2 + e) * 1024);
      if constexpr (HALF) glds16(bh_src + (size_t)kt * 64 * NTOT, base + A_BYTES + NPF * 8192 + cw * 1024);
#pragma unroll
      for (int e = 0; e < 4; ++e) glds16(a_src + (size_t)e * 8 * KD + kt * 64, base + (cw * 4 + e) * 1024);
    };
    TS(6);
    issue(0, 0);
    issue(1, 1);
    wait_vm<L>();
    TS(1);
    bar();
    static_for<2, S>([&](auto ic) { issue(ic.value, ic.value); });
    TS(7);
    if constexpr (NRES >= 1) {
#pragma unroll
      for (int e = 0; e < 4; ++e) {
        const int r = (cw * 4 + e) * 8 + (lane >> 3);
        glds16(g.res1 + (size_t)(m0 + r) * DM + n0 + (lane & 7) * 8, smem + OFF_RES + (cw * 4 + e) * 1024);
      }
    }
    if constexpr (NRES >= 2) {
#pragma unroll
      for (int e = 0; e < 4; ++e) {
        const int r = (cw * 4 + e) * 8 + (lane >> 3);
        glds16(g.res2 + (size_t)(m0 + r) * DM + n0 + (lane & 7) * 8, smem + OFF_RES + 16384 + (cw * 4 + e) * 1024);
      }
    }
    if constexpr (EPI == EPI_QKV) glds16(g.wphi + (size_t)ct * 2048 + cw * 512 + lane * 8, smem + OFF_WPHI + cw * 1024);
    if constexpr (FOLD) {
      int col = cw * 64 + (lane & 15) * 4;
      if (col > BN - 4) col = BN - 4;
      const int gcol = (EPI == EPI_QKV) ? (col >> 6) * DM + n0 + (col & 63) : n0 + col;
      if (lane < 16) glds16(g.colsum + gcol, smem + OFF_CS + cw * 256);
    }
    if constexpr (USE_STATS && !RP_CONS) load_stats();
    static_for<0, NK - 1>([&](auto ic) {
      constexpr int kt = ic.value;
      constexpr int young = (S - 2 < NK - 2 - kt) ? S - 2 : NK - 2 - kt;
      constexpr int ex = (kt + 1 <= S - 1 && kt + 1 < NK - 1) ? NEXTRA : 0;
      wait_vm<young * L + ex>();
      bar();
      if constexpr (kt + S < NK) issue(kt + S, kt % S);
      if constexpr (USE_STATS && !RP_CONS && kt >= 4 && kt < 7) row_stats(IC<kt - 4>{});
    });
    TS(2);
    bar();
  } else {
    const int fsw = ((q4 >> 1) & 1) | ((g4 & 1) << 1);
    int a_off[2], b_off[2], bh_off = 0;
#pragma unroll
    for (int k = 0; k < 2; ++k) {
      a_off[k] = (wr * 64 + (lane & 15)) * 128 + (((k * 4 + g4) ^ (lane & 7)) * 16);
      b_off[k] = A_BYTES + (8 * g4 + q4) * 128 + (((2 * k + wc) ^ fsw) * 32) + pp * 8;
    }
    if constexpr (HALF) bh_off = A_BYTES + NPF * 8192 + (8 * g4 + q4) * 64 + ((wc ^ (g4 & 1)) * 32) + pp * 8;
#pragma unroll
    for (int m = 0; m < 4; ++m)
#pragma unroll
      for (int i = 0; i < NI; ++i) acc[m][i] = (f32x4){0.f, 0.f, 0.f, 0.f};
    bf16x8 xa[2][4];
    s16x4 wlo[2][NI], whi[2][NI];
    const unsigned lbase = (unsigned)(size_t)smem;

    auto ldfrag = [&](auto setc, auto slotc, auto kc) {
      constexpr int set = decltype(setc)::value, slot = decltype(slotc)::value, k32 = decltype(kc)::value;
      const unsigned sb = lbase + slot * STAGE;
      const unsigned ba0 = sb + b_off[0], ba1 = sb + b_off[1], bah = sb + bh_off, aa = sb + a_off[k32];
      static_for<0, NI>([&](auto ic) {
        constexpr int i = decltype(ic)::value;
        if constexpr (HALF && i == NI - 1) {
          wlo[set][i] = lds_tr<k32 * 2048>(bah);
          whi[set][i] = lds_tr<k32 * 2048 + 256>(bah);
        } else {
          wlo[set][i] = lds_tr<(i >> 1) * 8192 + k32 * 4096>((i & 1) ? ba1 : ba0);
          whi[set][i] = lds_tr<(i >> 1) * 8192 + k32 * 4096 + 512>((i & 1) ? ba1 : ba0);
        }
      });
      static_for<0, 4>([&](auto mc) { constexpr int m = decltype(mc)::value; xa[set][m] = lds_b128<m * 2048>(aa); });
    };
    auto wait_set = [&](auto setc) {
      constexpr int set = decltype(setc)::value;
      asm volatile("s_waitcnt lgkmcnt(0)" : "+v"(xa[set][0]), "+v"(xa[set][1]), "+v"(xa[set][2]), "+v"(xa[set][3]) :: "memory");
#pragma unroll
      for (int i = 0; i < NI; ++i) asm volatile("" : "+v"(wlo[set][i]), "+v"(whi[set][i]));
      __builtin_amdgcn_sched_barrier(0);
    };
    auto ld1 = [&](auto setc, auto slotc, auto kc, auto rc) {
      constexpr int set = decltype(setc)::value, slot = decltype(slotc)::value, k32 = decltype(kc)::value, r = decltype(rc)::value;
      const unsigned sb = lbase + slot * STAGE;
      if constexpr (r < 2 * NI) {
        constexpr int i = r >> 1, hi = r & 1;
        s16x4 v;
        if constexpr (HALF && i == NI - 1) v = lds_tr<k32 * 2048 + hi * 256>(sb + bh_off);
        else v = lds_tr<(i >> 1) * 8192 + k32 * 4096 + hi * 512>(sb + b_off[i & 1]);
        if constexpr (hi) whi[set][i] = v; else wlo[set][i] = v;
      } else {
        constexpr int m = r - 2 * NI;
        xa[set][m] = lds_b128<m * 2048>(sb + a_off[k32]);
      }
    };
    auto mma_ld = [&](auto setc, auto ldc, auto slotc, auto kc) {
      constexpr int set = decltype(setc)::value;
      constexpr bool ld = decltype(ldc)::value != 0;
      static_for<0, 4 * NI>([&](auto tc) {
        constexpr int t = decltype(tc)::value, i = t >> 2, m = t & 3;
        acc[m][i] = mfma16(cat8(wlo[set][i], whi[set][i]), xa[set][m], acc[m][i]);
        if constexpr (ld && t < 2 * NI + 4) ld1(IC<set ^ 1>{}, slotc, kc, tc);
        __builtin_amdgcn_sched_barrier(0);
      });
    };

    if constexpr (USE_STATS && RP_CONS) load_stats();
    bar();
    TS(1);
    ldfrag(IC<0>{}, IC<0>{}, IC<0>{});
    static_for<0, NK>([&](auto ic) {
      constexpr int kt = ic.value;
      constexpr int slot = kt % S;
      wait_set(IC<0>{});
      if constexpr (USE_STATS && RP_CONS && kt >= 6 && kt < 9) { if constexpr (kt == 6) wait_vm<0>(); row_stats(IC<kt - 6>{}); }
      mma_ld(IC<0>{}, IC<1>{}, IC<slot>{}, IC<1>{});
      wait_set(IC<1>{});
      if constexpr (kt + 1 < NK) {
        bar();
        __builtin_amdgcn_sched_barrier(0);
        mma_ld(IC<1>{}, IC<1>{}, IC<(kt + 1) % S>{}, IC<0>{});
      } else {
        mma_ld(IC<1>{}, IC<0>{}, IC<0>{}, IC<0>{});
      }
    });
    TS(2);
    bar();
  }
  TS(3);

  constexpr int TSTR = BN * 2 + (EPI == EPI_QKV ? 32 : 16);
  char* T = smem;
  if (!prod) {
    float al[4] = {1.f, 1.f, 1.f, 1.f}, be[4] = {0.f, 0.f, 0.f, 0.f};
    if constexpr (FOLD) {
      if (ln_on) {
#pragma unroll
        for (int m = 0; m < 4; ++m) { const RowP r = rowp[wr * 64 + m * 16 + (lane & 15)]; al[m] = r.alpha; be[m] = r.beta; }
      }
    }
    f32x4 csv[NI];
    if constexpr (FOLD) {
#pragma unroll
      for (int i = 0; i < NI; ++i) csv[i] = *(const f32x4*)(smem + OFF_CS + ((2 * i + wc) * 16 + g4 * 4) * 4);
#pragma unroll
      for (int i = 0; i < NI; ++i) csv[i] = ln_on ? csv[i] : (f32x4){0.f, 0.f, 0.f, 0.f};
      __builtin_amdgcn_sched_barrier(0);
    }
#pragma unroll
    for (int i = 0; i < NI; ++i) {
      const int nb = 2 * i + wc;
      const int ncol = nb * 16 + g4 * 4;
      const f32x4 cs = csv[i];
#pragma unroll
      for (int m = 0; m < 4; ++m) {
        u32x2 pkd;
#pragma unroll
        for (int h = 0; h < 2; ++h) {
          f32x2 v = {acc[m][i][2 * h], acc[m][i][2 * h + 1]};
          if constexpr (FOLD) v = v * al[m] - (f32x2){cs[2 * h], cs[2 * h + 1]} * be[m];
          if constexpr (EPI == EPI_GELU_SHIFT || EPI == EPI_GELU) v = gelu2(rbf2(v));
          pkd[h] = pk2(v[0], v[1]);
        }
        *(u32x2*)(T + (wr * 64 + m * 16 + (lane & 15)) * TSTR + ncol * 2) = pkd;
      }
      if constexpr (EPI == EPI_GELU) {
        if ((i & 1) && i < NI - 1) { wait_lgkm0(); bar(); }
      }
    }
  } else if constexpr (EPI == EPI_QKV) {
    if (g.res1) {
      const int t = tid - 256;
      const u16* W4 = (const u16*)g.stats_out + (size_t)b * 14336 + t * 8;
      const u16* Wo = g.res1 + (size_t)b * 4096 + t * 8;
      const u16* W2 = g.res2 + (size_t)b * 4096 + t * 8;
      u32x4 v[11];
#pragma unroll
      for (int i = 0; i < 7; ++i) v[i] = *(const u32x4*)(W4 + i * 2048);
#pragma unroll
      for (int i = 0; i < 2; ++i) { v[7 + i] = *(const u32x4*)(Wo + i * 2048); v[9 + i] = *(const u32x4*)(W2 + i * 2048); }
#pragma unroll
      for (int i = 0; i < 11; ++i) asm volatile("" ::"v"(v[i]));
    }
  } else if constexpr (EPI == EPI_GELU) {
#pragma unroll
    for (int p = 0; p < NI / 2; ++p) {
      bar();
      asm volatile("" ::: "memory");
#pragma unroll
      for (int it = 0; it < 4; ++it) {
        const int r = it * 32 + ((tid - 256) >> 3), c = p * 8 + (tid & 7);
        st16_wt(g.out, SEQ * FF * 2, ((m0 + r) * FF + n0 + c * 8) * 2, *(const u32x4*)(T + r * TSTR + c * 16));
      }
    }
  }
  __syncthreads();
  TS(4);
  if constexpr (EPI == EPI_GELU) {
    const int r = tid >> 2, c = (NI / 2) * 8 + (tid & 3);
    st16_wt(g.out, SEQ * FF * 2, ((m0 + r) * FF + n0 + c * 8) * 2, *(const u32x4*)(T + r * TSTR + c * 16));
  } else if constexpr (EPI == EPI_GELU_SHIFT) {
    const int grp = n0 >> 8;
    const int sh = (grp == 0) ? 1 : (grp == 1) ? -1 : (grp == 2) ? 2 : -2;
#pragma unroll
    for (int it = 0; it < 2; ++it) {
      const int r = it * 64 + (tid >> 3), c = tid & 7;
      const u32x4 v = *(const u32x4*)(T + r * TSTR + c * 16);
      const int dr = m0 + r + sh;
      if (dr >= 0 && dr < SEQ) st16_wt(g.out, SEQ * DM * 2, (dr * DM + n0 + c * 8) * 2, v);
    }
    if (tid < 16) {
      const int zr = tid >> 3, c = tid & 7;
      const int az = sh > 0 ? sh : -sh;
      if (zr < az) {
        if (sh > 0 && rp == 0) *(u32x4*)(g.out + (size_t)zr * DM + n0 + c * 8) = (u32x4){0u, 0u, 0u, 0u};
        if (sh < 0 && rp == 15) *(u32x4*)(g.out + (size_t)(SEQ - 1 - zr) * DM + n0 + c * 8) = (u32x4){0u, 0u, 0u, 0u};
      }
    }
  } else if constexpr (EPI == EPI_RES || EPI == EPI_W2 || EPI == EPI_W4) {
#pragma unroll
    for (int it = 0; it < 2; ++it) {
      const int r = it * 64 + (tid >> 3), c = tid & 7;
      const u32x4 v = *(const u32x4*)(T + r * TSTR + c * 16);
      const u32x4 x = *(const u32x4*)(smem + OFF_RES + r * 128 + c * 16);
      f32x2 y[4];
      if constexpr (EPI == EPI_RES) {
#pragma unroll
        for (int e = 0; e < 4; ++e) y[e] = unpk(x[e]) + unpk(v[e]);
      } else if constexpr (EPI == EPI_W2) {
        const RowP rr = rowp[r];
#pragma unroll
        for (int e = 0; e < 4; ++e) y[e] = rbf2(rbf2(unpk(x[e]) - rr.mhat) * rr.alpha) + unpk(v[e]);
      } else {
        const u32x4 x1 = *(const u32x4*)(smem + OFF_RES + 16384 + r * 128 + c * 16);
#pragma unroll
        for (int e = 0; e < 4; ++e) y[e] = unpk(x[e]) + rbf2(unpk(x1[e]) + unpk(v[e]));
      }
      u32x4 o;
#pragma unroll
      for (int e = 0; e < 4; ++e) { o[e] = pk2(y[e][0], y[e][1]); y[e] = unpk(o[e]); }
      st16_wt(g.out, SEQ * DM * 2, ((m0 + r) * DM + n0 + c * 8) * 2, o);
      const f32x2 s2 = (y[0] + y[1]) + (y[2] + y[3]);
      const float s = sum8(s2[0] + s2[1]);
      const float mc = s * (1.0f / 64.0f);
      f32x2 q2 = {0.f, 0.f};
#pragma unroll
      for (int e = 0; e < 4; ++e) { const f32x2 d = y[e] - mc; q2 += d * d; }
      const float m2 = sum8(q2[0] + q2[1]);
      if (c == 0) *(f32x2*)(g.stats_out + ((size_t)ct * SEQ + m0 + r) * 2) = (f32x2){s, m2};
    }
  } else {
    constexpr int PKSTR = 160;
    char* PK = smem + 128 * TSTR;
#pragma unroll
    for (int it = 0; it < 2; ++it) {
      const int r = it * 64 + (tid >> 3), c = tid & 7;
      st16_wt(g.out, SEQ * DM * 2, ((m0 + r) * DM + n0 + c * 8) * 2, *(const u32x4*)(T + r * TSTR + c * 16));
    }
    {
      const int hh = w & 1, rq = w >> 1;
      bf16x8 wf[2];
#pragma unroll
      for (int fb = 0; fb < 2; ++fb) {
        const char* p = smem + OFF_WPHI + hh * 2048 + (8 * g4 + q4) * 64 + fb * 32 + pp * 8;
        wf[fb] = cat8(trread(p), trread(p + 256));
      }
#pragma unroll
      for (int mb = 0; mb < 2; ++mb) {
        const int row = rq * 32 + mb * 16 + (lane & 15);
        const bf16x8 xf = *(const bf16x8*)(T + row * TSTR + (64 + hh * 32 + g4 * 8) * 2);
        u32x4 pos, neg;
        feature_map(wf, xf, pos, neg);
        char* dst = PK + (hh * 128 + row) * PKSTR + g4 * 8;
        *(u32x2*)dst = (u32x2){pos[0], pos[1]}; *(u32x2*)(dst + 32) = (u32x2){pos[2], pos[3]};
        *(u32x2*)(dst + 64) = (u32x2){neg[0], neg[1]}; *(u32x2*)(dst + 96) = (u32x2){neg[2], neg[3]};
      }
    }
    __syncthreads();
    {
      const int hh = w >> 2, fa = w & 3;
      f32x4 kv[3];
#pragma unroll
      for (int d = 0; d < 3; ++d) kv[d] = (f32x4){0.f, 0.f, 0.f, 0.f};
      const short one = (short)0x3F80;
      const bf16x8 ones = {one, one, one, one, one, one, one, one};
#pragma unroll
      for (int ks = 0; ks < 4; ++ks) {
        const int row = ks * 32 + 8 * g4 + q4;
        bf16x8 vf[2];
#pragma unroll
        for (int d = 0; d < 2; ++d) {
          const char* p = T + row * TSTR + (128 + hh * 32 + d * 16) * 2 + pp * 8;
          vf[d] = cat8(trread(p), trread(p + 4 * TSTR));
        }
        const char* p = PK + (hh * 128 + row) * PKSTR + (fa * 16) * 2 + pp * 8;
        const bf16x8 pf = cat8(trread(p), trread(p + 4 * PKSTR));
        kv[0] = mfma16(pf, vf[0], kv[0]);
        kv[1] = mfma16(pf, vf[1], kv[1]);
        kv[2] = mfma16(pf, ones, kv[2]);
      }
      constexpr unsigned KVB = 16 * NH * 33 * 64 * 4;
      const int so = (rp * NH + ct * 2 + hh) * (33 * 64) + fa * 16 + g4 * 4;
      st16_wt(g.kvpart, KVB, (so + (lane & 15) * 64) * 4, __builtin_bit_cast(u32x4, kv[0]));
      st16_wt(g.kvpart, KVB, (so + (16 + (lane & 15)) * 64) * 4, __builtin_bit_cast(u32x4, kv[1]));
      if ((lane & 15) == 0) st16_wt(g.kvpart, KVB, (so + 32 * 64) * 4, __builtin_bit_cast(u32x4, kv[2]));
    }
  }
  TS(5);
}

__global__ __launch_bounds__(256) void ln0_k(const u16* __restrict__ x, u16* __restrict__ y, const u16* __restrict__ wqkv,
                                             const u16* __restrict__ w1, const u16* __restrict__ w3, float* __restrict__ colsum,
                                             const u16* __restrict__ wo, const u16* __restrict__ w2, const u16* __restrict__ w4) {
  if (blockIdx.x >= SEQ / 4 + 120) {
    const int t = blockIdx.x - (SEQ / 4 + 120);
    const u16* W = t < 32 ? wo + (size_t)t * 32768 : (t < 64 ? w2 + (size_t)(t - 32) * 32768 : w4 + (size_t)(t - 64) * 32768);
#pragma unroll
    for (int i = 0; i < 16; ++i) {
      const u32x4 v = *(const u32x4*)(W + (i * 256 + threadIdx.x) * 8);
      asm volatile("" ::"v"(v));
    }
    return;
  }
  if (blockIdx.x >= SEQ / 4) {
    __shared__ float red[32 * 64];
    const int tid = threadIdx.x;
    const int cg = blockIdx.x - SEQ / 4;
    const u16* W; int N, c0;
    if (cg < 48) { W = wqkv; N = 3 * DM; c0 = cg * 64; }
    else if (cg < 64) { W = w1; N = DM; c0 = (cg - 48) * 64; }
    else { W = w3; N = FF; c0 = (cg - 64) * 64; }
    const int c = tid & 7, rg = tid >> 3;
    f32x2 s[4] = {{0.f, 0.f}, {0.f, 0.f}, {0.f, 0.f}, {0.f, 0.f}};
#pragma unroll 16
    for (int i = 0; i < 32; ++i) {
      const u32x4 v = *(const u32x4*)(W + (size_t)(rg + i * 32) * N + c0 + c * 8);
#pragma unroll
      for (int e = 0; e < 4; ++e) s[e] += unpk(v[e]);
    }
#pragma unroll
    for (int e = 0; e < 4; ++e) *(f32x2*)(red + rg * 64 + c * 8 + 2 * e) = s[e];
    __syncthreads();
    if (tid < 64) {
      float t = 0.f;
#pragma unroll
      for (int i = 0; i < 32; ++i) t += red[i * 64 + tid];
      colsum[cg * 64 + tid] = t;
    }
    return;
  }
  const int lane = threadIdx.x & 63, row = blockIdx.x * 4 + (threadIdx.x >> 6);
  const u16* src = x + (size_t)row * DM;
  const u32x4 a = *(const u32x4*)(src + lane * 8), b = *(const u32x4*)(src + 512 + lane * 8);
  float v[16];
#pragma unroll
  for (int e = 0; e < 4; ++e) { v[2 * e] = bflo(a[e]); v[2 * e + 1] = bfhi(a[e]); v[8 + 2 * e] = bflo(b[e]); v[9 + 2 * e] = bfhi(b[e]); }
  float s = 0.f;
#pragma unroll
  for (int e = 0; e < 16; ++e) s += v[e];
  s = sum64(s);
  const float mean = s * (1.0f / 1024.0f);
  float m2 = 0.f;
#pragma unroll
  for (int e = 0; e < 16; ++e) { const float d = v[e] - mean; m2 += d * d; }
  m2 = sum64(m2);
  const RowP r = row_params(mean, m2 * (1.0f / 1024.0f), 1);
  u32x4 oa, ob;
#pragma unroll
  for (int e = 0; e < 4; ++e) {
    oa[e] = pk2(rbf(v[2 * e] - r.mhat) / r.dhat, rbf(v[2 * e + 1] - r.mhat) / r.dhat);
    ob[e] = pk2(rbf(v[8 + 2 * e] - r.mhat) / r.dhat, rbf(v[9 + 2 * e] - r.mhat) / r.dhat);
  }
  u16* dst = y + (size_t)row * DM;
  *(u32x4*)(dst + lane * 8) = oa;
  *(u32x4*)(dst + 512 + lane * 8) = ob;
}

__global__ __launch_bounds__(512) void attn_o_k(const u16* __restrict__ q, const u16* __restrict__ wphi, const float* __restrict__ kvpart,
                                                u16* __restrict__ o) {
  constexpr int KS = 80;
  __shared__ __attribute__((aligned(16))) char sm[33 * KS * 2 + 8];
  const int tid = threadIdx.x, lane = tid & 63, w = tid >> 6;
  constexpr int EPI = 6;
  TS(0);
  const int h = blockIdx.x & 31, rb = blockIdx.x >> 5;
  const int g4 = lane >> 4, r0 = rb * 256 + w * 32 + (lane & 15);
  bf16x8 qf[2], wf[2];
#pragma unroll
  for (int mb = 0; mb < 2; ++mb) qf[mb] = *(const bf16x8*)(q + (size_t)(r0 + mb * 16) * DM + h * 32 + g4 * 8);
#pragma unroll
  for (int fb = 0; fb < 2; ++fb)
#pragma unroll
    for (int j = 0; j < 8; ++j) wf[fb][j] = (short)wphi[h * 1024 + (g4 * 8 + j) * 32 + fb * 16 + (lane & 15)];
  __builtin_amdgcn_sched_barrier(0);
  f32x4 kp[16], kz[16];
  const float* slab = kvpart + (size_t)h * (33 * 64) + tid * 4;
#pragma unroll
  for (int p = 0; p < 16; ++p) kp[p] = *(const f32x4*)(slab + (size_t)p * NH * (33 * 64));
  const bool zw = __builtin_amdgcn_readfirstlane(w) == 0;
  if (zw) {
#pragma unroll
    for (int p = 0; p < 16; ++p) kz[p] = *(const f32x4*)(slab + (size_t)p * NH * (33 * 64) + 2048 - (tid & 48) * 4);
  }
  __builtin_amdgcn_sched_barrier(0);
  bf16x8 pq[2][2];
#pragma unroll
  for (int mb = 0; mb < 2; ++mb) {
    u32x4 pos, neg;
    feature_map(wf, qf[mb], pos, neg);
    pq[mb][0] = __builtin_bit_cast(bf16x8, pos);
    pq[mb][1] = __builtin_bit_cast(bf16x8, neg);
  }
  __builtin_amdgcn_sched_barrier(0);
  TS(1);
  u16* kvt = (u16*)sm;
  {
    f32x4 t = kp[0];
#pragma unroll
    for (int p = 1; p < 16; ++p) t += kp[p];
    *(u32x2*)(kvt + (tid >> 4) * KS + (tid & 15) * 4) = (u32x2){pk2(t[0], t[1]), pk2(t[2], t[3])};
    if (zw) {
      f32x4 z = kz[0];
#pragma unroll
      for (int p = 1; p < 16; ++p) z += kz[p];
      if (tid < 16) *(u32x2*)(kvt + 32 * KS + tid * 4) = (u32x2){pk2(z[0], z[1]), pk2(z[2], z[3])};
    }
  }
  TS(2);
  __syncthreads();
  TS(3);
  bf16x8 kf[3][2];
#pragma unroll
  for (int db = 0; db < 3; ++db)
#pragma unroll
    for (int ks = 0; ks < 2; ++ks) {
      const int d = (db < 2) ? ((lane >> 2) & 3) * 8 + db * 4 + (lane & 3) : 32;
      const u16* p = kvt + d * KS + ks * 32 + g4 * 4;
      kf[db][ks] = cat8(*(const s16x4*)p, *(const s16x4*)(p + 16));
    }
#pragma unroll
  for (int mb = 0; mb < 2; ++mb) {
    f32x4 a[3];
#pragma unroll
    for (int db = 0; db < 3; ++db) {
      a[db] = mfma16(kf[db][0], pq[mb][0], (f32x4){0.f, 0.f, 0.f, 0.f});
      a[db] = mfma16(kf[db][1], pq[mb][1], a[db]);
    }
    const float id = __builtin_amdgcn_rcpf(rbf(rbf(a[2][0]) + 1e-6f));
    u32x4 ov;
#pragma unroll
    for (int db = 0; db < 2; ++db) {
      const f32x2 lo = rbf2((f32x2){a[db][0], a[db][1]}) * id, hi = rbf2((f32x2){a[db][2], a[db][3]}) * id;
      ov[db * 2] = pk2(lo[0], lo[1]); ov[db * 2 + 1] = pk2(hi[0], hi[1]);
    }
    st16_wt(o, SEQ * DM * 2, ((r0 + mb * 16) * DM + h * 32 + g4 * 8) * 2, ov);
  }
  TS(4);
  TS(5);
}

template <int BN, int S, int KD, int EPI> static constexpr int smem_bytes() {
  constexpr bool USE_STATS = (EPI == EPI_QKV || EPI == EPI_GELU_SHIFT || EPI == EPI_W2 || EPI == EPI_GELU);
  constexpr int NRES = (EPI == EPI_RES || EPI == EPI_W2) ? 1 : (EPI == EPI_W4 ? 2 : 0);
  return S * (16384 + BN * 128) + NRES * 16384 + (EPI == EPI_QKV ? 4096 : 0) + 2048 + 1024;
}
template <int BN, int S, int KD, int EPI> static void launch_gemm(const GemmArgs& a, hipStream_t st) {
  constexpr int sb = smem_bytes<BN, S, KD, EPI>();
  static_assert(sb <= 163840, "LDS");
  (void)hipFuncSetAttribute((const void*)gemm_k<BN, S, KD, EPI>, hipFuncAttributeMaxDynamicSharedMemorySize, sb);
  gemm_k<BN, S, KD, EPI><<<256, 512, sb, st>>>(a);
}

extern "C" void kernel_launch(void* const* d_in, const int* in_sizes, int n_in, void* d_out, int out_size, void* d_ws,
                              size_t ws_size, hipStream_t stream) {
  const u16* x0 = (const u16*)d_in[0];
  const u16* wqkv = (const u16*)d_in[1];
  const u16* wphi = (const u16*)d_in[2];
  const u16* wo = (const u16*)d_in[3];
  const u16* w1 = (const u16*)d_in[5];
  const u16* w2 = (const u16*)d_in[6];
  const u16* w3 = (const u16*)d_in[7];
  const u16* w4 = (const u16*)d_in[8];
  char* ws = (char*)d_ws;
  const size_t MB = 1 << 20;
  u16* xln0 = (u16*)(ws + 0 * MB);
  u16* phiq = (u16*)(ws + 4 * MB);
  float* kvpart = (float*)(ws + 12 * MB);
  u16* obuf = (u16*)(ws + 17 * MB);
  u16* xa = (u16*)(ws + 21 * MB);
  u16* hsh = (u16*)(ws + 25 * MB);
  u16* x1 = (u16*)(ws + 29 * MB);
  u16* gbuf = (u16*)(ws + 33 * MB);
  u16* xb = (u16*)(ws + 48 * MB);
  float* st_xa = (float*)(ws + 52 * MB);
  float* st_x1 = (float*)(ws + 53 * MB);
  float* st_xb = (float*)(ws + 54 * MB);
  float* colsum = (float*)(ws + 55 * MB);

  ln0_k<<<SEQ / 4 + 120, 256, 0, stream>>>(x0, xln0, wqkv, w1, w3, colsum, wo, w2, w4);
  for (int layer = 0; layer < 2; ++layer) {
    const u16* xin = layer == 0 ? x0 : xb;
    u16* xout = layer == 0 ? xb : (u16*)d_out;
    GemmArgs a;
    a = GemmArgs{layer == 0 ? xln0 : xb, wqkv, phiq, layer == 0 ? wo : nullptr, w2, st_xb, (float*)w4, colsum, wphi, kvpart, layer == 0 ? 0 : 1, 0};
    launch_gemm<192, 3, 1024, EPI_QKV>(a, stream);
    attn_o_k<<<256, 512, 0, stream>>>(phiq, wphi, kvpart, obuf);
    a = GemmArgs{obuf, wo, xa, xin, nullptr, nullptr, st_xa, nullptr, nullptr, nullptr, 0, 0};
    launch_gemm<64, 5, 1024, EPI_RES>(a, stream);
    a = GemmArgs{xa, w1, hsh, nullptr, nullptr, st_xa, nullptr, colsum + 3072, nullptr, nullptr, 2, 0};
    launch_gemm<64, 5, 1024, EPI_GELU_SHIFT>(a, stream);
    a = GemmArgs{hsh, w2, x1, xa, nullptr, st_xa, st_x1, nullptr, nullptr, nullptr, 1, 0};
    launch_gemm<64, 5, 1024, EPI_W2>(a, stream);
    a = GemmArgs{x1, w3, gbuf, nullptr, nullptr, st_x1, nullptr, colsum + 4096, nullptr, nullptr, 1, 0};
    launch_gemm<224, 3, 1024, EPI_GELU>(a, stream);
    a = GemmArgs{gbuf, w4, xout, xa, x1, nullptr, st_xb, nullptr, nullptr, nullptr, 0, 0};
    launch_gemm<64, 5, 3584, EPI_W4>(a, stream);
  }
}
```

```cpp
#include <hip/hip_runtime.h>
#include <hip/hip_bf16.h>
#include <stdint.h>

typedef __attribute__((ext_vector_type(8))) short bf16x8;
typedef __attribute__((ext_vector_type(4))) short s16x4;
typedef __attribute__((ext_vector_type(4))) float f32x4;
typedef __attribute__((ext_vector_type(2))) float f32x2;
typedef __attribute__((ext_vector_type(4))) unsigned u32x4;
typedef __attribute__((ext_vector_type(2))) unsigned u32x2;
typedef __attribute__((ext_vector_type(2))) __bf16 bf16x2_t;
typedef unsigned short u16;
#define LDS_AS __attribute__((address_space(3)))
#define DI __device__ __forceinline__

static constexpr int SEQ = 2048, DM = 1024, NH = 32, FF = 3584;

DI float bf2f(unsigned v) { return __uint_as_float(v << 16); }
DI float bflo(unsigned v) { return __uint_as_float(v << 16); }
DI float bfhi(unsigned v) { return __uint_as_float(v & 0xffff0000u); }
DI unsigned pk2(float a, float b) {
  f32x2 v = {a, b};
  return __builtin_bit_cast(unsigned, __builtin_convertvector(v, bf16x2_t));
}
DI float rbf(float a) { return bflo(pk2(a, a)); }
template <int CTRL> DI float dppf(float v) { return __builtin_bit_cast(float, __builtin_amdgcn_update_dpp(0, __builtin_bit_cast(int, v), CTRL, 0xf, 0xf, true)); }
DI float sum8(float v) { v += dppf<0xB1>(v); v += dppf<0x4E>(v); return v + dppf<0x141>(v); }
DI f32x2 pair16(float v) { const unsigned u = __float_as_uint(v); const auto r = __builtin_amdgcn_permlane16_swap(u, u, false, false); return (f32x2){__uint_as_float(r[0]), __uint_as_float(r[1])}; }
DI f32x2 pair32(float v) { const unsigned u = __float_as_uint(v); const auto r = __builtin_amdgcn_permlane32_swap(u, u, false, false); return (f32x2){__uint_as_float(r[0]), __uint_as_float(r[1])}; }
DI float max4g(float v) { f32x2 t = pair16(v); t = pair32(fmaxf(t[0], t[1])); return fmaxf(t[0], t[1]); }
DI float min4g(float v) { f32x2 t = pair16(v); t = pair32(fminf(t[0], t[1])); return fminf(t[0], t[1]); }
DI float sum4g(float v) { f32x2 t = pair16(v); t = pair32(t[0] + t[1]); return t[0] + t[1]; }
DI float sum64(float v) { v = sum8(v); return sum4g(v + dppf<0x140>(v)); }

template <int I> struct IC { static constexpr int value = I; };
template <int B, int E, class F> DI void static_for(F&& f) {
  if constexpr (B < E) { f(IC<B>{}); static_for<B + 1, E>(f); }
}
template <int N> DI void wait_vm() { asm volatile("s_waitcnt vmcnt(%0)" ::"n"(N) : "memory"); }
DI void wait_lgkm0() { asm volatile("s_waitcnt lgkmcnt(0)" ::: "memory"); }
DI void bar() { __builtin_amdgcn_s_barrier(); }
DI void glds16(const void* src, char* lds_dst) {
  __builtin_amdgcn_global_load_lds((const unsigned*)src, (unsigned*)lds_dst, 16, 0, 0);
}
DI s16x4 trread(const char* p) {
  return __builtin_bit_cast(s16x4, __builtin_amdgcn_ds_read_tr16_b64_v4i16((LDS_AS s16x4*)p));
}
template <int OFF> DI s16x4 lds_tr(unsigned addr) {
  s16x4 r; asm volatile("ds_read_b64_tr_b16 %0, %1 offset:%2" : "=v"(r) : "v"(addr), "n"(OFF) : "memory"); return r;
}
template <int OFF> DI bf16x8 lds_b128(unsigned addr) {
  bf16x8 r; asm volatile("ds_read_b128 %0, %1 offset:%2" : "=v"(r) : "v"(addr), "n"(OFF) : "memory"); return r;
}
DI void st16_wt(void* base, unsigned bytes, unsigned off, u32x4 v) {
  __builtin_amdgcn_raw_buffer_store_b128(v, __builtin_amdgcn_make_buffer_rsrc(base, (short)0, (int)bytes, 0x00020000), (int)off, 0, 16);
}
DI bf16x8 cat8(s16x4 lo, s16x4 hi) { return (bf16x8){lo[0], lo[1], lo[2], lo[3], hi[0], hi[1], hi[2], hi[3]}; }
DI f32x4 mfma16(bf16x8 a, bf16x8 b, f32x4 c) { return __builtin_amdgcn_mfma_f32_16x16x32_bf16(a, b, c, 0, 0, 0); }

DI f32x2 gelu2(f32x2 x) {
  const float c0 = -2.302208198f, c1 = -2.302208198f * 0.044715f;
  const f32x2 a = x * (x * x * c1 + c0);
  const f32x2 d = (f32x2){__builtin_amdgcn_exp2f(a[0]), __builtin_amdgcn_exp2f(a[1])} + 1.0f;
  return x * (f32x2){__builtin_amdgcn_rcpf(d[0]), __builtin_amdgcn_rcpf(d[1])};
}
DI f32x2 rbf2(f32x2 v) { const unsigned p = pk2(v[0], v[1]); return (f32x2){bflo(p), bfhi(p)}; }
DI f32x2 unpk(unsigned p) { return (f32x2){bflo(p), bfhi(p)}; }

DI void feature_map(const bf16x8 (&wf)[2], bf16x8 xf, u32x4& pos, u32x4& neg) {
  f32x2 p[4];
#pragma unroll
  for (int fb = 0; fb < 2; ++fb) {
    const f32x4 d = mfma16(wf[fb], xf, (f32x4){0.f, 0.f, 0.f, 0.f});
    p[fb * 2] = rbf2((f32x2){d[0], d[1]});
    p[fb * 2 + 1] = rbf2((f32x2){d[2], d[3]});
  }
  float mx = fmaxf(p[0][0], p[0][1]), mn = fminf(p[0][0], p[0][1]);
#pragma unroll
  for (int e = 1; e < 4; ++e) { mx = fmaxf(mx, fmaxf(p[e][0], p[e][1])); mn = fminf(mn, fminf(p[e][0], p[e][1])); }
  mx = max4g(mx);
  mn = min4g(mn);
  const float l2e = 1.4426950408889634f;
  f32x2 ep[4], en[4], sp2 = {0.f, 0.f}, sn2 = {0.f, 0.f};
#pragma unroll
  for (int e = 0; e < 4; ++e) {
    const f32x2 a = (p[e] - mx) * l2e, b = (mn - p[e]) * l2e;
    ep[e] = (f32x2){__builtin_amdgcn_exp2f(a[0]), __builtin_amdgcn_exp2f(a[1])}; sp2 += ep[e];
    en[e] = (f32x2){__builtin_amdgcn_exp2f(b[0]), __builtin_amdgcn_exp2f(b[1])}; sn2 += en[e];
  }
  float sp = sp2[0] + sp2[1], sn = sn2[0] + sn2[1];
  sp = sum4g(sp);
  sn = sum4g(sn);
  const float ip = __builtin_amdgcn_rcpf(sp), in_ = __builtin_amdgcn_rcpf(sn);
#pragma unroll
  for (int e = 0; e < 4; ++e) {
    const f32x2 a = ep[e] * ip, b = en[e] * in_;
    pos[e] = pk2(a[0], a[1]); neg[e] = pk2(b[0], b[1]);
  }
}

#define TS(i)
struct GemmArgs {
  const u16* A;
  const u16* B;
  u16* out;
  const u16* res1;
  const u16* res2;
  const float* stats_in;
  float* stats_out;
  const float* colsum;
  const u16* wphi;
  float* kvpart;
  int ln_mode;
  int unused;
};

enum { EPI_QKV = 0, EPI_RES = 1, EPI_GELU_SHIFT = 2, EPI_W2 = 3, EPI_GELU = 4, EPI_W4 = 5 };

struct RowP { float alpha, beta, mhat, dhat; };
DI RowP row_params(float mean, float var, int mode) {
  RowP r;
  const float mh = rbf(mean);
  const float dh = rbf(__builtin_amdgcn_sqrtf(rbf(rbf(var) + 1e-5f)));
  const float i1 = __builtin_amdgcn_rcpf(dh);
  r.mhat = mh; r.dhat = dh;
  if (mode == 2) {
    const float m2 = (mean - mh) * i1;
    const float v2 = var * i1 * i1;
    const float mh2 = rbf(m2);
    const float dh2 = rbf(__builtin_amdgcn_sqrtf(rbf(rbf(v2) + 1e-5f)));
    const float i2 = __builtin_amdgcn_rcpf(dh2);
    r.alpha = i1 * i2;
    r.beta = (mh * i1 + mh2) * i2;
  } else {
    r.alpha = i1;
    r.beta = mh * i1;
  }
  return r;
}

template <int BN, int S, int KD, int EPI>
__global__ __launch_bounds__(512, 2) void gemm_k(GemmArgs g) {
  constexpr int NK = KD / 64;
  constexpr int NPF = BN / 64;
  constexpr bool HALF = (BN % 64) != 0;
  constexpr int NI = BN / 32;
  constexpr int A_BYTES = 16384;
  constexpr int STAGE = A_BYTES + BN * 128;
  constexpr int L = 4 + NPF * 2 + (HALF ? 1 : 0);
  constexpr int NTOT = (EPI == EPI_QKV) ? 3 * DM : (EPI == EPI_GELU ? FF : DM);
  constexpr bool USE_STATS = (EPI == EPI_QKV || EPI == EPI_GELU_SHIFT || EPI == EPI_W2 || EPI == EPI_GELU);
  constexpr bool FOLD = (EPI == EPI_QKV || EPI == EPI_GELU_SHIFT || EPI == EPI_GELU);
  constexpr int NRES = (EPI == EPI_RES || EPI == EPI_W2) ? 1 : (EPI == EPI_W4 ? 2 : 0);
  constexpr int OFF_STATS = S * STAGE;
  constexpr int OFF_RES = OFF_STATS;
  constexpr int OFF_WPHI = OFF_RES + NRES * 16384;
  constexpr int OFF_ROWP = OFF_WPHI + (EPI == EPI_QKV ? 4096 : 0);
  constexpr int OFF_CS = OFF_ROWP + 2048;
  constexpr int OFF_DUMP = OFF_CS + 1024;
  constexpr int NEXTRA = (USE_STATS && BN != 64 ? 8 : 0) + NRES * 4 + (EPI == EPI_QKV ? 1 : 0) + (FOLD ? 1 : 0);

  extern __shared__ __attribute__((aligned(16))) char smem[];
  const int tid = threadIdx.x, lane = tid & 63;
  const int w = __builtin_amdgcn_readfirstlane(tid >> 6);
  const bool prod = w >= 4;
  const int cw = w & 3, wr = cw >> 1, wc = cw & 1;

  const int b = blockIdx.x, xcd = b & 7, j = b >> 3;
  int rp, ct;
  if constexpr (EPI == EPI_QKV || EPI == EPI_GELU) { rp = (xcd >> 2) * 8 + (j >> 2); ct = (xcd & 3) * 4 + (j & 3); }
  else { rp = (xcd >> 1) * 4 + (j >> 3); ct = (xcd & 1) * 8 + (j & 7); }
  const int m0 = rp * 128;
  const int n0 = (EPI == EPI_QKV) ? ct * 64 : ct * BN;
  constexpr int PSTRIDE = (EPI == EPI_QKV) ? DM : 64;
  const int g4 = lane >> 4, q4 = (lane >> 2) & 3, pp = lane & 3;
  RowP* rowp = (RowP*)(smem + OFF_ROWP);
  const bool ln_on = USE_STATS && (g.ln_mode != 0 || EPI == EPI_W2);

  f32x4 acc[4][NI];
  TS(0);
  constexpr bool RP_CONS = (BN == 64);
  f32x2 sv[8];
  const int sr = cw * 32 + (lane >> 1), shf = lane & 1;
  auto load_stats = [&]() {
#pragma unroll
    for (int p = 0; p < 8; ++p)
      asm volatile("global_load_dwordx2 %0, %1, off" : "=v"(sv[p]) : "v"(g.stats_in + ((size_t)(shf * 8 + p) * SEQ + m0 + sr) * 2) : "memory");
  };
  float rs_s = 0.f, rs_m2 = 0.f;
  auto row_stats = [&](auto pc) {
    constexpr int piece = decltype(pc)::value;
    if constexpr (piece == 0) {
#pragma unroll
      for (int p = 0; p < 8; ++p) asm volatile("" : "+v"(sv[p]));
      float s = 0.f;
#pragma unroll
      for (int p = 0; p < 8; ++p) s += sv[p][0];
      rs_s = s + dppf<0xB1>(s);
    } else if constexpr (piece == 1) {
      const float mean = rs_s * (1.0f / 1024.0f);
      float m2 = 0.f;
#pragma unroll
      for (int p = 0; p < 8; ++p) { const float d = sv[p][0] * (1.0f / 64.0f) - mean; m2 += sv[p][1] + 64.0f * d * d; }
      rs_m2 = m2 + dppf<0xB1>(m2);
    } else {
      if (ln_on && shf == 0) rowp[sr] = row_params(rs_s * (1.0f / 1024.0f), rs_m2 * (1.0f / 1024.0f), EPI == EPI_W2 ? 1 : g.ln_mode);
      wait_lgkm0();
    }
    __builtin_amdgcn_sched_barrier(0);
  };
  if (__builtin_expect(prod, 1)) {
    const u16* a_src = g.A + (size_t)(m0 + cw * 32 + (lane >> 3)) * KD + (((lane & 7) ^ (lane >> 3)) * 8);
    const u16* b_src[2];
#pragma unroll
    for (int e = 0; e < 2; ++e) {
      const int r = lane >> 3;
      const int f = ((r >> 1) & 1) | (e << 1);
      const int c = ((((lane & 7) >> 1) ^ f) << 1) | (lane & 1);
      b_src[e] = g.B + (size_t)((cw * 2 + e) * 8 + r) * NTOT + n0 + c * 8;
    }
    const u16* bh_src = nullptr;
    if constexpr (HALF) {
      const int c = ((((lane >> 1) & 1) ^ ((lane >> 5) & 1)) << 1) | (lane & 1);
      bh_src = g.B + (size_t)(cw * 16 + (lane >> 2)) * NTOT + n0 + NPF * 64 + c * 8;
    }
    auto issue = [&](int kt, int slot) {
      char* base = smem + slot * STAGE;
#pragma unroll
      for (int p = 0; p < NPF; ++p)
#pragma unroll
        for (int e = 0; e < 2; ++e)
          glds16(b_src[e] + (size_t)kt * 64 * NTOT + p * PSTRIDE, base + A_BYTES + p * 8192 + (cw * 2 + e) * 1024);
      if constexpr (HALF) glds16(bh_src + (size_t)kt * 64 * NTOT, base + A_BYTES + NPF * 8192 + cw * 1024);
#pragma unroll
      for (int e = 0; e < 4; ++e) glds16(a_src + (size_t)e * 8 * KD + kt * 64, base + (cw * 4 + e) * 1024);
    };
    TS(6);
    issue(0, 0);
    issue(1, 1);
    wait_vm<L>();
    TS(1);
    bar();
    static_for<2, S>([&](auto ic) { issue(ic.value, ic.value); });
    TS(7);
    if constexpr (NRES >= 1) {
#pragma unroll
      for (int e = 0; e < 4; ++e) {
        const int r = (cw * 4 + e) * 8 + (lane >> 3);
        glds16(g.res1 + (size_t)(m0 + r) * DM + n0 + (lane & 7) * 8, smem + OFF_RES + (cw * 4 + e) * 1024);
      }
    }
    if constexpr (NRES >= 2) {
#pragma unroll
      for (int e = 0; e < 4; ++e) {
        const int r = (cw * 4 + e) * 8 + (lane >> 3);
        glds16(g.res2 + (size_t)(m0 + r) * DM + n0 + (lane & 7) * 8, smem + OFF_RES + 16384 + (cw * 4 + e) * 1024);
      }
    }
    if constexpr (EPI == EPI_QKV) glds16(g.wphi + (size_t)ct * 2048 + cw * 512 + lane * 8, smem + OFF_WPHI + cw * 1024);
    if constexpr (FOLD) {
      int col = cw * 64 + (lane & 15) * 4;
      if (col > BN - 4) col = BN - 4;
      const int gcol = (EPI == EPI_QKV) ? (col >> 6) * DM + n0 + (col & 63) : n0 + col;
      if (lane < 16) glds16(g.colsum + gcol, smem + OFF_CS + cw * 256);
    }
    if constexpr (USE_STATS && !RP_CONS) load_stats();
    static_for<0, NK - 1>([&](auto ic) {
      constexpr int kt = ic.value;
      constexpr int young = (S - 2 < NK - 2 - kt) ? S - 2 : NK - 2 - kt;
      constexpr int ex = (kt + 1 <= S - 1 && kt + 1 < NK - 1) ? NEXTRA : 0;
      wait_vm<young * L + ex>();
      bar();
      if constexpr (kt + S < NK) issue(kt + S, kt % S);
      if constexpr (USE_STATS && !RP_CONS && kt >= 4 && kt < 7) row_stats(IC<kt - 4>{});
    });
    TS(2);
    bar();
  } else {
    const int fsw = ((q4 >> 1) & 1) | ((g4 & 1) << 1);
    int a_off[2], b_off[2], bh_off = 0;
#pragma unroll
    for (int k = 0; k < 2; ++k) {
      a_off[k] = (wr * 64 + (lane & 15)) * 128 + (((k * 4 + g4) ^ (lane & 7)) * 16);
      b_off[k] = A_BYTES + (8 * g4 + q4) * 128 + (((2 * k + wc) ^ fsw) * 32) + pp * 8;
    }
    if constexpr (HALF) bh_off = A_BYTES + NPF * 8192 + (8 * g4 + q4) * 64 + ((wc ^ (g4 & 1)) * 32) + pp * 8;
#pragma unroll
    for (int m = 0; m < 4; ++m)
#pragma unroll
      for (int i = 0; i < NI; ++i) acc[m][i] = (f32x4){0.f, 0.f, 0.f, 0.f};
    bf16x8 xa[2][4];
    s16x4 wlo[2][NI], whi[2][NI];
    const unsigned lbase = (unsigned)(size_t)smem;

    auto ldfrag = [&](auto setc, auto slotc, auto kc) {
      constexpr int set = decltype(setc)::value, slot = decltype(slotc)::value, k32 = decltype(kc)::value;
      const unsigned sb = lbase + slot * STAGE;
      const unsigned ba0 = sb + b_off[0], ba1 = sb + b_off[1], bah = sb + bh_off, aa = sb + a_off[k32];
      static_for<0, NI>([&](auto ic) {
        constexpr int i = decltype(ic)::value;
        if constexpr (HALF && i == NI - 1) {
          wlo[set][i] = lds_tr<k32 * 2048>(bah);
          whi[set][i] = lds_tr<k32 * 2048 + 256>(bah);
        } else {
          wlo[set][i] = lds_tr<(i >> 1) * 8192 + k32 * 4096>((i & 1) ? ba1 : ba0);
          whi[set][i] = lds_tr<(i >> 1) * 8192 + k32 * 4096 + 512>((i & 1) ? ba1 : ba0);
        }
      });
      static_for<0, 4>([&](auto mc) { constexpr int m = decltype(mc)::value; xa[set][m] = lds_b128<m * 2048>(aa); });
    };
    auto wait_set = [&](auto setc) {
      constexpr int set = decltype(setc)::value;
      asm volatile("s_waitcnt lgkmcnt(0)" : "+v"(xa[set][0]), "+v"(xa[set][1]), "+v"(xa[set][2]), "+v"(xa[set][3]) :: "memory");
#pragma unroll
      for (int i = 0; i < NI; ++i) asm volatile("" : "+v"(wlo[set][i]), "+v"(whi[set][i]));
      __builtin_amdgcn_sched_barrier(0);
    };
    auto ld1 = [&](auto setc, auto slotc, auto kc, auto rc) {
      constexpr int set = decltype(setc)::value, slot = decltype(slotc)::value, k32 = decltype(kc)::value, r = decltype(rc)::value;
      const unsigned sb = lbase + slot * STAGE;
      if constexpr (r < 2 * NI) {
        constexpr int i = r >> 1, hi = r & 1;
        s16x4 v;
        if constexpr (HALF && i == NI - 1) v = lds_tr<k32 * 2048 + hi * 256>(sb + bh_off);
        else v = lds_tr<(i >> 1) * 8192 + k32 * 4096 + hi * 512>(sb + b_off[i & 1]);
        if constexpr (hi) whi[set][i] = v; else wlo[set][i] = v;
      } else {
        constexpr int m = r - 2 * NI;
        xa[set][m] = lds_b128<m * 2048>(sb + a_off[k32]);
      }
    };
    auto mma_ld = [&](auto setc, auto ldc, auto slotc, auto kc) {
      constexpr int set = decltype(setc)::value;
      constexpr bool ld = decltype(ldc)::value != 0;
      static_for<0, 4 * NI>([&](auto tc) {
        constexpr int t = decltype(tc)::value, i = t >> 2, m = t & 3;
        acc[m][i] = mfma16(cat8(wlo[set][i], whi[set][i]), xa[set][m], acc[m][i]);
        if constexpr (ld && t < 2 * NI + 4) ld1(IC<set ^ 1>{}, slotc, kc, tc);
        __builtin_amdgcn_sched_barrier(0);
      });
    };

    const char* tp = (const char*)g.A;
    unsigned tw = 0;
    bool ton = false;
    int tks = 0;
    if (cw == 0) {
      const int pl = lane >> 4;
      const int bl = (int)(((size_t)g.B >> 7) & 7);
      int off;
      if constexpr (BN == 64) { off = ((xcd & 1) * 8 + ((3 - bl) & 7)) * 128; ton = lane < 16; }
      else if constexpr (EPI == EPI_QKV) { const int c = (3 - bl - (xcd & 3) * 4) & 7; off = pl * 2048 + ((xcd & 3) * 4 + (c & 3)) * 128; ton = c < 4 && lane < 48; }
      else { const int l0 = (xcd & 3) * 14, c = (3 - bl - l0) & 7; off = (l0 + c + pl * 8) * 128; ton = lane < (c < 6 ? 32 : 16); }
      tks = (lane >> 1) & 7;
      if (ton) tp = (const char*)g.B + (size_t)(tks * 64 + j * 2 + (lane & 1)) * NTOT * 2 + off;
      tw = 8 * 64 * NTOT * 2;
    } else if (cw == 1) {
      tks = (3 - (int)(((size_t)g.A >> 7) & 7)) & 7;
      const int row = (BN == 64) ? (xcd >> 1) * 512 + j * 16 + (lane & 15) : (xcd >> 2) * 1024 + j * 32 + (lane & 31);
      ton = lane < (BN == 64 ? 16 : 32);
      tp = (const char*)g.A + (size_t)row * KD * 2 + tks * 128;
      tw = 1024;
    }
    auto touch = [&](auto wc_) {
      constexpr int wi = decltype(wc_)::value;
      if (cw < 2) {
        if (ton && (wi > 0 || tks >= S))
          __builtin_amdgcn_global_load_lds((const unsigned*)(tp + (size_t)(wi * tw)), (unsigned*)(smem + OFF_DUMP), 4, 0, 0);
      }
      __builtin_amdgcn_sched_barrier(0);
    };
    if constexpr (USE_STATS && RP_CONS) load_stats();
    touch(IC<0>{});
    if constexpr (BN == 64) touch(IC<1>{});
    bar();
    TS(1);
    ldfrag(IC<0>{}, IC<0>{}, IC<0>{});
    static_for<0, NK>([&](auto ic) {
      constexpr int kt = ic.value;
      constexpr int slot = kt % S;
      wait_set(IC<0>{});
      if constexpr (BN != 64 && kt == 0) touch(IC<1>{});
      if constexpr (kt % 8 == 4 && (kt + 12) / 8 < NK / 8) touch(IC<(kt + 12) / 8>{});
      if constexpr (USE_STATS && RP_CONS && kt >= 6 && kt < 9) { if constexpr (kt == 6) wait_vm<0>(); row_stats(IC<kt - 6>{}); }
      mma_ld(IC<0>{}, IC<1>{}, IC<slot>{}, IC<1>{});
      wait_set(IC<1>{});
      if constexpr (kt + 1 < NK) {
        bar();
        __builtin_amdgcn_sched_barrier(0);
        mma_ld(IC<1>{}, IC<1>{}, IC<(kt + 1) % S>{}, IC<0>{});
      } else {
        mma_ld(IC<1>{}, IC<0>{}, IC<0>{}, IC<0>{});
      }
    });
    TS(2);
    bar();
  }
  TS(3);

  constexpr int TSTR = BN * 2 + (EPI == EPI_QKV ? 32 : 16);
  char* T = smem;
  if (!prod) {
    float al[4] = {1.f, 1.f, 1.f, 1.f}, be[4] = {0.f, 0.f, 0.f, 0.f};
    if constexpr (FOLD) {
      if (ln_on) {
#pragma unroll
        for (int m = 0; m < 4; ++m) { const RowP r = rowp[wr * 64 + m * 16 + (lane & 15)]; al[m] = r.alpha; be[m] = r.beta; }
      }
    }
    f32x4 csv[NI];
    if constexpr (FOLD) {
#pragma unroll
      for (int i = 0; i < NI; ++i) csv[i] = *(const f32x4*)(smem + OFF_CS + ((2 * i + wc) * 16 + g4 * 4) * 4);
#pragma unroll
      for (int i = 0; i < NI; ++i) csv[i] = ln_on ? csv[i] : (f32x4){0.f, 0.f, 0.f, 0.f};
      __builtin_amdgcn_sched_barrier(0);
    }
#pragma unroll
    for (int i = 0; i < NI; ++i) {
      const int nb = 2 * i + wc;
      const int ncol = nb * 16 + g4 * 4;
      const f32x4 cs = csv[i];
#pragma unroll
      for (int m = 0; m < 4; ++m) {
        u32x2 pkd;
#pragma unroll
        for (int h = 0; h < 2; ++h) {
          f32x2 v = {acc[m][i][2 * h], acc[m][i][2 * h + 1]};
          if constexpr (FOLD) v = v * al[m] - (f32x2){cs[2 * h], cs[2 * h + 1]} * be[m];
          if constexpr (EPI == EPI_GELU_SHIFT || EPI == EPI_GELU) v = gelu2(rbf2(v));
          pkd[h] = pk2(v[0], v[1]);
        }
        *(u32x2*)(T + (wr * 64 + m * 16 + (lane & 15)) * TSTR + ncol * 2) = pkd;
      }
      if constexpr (EPI == EPI_GELU) {
        if ((i & 1) && i < NI - 1) { wait_lgkm0(); bar(); }
      }
    }
  } else if constexpr (EPI == EPI_QKV) {
    if (g.res1) {
      const int t = tid - 256;
      const u16* W4 = (const u16*)g.stats_out + (size_t)b * 14336 + t * 8;
      const u16* Wo = g.res1 + (size_t)b * 4096 + t * 8;
      const u16* W2 = g.res2 + (size_t)b * 4096 + t * 8;
      u32x4 v[11];
#pragma unroll
      for (int i = 0; i < 7; ++i) v[i] = *(const u32x4*)(W4 + i * 2048);
#pragma unroll
      for (int i = 0; i < 2; ++i) { v[7 + i] = *(const u32x4*)(Wo + i * 2048); v[9 + i] = *(const u32x4*)(W2 + i * 2048); }
#pragma unroll
      for (int i = 0; i < 11; ++i) asm volatile("" ::"v"(v[i]));
    }
  } else if constexpr (EPI == EPI_GELU) {
#pragma unroll
    for (int p = 0; p < NI / 2; ++p) {
      bar();
      asm volatile("" ::: "memory");
#pragma unroll
      for (int it = 0; it < 4; ++it) {
        const int r = it * 32 + ((tid - 256) >> 3), c = p * 8 + (tid & 7);
        st16_wt(g.out, SEQ * FF * 2, ((m0 + r) * FF + n0 + c * 8) * 2, *(const u32x4*)(T + r * TSTR + c * 16));
      }
    }
  }
  __syncthreads();
  TS(4);
  if constexpr (EPI == EPI_GELU) {
    const int r = tid >> 2, c = (NI / 2) * 8 + (tid & 3);
    st16_wt(g.out, SEQ * FF * 2, ((m0 + r) * FF + n0 + c * 8) * 2, *(const u32x4*)(T + r * TSTR + c * 16));
  } else if constexpr (EPI == EPI_GELU_SHIFT) {
    const int grp = n0 >> 8;
    const int sh = (grp == 0) ? 1 : (grp == 1) ? -1 : (grp == 2) ? 2 : -2;
#pragma unroll
    for (int it = 0; it < 2; ++it) {
      const int r = it * 64 + (tid >> 3), c = tid & 7;
      const u32x4 v = *(const u32x4*)(T + r * TSTR + c * 16);
      const int dr = m0 + r + sh;
      if (dr >= 0 && dr < SEQ) st16_wt(g.out, SEQ * DM * 2, (dr * DM + n0 + c * 8) * 2, v);
    }
    if (tid < 16) {
      const int zr = tid >> 3, c = tid & 7;
      const int az = sh > 0 ? sh : -sh;
      if (zr < az) {
        if (sh > 0 && rp == 0) *(u32x4*)(g.out + (size_t)zr * DM + n0 + c * 8) = (u32x4){0u, 0u, 0u, 0u};
        if (sh < 0 && rp == 15) *(u32x4*)(g.out + (size_t)(SEQ - 1 - zr) * DM + n0 + c * 8) = (u32x4){0u, 0u, 0u, 0u};
      }
    }
  } else if constexpr (EPI == EPI_RES || EPI == EPI_W2 || EPI == EPI_W4) {
#pragma unroll
    for (int it = 0; it < 2; ++it) {
      const int r = it * 64 + (tid >> 3), c = tid & 7;
      const u32x4 v = *(const u32x4*)(T + r * TSTR + c * 16);
      const u32x4 x = *(const u32x4*)(smem + OFF_RES + r * 128 + c * 16);
      f32x2 y[4];
      if constexpr (EPI == EPI_RES) {
#pragma unroll
        for (int e = 0; e < 4; ++e) y[e] = unpk(x[e]) + unpk(v[e]);
      } else if constexpr (EPI == EPI_W2) {
        const RowP rr = rowp[r];
#pragma unroll
        for (int e = 0; e < 4; ++e) y[e] = rbf2(rbf2(unpk(x[e]) - rr.mhat) * rr.alpha) + unpk(v[e]);
      } else {
        const u32x4 x1 = *(const u32x4*)(smem + OFF_RES + 16384 + r * 128 + c * 16);
#pragma unroll
        for (int e = 0; e < 4; ++e) y[e] = unpk(x[e]) + rbf2(unpk(x1[e]) + unpk(v[e]));
      }
      u32x4 o;
#pragma unroll
      for (int e = 0; e < 4; ++e) { o[e] = pk2(y[e][0], y[e][1]); y[e] = unpk(o[e]); }
      st16_wt(g.out, SEQ * DM * 2, ((m0 + r) * DM + n0 + c * 8) * 2, o);
      const f32x2 s2 = (y[0] + y[1]) + (y[2] + y[3]);
      const float s = sum8(s2[0] + s2[1]);
      const float mc = s * (1.0f / 64.0f);
      f32x2 q2 = {0.f, 0.f};
#pragma unroll
      for (int e = 0; e < 4; ++e) { const f32x2 d = y[e] - mc; q2 += d * d; }
      const float m2 = sum8(q2[0] + q2[1]);
      if (c == 0) *(f32x2*)(g.stats_out + ((size_t)ct * SEQ + m0 + r) * 2) = (f32x2){s, m2};
    }
  } else {
    constexpr int PKSTR = 160;
    char* PK = smem + 128 * TSTR;
#pragma unroll
    for (int it = 0; it < 2; ++it) {
      const int r = it * 64 + (tid >> 3), c = tid & 7;
      st16_wt(g.out, SEQ * DM * 2, ((m0 + r) * DM + n0 + c * 8) * 2, *(const u32x4*)(T + r * TSTR + c * 16));
    }
    {
      const int hh = w & 1, rq = w >> 1;
      bf16x8 wf[2];
#pragma unroll
      for (int fb = 0; fb < 2; ++fb) {
        const char* p = smem + OFF_WPHI + hh * 2048 + (8 * g4 + q4) * 64 + fb * 32 + pp * 8;
        wf[fb] = cat8(trread(p), trread(p + 256));
      }
#pragma unroll
      for (int mb = 0; mb < 2; ++mb) {
        const int row = rq * 32 + mb * 16 + (lane & 15);
        const bf16x8 xf = *(const bf16x8*)(T + row * TSTR + (64 + hh * 32 + g4 * 8) * 2);
        u32x4 pos, neg;
        feature_map(wf, xf, pos, neg);
        char* dst = PK + (hh * 128 + row) * PKSTR + g4 * 8;
        *(u32x2*)dst = (u32x2){pos[0], pos[1]}; *(u32x2*)(dst + 32) = (u32x2){pos[2], pos[3]};
        *(u32x2*)(dst + 64) = (u32x2){neg[0], neg[1]}; *(u32x2*)(dst + 96) = (u32x2){neg[2], neg[3]};
      }
    }
    __syncthreads();
    {
      const int hh = w >> 2, fa = w & 3;
      f32x4 kv[3];
#pragma unroll
      for (int d = 0; d < 3; ++d) kv[d] = (f32x4){0.f, 0.f, 0.f, 0.f};
      const short one = (short)0x3F80;
      const bf16x8 ones = {one, one, one, one, one, one, one, one};
#pragma unroll
      for (int ks = 0; ks < 4; ++ks) {
        const int row = ks * 32 + 8 * g4 + q4;
        bf16x8 vf[2];
#pragma unroll
        for (int d = 0; d < 2; ++d) {
          const char* p = T + row * TSTR + (128 + hh * 32 + d * 16) * 2 + pp * 8;
          vf[d] = cat8(trread(p), trread(p + 4 * TSTR));
        }
        const char* p = PK + (hh * 128 + row) * PKSTR + (fa * 16) * 2 + pp * 8;
        const bf16x8 pf = cat8(trread(p), trread(p + 4 * PKSTR));
        kv[0] = mfma16(pf, vf[0], kv[0]);
        kv[1] = mfma16(pf, vf[1], kv[1]);
        kv[2] = mfma16(pf, ones, kv[2]);
      }
      constexpr unsigned KVB = 16 * NH * 33 * 64 * 4;
      const int so = (rp * NH + ct * 2 + hh) * (33 * 64) + fa * 16 + g4 * 4;
      st16_wt(g.kvpart, KVB, (so + (lane & 15) * 64) * 4, __builtin_bit_cast(u32x4, kv[0]));
      st16_wt(g.kvpart, KVB, (so + (16 + (lane & 15)) * 64) * 4, __builtin_bit_cast(u32x4, kv[1]));
      if ((lane & 15) == 0) st16_wt(g.kvpart, KVB, (so + 32 * 64) * 4, __builtin_bit_cast(u32x4, kv[2]));
    }
  }
  TS(5);
}

__global__ __launch_bounds__(256) void ln0_k(const u16* __restrict__ x, u16* __restrict__ y, const u16* __restrict__ wqkv,
                                             const u16* __restrict__ w1, const u16* __restrict__ w3, float* __restrict__ colsum,
                                             const u16* __restrict__ wo, const u16* __restrict__ w2, const u16* __restrict__ w4) {
  if (blockIdx.x >= SEQ / 4 + 120) {
    const int t = blockIdx.x - (SEQ / 4 + 120);
    const u16* W = t < 32 ? wo + (size_t)t * 32768 : (t < 64 ? w2 + (size_t)(t - 32) * 32768 : w4 + (size_t)(t - 64) * 32768);
#pragma unroll
    for (int i = 0; i < 16; ++i) {
      const u32x4 v = *(const u32x4*)(W + (i * 256 + threadIdx.x) * 8);
      asm volatile("" ::"v"(v));
    }
    return;
  }
  if (blockIdx.x >= SEQ / 4) {
    __shared__ float red[32 * 64];
    const int tid = threadIdx.x;
    const int cg = blockIdx.x - SEQ / 4;
    const u16* W; int N, c0;
    if (cg < 48) { W = wqkv; N = 3 * DM; c0 = cg * 64; }
    else if (cg < 64) { W = w1; N = DM; c0 = (cg - 48) * 64; }
    else { W = w3; N = FF; c0 = (cg - 64) * 64; }
    const int c = tid & 7, rg = tid >> 3;
    f32x2 s[4] = {{0.f, 0.f}, {0.f, 0.f}, {0.f, 0.f}, {0.f, 0.f}};
#pragma unroll 16
    for (int i = 0; i < 32; ++i) {
      const u32x4 v = *(const u32x4*)(W + (size_t)(rg + i * 32) * N + c0 + c * 8);
#pragma unroll
      for (int e = 0; e < 4; ++e) s[e] += unpk(v[e]);
    }
#pragma unroll
    for (int e = 0; e < 4; ++e) *(f32x2*)(red + rg * 64 + c * 8 + 2 * e) = s[e];
    __syncthreads();
    if (tid < 64) {
      float t = 0.f;
#pragma unroll
      for (int i = 0; i < 32; ++i) t += red[i * 64 + tid];
      colsum[cg * 64 + tid] = t;
    }
    return;
  }
  const int lane = threadIdx.x & 63, row = blockIdx.x * 4 + (threadIdx.x >> 6);
  const u16* src = x + (size_t)row * DM;
  const u32x4 a = *(const u32x4*)(src + lane * 8), b = *(const u32x4*)(src + 512 + lane * 8);
  float v[16];
#pragma unroll
  for (int e = 0; e < 4; ++e) { v[2 * e] = bflo(a[e]); v[2 * e + 1] = bfhi(a[e]); v[8 + 2 * e] = bflo(b[e]); v[9 + 2 * e] = bfhi(b[e]); }
  float s = 0.f;
#pragma unroll
  for (int e = 0; e < 16; ++e) s += v[e];
  s = sum64(s);
  const float mean = s * (1.0f / 1024.0f);
  float m2 = 0.f;
#pragma unroll
  for (int e = 0; e < 16; ++e) { const float d = v[e] - mean; m2 += d * d; }
  m2 = sum64(m2);
  const RowP r = row_params(mean, m2 * (1.0f / 1024.0f), 1);
  u32x4 oa, ob;
#pragma unroll
  for (int e = 0; e < 4; ++e) {
    oa[e] = pk2(rbf(v[2 * e] - r.mhat) / r.dhat, rbf(v[2 * e + 1] - r.mhat) / r.dhat);
    ob[e] = pk2(rbf(v[8 + 2 * e] - r.mhat) / r.dhat, rbf(v[9 + 2 * e] - r.mhat) / r.dhat);
  }
  u16* dst = y + (size_t)row * DM;
  *(u32x4*)(dst + lane * 8) = oa;
  *(u32x4*)(dst + 512 + lane * 8) = ob;
}

__global__ __launch_bounds__(512) void attn_o_k(const u16* __restrict__ q, const u16* __restrict__ wphi, const float* __restrict__ kvpart,
                                                u16* __restrict__ o) {
  constexpr int KS = 80;
  __shared__ __attribute__((aligned(16))) char sm[33 * KS * 2 + 8];
  const int tid = threadIdx.x, lane = tid & 63, w = tid >> 6;
  constexpr int EPI = 6;
  TS(0);
  const int h = blockIdx.x & 31, rb = blockIdx.x >> 5;
  const int g4 = lane >> 4, r0 = rb * 256 + w * 32 + (lane & 15);
  bf16x8 qf[2], wf[2];
#pragma unroll
  for (int mb = 0; mb < 2; ++mb) qf[mb] = *(const bf16x8*)(q + (size_t)(r0 + mb * 16) * DM + h * 32 + g4 * 8);
#pragma unroll
  for (int fb = 0; fb < 2; ++fb)
#pragma unroll
    for (int j = 0; j < 8; ++j) wf[fb][j] = (short)wphi[h * 1024 + (g4 * 8 + j) * 32 + fb * 16 + (lane & 15)];
  __builtin_amdgcn_sched_barrier(0);
  f32x4 kp[16], kz[16];
  const float* slab = kvpart + (size_t)h * (33 * 64) + tid * 4;
#pragma unroll
  for (int p = 0; p < 16; ++p) kp[p] = *(const f32x4*)(slab + (size_t)p * NH * (33 * 64));
  const bool zw = __builtin_amdgcn_readfirstlane(w) == 0;
  if (zw) {
#pragma unroll
    for (int p = 0; p < 16; ++p) kz[p] = *(const f32x4*)(slab + (size_t)p * NH * (33 * 64) + 2048 - (tid & 48) * 4);
  }
  __builtin_amdgcn_sched_barrier(0);
  bf16x8 pq[2][2];
#pragma unroll
  for (int mb = 0; mb < 2; ++mb) {
    u32x4 pos, neg;
    feature_map(wf, qf[mb], pos, neg);
    pq[mb][0] = __builtin_bit_cast(bf16x8, pos);
    pq[mb][1] = __builtin_bit_cast(bf16x8, neg);
  }
  __builtin_amdgcn_sched_barrier(0);
  TS(1);
  u16* kvt = (u16*)sm;
  {
    f32x4 t = kp[0];
#pragma unroll
    for (int p = 1; p < 16; ++p) t += kp[p];
    *(u32x2*)(kvt + (tid >> 4) * KS + (tid & 15) * 4) = (u32x2){pk2(t[0], t[1]), pk2(t[2], t[3])};
    if (zw) {
      f32x4 z = kz[0];
#pragma unroll
      for (int p = 1; p < 16; ++p) z += kz[p];
      if (tid < 16) *(u32x2*)(kvt + 32 * KS + tid * 4) = (u32x2){pk2(z[0], z[1]), pk2(z[2], z[3])};
    }
  }
  TS(2);
  __syncthreads();
  TS(3);
  bf16x8 kf[3][2];
#pragma unroll
  for (int db = 0; db < 3; ++db)
#pragma unroll
    for (int ks = 0; ks < 2; ++ks) {
      const int d = (db < 2) ? ((lane >> 2) & 3) * 8 + db * 4 + (lane & 3) : 32;
      const u16* p = kvt + d * KS + ks * 32 + g4 * 4;
      kf[db][ks] = cat8(*(const s16x4*)p, *(const s16x4*)(p + 16));
    }
#pragma unroll
  for (int mb = 0; mb < 2; ++mb) {
    f32x4 a[3];
#pragma unroll
    for (int db = 0; db < 3; ++db) {
      a[db] = mfma16(kf[db][0], pq[mb][0], (f32x4){0.f, 0.f, 0.f, 0.f});
      a[db] = mfma16(kf[db][1], pq[mb][1], a[db]);
    }
    const float id = __builtin_amdgcn_rcpf(rbf(rbf(a[2][0]) + 1e-6f));
    u32x4 ov;
#pragma unroll
    for (int db = 0; db < 2; ++db) {
      const f32x2 lo = rbf2((f32x2){a[db][0], a[db][1]}) * id, hi = rbf2((f32x2){a[db][2], a[db][3]}) * id;
      ov[db * 2] = pk2(lo[0], lo[1]); ov[db * 2 + 1] = pk2(hi[0], hi[1]);
    }
    st16_wt(o, SEQ * DM * 2, ((r0 + mb * 16) * DM + h * 32 + g4 * 8) * 2, ov);
  }
  TS(4);
  TS(5);
}

template <int BN, int S, int KD, int EPI> static constexpr int smem_bytes() {
  constexpr bool USE_STATS = (EPI == EPI_QKV || EPI == EPI_GELU_SHIFT || EPI == EPI_W2 || EPI == EPI_GELU);
  constexpr int NRES = (EPI == EPI_RES || EPI == EPI_W2) ? 1 : (EPI == EPI_W4 ? 2 : 0);
  return S * (16384 + BN * 128) + NRES * 16384 + (EPI == EPI_QKV ? 4096 : 0) + 2048 + 1024 + 256;
}
template <int BN, int S, int KD, int EPI> static void launch_gemm(const GemmArgs& a, hipStream_t st) {
  constexpr int sb = smem_bytes<BN, S, KD, EPI>();
  static_assert(sb <= 163840, "LDS");
  (void)hipFuncSetAttribute((const void*)gemm_k<BN, S, KD, EPI>, hipFuncAttributeMaxDynamicSharedMemorySize, sb);
  gemm_k<BN, S, KD, EPI><<<256, 512, sb, st>>>(a);
}

extern "C" void kernel_launch(void* const* d_in, const int* in_sizes, int n_in, void* d_out, int out_size, void* d_ws,
                              size_t ws_size, hipStream_t stream) {
  const u16* x0 = (const u16*)d_in[0];
  const u16* wqkv = (const u16*)d_in[1];
  const u16* wphi = (const u16*)d_in[2];
  const u16* wo = (const u16*)d_in[3];
  const u16* w1 = (const u16*)d_in[5];
  const u16* w2 = (const u16*)d_in[6];
  const u16* w3 = (const u16*)d_in[7];
  const u16* w4 = (const u16*)d_in[8];
  char* ws = (char*)d_ws;
  const size_t MB = 1 << 20;
  u16* xln0 = (u16*)(ws + 0 * MB);
  u16* phiq = (u16*)(ws + 4 * MB);
  float* kvpart = (float*)(ws + 12 * MB);
  u16* obuf = (u16*)(ws + 17 * MB);
  u16* xa = (u16*)(ws + 21 * MB);
  u16* hsh = (u16*)(ws + 25 * MB);
  u16* x1 = (u16*)(ws + 29 * MB);
  u16* gbuf = (u16*)(ws + 33 * MB);
  u16* xb = (u16*)(ws + 48 * MB);
  float* st_xa = (float*)(ws + 52 * MB);
  float* st_x1 = (float*)(ws + 53 * MB);
  float* st_xb = (float*)(ws + 54 * MB);
  float* colsum = (float*)(ws + 55 * MB);

  ln0_k<<<SEQ / 4 + 120, 256, 0, stream>>>(x0, xln0, wqkv, w1, w3, colsum, wo, w2, w4);
  for (int layer = 0; layer < 2; ++layer) {
    const u16* xin = layer == 0 ? x0 : xb;
    u16* xout = layer == 0 ? xb : (u16*)d_out;
    GemmArgs a;
    a = GemmArgs{layer == 0 ? xln0 : xb, wqkv, phiq, layer == 0 ? wo : nullptr, w2, st_xb, (float*)w4, colsum, wphi, kvpart, layer == 0 ? 0 : 1, 0};
    launch_gemm<192, 3, 1024, EPI_QKV>(a, stream);
    attn_o_k<<<256, 512, 0, stream>>>(phiq, wphi, kvpart, obuf);
    a = GemmArgs{obuf, wo, xa, xin, nullptr, nullptr, st_xa, nullptr, nullptr, nullptr, 0, 0};
    launch_gemm<64, 5, 1024, EPI_RES>(a, stream);
    a = GemmArgs{xa, w1, hsh, nullptr, nullptr, st_xa, nullptr, colsum + 3072, nullptr, nullptr, 2, 0};
    launch_gemm<64, 5, 1024, EPI_GELU_SHIFT>(a, stream);
    a = GemmArgs{hsh, w2, x1, xa, nullptr, st_xa, st_x1, nullptr, nullptr, nullptr, 1, 0};
    launch_gemm<64, 5, 1024, EPI_W2>(a, stream);
    a = GemmArgs{x1, w3, gbuf, nullptr, nullptr, st_x1, nullptr, colsum + 4096, nullptr, nullptr, 1, 0};
    launch_gemm<224, 3, 1024, EPI_GELU>(a, stream);
    a = GemmArgs{gbuf, w4, xout, xa, x1, nullptr, st_xb, nullptr, nullptr, nullptr, 0, 0};
    launch_gemm<64, 5, 3584, EPI_W4>(a, stream);
  }
}
```

```cpp
#include <hip/hip_runtime.h>
#include <hip/hip_bf16.h>
#include <stdint.h>

typedef __attribute__((ext_vector_type(8))) short bf16x8;
typedef __attribute__((ext_vector_type(4))) short s16x4;
typedef __attribute__((ext_vector_type(4))) float f32x4;
typedef __attribute__((ext_vector_type(2))) float f32x2;
typedef __attribute__((ext_vector_type(4))) unsigned u32x4;
typedef __attribute__((ext_vector_type(2))) unsigned u32x2;
typedef __attribute__((ext_vector_type(2))) __bf16 bf16x2_t;
typedef unsigned short u16;
#define LDS_AS __attribute__((address_space(3)))
#define DI __device__ __forceinline__

static constexpr int SEQ = 2048, DM = 1024, NH = 32, FF = 3584;

DI float bf2f(unsigned v) { return __uint_as_float(v << 16); }
DI float bflo(unsigned v) { return __uint_as_float(v << 16); }
DI float bfhi(unsigned v) { return __uint_as_float(v & 0xffff0000u); }
DI unsigned pk2(float a, float b) {
  f32x2 v = {a, b};
  return __builtin_bit_cast(unsigned, __builtin_convertvector(v, bf16x2_t));
}
DI float rbf(float a) { return bflo(pk2(a, a)); }
template <int CTRL> DI float dppf(float v) { return __builtin_bit_cast(float, __builtin_amdgcn_update_dpp(0, __builtin_bit_cast(int, v), CTRL, 0xf, 0xf, true)); }
DI float sum8(float v) { v += dppf<0xB1>(v); v += dppf<0x4E>(v); return v + dppf<0x141>(v); }
DI f32x2 pair16(float v) { const unsigned u = __float_as_uint(v); const auto r = __builtin_amdgcn_permlane16_swap(u, u, false, false); return (f32x2){__uint_as_float(r[0]), __uint_as_float(r[1])}; }
DI f32x2 pair32(float v) { const unsigned u = __float_as_uint(v); const auto r = __builtin_amdgcn_permlane32_swap(u, u, false, false); return (f32x2){__uint_as_float(r[0]), __uint_as_float(r[1])}; }
DI float max4g(float v) { f32x2 t = pair16(v); t = pair32(fmaxf(t[0], t[1])); return fmaxf(t[0], t[1]); }
DI float min4g(float v) { f32x2 t = pair16(v); t = pair32(fminf(t[0], t[1])); return fminf(t[0], t[1]); }
DI float sum4g(float v) { f32x2 t = pair16(v); t = pair32(t[0] + t[1]); return t[0] + t[1]; }
DI float sum64(float v) { v = sum8(v); return sum4g(v + dppf<0x140>(v)); }

template <int I> struct IC { static constexpr int value = I; };
template <int B, int E, class F> DI void static_for(F&& f) {
  if constexpr (B < E) { f(IC<B>{}); static_for<B + 1, E>(f); }
}
template <int N> DI void wait_vm() { asm volatile("s_waitcnt vmcnt(%0)" ::"n"(N) : "memory"); }
DI void wait_lgkm0() { asm volatile("s_waitcnt lgkmcnt(0)" ::: "memory"); }
DI void bar() { __builtin_amdgcn_s_barrier(); }
DI void glds16(const void* src, char* lds_dst) {
  __builtin_amdgcn_global_load_lds((const unsigned*)src, (unsigned*)lds_dst, 16, 0, 0);
}
DI s16x4 trread(const char* p) {
  return __builtin_bit_cast(s16x4, __builtin_amdgcn_ds_read_tr16_b64_v4i16((LDS_AS s16x4*)p));
}
template <int OFF> DI s16x4 lds_tr(unsigned addr) {
  s16x4 r; asm volatile("ds_read_b64_tr_b16 %0, %1 offset:%2" : "=v"(r) : "v"(addr), "n"(OFF) : "memory"); return r;
}
template <int OFF> DI bf16x8 lds_b128(unsigned addr) {
  bf16x8 r; asm volatile("ds_read_b128 %0, %1 offset:%2" : "=v"(r) : "v"(addr), "n"(OFF) : "memory"); return r;
}
DI void st16_wt(void* base, unsigned bytes, unsigned off, u32x4 v) {
  __builtin_amdgcn_raw_buffer_store_b128(v, __builtin_amdgcn_make_buffer_rsrc(base, (short)0, (int)bytes, 0x00020000), (int)off, 0, 16);
}
DI bf16x8 cat8(s16x4 lo, s16x4 hi) { return (bf16x8){lo[0], lo[1], lo[2], lo[3], hi[0], hi[1], hi[2], hi[3]}; }
DI f32x4 mfma16(bf16x8 a, bf16x8 b, f32x4 c) { return __builtin_amdgcn_mfma_f32_16x16x32_bf16(a, b, c, 0, 0, 0); }

DI f32x2 gelu2(f32x2 x) {
  const float c0 = -2.302208198f, c1 = -2.302208198f * 0.044715f;
  const f32x2 a = x * (x * x * c1 + c0);
  const f32x2 d = (f32x2){__builtin_amdgcn_exp2f(a[0]), __builtin_amdgcn_exp2f(a[1])} + 1.0f;
  return x * (f32x2){__builtin_amdgcn_rcpf(d[0]), __builtin_amdgcn_rcpf(d[1])};
}
DI f32x2 rbf2(f32x2 v) { const unsigned p = pk2(v[0], v[1]); return (f32x2){bflo(p), bfhi(p)}; }
DI f32x2 unpk(unsigned p) { return (f32x2){bflo(p), bfhi(p)}; }

DI void feature_map(const bf16x8 (&wf)[2], bf16x8 xf, u32x4& pos, u32x4& neg) {
  f32x2 p[4];
#pragma unroll
  for (int fb = 0; fb < 2; ++fb) {
    const f32x4 d = mfma16(wf[fb], xf, (f32x4){0.f, 0.f, 0.f, 0.f});
    p[fb * 2] = rbf2((f32x2){d[0], d[1]});
    p[fb * 2 + 1] = rbf2((f32x2){d[2], d[3]});
  }
  float mx = fmaxf(p[0][0], p[0][1]), mn = fminf(p[0][0], p[0][1]);
#pragma unroll
  for (int e = 1; e < 4; ++e) { mx = fmaxf(mx, fmaxf(p[e][0], p[e][1])); mn = fminf(mn, fminf(p[e][0], p[e][1])); }
  mx = max4g(mx);
  mn = min4g(mn);
  const float l2e = 1.4426950408889634f;
  f32x2 ep[4], en[4], sp2 = {0.f, 0.f}, sn2 = {0.f, 0.f};
#pragma unroll
  for (int e = 0; e < 4; ++e) {
    const f32x2 a = (p[e] - mx) * l2e, b = (mn - p[e]) * l2e;
    ep[e] = (f32x2){__builtin_amdgcn_exp2f(a[0]), __builtin_amdgcn_exp2f(a[1])}; sp2 += ep[e];
    en[e] = (f32x2){__builtin_amdgcn_exp2f(b[0]), __builtin_amdgcn_exp2f(b[1])}; sn2 += en[e];
  }
  float sp = sp2[0] + sp2[1], sn = sn2[0] + sn2[1];
  sp = sum4g(sp);
  sn = sum4g(sn);
  const float ip = __builtin_amdgcn_rcpf(sp), in_ = __builtin_amdgcn_rcpf(sn);
#pragma unroll
  for (int e = 0; e < 4; ++e) {
    const f32x2 a = ep[e] * ip, b = en[e] * in_;
    pos[e] = pk2(a[0], a[1]); neg[e] = pk2(b[0], b[1]);
  }
}

#define TS(i)
struct GemmArgs {
  const u16* A;
  const u16* B;
  u16* out;
  const u16* res1;
  const u16* res2;
  const float* stats_in;
  float* stats_out;
  const float* colsum;
  const u16* wphi;
  float* kvpart;
  int ln_mode;
  int unused;
};

enum { EPI_QKV = 0, EPI_RES = 1, EPI_GELU_SHIFT = 2, EPI_W2 = 3, EPI_GELU = 4, EPI_W4 = 5 };

struct RowP { float alpha, beta, mhat, dhat; };
DI RowP row_params(float mean, float var, int mode) {
  RowP r;
  const float mh = rbf(mean);
  const float dh = rbf(__builtin_amdgcn_sqrtf(rbf(rbf(var) + 1e-5f)));
  const float i1 = __builtin_amdgcn_rcpf(dh);
  r.mhat = mh; r.dhat = dh;
  if (mode == 2) {
    const float m2 = (mean - mh) * i1;
    const float v2 = var * i1 * i1;
    const float mh2 = rbf(m2);
    const float dh2 = rbf(__builtin_amdgcn_sqrtf(rbf(rbf(v2) + 1e-5f)));
    const float i2 = __builtin_amdgcn_rcpf(dh2);
    r.alpha = i1 * i2;
    r.beta = (mh * i1 + mh2) * i2;
  } else {
    r.alpha = i1;
    r.beta = mh * i1;
  }
  return r;
}

template <int BN, int S, int KD, int EPI>
__global__ __launch_bounds__(512, 2) void gemm_k(GemmArgs g) {
  constexpr int NK = KD / 64;
  constexpr int NPF = BN / 64;
  constexpr bool HALF = (BN % 64) != 0;
  constexpr int NI = BN / 32;
  constexpr int A_BYTES = 16384;
  constexpr int STAGE = A_BYTES + BN * 128;
  constexpr int L = 4 + NPF * 2 + (HALF ? 1 : 0);
  constexpr int NTOT = (EPI == EPI_QKV) ? 3 * DM : (EPI == EPI_GELU ? FF : DM);
  constexpr bool USE_STATS = (EPI == EPI_QKV || EPI == EPI_GELU_SHIFT || EPI == EPI_W2 || EPI == EPI_GELU);
  constexpr bool FOLD = (EPI == EPI_QKV || EPI == EPI_GELU_SHIFT || EPI == EPI_GELU);
  constexpr int NRES = (EPI == EPI_RES || EPI == EPI_W2) ? 1 : (EPI == EPI_W4 ? 2 : 0);
  constexpr int OFF_STATS = S * STAGE;
  constexpr int OFF_RES = OFF_STATS;
  constexpr int OFF_WPHI = OFF_RES + NRES * 16384;
  constexpr int OFF_ROWP = OFF_WPHI + (EPI == EPI_QKV ? 4096 : 0);
  constexpr int OFF_CS = OFF_ROWP + 2048;
  constexpr int OFF_DUMP = OFF_CS + 1024;
  constexpr int NEXTRA = (USE_STATS && BN != 64 ? 8 : 0) + NRES * 4 + (EPI == EPI_QKV ? 1 : 0) + (FOLD ? 1 : 0);

  extern __shared__ __attribute__((aligned(16))) char smem[];
  const int tid = threadIdx.x, lane = tid & 63;
  const int w = __builtin_amdgcn_readfirstlane(tid >> 6);
  const bool prod = w >= 4;
  const int cw = w & 3, wr = cw >> 1, wc = cw & 1;

  const int b = blockIdx.x, xcd = b & 7, j = b >> 3;
  int rp, ct;
  if constexpr (EPI == EPI_QKV || EPI == EPI_GELU) { rp = (xcd >> 2) * 8 + (j >> 2); ct = (xcd & 3) * 4 + (j & 3); }
  else { rp = (xcd >> 1) * 4 + (j >> 3); ct = (xcd & 1) * 8 + (j & 7); }
  const int m0 = rp * 128;
  const int n0 = (EPI == EPI_QKV) ? ct * 64 : ct * BN;
  constexpr int PSTRIDE = (EPI == EPI_QKV) ? DM : 64;
  const int nf = HALF ? n0 + (ct & 1) * 32 : n0;
  const int nh = (ct & 1) ? n0 : n0 + NPF * 64;
  const int g4 = lane >> 4, q4 = (lane >> 2) & 3, pp = lane & 3;
  RowP* rowp = (RowP*)(smem + OFF_ROWP);
  const bool ln_on = USE_STATS && (g.ln_mode != 0 || EPI == EPI_W2);

  f32x4 acc[4][NI];
  TS(0);
  constexpr bool RP_CONS = (BN == 64);
  f32x2 sv[8];
  const int sr = cw * 32 + (lane >> 1), shf = lane & 1;
  auto load_stats = [&]() {
#pragma unroll
    for (int p = 0; p < 8; ++p)
      asm volatile("global_load_dwordx2 %0, %1, off" : "=v"(sv[p]) : "v"(g.stats_in + ((size_t)(shf * 8 + p) * SEQ + m0 + sr) * 2) : "memory");
  };
  float rs_s = 0.f, rs_m2 = 0.f;
  auto row_stats = [&](auto pc) {
    constexpr int piece = decltype(pc)::value;
    if constexpr (piece == 0) {
#pragma unroll
      for (int p = 0; p < 8; ++p) asm volatile("" : "+v"(sv[p]));
      float s = 0.f;
#pragma unroll
      for (int p = 0; p < 8; ++p) s += sv[p][0];
      rs_s = s + dppf<0xB1>(s);
    } else if constexpr (piece == 1) {
      const float mean = rs_s * (1.0f / 1024.0f);
      float m2 = 0.f;
#pragma unroll
      for (int p = 0; p < 8; ++p) { const float d = sv[p][0] * (1.0f / 64.0f) - mean; m2 += sv[p][1] + 64.0f * d * d; }
      rs_m2 = m2 + dppf<0xB1>(m2);
    } else {
      if (ln_on && shf == 0) rowp[sr] = row_params(rs_s * (1.0f / 1024.0f), rs_m2 * (1.0f / 1024.0f), EPI == EPI_W2 ? 1 : g.ln_mode);
      wait_lgkm0();
    }
    __builtin_amdgcn_sched_barrier(0);
  };
  if (__builtin_expect(prod, 1)) {
    const u16* a_src = g.A + (size_t)(m0 + cw * 32 + (lane >> 3)) * KD + (((lane & 7) ^ (lane >> 3)) * 8);
    const u16* b_src[2];
#pragma unroll
    for (int e = 0; e < 2; ++e) {
      const int r = lane >> 3;
      const int f = ((r >> 1) & 1) | (e << 1);
      const int c = ((((lane & 7) >> 1) ^ f) << 1) | (lane & 1);
      b_src[e] = g.B + (size_t)((cw * 2 + e) * 8 + r) * NTOT + nf + c * 8;
    }
    const u16* bh_src = nullptr;
    if constexpr (HALF) {
      const int c = ((((lane >> 1) & 1) ^ ((lane >> 5) & 1)) << 1) | (lane & 1);
      bh_src = g.B + (size_t)(cw * 16 + (lane >> 2)) * NTOT + nh + c * 8;
    }
    auto issue = [&](int kt, int slot) {
      char* base = smem + slot * STAGE;
#pragma unroll
      for (int p = 0; p < NPF; ++p)
#pragma unroll
        for (int e = 0; e < 2; ++e)
          glds16(b_src[e] + (size_t)kt * 64 * NTOT + p * PSTRIDE, base + A_BYTES + p * 8192 + (cw * 2 + e) * 1024);
      if constexpr (HALF) glds16(bh_src + (size_t)kt * 64 * NTOT, base + A_BYTES + NPF * 8192 + cw * 1024);
#pragma unroll
      for (int e = 0; e < 4; ++e) glds16(a_src + (size_t)e * 8 * KD + kt * 64, base + (cw * 4 + e) * 1024);
    };
    TS(6);
    issue(0, 0);
    issue(1, 1);
    wait_vm<L>();
    TS(1);
    bar();
    static_for<2, S>([&](auto ic) { issue(ic.value, ic.value); });
    TS(7);
    if constexpr (NRES >= 1) {
#pragma unroll
      for (int e = 0; e < 4; ++e) {
        const int r = (cw * 4 + e) * 8 + (lane >> 3);
        glds16(g.res1 + (size_t)(m0 + r) * DM + n0 + (lane & 7) * 8, smem + OFF_RES + (cw * 4 + e) * 1024);
      }
    }
    if constexpr (NRES >= 2) {
#pragma unroll
      for (int e = 0; e < 4; ++e) {
        const int r = (cw * 4 + e) * 8 + (lane >> 3);
        glds16(g.res2 + (size_t)(m0 + r) * DM + n0 + (lane & 7) * 8, smem + OFF_RES + 16384 + (cw * 4 + e) * 1024);
      }
    }
    if constexpr (EPI == EPI_QKV) glds16(g.wphi + (size_t)ct * 2048 + cw * 512 + lane * 8, smem + OFF_WPHI + cw * 1024);
    if constexpr (FOLD) {
      int col = cw * 64 + (lane & 15) * 4;
      if (col > BN - 4) col = BN - 4;
      const int gcol = (EPI == EPI_QKV) ? (col >> 6) * DM + n0 + (col & 63) : (HALF && col >= NPF * 64) ? nh + col - NPF * 64 : nf + col;
      if (lane < 16) glds16(g.colsum + gcol, smem + OFF_CS + cw * 256);
    }
    if constexpr (USE_STATS && !RP_CONS) load_stats();
    static_for<0, NK - 1>([&](auto ic) {
      constexpr int kt = ic.value;
      constexpr int young = (S - 2 < NK - 2 - kt) ? S - 2 : NK - 2 - kt;
      constexpr int ex = (kt + 1 <= S - 1 && kt + 1 < NK - 1) ? NEXTRA : 0;
      wait_vm<young * L + ex>();
      bar();
      if constexpr (kt + S < NK) issue(kt + S, kt % S);
      if constexpr (USE_STATS && !RP_CONS && kt >= 4 && kt < 7) row_stats(IC<kt - 4>{});
    });
    TS(2);
    bar();
  } else {
    const int fsw = ((q4 >> 1) & 1) | ((g4 & 1) << 1);
    int a_off[2], b_off[2], bh_off = 0;
#pragma unroll
    for (int k = 0; k < 2; ++k) {
      a_off[k] = (wr * 64 + (lane & 15)) * 128 + (((k * 4 + g4) ^ (lane & 7)) * 16);
      b_off[k] = A_BYTES + (8 * g4 + q4) * 128 + (((2 * k + wc) ^ fsw) * 32) + pp * 8;
    }
    if constexpr (HALF) bh_off = A_BYTES + NPF * 8192 + (8 * g4 + q4) * 64 + ((wc ^ (g4 & 1)) * 32) + pp * 8;
#pragma unroll
    for (int m = 0; m < 4; ++m)
#pragma unroll
      for (int i = 0; i < NI; ++i) acc[m][i] = (f32x4){0.f, 0.f, 0.f, 0.f};
    bf16x8 xa[2][4];
    s16x4 wlo[2][NI], whi[2][NI];
    const unsigned lbase = (unsigned)(size_t)smem;

    auto ldfrag = [&](auto setc, auto slotc, auto kc) {
      constexpr int set = decltype(setc)::value, slot = decltype(slotc)::value, k32 = decltype(kc)::value;
      const unsigned sb = lbase + slot * STAGE;
      const unsigned ba0 = sb + b_off[0], ba1 = sb + b_off[1], bah = sb + bh_off, aa = sb + a_off[k32];
      static_for<0, NI>([&](auto ic) {
        constexpr int i = decltype(ic)::value;
        if constexpr (HALF && i == NI - 1) {
          wlo[set][i] = lds_tr<k32 * 2048>(bah);
          whi[set][i] = lds_tr<k32 * 2048 + 256>(bah);
        } else {
          wlo[set][i] = lds_tr<(i >> 1) * 8192 + k32 * 4096>((i & 1) ? ba1 : ba0);
          whi[set][i] = lds_tr<(i >> 1) * 8192 + k32 * 4096 + 512>((i & 1) ? ba1 : ba0);
        }
      });
      static_for<0, 4>([&](auto mc) { constexpr int m = decltype(mc)::value; xa[set][m] = lds_b128<m * 2048>(aa); });
    };
    auto wait_set = [&](auto setc) {
      constexpr int set = decltype(setc)::value;
      asm volatile("s_waitcnt lgkmcnt(0)" : "+v"(xa[set][0]), "+v"(xa[set][1]), "+v"(xa[set][2]), "+v"(xa[set][3]) :: "memory");
#pragma unroll
      for (int i = 0; i < NI; ++i) asm volatile("" : "+v"(wlo[set][i]), "+v"(whi[set][i]));
      __builtin_amdgcn_sched_barrier(0);
    };
    auto ld1 = [&](auto setc, auto slotc, auto kc, auto rc) {
      constexpr int set = decltype(setc)::value, slot = decltype(slotc)::value, k32 = decltype(kc)::value, r = decltype(rc)::value;
      const unsigned sb = lbase + slot * STAGE;
      if constexpr (r < 2 * NI) {
        constexpr int i = r >> 1, hi = r & 1;
        s16x4 v;
        if constexpr (HALF && i == NI - 1) v = lds_tr<k32 * 2048 + hi * 256>(sb + bh_off);
        else v = lds_tr<(i >> 1) * 8192 + k32 * 4096 + hi * 512>(sb + b_off[i & 1]);
        if constexpr (hi) whi[set][i] = v; else wlo[set][i] = v;
      } else {
        constexpr int m = r - 2 * NI;
        xa[set][m] = lds_b128<m * 2048>(sb + a_off[k32]);
      }
    };
    auto mma_ld = [&](auto setc, auto ldc, auto slotc, auto kc) {
      constexpr int set = decltype(setc)::value;
      constexpr bool ld = decltype(ldc)::value != 0;
      static_for<0, 4 * NI>([&](auto tc) {
        constexpr int t = decltype(tc)::value, i = t >> 2, m = t & 3;
        acc[m][i] = mfma16(cat8(wlo[set][i], whi[set][i]), xa[set][m], acc[m][i]);
        if constexpr (ld && t < 2 * NI + 4) ld1(IC<set ^ 1>{}, slotc, kc, tc);
        __builtin_amdgcn_sched_barrier(0);
      });
    };

    const char* tp = (const char*)g.A;
    unsigned tw = 0;
    bool ton = false;
    int tks = 0;
    if (cw == 0) {
      const int pl = lane >> 4;
      const int bl = (int)(((size_t)g.B >> 7) & 7);
      int off;
      if constexpr (BN == 64) { off = ((xcd & 1) * 8 + ((3 - bl) & 7)) * 128; ton = lane < 16; }
      else if constexpr (EPI == EPI_QKV) { const int c = (3 - bl - (xcd & 3) * 4) & 7; off = pl * 2048 + ((xcd & 3) * 4 + (c & 3)) * 128; ton = c < 4 && lane < 48; }
      else { const int l0 = (xcd & 3) * 14, c = (3 - bl - l0) & 7; off = (l0 + c + pl * 8) * 128; ton = lane < (c < 6 ? 32 : 16); }
      tks = (lane >> 1) & 7;
      if (ton) tp = (const char*)g.B + (size_t)(tks * 64 + j * 2 + (lane & 1)) * NTOT * 2 + off;
      tw = 8 * 64 * NTOT * 2;
    } else if (cw == 1) {
      tks = (3 - (int)(((size_t)g.A >> 7) & 7)) & 7;
      const int row = (BN == 64) ? (xcd >> 1) * 512 + j * 16 + (lane & 15) : (xcd >> 2) * 1024 + j * 32 + (lane & 31);
      ton = lane < (BN == 64 ? 16 : 32);
      tp = (const char*)g.A + (size_t)row * KD * 2 + tks * 128;
      tw = 1024;
    }
    auto touch = [&](auto wc_) {
      constexpr int wi = decltype(wc_)::value;
      if (cw < 2) {
        if (ton && (wi > 0 || tks >= S))
          __builtin_amdgcn_global_load_lds((const unsigned*)(tp + (size_t)(wi * tw)), (unsigned*)(smem + OFF_DUMP), 4, 0, 0);
      }
      __builtin_amdgcn_sched_barrier(0);
    };
    if constexpr (USE_STATS && RP_CONS) load_stats();
    touch(IC<0>{});
    if constexpr (BN == 64) touch(IC<1>{});
    bar();
    TS(1);
    ldfrag(IC<0>{}, IC<0>{}, IC<0>{});
    static_for<0, NK>([&](auto ic) {
      constexpr int kt = ic.value;
      constexpr int slot = kt % S;
      wait_set(IC<0>{});
      if constexpr (BN != 64 && kt == 0) touch(IC<1>{});
      if constexpr (kt % 8 == 4 && (kt + 12) / 8 < NK / 8) touch(IC<(kt + 12) / 8>{});
      if constexpr (USE_STATS && RP_CONS && kt >= 6 && kt < 9) { if constexpr (kt == 6) wait_vm<0>(); row_stats(IC<kt - 6>{}); }
      mma_ld(IC<0>{}, IC<1>{}, IC<slot>{}, IC<1>{});
      wait_set(IC<1>{});
      if constexpr (kt + 1 < NK) {
        bar();
        __builtin_amdgcn_sched_barrier(0);
        mma_ld(IC<1>{}, IC<1>{}, IC<(kt + 1) % S>{}, IC<0>{});
      } else {
        mma_ld(IC<1>{}, IC<0>{}, IC<0>{}, IC<0>{});
      }
    });
    TS(2);
    bar();
  }
  TS(3);

  constexpr int TSTR = BN * 2 + (EPI == EPI_QKV ? 32 : 16);
  char* T = smem;
  if (!prod) {
    float al[4] = {1.f, 1.f, 1.f, 1.f}, be[4] = {0.f, 0.f, 0.f, 0.f};
    if constexpr (FOLD) {
      if (ln_on) {
#pragma unroll
        for (int m = 0; m < 4; ++m) { const RowP r = rowp[wr * 64 + m * 16 + (lane & 15)]; al[m] = r.alpha; be[m] = r.beta; }
      }
    }
    f32x4 csv[NI];
    if constexpr (FOLD) {
#pragma unroll
      for (int i = 0; i < NI; ++i) csv[i] = *(const f32x4*)(smem + OFF_CS + ((2 * i + wc) * 16 + g4 * 4) * 4);
#pragma unroll
      for (int i = 0; i < NI; ++i) csv[i] = ln_on ? csv[i] : (f32x4){0.f, 0.f, 0.f, 0.f};
      __builtin_amdgcn_sched_barrier(0);
    }
#pragma unroll
    for (int i = 0; i < NI; ++i) {
      const int nb = 2 * i + wc;
      const int ncol = nb * 16 + g4 * 4;
      const f32x4 cs = csv[i];
#pragma unroll
      for (int m = 0; m < 4; ++m) {
        u32x2 pkd;
#pragma unroll
        for (int h = 0; h < 2; ++h) {
          f32x2 v = {acc[m][i][2 * h], acc[m][i][2 * h + 1]};
          if constexpr (FOLD) v = v * al[m] - (f32x2){cs[2 * h], cs[2 * h + 1]} * be[m];
          if constexpr (EPI == EPI_GELU_SHIFT || EPI == EPI_GELU) v = gelu2(rbf2(v));
          pkd[h] = pk2(v[0], v[1]);
        }
        *(u32x2*)(T + (wr * 64 + m * 16 + (lane & 15)) * TSTR + ncol * 2) = pkd;
      }
      if constexpr (EPI == EPI_GELU) {
        if ((i & 1) && i < NI - 1) { wait_lgkm0(); bar(); }
      }
    }
  } else if constexpr (EPI == EPI_QKV) {
    if (g.res1) {
      const int t = tid - 256;
      const u16* W4 = (const u16*)g.stats_out + (size_t)b * 14336 + t * 8;
      const u16* Wo = g.res1 + (size_t)b * 4096 + t * 8;
      const u16* W2 = g.res2 + (size_t)b * 4096 + t * 8;
      u32x4 v[11];
#pragma unroll
      for (int i = 0; i < 7; ++i) v[i] = *(const u32x4*)(W4 + i * 2048);
#pragma unroll
      for (int i = 0; i < 2; ++i) { v[7 + i] = *(const u32x4*)(Wo + i * 2048); v[9 + i] = *(const u32x4*)(W2 + i * 2048); }
#pragma unroll
      for (int i = 0; i < 11; ++i) asm volatile("" ::"v"(v[i]));
    }
  } else if constexpr (EPI == EPI_GELU) {
#pragma unroll
    for (int p = 0; p < NI / 2; ++p) {
      bar();
      asm volatile("" ::: "memory");
#pragma unroll
      for (int it = 0; it < 4; ++it) {
        const int r = it * 32 + ((tid - 256) >> 3), c = p * 8 + (tid & 7);
        st16_wt(g.out, SEQ * FF * 2, ((m0 + r) * FF + nf + c * 8) * 2, *(const u32x4*)(T + r * TSTR + c * 16));
      }
    }
  }
  __syncthreads();
  TS(4);
  if constexpr (EPI == EPI_GELU) {
    const int r = tid >> 2, c = (NI / 2) * 8 + (tid & 3);
    st16_wt(g.out, SEQ * FF * 2, ((m0 + r) * FF + nh + (tid & 3) * 8) * 2, *(const u32x4*)(T + r * TSTR + c * 16));
  } else if constexpr (EPI == EPI_GELU_SHIFT) {
    const int grp = n0 >> 8;
    const int sh = (grp == 0) ? 1 : (grp == 1) ? -1 : (grp == 2) ? 2 : -2;
#pragma unroll
    for (int it = 0; it < 2; ++it) {
      const int r = it * 64 + (tid >> 3), c = tid & 7;
      const u32x4 v = *(const u32x4*)(T + r * TSTR + c * 16);
      const int dr = m0 + r + sh;
      if (dr >= 0 && dr < SEQ) st16_wt(g.out, SEQ * DM * 2, (dr * DM + n0 + c * 8) * 2, v);
    }
    if (tid < 16) {
      const int zr = tid >> 3, c = tid & 7;
      const int az = sh > 0 ? sh : -sh;
      if (zr < az) {
        if (sh > 0 && rp == 0) *(u32x4*)(g.out + (size_t)zr * DM + n0 + c * 8) = (u32x4){0u, 0u, 0u, 0u};
        if (sh < 0 && rp == 15) *(u32x4*)(g.out + (size_t)(SEQ - 1 - zr) * DM + n0 + c * 8) = (u32x4){0u, 0u, 0u, 0u};
      }
    }
  } else if constexpr (EPI == EPI_RES || EPI == EPI_W2 || EPI == EPI_W4) {
#pragma unroll
    for (int it = 0; it < 2; ++it) {
      const int r = it * 64 + (tid >> 3), c = tid & 7;
      const u32x4 v = *(const u32x4*)(T + r * TSTR + c * 16);
      const u32x4 x = *(const u32x4*)(smem + OFF_RES + r * 128 + c * 16);
      f32x2 y[4];
      if constexpr (EPI == EPI_RES) {
#pragma unroll
        for (int e = 0; e < 4; ++e) y[e] = unpk(x[e]) + unpk(v[e]);
      } else if constexpr (EPI == EPI_W2) {
        const RowP rr = rowp[r];
#pragma unroll
        for (int e = 0; e < 4; ++e) y[e] = rbf2(rbf2(unpk(x[e]) - rr.mhat) * rr.alpha) + unpk(v[e]);
      } else {
        const u32x4 x1 = *(const u32x4*)(smem + OFF_RES + 16384 + r * 128 + c * 16);
#pragma unroll
        for (int e = 0; e < 4; ++e) y[e] = unpk(x[e]) + rbf2(unpk(x1[e]) + unpk(v[e]));
      }
      u32x4 o;
#pragma unroll
      for (int e = 0; e < 4; ++e) { o[e] = pk2(y[e][0], y[e][1]); y[e] = unpk(o[e]); }
      st16_wt(g.out, SEQ * DM * 2, ((m0 + r) * DM + n0 + c * 8) * 2, o);
      const f32x2 s2 = (y[0] + y[1]) + (y[2] + y[3]);
      const float s = sum8(s2[0] + s2[1]);
      const float mc = s * (1.0f / 64.0f);
      f32x2 q2 = {0.f, 0.f};
#pragma unroll
      for (int e = 0; e < 4; ++e) { const f32x2 d = y[e] - mc; q2 += d * d; }
      const float m2 = sum8(q2[0] + q2[1]);
      if (c == 0) *(f32x2*)(g.stats_out + ((size_t)ct * SEQ + m0 + r) * 2) = (f32x2){s, m2};
    }
  } else {
    constexpr int PKSTR = 160;
    char* PK = smem + 128 * TSTR;
#pragma unroll
    for (int it = 0; it < 2; ++it) {
      const int r = it * 64 + (tid >> 3), c = tid & 7;
      st16_wt(g.out, SEQ * DM * 2, ((m0 + r) * DM + n0 + c * 8) * 2, *(const u32x4*)(T + r * TSTR + c * 16));
    }
    {
      const int hh = w & 1, rq = w >> 1;
      bf16x8 wf[2];
#pragma unroll
      for (int fb = 0; fb < 2; ++fb) {
        const char* p = smem + OFF_WPHI + hh * 2048 + (8 * g4 + q4) * 64 + fb * 32 + pp * 8;
        wf[fb] = cat8(trread(p), trread(p + 256));
      }
#pragma unroll
      for (int mb = 0; mb < 2; ++mb) {
        const int row = rq * 32 + mb * 16 + (lane & 15);
        const bf16x8 xf = *(const bf16x8*)(T + row * TSTR + (64 + hh * 32 + g4 * 8) * 2);
        u32x4 pos, neg;
        feature_map(wf, xf, pos, neg);
        char* dst = PK + (hh * 128 + row) * PKSTR + g4 * 8;
        *(u32x2*)dst = (u32x2){pos[0], pos[1]}; *(u32x2*)(dst + 32) = (u32x2){pos[2], pos[3]};
        *(u32x2*)(dst + 64) = (u32x2){neg[0], neg[1]}; *(u32x2*)(dst + 96) = (u32x2){neg[2], neg[3]};
      }
    }
    __syncthreads();
    {
      const int hh = w >> 2, fa = w & 3;
      f32x4 kv[3];
#pragma unroll
      for (int d = 0; d < 3; ++d) kv[d] = (f32x4){0.f, 0.f, 0.f, 0.f};
      const short one = (short)0x3F80;
      const bf16x8 ones = {one, one, one, one, one, one, one, one};
#pragma unroll
      for (int ks = 0; ks < 4; ++ks) {
        const int row = ks * 32 + 8 * g4 + q4;
        bf16x8 vf[2];
#pragma unroll
        for (int d = 0; d < 2; ++d) {
          const char* p = T + row * TSTR + (128 + hh * 32 + d * 16) * 2 + pp * 8;
          vf[d] = cat8(trread(p), trread(p + 4 * TSTR));
        }
        const char* p = PK + (hh * 128 + row) * PKSTR + (fa * 16) * 2 + pp * 8;
        const bf16x8 pf = cat8(trread(p), trread(p + 4 * PKSTR));
        kv[0] = mfma16(pf, vf[0], kv[0]);
        kv[1] = mfma16(pf, vf[1], kv[1]);
        kv[2] = mfma16(pf, ones, kv[2]);
      }
      constexpr unsigned KVB = 16 * NH * 33 * 64 * 4;
      const int so = (rp * NH + ct * 2 + hh) * (33 * 64) + fa * 16 + g4 * 4;
      st16_wt(g.kvpart, KVB, (so + (lane & 15) * 64) * 4, __builtin_bit_cast(u32x4, kv[0]));
      st16_wt(g.kvpart, KVB, (so + (16 + (lane & 15)) * 64) * 4, __builtin_bit_cast(u32x4, kv[1]));
      if ((lane & 15) == 0) st16_wt(g.kvpart, KVB, (so + 32 * 64) * 4, __builtin_bit_cast(u32x4, kv[2]));
    }
  }
  TS(5);
}

__global__ __launch_bounds__(256) void ln0_k(const u16* __restrict__ x, u16* __restrict__ y, const u16* __restrict__ wqkv,
                                             const u16* __restrict__ w1, const u16* __restrict__ w3, float* __restrict__ colsum,
                                             const u16* __restrict__ wo, const u16* __restrict__ w2, const u16* __restrict__ w4) {
  if (blockIdx.x >= SEQ / 4 + 120) {
    const int t = blockIdx.x - (SEQ / 4 + 120);
    const u16* W = t < 32 ? wo + (size_t)t * 32768 : (t < 64 ? w2 + (size_t)(t - 32) * 32768 : w4 + (size_t)(t - 64) * 32768);
#pragma unroll
    for (int i = 0; i < 16; ++i) {
      const u32x4 v = *(const u32x4*)(W + (i * 256 + threadIdx.x) * 8);
      asm volatile("" ::"v"(v));
    }
    return;
  }
  if (blockIdx.x >= SEQ / 4) {
    __shared__ float red[32 * 64];
    const int tid = threadIdx.x;
    const int cg = blockIdx.x - SEQ / 4;
    const u16* W; int N, c0;
    if (cg < 48) { W = wqkv; N = 3 * DM; c0 = cg * 64; }
    else if (cg < 64) { W = w1; N = DM; c0 = (cg - 48) * 64; }
    else { W = w3; N = FF; c0 = (cg - 64) * 64; }
    const int c = tid & 7, rg = tid >> 3;
    f32x2 s[4] = {{0.f, 0.f}, {0.f, 0.f}, {0.f, 0.f}, {0.f, 0.f}};
#pragma unroll 16
    for (int i = 0; i < 32; ++i) {
      const u32x4 v = *(const u32x4*)(W + (size_t)(rg + i * 32) * N + c0 + c * 8);
#pragma unroll
      for (int e = 0; e < 4; ++e) s[e] += unpk(v[e]);
    }
#pragma unroll
    for (int e = 0; e < 4; ++e) *(f32x2*)(red + rg * 64 + c * 8 + 2 * e) = s[e];
    __syncthreads();
    if (tid < 64) {
      float t = 0.f;
#pragma unroll
      for (int i = 0; i < 32; ++i) t += red[i * 64 + tid];
      colsum[cg * 64 + tid] = t;
    }
    return;
  }
  const int lane = threadIdx.x & 63, row = blockIdx.x * 4 + (threadIdx.x >> 6);
  const u16* src = x + (size_t)row * DM;
  const u32x4 a = *(const u32x4*)(src + lane * 8), b = *(const u32x4*)(src + 512 + lane * 8);
  float v[16];
#pragma unroll
  for (int e = 0; e < 4; ++e) { v[2 * e] = bflo(a[e]); v[2 * e + 1] = bfhi(a[e]); v[8 + 2 * e] = bflo(b[e]); v[9 + 2 * e] = bfhi(b[e]); }
  float s = 0.f;
#pragma unroll
  for (int e = 0; e < 16; ++e) s += v[e];
  s = sum64(s);
  const float mean = s * (1.0f / 1024.0f);
  float m2 = 0.f;
#pragma unroll
  for (int e = 0; e < 16; ++e) { const float d = v[e] - mean; m2 += d * d; }
  m2 = sum64(m2);
  const RowP r = row_params(mean, m2 * (1.0f / 1024.0f), 1);
  u32x4 oa, ob;
#pragma unroll
  for (int e = 0; e < 4; ++e) {
    oa[e] = pk2(rbf(v[2 * e] - r.mhat) / r.dhat, rbf(v[2 * e + 1] - r.mhat) / r.dhat);
    ob[e] = pk2(rbf(v[8 + 2 * e] - r.mhat) / r.dhat, rbf(v[9 + 2 * e] - r.mhat) / r.dhat);
  }
  u16* dst = y + (size_t)row * DM;
  *(u32x4*)(dst + lane * 8) = oa;
  *(u32x4*)(dst + 512 + lane * 8) = ob;
}

__global__ __launch_bounds__(512) void attn_o_k(const u16* __restrict__ q, const u16* __restrict__ wphi, const float* __restrict__ kvpart,
                                                u16* __restrict__ o) {
  constexpr int KS = 80;
  __shared__ __attribute__((aligned(16))) char sm[33 * KS * 2 + 8];
  const int tid = threadIdx.x, lane = tid & 63, w = tid >> 6;
  constexpr int EPI = 6;
  TS(0);
  const int h = blockIdx.x & 31, rb = blockIdx.x >> 5;
  const int g4 = lane >> 4, r0 = rb * 256 + w * 32 + (lane & 15);
  bf16x8 qf[2], wf[2];
#pragma unroll
  for (int mb = 0; mb < 2; ++mb) qf[mb] = *(const bf16x8*)(q + (size_t)(r0 + mb * 16) * DM + h * 32 + g4 * 8);
#pragma unroll
  for (int fb = 0; fb < 2; ++fb)
#pragma unroll
    for (int j = 0; j < 8; ++j) wf[fb][j] = (short)wphi[h * 1024 + (g4 * 8 + j) * 32 + fb * 16 + (lane & 15)];
  __builtin_amdgcn_sched_barrier(0);
  f32x4 kp[16], kz[16];
  const float* slab = kvpart + (size_t)h * (33 * 64) + tid * 4;
#pragma unroll
  for (int p = 0; p < 16; ++p) kp[p] = *(const f32x4*)(slab + (size_t)p * NH * (33 * 64));
  const bool zw = __builtin_amdgcn_readfirstlane(w) == 0;
  if (zw) {
#pragma unroll
    for (int p = 0; p < 16; ++p) kz[p] = *(const f32x4*)(slab + (size_t)p * NH * (33 * 64) + 2048 - (tid & 48) * 4);
  }
  __builtin_amdgcn_sched_barrier(0);
  bf16x8 pq[2][2];
#pragma unroll
  for (int mb = 0; mb < 2; ++mb) {
    u32x4 pos, neg;
    feature_map(wf, qf[mb], pos, neg);
    pq[mb][0] = __builtin_bit_cast(bf16x8, pos);
    pq[mb][1] = __builtin_bit_cast(bf16x8, neg);
  }
  __builtin_amdgcn_sched_barrier(0);
  TS(1);
  u16* kvt = (u16*)sm;
  {
    f32x4 t = kp[0];
#pragma unroll
    for (int p = 1; p < 16; ++p) t += kp[p];
    *(u32x2*)(kvt + (tid >> 4) * KS + (tid & 15) * 4) = (u32x2){pk2(t[0], t[1]), pk2(t[2], t[3])};
    if (zw) {
      f32x4 z = kz[0];
#pragma unroll
      for (int p = 1; p < 16; ++p) z += kz[p];
      if (tid < 16) *(u32x2*)(kvt + 32 * KS + tid * 4) = (u32x2){pk2(z[0], z[1]), pk2(z[2], z[3])};
    }
  }
  TS(2);
  __syncthreads();
  TS(3);
  bf16x8 kf[3][2];
#pragma unroll
  for (int db = 0; db < 3; ++db)
#pragma unroll
    for (int ks = 0; ks < 2; ++ks) {
      const int d = (db < 2) ? ((lane >> 2) & 3) * 8 + db * 4 + (lane & 3) : 32;
      const u16* p = kvt + d * KS + ks * 32 + g4 * 4;
      kf[db][ks] = cat8(*(const s16x4*)p, *(const s16x4*)(p + 16));
    }
#pragma unroll
  for (int mb = 0; mb < 2; ++mb) {
    f32x4 a[3];
#pragma unroll
    for (int db = 0; db < 3; ++db) {
      a[db] = mfma16(kf[db][0], pq[mb][0], (f32x4){0.f, 0.f, 0.f, 0.f});
      a[db] = mfma16(kf[db][1], pq[mb][1], a[db]);
    }
    const float id = __builtin_amdgcn_rcpf(rbf(rbf(a[2][0]) + 1e-6f));
    u32x4 ov;
#pragma unroll
    for (int db = 0; db < 2; ++db) {
      const f32x2 lo = rbf2((f32x2){a[db][0], a[db][1]}) * id, hi = rbf2((f32x2){a[db][2], a[db][3]}) * id;
      ov[db * 2] = pk2(lo[0], lo[1]); ov[db * 2 + 1] = pk2(hi[0], hi[1]);
    }
    st16_wt(o, SEQ * DM * 2, ((r0 + mb * 16) * DM + h * 32 + g4 * 8) * 2, ov);
  }
  TS(4);
  TS(5);
}

template <int BN, int S, int KD, int EPI> static constexpr int smem_bytes() {
  constexpr bool USE_STATS = (EPI == EPI_QKV || EPI == EPI_GELU_SHIFT || EPI == EPI_W2 || EPI == EPI_GELU);
  constexpr int NRES = (EPI == EPI_RES || EPI == EPI_W2) ? 1 : (EPI == EPI_W4 ? 2 : 0);
  return S * (16384 + BN * 128) + NRES * 16384 + (EPI == EPI_QKV ? 4096 : 0) + 2048 + 1024 + 256;
}
template <int BN, int S, int KD, int EPI> static void launch_gemm(const GemmArgs& a, hipStream_t st) {
  constexpr int sb = smem_bytes<BN, S, KD, EPI>();
  static_assert(sb <= 163840, "LDS");
  (void)hipFuncSetAttribute((const void*)gemm_k<BN, S, KD, EPI>, hipFuncAttributeMaxDynamicSharedMemorySize, sb);
  gemm_k<BN, S, KD, EPI><<<256, 512, sb, st>>>(a);
}

extern "C" void kernel_launch(void* const* d_in, const int* in_sizes, int n_in, void* d_out, int out_size, void* d_ws,
                              size_t ws_size, hipStream_t stream) {
  const u16* x0 = (const u16*)d_in[0];
  const u16* wqkv = (const u16*)d_in[1];
  const u16* wphi = (const u16*)d_in[2];
  const u16* wo = (const u16*)d_in[3];
  const u16* w1 = (const u16*)d_in[5];
  const u16* w2 = (const u16*)d_in[6];
  const u16* w3 = (const u16*)d_in[7];
  const u16* w4 = (const u16*)d_in[8];
  char* ws = (char*)d_ws;
  const size_t MB = 1 << 20;
  u16* xln0 = (u16*)(ws + 0 * MB);
  u16* phiq = (u16*)(ws + 4 * MB);
  float* kvpart = (float*)(ws + 12 * MB);
  u16* obuf = (u16*)(ws + 17 * MB);
  u16* xa = (u16*)(ws + 21 * MB);
  u16* hsh = (u16*)(ws + 25 * MB);
  u16* x1 = (u16*)(ws + 29 * MB);
  u16* gbuf = (u16*)(ws + 33 * MB);
  u16* xb = (u16*)(ws + 48 * MB);
  float* st_xa = (float*)(ws + 52 * MB);
  float* st_x1 = (float*)(ws + 53 * MB);
  float* st_xb = (float*)(ws + 54 * MB);
  float* colsum = (float*)(ws + 55 * MB);

  ln0_k<<<SEQ / 4 + 120, 256, 0, stream>>>(x0, xln0, wqkv, w1, w3, colsum, wo, w2, w4);
  for (int layer = 0; layer < 2; ++layer) {
    const u16* xin = layer == 0 ? x0 : xb;
    u16* xout = layer == 0 ? xb : (u16*)d_out;
    GemmArgs a;
    a = GemmArgs{layer == 0 ? xln0 : xb, wqkv, phiq, layer == 0 ? wo : nullptr, w2, st_xb, (float*)w4, colsum, wphi, kvpart, layer == 0 ? 0 : 1, 0};
    launch_gemm<192, 3, 1024, EPI_QKV>(a, stream);
    attn_o_k<<<256, 512, 0, stream>>>(phiq, wphi, kvpart, obuf);
    a = GemmArgs{obuf, wo, xa, xin, nullptr, nullptr, st_xa, nullptr, nullptr, nullptr, 0, 0};
    launch_gemm<64, 5, 1024, EPI_RES>(a, stream);
    a = GemmArgs{xa, w1, hsh, nullptr, nullptr, st_xa, nullptr, colsum + 3072, nullptr, nullptr, 2, 0};
    launch_gemm<64, 5, 1024, EPI_GELU_SHIFT>(a, stream);
    a = GemmArgs{hsh, w2, x1, xa, nullptr, st_xa, st_x1, nullptr, nullptr, nullptr, 1, 0};
    launch_gemm<64, 5, 1024, EPI_W2>(a, stream);
    a = GemmArgs{x1, w3, gbuf, nullptr, nullptr, st_x1, nullptr, colsum + 4096, nullptr, nullptr, 1, 0};
    launch_gemm<224, 3, 1024, EPI_GELU>(a, stream);
    a = GemmArgs{gbuf, w4, xout, xa, x1, nullptr, st_xb, nullptr, nullptr, nullptr, 0, 0};
    launch_gemm<64, 5, 3584, EPI_W4>(a, stream);
  }
}
```

```cpp
#include <hip/hip_runtime.h>
#include <hip/hip_bf16.h>
#include <stdint.h>

typedef __attribute__((ext_vector_type(8))) short bf16x8;
typedef __attribute__((ext_vector_type(4))) short s16x4;
typedef __attribute__((ext_vector_type(4))) float f32x4;
typedef __attribute__((ext_vector_type(2))) float f32x2;
typedef __attribute__((ext_vector_type(4))) unsigned u32x4;
typedef __attribute__((ext_vector_type(2))) unsigned u32x2;
typedef __attribute__((ext_vector_type(2))) __bf16 bf16x2_t;
typedef unsigned short u16;
#define LDS_AS __attribute__((address_space(3)))
#define DI __device__ __forceinline__

static constexpr int SEQ = 2048, DM = 1024, NH = 32, FF = 3584;

DI float bf2f(unsigned v) { return __uint_as_float(v << 16); }
DI float bflo(unsigned v) { return __uint_as_float(v << 16); }
DI float bfhi(unsigned v) { return __uint_as_float(v & 0xffff0000u); }
DI unsigned pk2(float a, float b) {
  f32x2 v = {a, b};
  return __builtin_bit_cast(unsigned, __builtin_convertvector(v, bf16x2_t));
}
DI float rbf(float a) { return bflo(pk2(a, a)); }
template <int CTRL> DI float dppf(float v) { return __builtin_bit_cast(float, __builtin_amdgcn_update_dpp(0, __builtin_bit_cast(int, v), CTRL, 0xf, 0xf, true)); }
DI float sum8(float v) { v += dppf<0xB1>(v); v += dppf<0x4E>(v); return v + dppf<0x141>(v); }
DI f32x2 pair16(float v) { const unsigned u = __float_as_uint(v); const auto r = __builtin_amdgcn_permlane16_swap(u, u, false, false); return (f32x2){__uint_as_float(r[0]), __uint_as_float(r[1])}; }
DI f32x2 pair32(float v) { const unsigned u = __float_as_uint(v); const auto r = __builtin_amdgcn_permlane32_swap(u, u, false, false); return (f32x2){__uint_as_float(r[0]), __uint_as_float(r[1])}; }
DI float max4g(float v) { f32x2 t = pair16(v); t = pair32(fmaxf(t[0], t[1])); return fmaxf(t[0], t[1]); }
DI float min4g(float v) { f32x2 t = pair16(v); t = pair32(fminf(t[0], t[1])); return fminf(t[0], t[1]); }
DI float sum4g(float v) { f32x2 t = pair16(v); t = pair32(t[0] + t[1]); return t[0] + t[1]; }
DI float sum64(float v) { v = sum8(v); return sum4g(v + dppf<0x140>(v)); }

template <int I> struct IC { static constexpr int value = I; };
template <int B, int E, class F> DI void static_for(F&& f) {
  if constexpr (B < E) { f(IC<B>{}); static_for<B + 1, E>(f); }
}
template <int N> DI void wait_vm() { asm volatile("s_waitcnt vmcnt(%0)" ::"n"(N) : "memory"); }
DI void wait_lgkm0() { asm volatile("s_waitcnt lgkmcnt(0)" ::: "memory"); }
DI void bar() { __builtin_amdgcn_s_barrier(); }
DI void glds16(const void* src, char* lds_dst) {
  __builtin_amdgcn_global_load_lds((const unsigned*)src, (unsigned*)lds_dst, 16, 0, 0);
}
DI s16x4 trread(const char* p) {
  return __builtin_bit_cast(s16x4, __builtin_amdgcn_ds_read_tr16_b64_v4i16((LDS_AS s16x4*)p));
}
template <int OFF> DI s16x4 lds_tr(unsigned addr) {
  s16x4 r; asm volatile("ds_read_b64_tr_b16 %0, %1 offset:%2" : "=v"(r) : "v"(addr), "n"(OFF) : "memory"); return r;
}
template <int OFF> DI bf16x8 lds_b128(unsigned addr) {
  bf16x8 r; asm volatile("ds_read_b128 %0, %1 offset:%2" : "=v"(r) : "v"(addr), "n"(OFF) : "memory"); return r;
}
DI void st16_wt(void* base, unsigned bytes, unsigned off, u32x4 v) {
  __builtin_amdgcn_raw_buffer_store_b128(v, __builtin_amdgcn_make_buffer_rsrc(base, (short)0, (int)bytes, 0x00020000), (int)off, 0, 16);
}
DI bf16x8 cat8(s16x4 lo, s16x4 hi) { return (bf16x8){lo[0], lo[1], lo[2], lo[3], hi[0], hi[1], hi[2], hi[3]}; }
DI f32x4 mfma16(bf16x8 a, bf16x8 b, f32x4 c) { return __builtin_amdgcn_mfma_f32_16x16x32_bf16(a, b, c, 0, 0, 0); }

DI f32x2 gelu2(f32x2 x) {
  const float c0 = -2.302208198f, c1 = -2.302208198f * 0.044715f;
  const f32x2 a = x * (x * x * c1 + c0);
  const f32x2 d = (f32x2){__builtin_amdgcn_exp2f(a[0]), __builtin_amdgcn_exp2f(a[1])} + 1.0f;
  return x * (f32x2){__builtin_amdgcn_rcpf(d[0]), __builtin_amdgcn_rcpf(d[1])};
}
DI f32x2 rbf2(f32x2 v) { const unsigned p = pk2(v[0], v[1]); return (f32x2){bflo(p), bfhi(p)}; }
DI f32x2 unpk(unsigned p) { return (f32x2){bflo(p), bfhi(p)}; }

DI void feature_map(const bf16x8 (&wf)[2], bf16x8 xf, u32x4& pos, u32x4& neg) {
  f32x2 p[4];
#pragma unroll
  for (int fb = 0; fb < 2; ++fb) {
    const f32x4 d = mfma16(wf[fb], xf, (f32x4){0.f, 0.f, 0.f, 0.f});
    p[fb * 2] = rbf2((f32x2){d[0], d[1]});
    p[fb * 2 + 1] = rbf2((f32x2){d[2], d[3]});
  }
  float mx = fmaxf(p[0][0], p[0][1]), mn = fminf(p[0][0], p[0][1]);
#pragma unroll
  for (int e = 1; e < 4; ++e) { mx = fmaxf(mx, fmaxf(p[e][0], p[e][1])); mn = fminf(mn, fminf(p[e][0], p[e][1])); }
  mx = max4g(mx);
  mn = min4g(mn);
  const float l2e = 1.4426950408889634f;
  f32x2 ep[4], en[4], sp2 = {0.f, 0.f}, sn2 = {0.f, 0.f};
#pragma unroll
  for (int e = 0; e < 4; ++e) {
    const f32x2 a = (p[e] - mx) * l2e, b = (mn - p[e]) * l2e;
    ep[e] = (f32x2){__builtin_amdgcn_exp2f(a[0]), __builtin_amdgcn_exp2f(a[1])}; sp2 += ep[e];
    en[e] = (f32x2){__builtin_amdgcn_exp2f(b[0]), __builtin_amdgcn_exp2f(b[1])}; sn2 += en[e];
  }
  float sp = sp2[0] + sp2[1], sn = sn2[0] + sn2[1];
  sp = sum4g(sp);
  sn = sum4g(sn);
  const float ip = __builtin_amdgcn_rcpf(sp), in_ = __builtin_amdgcn_rcpf(sn);
#pragma unroll
  for (int e = 0; e < 4; ++e) {
    const f32x2 a = ep[e] * ip, b = en[e] * in_;
    pos[e] = pk2(a[0], a[1]); neg[e] = pk2(b[0], b[1]);
  }
}

#define TS(i)
struct GemmArgs {
  const u16* A;
  const u16* B;
  u16* out;
  const u16* res1;
  const u16* res2;
  const float* stats_in;
  float* stats_out;
  const float* colsum;
  const u16* wphi;
  float* kvpart;
  int ln_mode;
  int unused;
};

enum { EPI_QKV = 0, EPI_RES = 1, EPI_GELU_SHIFT = 2, EPI_W2 = 3, EPI_GELU = 4, EPI_W4 = 5 };

struct RowP { float alpha, beta, mhat, dhat; };
DI RowP row_params(float mean, float var, int mode) {
  RowP r;
  const float mh = rbf(mean);
  const float dh = rbf(__builtin_amdgcn_sqrtf(rbf(rbf(var) + 1e-5f)));
  const float i1 = __builtin_amdgcn_rcpf(dh);
  r.mhat = mh; r.dhat = dh;
  if (mode == 2) {
    const float m2 = (mean - mh) * i1;
    const float v2 = var * i1 * i1;
    const float mh2 = rbf(m2);
    const float dh2 = rbf(__builtin_amdgcn_sqrtf(rbf(rbf(v2) + 1e-5f)));
    const float i2 = __builtin_amdgcn_rcpf(dh2);
    r.alpha = i1 * i2;
    r.beta = (mh * i1 + mh2) * i2;
  } else {
    r.alpha = i1;
    r.beta = mh * i1;
  }
  return r;
}

template <int BN, int S, int KD, int EPI>
__global__ __launch_bounds__(512, 2) void gemm_k(GemmArgs g) {
  constexpr int NK = KD / 64;
  constexpr int NPF = BN / 64;
  constexpr bool HALF = (BN % 64) != 0;
  constexpr int NI = BN / 32;
  constexpr int A_BYTES = 16384;
  constexpr int STAGE = A_BYTES + BN * 128;
  constexpr int L = 4 + NPF * 2 + (HALF ? 1 : 0);
  constexpr int NTOT = (EPI == EPI_QKV) ? 3 * DM : (EPI == EPI_GELU ? FF : DM);
  constexpr bool USE_STATS = (EPI == EPI_QKV || EPI == EPI_GELU_SHIFT || EPI == EPI_W2 || EPI == EPI_GELU);
  constexpr bool FOLD = (EPI == EPI_QKV || EPI == EPI_GELU_SHIFT || EPI == EPI_GELU);
  constexpr int NRES = (EPI == EPI_RES || EPI == EPI_W2) ? 1 : (EPI == EPI_W4 ? 2 : 0);
  constexpr int OFF_STATS = S * STAGE;
  constexpr int OFF_RES = OFF_STATS;
  constexpr int OFF_WPHI = OFF_RES + NRES * 16384;
  constexpr int OFF_ROWP = OFF_WPHI + (EPI == EPI_QKV ? 4096 : 0);
  constexpr int OFF_CS = OFF_ROWP + 2048;
  constexpr int OFF_DUMP = OFF_CS + 1024;
  constexpr int NEXTRA = (USE_STATS && BN != 64 ? 8 : 0) + NRES * 4 + (EPI == EPI_QKV ? 1 : 0) + (FOLD ? 1 : 0);

  extern __shared__ __attribute__((aligned(16))) char smem[];
  const int tid = threadIdx.x, lane = tid & 63;
  const int w = __builtin_amdgcn_readfirstlane(tid >> 6);
  const bool prod = w >= 4;
  const int cw = w & 3, wr = cw >> 1, wc = cw & 1;

  const int b = blockIdx.x, xcd = b & 7, j = b >> 3;
  int rp, ct;
  if constexpr (EPI == EPI_QKV || EPI == EPI_GELU) { rp = (xcd >> 2) * 8 + (j >> 2); ct = (xcd & 3) * 4 + (j & 3); }
  else { rp = (xcd >> 1) * 4 + (j >> 3); ct = (xcd & 1) * 8 + (j & 7); }
  const int m0 = rp * 128;
  const int n0 = (EPI == EPI_QKV) ? ct * 64 : ct * BN;
  constexpr int PSTRIDE = (EPI == EPI_QKV) ? DM : 64;
  const int nf = HALF ? n0 + (ct & 1) * 32 : n0;
  const int nh = (ct & 1) ? n0 : n0 + NPF * 64;
  const int g4 = lane >> 4, q4 = (lane >> 2) & 3, pp = lane & 3;
  RowP* rowp = (RowP*)(smem + OFF_ROWP);
  const bool ln_on = USE_STATS && (g.ln_mode != 0 || EPI == EPI_W2);

  f32x4 acc[4][NI];
  TS(0);
  constexpr bool RP_CONS = (BN == 64);
  f32x2 sv[8];
  const int sr = cw * 32 + (lane >> 1), shf = lane & 1;
  auto load_stats = [&]() {
#pragma unroll
    for (int p = 0; p < 8; ++p)
      asm volatile("global_load_dwordx2 %0, %1, off" : "=v"(sv[p]) : "v"(g.stats_in + ((size_t)(shf * 8 + p) * SEQ + m0 + sr) * 2) : "memory");
  };
  float rs_s = 0.f, rs_m2 = 0.f;
  auto row_stats = [&](auto pc) {
    constexpr int piece = decltype(pc)::value;
    if constexpr (piece == 0) {
#pragma unroll
      for (int p = 0; p < 8; ++p) asm volatile("" : "+v"(sv[p]));
      float s = 0.f;
#pragma unroll
      for (int p = 0; p < 8; ++p) s += sv[p][0];
      rs_s = s + dppf<0xB1>(s);
    } else if constexpr (piece == 1) {
      const float mean = rs_s * (1.0f / 1024.0f);
      float m2 = 0.f;
#pragma unroll
      for (int p = 0; p < 8; ++p) { const float d = sv[p][0] * (1.0f / 64.0f) - mean; m2 += sv[p][1] + 64.0f * d * d; }
      rs_m2 = m2 + dppf<0xB1>(m2);
    } else {
      if (ln_on && shf == 0) rowp[sr] = row_params(rs_s * (1.0f / 1024.0f), rs_m2 * (1.0f / 1024.0f), EPI == EPI_W2 ? 1 : g.ln_mode);
      wait_lgkm0();
    }
    __builtin_amdgcn_sched_barrier(0);
  };
  if (__builtin_expect(prod, 1)) {
    const u16* a_src = g.A + (size_t)(m0 + cw * 32 + (lane >> 3)) * KD + (((lane & 7) ^ (lane >> 3)) * 8);
    const u16* b_src[2];
#pragma unroll
    for (int e = 0; e < 2; ++e) {
      const int r = lane >> 3;
      const int f = ((r >> 1) & 1) | (e << 1);
      const int c = ((((lane & 7) >> 1) ^ f) << 1) | (lane & 1);
      b_src[e] = g.B + (size_t)((cw * 2 + e) * 8 + r) * NTOT + nf + c * 8;
    }
    const u16* bh_src = nullptr;
    if constexpr (HALF) {
      const int c = ((((lane >> 1) & 1) ^ ((lane >> 5) & 1)) << 1) | (lane & 1);
      bh_src = g.B + (size_t)(cw * 16 + (lane >> 2)) * NTOT + nh + c * 8;
    }
    auto issue = [&](int kt, int slot) {
      char* base = smem + slot * STAGE;
#pragma unroll
      for (int p = 0; p < NPF; ++p)
#pragma unroll
        for (int e = 0; e < 2; ++e)
          glds16(b_src[e] + (size_t)kt * 64 * NTOT + p * PSTRIDE, base + A_BYTES + p * 8192 + (cw * 2 + e) * 1024);
      if constexpr (HALF) glds16(bh_src + (size_t)kt * 64 * NTOT, base + A_BYTES + NPF * 8192 + cw * 1024);
#pragma unroll
      for (int e = 0; e < 4; ++e) glds16(a_src + (size_t)e * 8 * KD + kt * 64, base + (cw * 4 + e) * 1024);
    };
    TS(6);
    issue(0, 0);
    issue(1, 1);
    wait_vm<L>();
    TS(1);
    bar();
    static_for<2, S>([&](auto ic) { issue(ic.value, ic.value); });
    TS(7);
    if constexpr (NRES >= 1) {
#pragma unroll
      for (int e = 0; e < 4; ++e) {
        const int r = (cw * 4 + e) * 8 + (lane >> 3);
        glds16(g.res1 + (size_t)(m0 + r) * DM + n0 + (lane & 7) * 8, smem + OFF_RES + (cw * 4 + e) * 1024);
      }
    }
    if constexpr (NRES >= 2) {
#pragma unroll
      for (int e = 0; e < 4; ++e) {
        const int r = (cw * 4 + e) * 8 + (lane >> 3);
        glds16(g.res2 + (size_t)(m0 + r) * DM + n0 + (lane & 7) * 8, smem + OFF_RES + 16384 + (cw * 4 + e) * 1024);
      }
    }
    if constexpr (EPI == EPI_QKV) glds16(g.wphi + (size_t)ct * 2048 + cw * 512 + lane * 8, smem + OFF_WPHI + cw * 1024);
    if constexpr (FOLD) {
      int col = cw * 64 + (lane & 15) * 4;
      if (col > BN - 4) col = BN - 4;
      const int gcol = (EPI == EPI_QKV) ? (col >> 6) * DM + n0 + (col & 63) : (HALF && col >= NPF * 64) ? nh + col - NPF * 64 : nf + col;
      if (lane < 16) glds16(g.colsum + gcol, smem + OFF_CS + cw * 256);
    }
    if constexpr (USE_STATS && !RP_CONS) load_stats();
    static_for<0, NK - 1>([&](auto ic) {
      constexpr int kt = ic.value;
      constexpr int young = (S - 2 < NK - 2 - kt) ? S - 2 : NK - 2 - kt;
      constexpr int ex = (kt + 1 <= S - 1 && kt + 1 < NK - 1) ? NEXTRA : 0;
      wait_vm<young * L + ex>();
      bar();
      if constexpr (kt + S < NK) issue(kt + S, kt % S);
      if constexpr (USE_STATS && !RP_CONS && kt >= 4 && kt < 7) row_stats(IC<kt - 4>{});
    });
    TS(2);
    bar();
  } else {
    const int fsw = ((q4 >> 1) & 1) | ((g4 & 1) << 1);
    int a_off[2], b_off[2], bh_off = 0;
#pragma unroll
    for (int k = 0; k < 2; ++k) {
      a_off[k] = (wr * 64 + (lane & 15)) * 128 + (((k * 4 + g4) ^ (lane & 7)) * 16);
      b_off[k] = A_BYTES + (8 * g4 + q4) * 128 + (((2 * k + wc) ^ fsw) * 32) + pp * 8;
    }
    if constexpr (HALF) bh_off = A_BYTES + NPF * 8192 + (8 * g4 + q4) * 64 + ((wc ^ (g4 & 1)) * 32) + pp * 8;
#pragma unroll
    for (int m = 0; m < 4; ++m)
#pragma unroll
      for (int i = 0; i < NI; ++i) acc[m][i] = (f32x4){0.f, 0.f, 0.f, 0.f};
    bf16x8 xa[2][4];
    s16x4 wlo[2][NI], whi[2][NI];
    const unsigned lbase = (unsigned)(size_t)smem;

    auto ldfrag = [&](auto setc, auto slotc, auto kc) {
      constexpr int set = decltype(setc)::value, slot = decltype(slotc)::value, k32 = decltype(kc)::value;
      const unsigned sb = lbase + slot * STAGE;
      const unsigned ba0 = sb + b_off[0], ba1 = sb + b_off[1], bah = sb + bh_off, aa = sb + a_off[k32];
      static_for<0, NI>([&](auto ic) {
        constexpr int i = decltype(ic)::value;
        if constexpr (HALF && i == NI - 1) {
          wlo[set][i] = lds_tr<k32 * 2048>(bah);
          whi[set][i] = lds_tr<k32 * 2048 + 256>(bah);
        } else {
          wlo[set][i] = lds_tr<(i >> 1) * 8192 + k32 * 4096>((i & 1) ? ba1 : ba0);
          whi[set][i] = lds_tr<(i >> 1) * 8192 + k32 * 4096 + 512>((i & 1) ? ba1 : ba0);
        }
      });
      static_for<0, 4>([&](auto mc) { constexpr int m = decltype(mc)::value; xa[set][m] = lds_b128<m * 2048>(aa); });
    };
    auto wait_set = [&](auto setc) {
      constexpr int set = decltype(setc)::value;
      asm volatile("s_waitcnt lgkmcnt(0)" : "+v"(xa[set][0]), "+v"(xa[set][1]), "+v"(xa[set][2]), "+v"(xa[set][3]) :: "memory");
#pragma unroll
      for (int i = 0; i < NI; ++i) asm volatile("" : "+v"(wlo[set][i]), "+v"(whi[set][i]));
      __builtin_amdgcn_sched_barrier(0);
    };
    auto ld1 = [&](auto setc, auto slotc, auto kc, auto rc) {
      constexpr int set = decltype(setc)::value, slot = decltype(slotc)::value, k32 = decltype(kc)::value, r = decltype(rc)::value;
      const unsigned sb = lbase + slot * STAGE;
      if constexpr (r < 2 * NI) {
        constexpr int i = r >> 1, hi = r & 1;
        s16x4 v;
        if constexpr (HALF && i == NI - 1) v = lds_tr<k32 * 2048 + hi * 256>(sb + bh_off);
        else v = lds_tr<(i >> 1) * 8192 + k32 * 4096 + hi * 512>(sb + b_off[i & 1]);
        if constexpr (hi) whi[set][i] = v; else wlo[set][i] = v;
      } else {
        constexpr int m = r - 2 * NI;
        xa[set][m] = lds_b128<m * 2048>(sb + a_off[k32]);
      }
    };
    auto mma_ld = [&](auto setc, auto ldc, auto slotc, auto kc) {
      constexpr int set = decltype(setc)::value;
      constexpr bool ld = decltype(ldc)::value != 0;
      static_for<0, 4 * NI>([&](auto tc) {
        constexpr int t = decltype(tc)::value, i = t >> 2, m = t & 3;
        acc[m][i] = mfma16(cat8(wlo[set][i], whi[set][i]), xa[set][m], acc[m][i]);
        if constexpr (ld && t < 2 * NI + 4) ld1(IC<set ^ 1>{}, slotc, kc, tc);
        __builtin_amdgcn_sched_barrier(0);
      });
    };

    const char* tp = (const char*)g.A;
    unsigned tw = 0;
    bool ton = false;
    int tks = 0;
    if (cw == 0) {
      const int pl = lane >> 4;
      const int bl = (int)(((size_t)g.B >> 7) & 7);
      int off;
      if constexpr (BN == 64) { off = ((xcd & 1) * 8 + ((3 - bl) & 7)) * 128; ton = lane < 16; }
      else if constexpr (EPI == EPI_QKV) { const int c = (3 - bl - (xcd & 3) * 4) & 7; off = pl * 2048 + ((xcd & 3) * 4 + (c & 3)) * 128; ton = c < 4 && lane < 48; }
      else { const int l0 = (xcd & 3) * 14, c = (3 - bl - l0) & 7; off = (l0 + c + pl * 8) * 128; ton = lane < (c < 6 ? 32 : 16); }
      tks = (lane >> 1) & 7;
      if (ton) tp = (const char*)g.B + (size_t)(tks * 64 + j * 2 + (lane & 1)) * NTOT * 2 + off;
      tw = 8 * 64 * NTOT * 2;
    } else if (cw == 1) {
      tks = (3 - (int)(((size_t)g.A >> 7) & 7)) & 7;
      const int row = (BN == 64) ? (xcd >> 1) * 512 + j * 16 + (lane & 15) : (xcd >> 2) * 1024 + j * 32 + (lane & 31);
      ton = lane < (BN == 64 ? 16 : 32);
      tp = (const char*)g.A + (size_t)row * KD * 2 + tks * 128;
      tw = 1024;
    }
    auto touch = [&](auto wc_) {
      constexpr int wi = decltype(wc_)::value;
      if (cw < 2) {
        if (ton && (wi > 0 || tks >= S))
          __builtin_amdgcn_global_load_lds((const unsigned*)(tp + (size_t)(wi * tw)), (unsigned*)(smem + OFF_DUMP), 4, 0, 0);
      }
      __builtin_amdgcn_sched_barrier(0);
    };
    if constexpr (USE_STATS && RP_CONS) load_stats();
    touch(IC<0>{});
    if constexpr (BN == 64) touch(IC<1>{});
    bar();
    TS(1);
    ldfrag(IC<0>{}, IC<0>{}, IC<0>{});
    static_for<0, NK>([&](auto ic) {
      constexpr int kt = ic.value;
      constexpr int slot = kt % S;
      wait_set(IC<0>{});
      if constexpr (BN != 64 && kt == 0) touch(IC<1>{});
      if constexpr (kt % 8 == 4 && (kt + 12) / 8 < NK / 8) touch(IC<(kt + 12) / 8>{});
      if constexpr (USE_STATS && RP_CONS && kt >= 6 && kt < 9) { if constexpr (kt == 6) wait_vm<0>(); row_stats(IC<kt - 6>{}); }
      mma_ld(IC<0>{}, IC<1>{}, IC<slot>{}, IC<1>{});
      wait_set(IC<1>{});
      if constexpr (kt + 1 < NK) {
        bar();
        __builtin_amdgcn_sched_barrier(0);
        mma_ld(IC<1>{}, IC<1>{}, IC<(kt + 1) % S>{}, IC<0>{});
      } else {
        mma_ld(IC<1>{}, IC<0>{}, IC<0>{}, IC<0>{});
      }
    });
    TS(2);
    bar();
  }
  TS(3);
  if constexpr (EPI == EPI_QKV) __builtin_amdgcn_s_waitcnt(0x0F70);

  constexpr int TSTR = BN * 2 + (EPI == EPI_QKV ? 32 : 16);
  char* T = smem;
  u32x4 pv[EPI == EPI_QKV ? 11 : 1];
  if (!prod) {
    float al[4] = {1.f, 1.f, 1.f, 1.f}, be[4] = {0.f, 0.f, 0.f, 0.f};
    if constexpr (FOLD) {
      if (ln_on) {
#pragma unroll
        for (int m = 0; m < 4; ++m) { const RowP r = rowp[wr * 64 + m * 16 + (lane & 15)]; al[m] = r.alpha; be[m] = r.beta; }
      }
    }
    f32x4 csv[NI];
    if constexpr (FOLD) {
#pragma unroll
      for (int i = 0; i < NI; ++i) csv[i] = *(const f32x4*)(smem + OFF_CS + ((2 * i + wc) * 16 + g4 * 4) * 4);
#pragma unroll
      for (int i = 0; i < NI; ++i) csv[i] = ln_on ? csv[i] : (f32x4){0.f, 0.f, 0.f, 0.f};
      __builtin_amdgcn_sched_barrier(0);
    }
#pragma unroll
    for (int i = 0; i < NI; ++i) {
      const int nb = 2 * i + wc;
      const int ncol = nb * 16 + g4 * 4;
      const f32x4 cs = csv[i];
#pragma unroll
      for (int m = 0; m < 4; ++m) {
        u32x2 pkd;
#pragma unroll
        for (int h = 0; h < 2; ++h) {
          f32x2 v = {acc[m][i][2 * h], acc[m][i][2 * h + 1]};
          if constexpr (FOLD) v = v * al[m] - (f32x2){cs[2 * h], cs[2 * h + 1]} * be[m];
          if constexpr (EPI == EPI_GELU_SHIFT || EPI == EPI_GELU) v = gelu2(rbf2(v));
          pkd[h] = pk2(v[0], v[1]);
        }
        *(u32x2*)(T + (wr * 64 + m * 16 + (lane & 15)) * TSTR + ncol * 2) = pkd;
      }
      if constexpr (EPI == EPI_GELU) {
        if ((i & 1) && i < NI - 1) { wait_lgkm0(); bar(); }
      }
    }
  } else if constexpr (EPI == EPI_QKV) {
    if (g.res1) {
      const int t = tid - 256;
      const u16* W4 = (const u16*)g.stats_out + (size_t)b * 14336 + t * 8;
      const u16* Wo = g.res1 + (size_t)b * 4096 + t * 8;
      const u16* W2 = g.res2 + (size_t)b * 4096 + t * 8;
#pragma unroll
      for (int i = 0; i < 2; ++i) { pv[7 + i] = *(const u32x4*)(Wo + i * 2048); pv[9 + i] = *(const u32x4*)(W2 + i * 2048); }
#pragma unroll
      for (int i = 0; i < 7; ++i) pv[i] = *(const u32x4*)(W4 + i * 2048);
    }
  } else if constexpr (EPI == EPI_GELU) {
#pragma unroll
    for (int p = 0; p < NI / 2; ++p) {
      bar();
      asm volatile("" ::: "memory");
#pragma unroll
      for (int it = 0; it < 4; ++it) {
        const int r = it * 32 + ((tid - 256) >> 3), c = p * 8 + (tid & 7);
        st16_wt(g.out, SEQ * FF * 2, ((m0 + r) * FF + nf + c * 8) * 2, *(const u32x4*)(T + r * TSTR + c * 16));
      }
    }
  }
  if constexpr (EPI == EPI_QKV) { wait_lgkm0(); bar(); asm volatile("" ::: "memory"); }
  else __syncthreads();
  TS(4);
  if constexpr (EPI == EPI_GELU) {
    const int r = tid >> 2, c = (NI / 2) * 8 + (tid & 3);
    st16_wt(g.out, SEQ * FF * 2, ((m0 + r) * FF + nh + (tid & 3) * 8) * 2, *(const u32x4*)(T + r * TSTR + c * 16));
  } else if constexpr (EPI == EPI_GELU_SHIFT) {
    const int grp = n0 >> 8;
    const int sh = (grp == 0) ? 1 : (grp == 1) ? -1 : (grp == 2) ? 2 : -2;
#pragma unroll
    for (int it = 0; it < 2; ++it) {
      const int r = it * 64 + (tid >> 3), c = tid & 7;
      const u32x4 v = *(const u32x4*)(T + r * TSTR + c * 16);
      const int dr = m0 + r + sh;
      if (dr >= 0 && dr < SEQ) st16_wt(g.out, SEQ * DM * 2, (dr * DM + n0 + c * 8) * 2, v);
    }
    if (tid < 16) {
      const int zr = tid >> 3, c = tid & 7;
      const int az = sh > 0 ? sh : -sh;
      if (zr < az) {
        if (sh > 0 && rp == 0) *(u32x4*)(g.out + (size_t)zr * DM + n0 + c * 8) = (u32x4){0u, 0u, 0u, 0u};
        if (sh < 0 && rp == 15) *(u32x4*)(g.out + (size_t)(SEQ - 1 - zr) * DM + n0 + c * 8) = (u32x4){0u, 0u, 0u, 0u};
      }
    }
  } else if constexpr (EPI == EPI_RES || EPI == EPI_W2 || EPI == EPI_W4) {
#pragma unroll
    for (int it = 0; it < 2; ++it) {
      const int r = it * 64 + (tid >> 3), c = tid & 7;
      const u32x4 v = *(const u32x4*)(T + r * TSTR + c * 16);
      const u32x4 x = *(const u32x4*)(smem + OFF_RES + r * 128 + c * 16);
      f32x2 y[4];
      if constexpr (EPI == EPI_RES) {
#pragma unroll
        for (int e = 0; e < 4; ++e) y[e] = unpk(x[e]) + unpk(v[e]);
      } else if constexpr (EPI == EPI_W2) {
        const RowP rr = rowp[r];
#pragma unroll
        for (int e = 0; e < 4; ++e) y[e] = rbf2(rbf2(unpk(x[e]) - rr.mhat) * rr.alpha) + unpk(v[e]);
      } else {
        const u32x4 x1 = *(const u32x4*)(smem + OFF_RES + 16384 + r * 128 + c * 16);
#pragma unroll
        for (int e = 0; e < 4; ++e) y[e] = unpk(x[e]) + rbf2(unpk(x1[e]) + unpk(v[e]));
      }
      u32x4 o;
#pragma unroll
      for (int e = 0; e < 4; ++e) { o[e] = pk2(y[e][0], y[e][1]); y[e] = unpk(o[e]); }
      st16_wt(g.out, SEQ * DM * 2, ((m0 + r) * DM + n0 + c * 8) * 2, o);
      const f32x2 s2 = (y[0] + y[1]) + (y[2] + y[3]);
      const float s = sum8(s2[0] + s2[1]);
      const float mc = s * (1.0f / 64.0f);
      f32x2 q2 = {0.f, 0.f};
#pragma unroll
      for (int e = 0; e < 4; ++e) { const f32x2 d = y[e] - mc; q2 += d * d; }
      const float m2 = sum8(q2[0] + q2[1]);
      if (c == 0) *(f32x2*)(g.stats_out + ((size_t)ct * SEQ + m0 + r) * 2) = (f32x2){s, m2};
    }
  } else {
    constexpr int PKSTR = 160;
    char* PK = smem + 128 * TSTR;
#pragma unroll
    for (int it = 0; it < 2; ++it) {
      const int r = it * 64 + (tid >> 3), c = tid & 7;
      st16_wt(g.out, SEQ * DM * 2, ((m0 + r) * DM + n0 + c * 8) * 2, *(const u32x4*)(T + r * TSTR + c * 16));
    }
    {
      const int hh = w & 1, rq = w >> 1;
      bf16x8 wf[2];
#pragma unroll
      for (int fb = 0; fb < 2; ++fb) {
        const char* p = smem + OFF_WPHI + hh * 2048 + (8 * g4 + q4) * 64 + fb * 32 + pp * 8;
        wf[fb] = cat8(trread(p), trread(p + 256));
      }
#pragma unroll
      for (int mb = 0; mb < 2; ++mb) {
        const int row = rq * 32 + mb * 16 + (lane & 15);
        const bf16x8 xf = *(const bf16x8*)(T + row * TSTR + (64 + hh * 32 + g4 * 8) * 2);
        u32x4 pos, neg;
        feature_map(wf, xf, pos, neg);
        char* dst = PK + (hh * 128 + row) * PKSTR + g4 * 8;
        *(u32x2*)dst = (u32x2){pos[0], pos[1]}; *(u32x2*)(dst + 32) = (u32x2){pos[2], pos[3]};
        *(u32x2*)(dst + 64) = (u32x2){neg[0], neg[1]}; *(u32x2*)(dst + 96) = (u32x2){neg[2], neg[3]};
      }
    }
    __syncthreads();
    {
      const int hh = w >> 2, fa = w & 3;
      f32x4 kv[3];
#pragma unroll
      for (int d = 0; d < 3; ++d) kv[d] = (f32x4){0.f, 0.f, 0.f, 0.f};
      const short one = (short)0x3F80;
      const bf16x8 ones = {one, one, one, one, one, one, one, one};
#pragma unroll
      for (int ks = 0; ks < 4; ++ks) {
        const int row = ks * 32 + 8 * g4 + q4;
        bf16x8 vf[2];
#pragma unroll
        for (int d = 0; d < 2; ++d) {
          const char* p = T + row * TSTR + (128 + hh * 32 + d * 16) * 2 + pp * 8;
          vf[d] = cat8(trread(p), trread(p + 4 * TSTR));
        }
        const char* p = PK + (hh * 128 + row) * PKSTR + (fa * 16) * 2 + pp * 8;
        const bf16x8 pf = cat8(trread(p), trread(p + 4 * PKSTR));
        kv[0] = mfma16(pf, vf[0], kv[0]);
        kv[1] = mfma16(pf, vf[1], kv[1]);
        kv[2] = mfma16(pf, ones, kv[2]);
      }
      constexpr unsigned KVB = 16 * NH * 33 * 64 * 4;
      const int so = (rp * NH + ct * 2 + hh) * (33 * 64) + fa * 16 + g4 * 4;
      st16_wt(g.kvpart, KVB, (so + (lane & 15) * 64) * 4, __builtin_bit_cast(u32x4, kv[0]));
      st16_wt(g.kvpart, KVB, (so + (16 + (lane & 15)) * 64) * 4, __builtin_bit_cast(u32x4, kv[1]));
      if ((lane & 15) == 0) st16_wt(g.kvpart, KVB, (so + 32 * 64) * 4, __builtin_bit_cast(u32x4, kv[2]));
    }
    if (prod && g.res1) {
#pragma unroll
      for (int i = 0; i < 11; ++i) asm volatile("" ::"v"(pv[i]));
    }
  }
  TS(5);
}

__global__ __launch_bounds__(256) void ln0_k(const u16* __restrict__ x, u16* __restrict__ y, const u16* __restrict__ wqkv,
                                             const u16* __restrict__ w1, const u16* __restrict__ w3, float* __restrict__ colsum,
                                             const u16* __restrict__ wo, const u16* __restrict__ w2, const u16* __restrict__ w4) {
  if (blockIdx.x >= SEQ / 4 + 120) {
    const int t = blockIdx.x - (SEQ / 4 + 120);
    const u16* W = t < 32 ? wo + (size_t)t * 32768 : (t < 64 ? w2 + (size_t)(t - 32) * 32768 : w4 + (size_t)(t - 64) * 32768);
#pragma unroll
    for (int i = 0; i < 16; ++i) {
      const u32x4 v = *(const u32x4*)(W + (i * 256 + threadIdx.x) * 8);
      asm volatile("" ::"v"(v));
    }
    return;
  }
  if (blockIdx.x >= SEQ / 4) {
    __shared__ float red[32 * 64];
    const int tid = threadIdx.x;
    const int cg = blockIdx.x - SEQ / 4;
    const u16* W; int N, c0;
    if (cg < 48) { W = wqkv; N = 3 * DM; c0 = cg * 64; }
    else if (cg < 64) { W = w1; N = DM; c0 = (cg - 48) * 64; }
    else { W = w3; N = FF; c0 = (cg - 64) * 64; }
    const int c = tid & 7, rg = tid >> 3;
    f32x2 s[4] = {{0.f, 0.f}, {0.f, 0.f}, {0.f, 0.f}, {0.f, 0.f}};
#pragma unroll 16
    for (int i = 0; i < 32; ++i) {
      const u32x4 v = *(const u32x4*)(W + (size_t)(rg + i * 32) * N + c0 + c * 8);
#pragma unroll
      for (int e = 0; e < 4; ++e) s[e] += unpk(v[e]);
    }
#pragma unroll
    for (int e = 0; e < 4; ++e) *(f32x2*)(red + rg * 64 + c * 8 + 2 * e) = s[e];
    __syncthreads();
    if (tid < 64) {
      float t = 0.f;
#pragma unroll
      for (int i = 0; i < 32; ++i) t += red[i * 64 + tid];
      colsum[cg * 64 + tid] = t;
    }
    return;
  }
  const int lane = threadIdx.x & 63, row = blockIdx.x * 4 + (threadIdx.x >> 6);
  const u16* src = x + (size_t)row * DM;
  const u32x4 a = *(const u32x4*)(src + lane * 8), b = *(const u32x4*)(src + 512 + lane * 8);
  float v[16];
#pragma unroll
  for (int e = 0; e < 4; ++e) { v[2 * e] = bflo(a[e]); v[2 * e + 1] = bfhi(a[e]); v[8 + 2 * e] = bflo(b[e]); v[9 + 2 * e] = bfhi(b[e]); }
  float s = 0.f;
#pragma unroll
  for (int e = 0; e < 16; ++e) s += v[e];
  s = sum64(s);
  const float mean = s * (1.0f / 1024.0f);
  float m2 = 0.f;
#pragma unroll
  for (int e = 0; e < 16; ++e) { const float d = v[e] - mean; m2 += d * d; }
  m2 = sum64(m2);
  const RowP r = row_params(mean, m2 * (1.0f / 1024.0f), 1);
  u32x4 oa, ob;
#pragma unroll
  for (int e = 0; e < 4; ++e) {
    oa[e] = pk2(rbf(v[2 * e] - r.mhat) / r.dhat, rbf(v[2 * e + 1] - r.mhat) / r.dhat);
    ob[e] = pk2(rbf(v[8 + 2 * e] - r.mhat) / r.dhat, rbf(v[9 + 2 * e] - r.mhat) / r.dhat);
  }
  u16* dst = y + (size_t)row * DM;
  *(u32x4*)(dst + lane * 8) = oa;
  *(u32x4*)(dst + 512 + lane * 8) = ob;
}

__global__ __launch_bounds__(512) void attn_o_k(const u16* __restrict__ q, const u16* __restrict__ wphi, const float* __restrict__ kvpart,
                                                u16* __restrict__ o) {
  constexpr int KS = 80;
  __shared__ __attribute__((aligned(16))) char sm[33 * KS * 2 + 8];
  const int tid = threadIdx.x, lane = tid & 63, w = tid >> 6;
  constexpr int EPI = 6;
  TS(0);
  const int h = blockIdx.x & 31, rb = blockIdx.x >> 5;
  const int g4 = lane >> 4, r0 = rb * 256 + w * 32 + (lane & 15);
  bf16x8 qf[2], wf[2];
#pragma unroll
  for (int mb = 0; mb < 2; ++mb) qf[mb] = *(const bf16x8*)(q + (size_t)(r0 + mb * 16) * DM + h * 32 + g4 * 8);
#pragma unroll
  for (int fb = 0; fb < 2; ++fb)
#pragma unroll
    for (int j = 0; j < 8; ++j) wf[fb][j] = (short)wphi[h * 1024 + (g4 * 8 + j) * 32 + fb * 16 + (lane & 15)];
  __builtin_amdgcn_sched_barrier(0);
  f32x4 kp[16], kz[16];
  const float* slab = kvpart + (size_t)h * (33 * 64) + tid * 4;
#pragma unroll
  for (int p = 0; p < 16; ++p) kp[p] = *(const f32x4*)(slab + (size_t)p * NH * (33 * 64));
  const bool zw = __builtin_amdgcn_readfirstlane(w) == 0;
  if (zw) {
#pragma unroll
    for (int p = 0; p < 16; ++p) kz[p] = *(const f32x4*)(slab + (size_t)p * NH * (33 * 64) + 2048 - (tid & 48) * 4);
  }
  __builtin_amdgcn_sched_barrier(0);
  bf16x8 pq[2][2];
#pragma unroll
  for (int mb = 0; mb < 2; ++mb) {
    u32x4 pos, neg;
    feature_map(wf, qf[mb], pos, neg);
    pq[mb][0] = __builtin_bit_cast(bf16x8, pos);
    pq[mb][1] = __builtin_bit_cast(bf16x8, neg);
  }
  __builtin_amdgcn_sched_barrier(0);
  TS(1);
  u16* kvt = (u16*)sm;
  {
    f32x4 t = kp[0];
#pragma unroll
    for (int p = 1; p < 16; ++p) t += kp[p];
    *(u32x2*)(kvt + (tid >> 4) * KS + (tid & 15) * 4) = (u32x2){pk2(t[0], t[1]), pk2(t[2], t[3])};
    if (zw) {
      f32x4 z = kz[0];
#pragma unroll
      for (int p = 1; p < 16; ++p) z += kz[p];
      if (tid < 16) *(u32x2*)(kvt + 32 * KS + tid * 4) = (u32x2){pk2(z[0], z[1]), pk2(z[2], z[3])};
    }
  }
  TS(2);
  __syncthreads();
  TS(3);
  bf16x8 kf[3][2];
#pragma unroll
  for (int db = 0; db < 3; ++db)
#pragma unroll
    for (int ks = 0; ks < 2; ++ks) {
      const int d = (db < 2) ? ((lane >> 2) & 3) * 8 + db * 4 + (lane & 3) : 32;
      const u16* p = kvt + d * KS + ks * 32 + g4 * 4;
      kf[db][ks] = cat8(*(const s16x4*)p, *(const s16x4*)(p + 16));
    }
#pragma unroll
  for (int mb = 0; mb < 2; ++mb) {
    f32x4 a[3];
#pragma unroll
    for (int db = 0; db < 3; ++db) {
      a[db] = mfma16(kf[db][0], pq[mb][0], (f32x4){0.f, 0.f, 0.f, 0.f});
      a[db] = mfma16(kf[db][1], pq[mb][1], a[db]);
    }
    const float id = __builtin_amdgcn_rcpf(rbf(rbf(a[2][0]) + 1e-6f));
    u32x4 ov;
#pragma unroll
    for (int db = 0; db < 2; ++db) {
      const f32x2 lo = rbf2((f32x2){a[db][0], a[db][1]}) * id, hi = rbf2((f32x2){a[db][2], a[db][3]}) * id;
      ov[db * 2] = pk2(lo[0], lo[1]); ov[db * 2 + 1] = pk2(hi[0], hi[1]);
    }
    st16_wt(o, SEQ * DM * 2, ((r0 + mb * 16) * DM + h * 32 + g4 * 8) * 2, ov);
  }
  TS(4);
  TS(5);
}

template <int BN, int S, int KD, int EPI> static constexpr int smem_bytes() {
  constexpr bool USE_STATS = (EPI == EPI_QKV || EPI == EPI_GELU_SHIFT || EPI == EPI_W2 || EPI == EPI_GELU);
  constexpr int NRES = (EPI == EPI_RES || EPI == EPI_W2) ? 1 : (EPI == EPI_W4 ? 2 : 0);
  return S * (16384 + BN * 128) + NRES * 16384 + (EPI == EPI_QKV ? 4096 : 0) + 2048 + 1024 + 256;
}
template <int BN, int S, int KD, int EPI> static void launch_gemm(const GemmArgs& a, hipStream_t st) {
  constexpr int sb = smem_bytes<BN, S, KD, EPI>();
  static_assert(sb <= 163840, "LDS");
  (void)hipFuncSetAttribute((const void*)gemm_k<BN, S, KD, EPI>, hipFuncAttributeMaxDynamicSharedMemorySize, sb);
  gemm_k<BN, S, KD, EPI><<<256, 512, sb, st>>>(a);
}

extern "C" void kernel_launch(void* const* d_in, const int* in_sizes, int n_in, void* d_out, int out_size, void* d_ws,
                              size_t ws_size, hipStream_t stream) {
  const u16* x0 = (const u16*)d_in[0];
  const u16* wqkv = (const u16*)d_in[1];
  const u16* wphi = (const u16*)d_in[2];
  const u16* wo = (const u16*)d_in[3];
  const u16* w1 = (const u16*)d_in[5];
  const u16* w2 = (const u16*)d_in[6];
  const u16* w3 = (const u16*)d_in[7];
  const u16* w4 = (const u16*)d_in[8];
  char* ws = (char*)d_ws;
  const size_t MB = 1 << 20;
  u16* xln0 = (u16*)(ws + 0 * MB);
  u16* phiq = (u16*)(ws + 4 * MB);
  float* kvpart = (float*)(ws + 12 * MB);
  u16* obuf = (u16*)(ws + 17 * MB);
  u16* xa = (u16*)(ws + 21 * MB);
  u16* hsh = (u16*)(ws + 25 * MB);
  u16* x1 = (u16*)(ws + 29 * MB);
  u16* gbuf = (u16*)(ws + 33 * MB);
  u16* xb = (u16*)(ws + 48 * MB);
  float* st_xa = (float*)(ws + 52 * MB);
  float* st_x1 = (float*)(ws + 53 * MB);
  float* st_xb = (float*)(ws + 54 * MB);
  float* colsum = (float*)(ws + 55 * MB);

  ln0_k<<<SEQ / 4 + 120, 256, 0, stream>>>(x0, xln0, wqkv, w1, w3, colsum, wo, w2, w4);
  for (int layer = 0; layer < 2; ++layer) {
    const u16* xin = layer == 0 ? x0 : xb;
    u16* xout = layer == 0 ? xb : (u16*)d_out;
    GemmArgs a;
    a = GemmArgs{layer == 0 ? xln0 : xb, wqkv, phiq, layer == 0 ? wo : nullptr, w2, st_xb, (float*)w4, colsum, wphi, kvpart, layer == 0 ? 0 : 1, 0};
    launch_gemm<192, 3, 1024, EPI_QKV>(a, stream);
    attn_o_k<<<256, 512, 0, stream>>>(phiq, wphi, kvpart, obuf);
    a = GemmArgs{obuf, wo, xa, xin, nullptr, nullptr, st_xa, nullptr, nullptr, nullptr, 0, 0};
    launch_gemm<64, 5, 1024, EPI_RES>(a, stream);
    a = GemmArgs{xa, w1, hsh, nullptr, nullptr, st_xa, nullptr, colsum + 3072, nullptr, nullptr, 2, 0};
    launch_gemm<64, 5, 1024, EPI_GELU_SHIFT>(a, stream);
    a = GemmArgs{hsh, w2, x1, xa, nullptr, st_xa, st_x1, nullptr, nullptr, nullptr, 1, 0};
    launch_gemm<64, 5, 1024, EPI_W2>(a, stream);
    a = GemmArgs{x1, w3, gbuf, nullptr, nullptr, st_x1, nullptr, colsum + 4096, nullptr, nullptr, 1, 0};
    launch_gemm<224, 3, 1024, EPI_GELU>(a, stream);
    a = GemmArgs{gbuf, w4, xout, xa, x1, nullptr, st_xb, nullptr, nullptr, nullptr, 0, 0};
    launch_gemm<64, 5, 3584, EPI_W4>(a, stream);
  }
}
```

```cpp
#include <hip/hip_runtime.h>
#include <hip/hip_bf16.h>
#include <stdint.h>

typedef __attribute__((ext_vector_type(8))) short bf16x8;
typedef __attribute__((ext_vector_type(4))) short s16x4;
typedef __attribute__((ext_vector_type(4))) float f32x4;
typedef __attribute__((ext_vector_type(2))) float f32x2;
typedef __attribute__((ext_vector_type(4))) unsigned u32x4;
typedef __attribute__((ext_vector_type(2))) unsigned u32x2;
typedef __attribute__((ext_vector_type(2))) __bf16 bf16x2_t;
typedef unsigned short u16;
#define LDS_AS __attribute__((address_space(3)))
#define DI __device__ __forceinline__

static constexpr int SEQ = 2048, DM = 1024, NH = 32, FF = 3584, NCS = 3 * DM + DM + FF;

DI float bf2f(unsigned v) { return __uint_as_float(v << 16); }
DI float bflo(unsigned v) { return __uint_as_float(v << 16); }
DI float bfhi(unsigned v) { return __uint_as_float(v & 0xffff0000u); }
DI unsigned pk2(float a, float b) {
  f32x2 v = {a, b};
  return __builtin_bit_cast(unsigned, __builtin_convertvector(v, bf16x2_t));
}
DI float rbf(float a) { return bflo(pk2(a, a)); }
template <int CTRL> DI float dppf(float v) { return __builtin_bit_cast(float, __builtin_amdgcn_update_dpp(0, __builtin_bit_cast(int, v), CTRL, 0xf, 0xf, true)); }
DI float sum8(float v) { v += dppf<0xB1>(v); v += dppf<0x4E>(v); return v + dppf<0x141>(v); }
DI f32x2 pair16(float v) { const unsigned u = __float_as_uint(v); const auto r = __builtin_amdgcn_permlane16_swap(u, u, false, false); return (f32x2){__uint_as_float(r[0]), __uint_as_float(r[1])}; }
DI f32x2 pair32(float v) { const unsigned u = __float_as_uint(v); const auto r = __builtin_amdgcn_permlane32_swap(u, u, false, false); return (f32x2){__uint_as_float(r[0]), __uint_as_float(r[1])}; }
DI float max4g(float v) { f32x2 t = pair16(v); t = pair32(fmaxf(t[0], t[1])); return fmaxf(t[0], t[1]); }
DI float min4g(float v) { f32x2 t = pair16(v); t = pair32(fminf(t[0], t[1])); return fminf(t[0], t[1]); }
DI float sum4g(float v) { f32x2 t = pair16(v); t = pair32(t[0] + t[1]); return t[0] + t[1]; }
DI float sum64(float v) { v = sum8(v); return sum4g(v + dppf<0x140>(v)); }

template <int I> struct IC { static constexpr int value = I; };
template <int B, int E, class F> DI void static_for(F&& f) {
  if constexpr (B < E) { f(IC<B>{}); static_for<B + 1, E>(f); }
}
template <int N> DI void wait_vm() { asm volatile("s_waitcnt vmcnt(%0)" ::"n"(N) : "memory"); }
DI void wait_lgkm0() { asm volatile("s_waitcnt lgkmcnt(0)" ::: "memory"); }
DI void bar() { __builtin_amdgcn_s_barrier(); }
DI void glds16(const void* src, char* lds_dst) {
  __builtin_amdgcn_global_load_lds((const unsigned*)src, (unsigned*)lds_dst, 16, 0, 0);
}
DI s16x4 trread(const char* p) {
  return __builtin_bit_cast(s16x4, __builtin_amdgcn_ds_read_tr16_b64_v4i16((LDS_AS s16x4*)p));
}
template <int OFF> DI s16x4 lds_tr(unsigned addr) {
  s16x4 r; asm volatile("ds_read_b64_tr_b16 %0, %1 offset:%2" : "=v"(r) : "v"(addr), "n"(OFF) : "memory"); return r;
}
template <int OFF> DI bf16x8 lds_b128(unsigned addr) {
  bf16x8 r; asm volatile("ds_read_b128 %0, %1 offset:%2" : "=v"(r) : "v"(addr), "n"(OFF) : "memory"); return r;
}
DI void st16_wt(void* base, unsigned bytes, unsigned off, u32x4 v) {
  __builtin_amdgcn_raw_buffer_store_b128(v, __builtin_amdgcn_make_buffer_rsrc(base, (short)0, (int)bytes, 0x00020000), (int)off, 0, 16);
}
DI bf16x8 cat8(s16x4 lo, s16x4 hi) { return (bf16x8){lo[0], lo[1], lo[2], lo[3], hi[0], hi[1], hi[2], hi[3]}; }
DI f32x4 mfma16(bf16x8 a, bf16x8 b, f32x4 c) { return __builtin_amdgcn_mfma_f32_16x16x32_bf16(a, b, c, 0, 0, 0); }

DI f32x2 gelu2(f32x2 x) {
  const float c0 = -2.302208198f, c1 = -2.302208198f * 0.044715f;
  const f32x2 a = x * (x * x * c1 + c0);
  const f32x2 d = (f32x2){__builtin_amdgcn_exp2f(a[0]), __builtin_amdgcn_exp2f(a[1])} + 1.0f;
  return x * (f32x2){__builtin_amdgcn_rcpf(d[0]), __builtin_amdgcn_rcpf(d[1])};
}
DI f32x2 rbf2(f32x2 v) { const unsigned p = pk2(v[0], v[1]); return (f32x2){bflo(p), bfhi(p)}; }
DI f32x2 unpk(unsigned p) { return (f32x2){bflo(p), bfhi(p)}; }

DI void feature_map(const bf16x8 (&wf)[2], bf16x8 xf, u32x4& pos, u32x4& neg) {
  f32x2 p[4];
#pragma unroll
  for (int fb = 0; fb < 2; ++fb) {
    const f32x4 d = mfma16(wf[fb], xf, (f32x4){0.f, 0.f, 0.f, 0.f});
    p[fb * 2] = rbf2((f32x2){d[0], d[1]});
    p[fb * 2 + 1] = rbf2((f32x2){d[2], d[3]});
  }
  float mx = fmaxf(p[0][0], p[0][1]), mn = fminf(p[0][0], p[0][1]);
#pragma unroll
  for (int e = 1; e < 4; ++e) { mx = fmaxf(mx, fmaxf(p[e][0], p[e][1])); mn = fminf(mn, fminf(p[e][0], p[e][1])); }
  mx = max4g(mx);
  mn = min4g(mn);
  const float l2e = 1.4426950408889634f;
  f32x2 ep[4], en[4], sp2 = {0.f, 0.f}, sn2 = {0.f, 0.f};
#pragma unroll
  for (int e = 0; e < 4; ++e) {
    const f32x2 a = (p[e] - mx) * l2e, b = (mn - p[e]) * l2e;
    ep[e] = (f32x2){__builtin_amdgcn_exp2f(a[0]), __builtin_amdgcn_exp2f(a[1])}; sp2 += ep[e];
    en[e] = (f32x2){__builtin_amdgcn_exp2f(b[0]), __builtin_amdgcn_exp2f(b[1])}; sn2 += en[e];
  }
  float sp = sp2[0] + sp2[1], sn = sn2[0] + sn2[1];
  sp = sum4g(sp);
  sn = sum4g(sn);
  const float ip = __builtin_amdgcn_rcpf(sp), in_ = __builtin_amdgcn_rcpf(sn);
#pragma unroll
  for (int e = 0; e < 4; ++e) {
    const f32x2 a = ep[e] * ip, b = en[e] * in_;
    pos[e] = pk2(a[0], a[1]); neg[e] = pk2(b[0], b[1]);
  }
}

#define TS(i)
struct GemmArgs {
  const u16* A;
  const u16* B;
  u16* out;
  const u16* res1;
  const u16* res2;
  const float* stats_in;
  float* stats_out;
  const float* colsum;
  const u16* wphi;
  float* kvpart;
  int ln_mode;
  int unused;
};

enum { EPI_QKV = 0, EPI_RES = 1, EPI_GELU_SHIFT = 2, EPI_W2 = 3, EPI_GELU = 4, EPI_W4 = 5 };

struct RowP { float alpha, beta, mhat, dhat; };
DI RowP row_params(float mean, float var, int mode) {
  RowP r;
  const float mh = rbf(mean);
  const float dh = rbf(__builtin_amdgcn_sqrtf(rbf(rbf(var) + 1e-5f)));
  const float i1 = __builtin_amdgcn_rcpf(dh);
  r.mhat = mh; r.dhat = dh;
  if (mode == 2) {
    const float m2 = (mean - mh) * i1;
    const float v2 = var * i1 * i1;
    const float mh2 = rbf(m2);
    const float dh2 = rbf(__builtin_amdgcn_sqrtf(rbf(rbf(v2) + 1e-5f)));
    const float i2 = __builtin_amdgcn_rcpf(dh2);
    r.alpha = i1 * i2;
    r.beta = (mh * i1 + mh2) * i2;
  } else {
    r.alpha = i1;
    r.beta = mh * i1;
  }
  return r;
}

template <int BN, int S, int KD, int EPI>
__global__ __launch_bounds__(512, 2) void gemm_k(GemmArgs g) {
  constexpr int NK = KD / 64;
  constexpr int NPF = BN / 64;
  constexpr bool HALF = (BN % 64) != 0;
  constexpr int NI = BN / 32;
  constexpr int A_BYTES = 16384;
  constexpr int STAGE = A_BYTES + BN * 128;
  constexpr int L = 4 + NPF * 2 + (HALF ? 1 : 0);
  constexpr int NTOT = (EPI == EPI_QKV) ? 3 * DM : (EPI == EPI_GELU ? FF : DM);
  constexpr bool USE_STATS = (EPI == EPI_QKV || EPI == EPI_GELU_SHIFT || EPI == EPI_W2 || EPI == EPI_GELU);
  constexpr bool FOLD = (EPI == EPI_QKV || EPI == EPI_GELU_SHIFT || EPI == EPI_GELU);
  constexpr int NRES = (EPI == EPI_RES || EPI == EPI_W2) ? 1 : (EPI == EPI_W4 ? 2 : 0);
  constexpr int OFF_STATS = S * STAGE;
  constexpr int OFF_RES = OFF_STATS;
  constexpr int OFF_WPHI = OFF_RES + NRES * 16384;
  constexpr int OFF_ROWP = OFF_WPHI + (EPI == EPI_QKV ? 4096 : 0);
  constexpr int OFF_CS = OFF_ROWP + 2048;
  constexpr int OFF_DUMP = OFF_CS + 4096;
  constexpr int NEXTRA = (USE_STATS && BN != 64 ? 8 : 0) + NRES * 4 + (EPI == EPI_QKV ? 1 : 0) + (FOLD ? 1 : 0);

  extern __shared__ __attribute__((aligned(16))) char smem[];
  const int tid = threadIdx.x, lane = tid & 63;
  const int w = __builtin_amdgcn_readfirstlane(tid >> 6);
  const bool prod = w >= 4;
  const int cw = w & 3, wr = cw >> 1, wc = cw & 1;

  const int b = blockIdx.x, xcd = b & 7, j = b >> 3;
  int rp, ct;
  if constexpr (EPI == EPI_QKV || EPI == EPI_GELU) { rp = (xcd >> 2) * 8 + (j >> 2); ct = (xcd & 3) * 4 + (j & 3); }
  else { rp = (xcd >> 1) * 4 + (j >> 3); ct = (xcd & 1) * 8 + (j & 7); }
  const int m0 = rp * 128;
  const int n0 = (EPI == EPI_QKV) ? ct * 64 : ct * BN;
  constexpr int PSTRIDE = (EPI == EPI_QKV) ? DM : 64;
  const int nf = HALF ? n0 + (ct & 1) * 32 : n0;
  const int nh = (ct & 1) ? n0 : n0 + NPF * 64;
  const int g4 = lane >> 4, q4 = (lane >> 2) & 3, pp = lane & 3;
  RowP* rowp = (RowP*)(smem + OFF_ROWP);
  const bool ln_on = USE_STATS && (g.ln_mode != 0 || EPI == EPI_W2);

  f32x4 acc[4][NI];
  TS(0);
  constexpr bool RP_CONS = (BN == 64);
  f32x2 sv[8];
  const int sr = cw * 32 + (lane >> 1), shf = lane & 1;
  auto load_stats = [&]() {
#pragma unroll
    for (int p = 0; p < 8; ++p)
      asm volatile("global_load_dwordx2 %0, %1, off" : "=v"(sv[p]) : "v"(g.stats_in + ((size_t)(shf * 8 + p) * SEQ + m0 + sr) * 2) : "memory");
  };
  float rs_s = 0.f, rs_m2 = 0.f;
  auto row_stats = [&](auto pc) {
    constexpr int piece = decltype(pc)::value;
    if constexpr (piece == 0) {
#pragma unroll
      for (int p = 0; p < 8; ++p) asm volatile("" : "+v"(sv[p]));
      float s = 0.f;
#pragma unroll
      for (int p = 0; p < 8; ++p) s += sv[p][0];
      rs_s = s + dppf<0xB1>(s);
    } else if constexpr (piece == 1) {
      const float mean = rs_s * (1.0f / 1024.0f);
      float m2 = 0.f;
#pragma unroll
      for (int p = 0; p < 8; ++p) { const float d = sv[p][0] * (1.0f / 64.0f) - mean; m2 += sv[p][1] + 64.0f * d * d; }
      rs_m2 = m2 + dppf<0xB1>(m2);
    } else {
      if (ln_on && shf == 0) rowp[sr] = row_params(rs_s * (1.0f / 1024.0f), rs_m2 * (1.0f / 1024.0f), EPI == EPI_W2 ? 1 : g.ln_mode);
      wait_lgkm0();
    }
    __builtin_amdgcn_sched_barrier(0);
  };
  if (__builtin_expect(prod, 1)) {
    const u16* a_src = g.A + (size_t)(m0 + cw * 32 + (lane >> 3)) * KD + (((lane & 7) ^ (lane >> 3)) * 8);
    const u16* b_src[2];
#pragma unroll
    for (int e = 0; e < 2; ++e) {
      const int r = lane >> 3;
      const int f = ((r >> 1) & 1) | (e << 1);
      const int c = ((((lane & 7) >> 1) ^ f) << 1) | (lane & 1);
      b_src[e] = g.B + (size_t)((cw * 2 + e) * 8 + r) * NTOT + nf + c * 8;
    }
    const u16* bh_src = nullptr;
    if constexpr (HALF) {
      const int c = ((((lane >> 1) & 1) ^ ((lane >> 5) & 1)) << 1) | (lane & 1);
      bh_src = g.B + (size_t)(cw * 16 + (lane >> 2)) * NTOT + nh + c * 8;
    }
    auto issue = [&](int kt, int slot) {
      char* base = smem + slot * STAGE;
#pragma unroll
      for (int p = 0; p < NPF; ++p)
#pragma unroll
        for (int e = 0; e < 2; ++e)
          glds16(b_src[e] + (size_t)kt * 64 * NTOT + p * PSTRIDE, base + A_BYTES + p * 8192 + (cw * 2 + e) * 1024);
      if constexpr (HALF) glds16(bh_src + (size_t)kt * 64 * NTOT, base + A_BYTES + NPF * 8192 + cw * 1024);
#pragma unroll
      for (int e = 0; e < 4; ++e) glds16(a_src + (size_t)e * 8 * KD + kt * 64, base + (cw * 4 + e) * 1024);
    };
    TS(6);
    issue(0, 0);
    issue(1, 1);
    wait_vm<L>();
    TS(1);
    bar();
    static_for<2, S>([&](auto ic) { issue(ic.value, ic.value); });
    TS(7);
    if constexpr (NRES >= 1) {
#pragma unroll
      for (int e = 0; e < 4; ++e) {
        const int r = (cw * 4 + e) * 8 + (lane >> 3);
        glds16(g.res1 + (size_t)(m0 + r) * DM + n0 + (lane & 7) * 8, smem + OFF_RES + (cw * 4 + e) * 1024);
      }
    }
    if constexpr (NRES >= 2) {
#pragma unroll
      for (int e = 0; e < 4; ++e) {
        const int r = (cw * 4 + e) * 8 + (lane >> 3);
        glds16(g.res2 + (size_t)(m0 + r) * DM + n0 + (lane & 7) * 8, smem + OFF_RES + 16384 + (cw * 4 + e) * 1024);
      }
    }
    if constexpr (EPI == EPI_QKV) glds16(g.wphi + (size_t)ct * 2048 + cw * 512 + lane * 8, smem + OFF_WPHI + cw * 1024);
    if constexpr (FOLD) {
      int col = cw * 64 + (lane & 15) * 4;
      if (col > BN - 4) col = BN - 4;
      const int gcol = (EPI == EPI_QKV) ? (col >> 6) * DM + n0 + (col & 63) : (HALF && col >= NPF * 64) ? nh + col - NPF * 64 : nf + col;
      glds16(g.colsum + (lane >> 4) * NCS + gcol, smem + OFF_CS + cw * 1024);
    }
    if constexpr (USE_STATS && !RP_CONS) load_stats();
    static_for<0, NK - 1>([&](auto ic) {
      constexpr int kt = ic.value;
      constexpr int young = (S - 2 < NK - 2 - kt) ? S - 2 : NK - 2 - kt;
      constexpr int ex = (kt + 1 <= S - 1 && kt + 1 < NK - 1) ? NEXTRA : 0;
      wait_vm<young * L + ex>();
      bar();
      if constexpr (kt + S < NK) issue(kt + S, kt % S);
      if constexpr (USE_STATS && !RP_CONS && kt >= 4 && kt < 7) row_stats(IC<kt - 4>{});
    });
    TS(2);
    bar();
  } else {
    const int fsw = ((q4 >> 1) & 1) | ((g4 & 1) << 1);
    int a_off[2], b_off[2], bh_off = 0;
#pragma unroll
    for (int k = 0; k < 2; ++k) {
      a_off[k] = (wr * 64 + (lane & 15)) * 128 + (((k * 4 + g4) ^ (lane & 7)) * 16);
      b_off[k] = A_BYTES + (8 * g4 + q4) * 128 + (((2 * k + wc) ^ fsw) * 32) + pp * 8;
    }
    if constexpr (HALF) bh_off = A_BYTES + NPF * 8192 + (8 * g4 + q4) * 64 + ((wc ^ (g4 & 1)) * 32) + pp * 8;
#pragma unroll
    for (int m = 0; m < 4; ++m)
#pragma unroll
      for (int i = 0; i < NI; ++i) acc[m][i] = (f32x4){0.f, 0.f, 0.f, 0.f};
    bf16x8 xa[2][4];
    s16x4 wlo[2][NI], whi[2][NI];
    const unsigned lbase = (unsigned)(size_t)smem;

    auto ldfrag = [&](auto setc, auto slotc, auto kc) {
      constexpr int set = decltype(setc)::value, slot = decltype(slotc)::value, k32 = decltype(kc)::value;
      const unsigned sb = lbase + slot * STAGE;
      const unsigned ba0 = sb + b_off[0], ba1 = sb + b_off[1], bah = sb + bh_off, aa = sb + a_off[k32];
      static_for<0, NI>([&](auto ic) {
        constexpr int i = decltype(ic)::value;
        if constexpr (HALF && i == NI - 1) {
          wlo[set][i] = lds_tr<k32 * 2048>(bah);
          whi[set][i] = lds_tr<k32 * 2048 + 256>(bah);
        } else {
          wlo[set][i] = lds_tr<(i >> 1) * 8192 + k32 * 4096>((i & 1) ? ba1 : ba0);
          whi[set][i] = lds_tr<(i >> 1) * 8192 + k32 * 4096 + 512>((i & 1) ? ba1 : ba0);
        }
      });
      static_for<0, 4>([&](auto mc) { constexpr int m = decltype(mc)::value; xa[set][m] = lds_b128<m * 2048>(aa); });
    };
    auto wait_set = [&](auto setc) {
      constexpr int set = decltype(setc)::value;
      asm volatile("s_waitcnt lgkmcnt(0)" : "+v"(xa[set][0]), "+v"(xa[set][1]), "+v"(xa[set][2]), "+v"(xa[set][3]) :: "memory");
#pragma unroll
      for (int i = 0; i < NI; ++i) asm volatile("" : "+v"(wlo[set][i]), "+v"(whi[set][i]));
      __builtin_amdgcn_sched_barrier(0);
    };
    auto ld1 = [&](auto setc, auto slotc, auto kc, auto rc) {
      constexpr int set = decltype(setc)::value, slot = decltype(slotc)::value, k32 = decltype(kc)::value, r = decltype(rc)::value;
      const unsigned sb = lbase + slot * STAGE;
      if constexpr (r < 2 * NI) {
        constexpr int i = r >> 1, hi = r & 1;
        s16x4 v;
        if constexpr (HALF && i == NI - 1) v = lds_tr<k32 * 2048 + hi * 256>(sb + bh_off);
        else v = lds_tr<(i >> 1) * 8192 + k32 * 4096 + hi * 512>(sb + b_off[i & 1]);
        if constexpr (hi) whi[set][i] = v; else wlo[set][i] = v;
      } else {
        constexpr int m = r - 2 * NI;
        xa[set][m] = lds_b128<m * 2048>(sb + a_off[k32]);
      }
    };
    auto mma_ld = [&](auto setc, auto ldc, auto slotc, auto kc) {
      constexpr int set = decltype(setc)::value;
      constexpr bool ld = decltype(ldc)::value != 0;
      static_for<0, 4 * NI>([&](auto tc) {
        constexpr int t = decltype(tc)::value, i = t >> 2, m = t & 3;
        acc[m][i] = mfma16(cat8(wlo[set][i], whi[set][i]), xa[set][m], acc[m][i]);
        if constexpr (ld && t < 2 * NI + 4) ld1(IC<set ^ 1>{}, slotc, kc, tc);
        __builtin_amdgcn_sched_barrier(0);
      });
    };

    const char* tp = (const char*)g.A;
    unsigned tw = 0;
    bool ton = false;
    int tks = 0;
    if (cw == 0) {
      const int pl = lane >> 4;
      const int bl = (int)(((size_t)g.B >> 7) & 7);
      int off;
      if constexpr (BN == 64) { off = ((xcd & 1) * 8 + ((3 - bl) & 7)) * 128; ton = lane < 16; }
      else if constexpr (EPI == EPI_QKV) { const int c = (3 - bl - (xcd & 3) * 4) & 7; off = pl * 2048 + ((xcd & 3) * 4 + (c & 3)) * 128; ton = c < 4 && lane < 48; }
      else { const int l0 = (xcd & 3) * 14, c = (3 - bl - l0) & 7; off = (l0 + c + pl * 8) * 128; ton = lane < (c < 6 ? 32 : 16); }
      tks = (lane >> 1) & 7;
      if (ton) tp = (const char*)g.B + (size_t)(tks * 64 + j * 2 + (lane & 1)) * NTOT * 2 + off;
      tw = 8 * 64 * NTOT * 2;
    } else if (cw == 1) {
      tks = (3 - (int)(((size_t)g.A >> 7) & 7)) & 7;
      const int row = (BN == 64) ? (xcd >> 1) * 512 + j * 16 + (lane & 15) : (xcd >> 2) * 1024 + j * 32 + (lane & 31);
      ton = lane < (BN == 64 ? 16 : 32);
      tp = (const char*)g.A + (size_t)row * KD * 2 + tks * 128;
      tw = 1024;
    }
    auto touch = [&](auto wc_) {
      constexpr int wi = decltype(wc_)::value;
      if (cw < 2) {
        if (ton && (wi > 0 || tks >= S))
          __builtin_amdgcn_global_load_lds((const unsigned*)(tp + (size_t)(wi * tw)), (unsigned*)(smem + OFF_DUMP), 4, 0, 0);
      }
      __builtin_amdgcn_sched_barrier(0);
    };
    if constexpr (USE_STATS && RP_CONS) load_stats();
    touch(IC<0>{});
    if constexpr (BN == 64) touch(IC<1>{});
    bar();
    TS(1);
    ldfrag(IC<0>{}, IC<0>{}, IC<0>{});
    static_for<0, NK>([&](auto ic) {
      constexpr int kt = ic.value;
      constexpr int slot = kt % S;
      wait_set(IC<0>{});
      if constexpr (BN != 64 && kt == 0) touch(IC<1>{});
      if constexpr (kt % 8 == 4 && (kt + 12) / 8 < NK / 8) touch(IC<(kt + 12) / 8>{});
      if constexpr (USE_STATS && RP_CONS && kt >= 6 && kt < 9) { if constexpr (kt == 6) wait_vm<0>(); row_stats(IC<kt - 6>{}); }
      mma_ld(IC<0>{}, IC<1>{}, IC<slot>{}, IC<1>{});
      wait_set(IC<1>{});
      if constexpr (kt + 1 < NK) {
        bar();
        __builtin_amdgcn_sched_barrier(0);
        mma_ld(IC<1>{}, IC<1>{}, IC<(kt + 1) % S>{}, IC<0>{});
      } else {
        mma_ld(IC<1>{}, IC<0>{}, IC<0>{}, IC<0>{});
      }
    });
    TS(2);
    bar();
  }
  TS(3);
  if constexpr (EPI == EPI_QKV) __builtin_amdgcn_s_waitcnt(0x0F70);

  constexpr int TSTR = BN * 2 + (EPI == EPI_QKV ? 32 : 16);
  char* T = smem;
  u32x4 pv[EPI == EPI_QKV ? 11 : 1];
  if (!prod) {
    float al[4] = {1.f, 1.f, 1.f, 1.f}, be[4] = {0.f, 0.f, 0.f, 0.f};
    if constexpr (FOLD) {
      if (ln_on) {
#pragma unroll
        for (int m = 0; m < 4; ++m) { const RowP r = rowp[wr * 64 + m * 16 + (lane & 15)]; al[m] = r.alpha; be[m] = r.beta; }
      }
    }
    f32x4 csv[NI];
    if constexpr (FOLD) {
#pragma unroll
      for (int i = 0; i < NI; ++i) {
        const char* cp = smem + OFF_CS + (i >> 1) * 1024 + ((2 * (i & 1) + wc) * 16 + g4 * 4) * 4;
        csv[i] = (*(const f32x4*)cp + *(const f32x4*)(cp + 256)) + (*(const f32x4*)(cp + 512) + *(const f32x4*)(cp + 768));
      }
#pragma unroll
      for (int i = 0; i < NI; ++i) csv[i] = ln_on ? csv[i] : (f32x4){0.f, 0.f, 0.f, 0.f};
      __builtin_amdgcn_sched_barrier(0);
    }
#pragma unroll
    for (int i = 0; i < NI; ++i) {
      const int nb = 2 * i + wc;
      const int ncol = nb * 16 + g4 * 4;
      const f32x4 cs = csv[i];
#pragma unroll
      for (int m = 0; m < 4; ++m) {
        u32x2 pkd;
#pragma unroll
        for (int h = 0; h < 2; ++h) {
          f32x2 v = {acc[m][i][2 * h], acc[m][i][2 * h + 1]};
          if constexpr (FOLD) v = v * al[m] - (f32x2){cs[2 * h], cs[2 * h + 1]} * be[m];
          if constexpr (EPI == EPI_GELU_SHIFT || EPI == EPI_GELU) v = gelu2(rbf2(v));
          pkd[h] = pk2(v[0], v[1]);
        }
        *(u32x2*)(T + (wr * 64 + m * 16 + (lane & 15)) * TSTR + ncol * 2) = pkd;
      }
      if constexpr (EPI == EPI_GELU) {
        if ((i & 1) && i < NI - 1) { wait_lgkm0(); bar(); }
      }
    }
  } else if constexpr (EPI == EPI_QKV) {
    if (g.res1) {
      const int t = tid - 256;
      const u16* W4 = (const u16*)g.stats_out + (size_t)b * 14336 + t * 8;
      const u16* Wo = g.res1 + (size_t)b * 4096 + t * 8;
      const u16* W2 = g.res2 + (size_t)b * 4096 + t * 8;
#pragma unroll
      for (int i = 0; i < 2; ++i) { pv[7 + i] = *(const u32x4*)(Wo + i * 2048); pv[9 + i] = *(const u32x4*)(W2 + i * 2048); }
#pragma unroll
      for (int i = 0; i < 7; ++i) pv[i] = *(const u32x4*)(W4 + i * 2048);
    }
  } else if constexpr (EPI == EPI_GELU) {
#pragma unroll
    for (int p = 0; p < NI / 2; ++p) {
      bar();
      asm volatile("" ::: "memory");
#pragma unroll
      for (int it = 0; it < 4; ++it) {
        const int r = it * 32 + ((tid - 256) >> 3), c = p * 8 + (tid & 7);
        st16_wt(g.out, SEQ * FF * 2, ((m0 + r) * FF + nf + c * 8) * 2, *(const u32x4*)(T + r * TSTR + c * 16));
      }
    }
  }
  if constexpr (EPI == EPI_QKV) { wait_lgkm0(); bar(); asm volatile("" ::: "memory"); }
  else __syncthreads();
  TS(4);
  if constexpr (EPI == EPI_GELU) {
    const int r = tid >> 2, c = (NI / 2) * 8 + (tid & 3);
    st16_wt(g.out, SEQ * FF * 2, ((m0 + r) * FF + nh + (tid & 3) * 8) * 2, *(const u32x4*)(T + r * TSTR + c * 16));
  } else if constexpr (EPI == EPI_GELU_SHIFT) {
    const int grp = n0 >> 8;
    const int sh = (grp == 0) ? 1 : (grp == 1) ? -1 : (grp == 2) ? 2 : -2;
#pragma unroll
    for (int it = 0; it < 2; ++it) {
      const int r = it * 64 + (tid >> 3), c = tid & 7;
      const u32x4 v = *(const u32x4*)(T + r * TSTR + c * 16);
      const int dr = m0 + r + sh;
      if (dr >= 0 && dr < SEQ) st16_wt(g.out, SEQ * DM * 2, (dr * DM + n0 + c * 8) * 2, v);
    }
    if (tid < 16) {
      const int zr = tid >> 3, c = tid & 7;
      const int az = sh > 0 ? sh : -sh;
      if (zr < az) {
        if (sh > 0 && rp == 0) *(u32x4*)(g.out + (size_t)zr * DM + n0 + c * 8) = (u32x4){0u, 0u, 0u, 0u};
        if (sh < 0 && rp == 15) *(u32x4*)(g.out + (size_t)(SEQ - 1 - zr) * DM + n0 + c * 8) = (u32x4){0u, 0u, 0u, 0u};
      }
    }
  } else if constexpr (EPI == EPI_RES || EPI == EPI_W2 || EPI == EPI_W4) {
#pragma unroll
    for (int it = 0; it < 2; ++it) {
      const int r = it * 64 + (tid >> 3), c = tid & 7;
      const u32x4 v = *(const u32x4*)(T + r * TSTR + c * 16);
      const u32x4 x = *(const u32x4*)(smem + OFF_RES + r * 128 + c * 16);
      f32x2 y[4];
      if constexpr (EPI == EPI_RES) {
#pragma unroll
        for (int e = 0; e < 4; ++e) y[e] = unpk(x[e]) + unpk(v[e]);
      } else if constexpr (EPI == EPI_W2) {
        const RowP rr = rowp[r];
#pragma unroll
        for (int e = 0; e < 4; ++e) y[e] = rbf2(rbf2(unpk(x[e]) - rr.mhat) * rr.alpha) + unpk(v[e]);
      } else {
        const u32x4 x1 = *(const u32x4*)(smem + OFF_RES + 16384 + r * 128 + c * 16);
#pragma unroll
        for (int e = 0; e < 4; ++e) y[e] = unpk(x[e]) + rbf2(unpk(x1[e]) + unpk(v[e]));
      }
      u32x4 o;
#pragma unroll
      for (int e = 0; e < 4; ++e) { o[e] = pk2(y[e][0], y[e][1]); y[e] = unpk(o[e]); }
      st16_wt(g.out, SEQ * DM * 2, ((m0 + r) * DM + n0 + c * 8) * 2, o);
      const f32x2 s2 = (y[0] + y[1]) + (y[2] + y[3]);
      const float s = sum8(s2[0] + s2[1]);
      const float mc = s * (1.0f / 64.0f);
      f32x2 q2 = {0.f, 0.f};
#pragma unroll
      for (int e = 0; e < 4; ++e) { const f32x2 d = y[e] - mc; q2 += d * d; }
      const float m2 = sum8(q2[0] + q2[1]);
      if (c == 0) *(f32x2*)(g.stats_out + ((size_t)ct * SEQ + m0 + r) * 2) = (f32x2){s, m2};
    }
  } else {
    constexpr int PKSTR = 160;
    char* PK = smem + 128 * TSTR;
#pragma unroll
    for (int it = 0; it < 2; ++it) {
      const int r = it * 64 + (tid >> 3), c = tid & 7;
      st16_wt(g.out, SEQ * DM * 2, ((m0 + r) * DM + n0 + c * 8) * 2, *(const u32x4*)(T + r * TSTR + c * 16));
    }
    {
      const int hh = w & 1, rq = w >> 1;
      bf16x8 wf[2];
#pragma unroll
      for (int fb = 0; fb < 2; ++fb) {
        const char* p = smem + OFF_WPHI + hh * 2048 + (8 * g4 + q4) * 64 + fb * 32 + pp * 8;
        wf[fb] = cat8(trread(p), trread(p + 256));
      }
#pragma unroll
      for (int mb = 0; mb < 2; ++mb) {
        const int row = rq * 32 + mb * 16 + (lane & 15);
        const bf16x8 xf = *(const bf16x8*)(T + row * TSTR + (64 + hh * 32 + g4 * 8) * 2);
        u32x4 pos, neg;
        feature_map(wf, xf, pos, neg);
        char* dst = PK + (hh * 128 + row) * PKSTR + g4 * 8;
        *(u32x2*)dst = (u32x2){pos[0], pos[1]}; *(u32x2*)(dst + 32) = (u32x2){pos[2], pos[3]};
        *(u32x2*)(dst + 64) = (u32x2){neg[0], neg[1]}; *(u32x2*)(dst + 96) = (u32x2){neg[2], neg[3]};
      }
    }
    __syncthreads();
    {
      const int hh = w >> 2, fa = w & 3;
      f32x4 kv[3];
#pragma unroll
      for (int d = 0; d < 3; ++d) kv[d] = (f32x4){0.f, 0.f, 0.f, 0.f};
      const short one = (short)0x3F80;
      const bf16x8 ones = {one, one, one, one, one, one, one, one};
#pragma unroll
      for (int ks = 0; ks < 4; ++ks) {
        const int row = ks * 32 + 8 * g4 + q4;
        bf16x8 vf[2];
#pragma unroll
        for (int d = 0; d < 2; ++d) {
          const char* p = T + row * TSTR + (128 + hh * 32 + d * 16) * 2 + pp * 8;
          vf[d] = cat8(trread(p), trread(p + 4 * TSTR));
        }
        const char* p = PK + (hh * 128 + row) * PKSTR + (fa * 16) * 2 + pp * 8;
        const bf16x8 pf = cat8(trread(p), trread(p + 4 * PKSTR));
        kv[0] = mfma16(pf, vf[0], kv[0]);
        kv[1] = mfma16(pf, vf[1], kv[1]);
        kv[2] = mfma16(pf, ones, kv[2]);
      }
      constexpr unsigned KVB = 16 * NH * 33 * 64 * 4;
      const int so = (rp * NH + ct * 2 + hh) * (33 * 64) + fa * 16 + g4 * 4;
      st16_wt(g.kvpart, KVB, (so + (lane & 15) * 64) * 4, __builtin_bit_cast(u32x4, kv[0]));
      st16_wt(g.kvpart, KVB, (so + (16 + (lane & 15)) * 64) * 4, __builtin_bit_cast(u32x4, kv[1]));
      if ((lane & 15) == 0) st16_wt(g.kvpart, KVB, (so + 32 * 64) * 4, __builtin_bit_cast(u32x4, kv[2]));
    }
    if (prod && g.res1) {
#pragma unroll
      for (int i = 0; i < 11; ++i) asm volatile("" ::"v"(pv[i]));
    }
  }
  TS(5);
}

__global__ __launch_bounds__(256) void ln0_k(const u16* __restrict__ x, u16* __restrict__ y, const u16* __restrict__ wqkv,
                                             const u16* __restrict__ w1, const u16* __restrict__ w3, float* __restrict__ colsum,
                                             const u16* __restrict__ wo, const u16* __restrict__ w2, const u16* __restrict__ w4) {
  if (blockIdx.x < 240) {
    __shared__ float red[16 * 128];
    const int tid = threadIdx.x;
    const int cg = blockIdx.x >> 2, q = blockIdx.x & 3;
    const u16* W; int N, c0;
    if (cg < 24) { W = wqkv; N = 3 * DM; c0 = cg * 128; }
    else if (cg < 32) { W = w1; N = DM; c0 = (cg - 24) * 128; }
    else { W = w3; N = FF; c0 = (cg - 32) * 128; }
    const int c = tid & 15, rg = tid >> 4;
    u32x4 v[16];
#pragma unroll
    for (int i = 0; i < 16; ++i) v[i] = *(const u32x4*)(W + (size_t)(q * 256 + rg + i * 16) * N + c0 + c * 8);
    f32x2 s[4] = {{0.f, 0.f}, {0.f, 0.f}, {0.f, 0.f}, {0.f, 0.f}};
#pragma unroll
    for (int i = 0; i < 16; ++i)
#pragma unroll
      for (int e = 0; e < 4; ++e) s[e] += unpk(v[i][e]);
#pragma unroll
    for (int e = 0; e < 4; ++e) *(f32x2*)(red + rg * 128 + c * 8 + 2 * e) = s[e];
    __syncthreads();
    if (tid < 128) {
      float t = 0.f;
#pragma unroll
      for (int i = 0; i < 16; ++i) t += red[i * 128 + tid];
      colsum[q * NCS + cg * 128 + tid] = t;
    }
    return;
  }
  const int lane = threadIdx.x & 63, row = (blockIdx.x - 240) * 4 + (threadIdx.x >> 6);
  const u16* src = x + (size_t)row * DM;
  const u32x4 a = *(const u32x4*)(src + lane * 8), b = *(const u32x4*)(src + 512 + lane * 8);
  float v[16];
#pragma unroll
  for (int e = 0; e < 4; ++e) { v[2 * e] = bflo(a[e]); v[2 * e + 1] = bfhi(a[e]); v[8 + 2 * e] = bflo(b[e]); v[9 + 2 * e] = bfhi(b[e]); }
  float s = 0.f;
#pragma unroll
  for (int e = 0; e < 16; ++e) s += v[e];
  s = sum64(s);
  const float mean = s * (1.0f / 1024.0f);
  float m2 = 0.f;
#pragma unroll
  for (int e = 0; e < 16; ++e) { const float d = v[e] - mean; m2 += d * d; }
  m2 = sum64(m2);
  const RowP r = row_params(mean, m2 * (1.0f / 1024.0f), 1);
  u32x4 oa, ob;
#pragma unroll
  for (int e = 0; e < 4; ++e) {
    oa[e] = pk2(rbf(v[2 * e] - r.mhat) / r.dhat, rbf(v[2 * e + 1] - r.mhat) / r.dhat);
    ob[e] = pk2(rbf(v[8 + 2 * e] - r.mhat) / r.dhat, rbf(v[9 + 2 * e] - r.mhat) / r.dhat);
  }
  u16* dst = y + (size_t)row * DM;
  *(u32x4*)(dst + lane * 8) = oa;
  *(u32x4*)(dst + 512 + lane * 8) = ob;
}

__global__ __launch_bounds__(512) void attn_o_k(const u16* __restrict__ q, const u16* __restrict__ wphi, const float* __restrict__ kvpart,
                                                u16* __restrict__ o) {
  constexpr int KS = 80;
  __shared__ __attribute__((aligned(16))) char sm[33 * KS * 2 + 8];
  const int tid = threadIdx.x, lane = tid & 63, w = tid >> 6;
  constexpr int EPI = 6;
  TS(0);
  const int h = blockIdx.x & 31, rb = blockIdx.x >> 5;
  const int g4 = lane >> 4, r0 = rb * 256 + w * 32 + (lane & 15);
  bf16x8 qf[2], wf[2];
#pragma unroll
  for (int mb = 0; mb < 2; ++mb) qf[mb] = *(const bf16x8*)(q + (size_t)(r0 + mb * 16) * DM + h * 32 + g4 * 8);
#pragma unroll
  for (int fb = 0; fb < 2; ++fb)
#pragma unroll
    for (int j = 0; j < 8; ++j) wf[fb][j] = (short)wphi[h * 1024 + (g4 * 8 + j) * 32 + fb * 16 + (lane & 15)];
  __builtin_amdgcn_sched_barrier(0);
  f32x4 kp[16], kz[16];
  const float* slab = kvpart + (size_t)h * (33 * 64) + tid * 4;
#pragma unroll
  for (int p = 0; p < 16; ++p) kp[p] = *(const f32x4*)(slab + (size_t)p * NH * (33 * 64));
  const bool zw = __builtin_amdgcn_readfirstlane(w) == 0;
  if (zw) {
#pragma unroll
    for (int p = 0; p < 16; ++p) kz[p] = *(const f32x4*)(slab + (size_t)p * NH * (33 * 64) + 2048 - (tid & 48) * 4);
  }
  __builtin_amdgcn_sched_barrier(0);
  bf16x8 pq[2][2];
#pragma unroll
  for (int mb = 0; mb < 2; ++mb) {
    u32x4 pos, neg;
    feature_map(wf, qf[mb], pos, neg);
    pq[mb][0] = __builtin_bit_cast(bf16x8, pos);
    pq[mb][1] = __builtin_bit_cast(bf16x8, neg);
  }
  __builtin_amdgcn_sched_barrier(0);
  TS(1);
  u16* kvt = (u16*)sm;
  {
    f32x4 t = kp[0];
#pragma unroll
    for (int p = 1; p < 16; ++p) t += kp[p];
    *(u32x2*)(kvt + (tid >> 4) * KS + (tid & 15) * 4) = (u32x2){pk2(t[0], t[1]), pk2(t[2], t[3])};
    if (zw) {
      f32x4 z = kz[0];
#pragma unroll
      for (int p = 1; p < 16; ++p) z += kz[p];
      if (tid < 16) *(u32x2*)(kvt + 32 * KS + tid * 4) = (u32x2){pk2(z[0], z[1]), pk2(z[2], z[3])};
    }
  }
  TS(2);
  __syncthreads();
  TS(3);
  bf16x8 kf[3][2];
#pragma unroll
  for (int db = 0; db < 3; ++db)
#pragma unroll
    for (int ks = 0; ks < 2; ++ks) {
      const int d = (db < 2) ? ((lane >> 2) & 3) * 8 + db * 4 + (lane & 3) : 32;
      const u16* p = kvt + d * KS + ks * 32 + g4 * 4;
      kf[db][ks] = cat8(*(const s16x4*)p, *(const s16x4*)(p + 16));
    }
#pragma unroll
  for (int mb = 0; mb < 2; ++mb) {
    f32x4 a[3];
#pragma unroll
    for (int db = 0; db < 3; ++db) {
      a[db] = mfma16(kf[db][0], pq[mb][0], (f32x4){0.f, 0.f, 0.f, 0.f});
      a[db] = mfma16(kf[db][1], pq[mb][1], a[db]);
    }
    const float id = __builtin_amdgcn_rcpf(rbf(rbf(a[2][0]) + 1e-6f));
    u32x4 ov;
#pragma unroll
    for (int db = 0; db < 2; ++db) {
      const f32x2 lo = rbf2((f32x2){a[db][0], a[db][1]}) * id, hi = rbf2((f32x2){a[db][2], a[db][3]}) * id;
      ov[db * 2] = pk2(lo[0], lo[1]); ov[db * 2 + 1] = pk2(hi[0], hi[1]);
    }
    st16_wt(o, SEQ * DM * 2, ((r0 + mb * 16) * DM + h * 32 + g4 * 8) * 2, ov);
  }
  TS(4);
  TS(5);
}

template <int BN, int S, int KD, int EPI> static constexpr int smem_bytes() {
  constexpr bool USE_STATS = (EPI == EPI_QKV || EPI == EPI_GELU_SHIFT || EPI == EPI_W2 || EPI == EPI_GELU);
  constexpr int NRES = (EPI == EPI_RES || EPI == EPI_W2) ? 1 : (EPI == EPI_W4 ? 2 : 0);
  return S * (16384 + BN * 128) + NRES * 16384 + (EPI == EPI_QKV ? 4096 : 0) + 2048 + 4096 + 256;
}
template <int BN, int S, int KD, int EPI> static void launch_gemm(const GemmArgs& a, hipStream_t st) {
  constexpr int sb = smem_bytes<BN, S, KD, EPI>();
  static_assert(sb <= 163840, "LDS");
  (void)hipFuncSetAttribute((const void*)gemm_k<BN, S, KD, EPI>, hipFuncAttributeMaxDynamicSharedMemorySize, sb);
  gemm_k<BN, S, KD, EPI><<<256, 512, sb, st>>>(a);
}

extern "C" void kernel_launch(void* const* d_in, const int* in_sizes, int n_in, void* d_out, int out_size, void* d_ws,
                              size_t ws_size, hipStream_t stream) {
  const u16* x0 = (const u16*)d_in[0];
  const u16* wqkv = (const u16*)d_in[1];
  const u16* wphi = (const u16*)d_in[2];
  const u16* wo = (const u16*)d_in[3];
  const u16* w1 = (const u16*)d_in[5];
  const u16* w2 = (const u16*)d_in[6];
  const u16* w3 = (const u16*)d_in[7];
  const u16* w4 = (const u16*)d_in[8];
  char* ws = (char*)d_ws;
  const size_t MB = 1 << 20;
  u16* xln0 = (u16*)(ws + 0 * MB);
  u16* phiq = (u16*)(ws + 4 * MB);
  float* kvpart = (float*)(ws + 12 * MB);
  u16* obuf = (u16*)(ws + 17 * MB);
  u16* xa = (u16*)(ws + 21 * MB);
  u16* hsh = (u16*)(ws + 25 * MB);
  u16* x1 = (u16*)(ws + 29 * MB);
  u16* gbuf = (u16*)(ws + 33 * MB);
  u16* xb = (u16*)(ws + 48 * MB);
  float* st_xa = (float*)(ws + 52 * MB);
  float* st_x1 = (float*)(ws + 53 * MB);
  float* st_xb = (float*)(ws + 54 * MB);
  float* colsum = (float*)(ws + 55 * MB);

  ln0_k<<<SEQ / 4 + 240, 256, 0, stream>>>(x0, xln0, wqkv, w1, w3, colsum, wo, w2, w4);
  for (int layer = 0; layer < 2; ++layer) {
    const u16* xin = layer == 0 ? x0 : xb;
    u16* xout = layer == 0 ? xb : (u16*)d_out;
    GemmArgs a;
    a = GemmArgs{layer == 0 ? xln0 : xb, wqkv, phiq, layer == 0 ? wo : nullptr, w2, st_xb, (float*)w4, colsum, wphi, kvpart, layer == 0 ? 0 : 1, 0};
    launch_gemm<192, 3, 1024, EPI_QKV>(a, stream);
    attn_o_k<<<256, 512, 0, stream>>>(phiq, wphi, kvpart, obuf);
    a = GemmArgs{obuf, wo, xa, xin, nullptr, nullptr, st_xa, nullptr, nullptr, nullptr, 0, 0};
    launch_gemm<64, 5, 1024, EPI_RES>(a, stream);
    a = GemmArgs{xa, w1, hsh, nullptr, nullptr, st_xa, nullptr, colsum + 3072, nullptr, nullptr, 2, 0};
    launch_gemm<64, 5, 1024, EPI_GELU_SHIFT>(a, stream);
    a = GemmArgs{hsh, w2, x1, xa, nullptr, st_xa, st_x1, nullptr, nullptr, nullptr, 1, 0};
    launch_gemm<64, 5, 1024, EPI_W2>(a, stream);
    a = GemmArgs{x1, w3, gbuf, nullptr, nullptr, st_x1, nullptr, colsum + 4096, nullptr, nullptr, 1, 0};
    launch_gemm<224, 3, 1024, EPI_GELU>(a, stream);
    a = GemmArgs{gbuf, w4, xout, xa, x1, nullptr, st_xb, nullptr, nullptr, nullptr, 0, 0};
    launch_gemm<64, 5, 3584, EPI_W4>(a, stream);
  }
}
```

```cpp
#include <hip/hip_runtime.h>
#include <hip/hip_bf16.h>
#include <stdint.h>

typedef __attribute__((ext_vector_type(8))) short bf16x8;
typedef __attribute__((ext_vector_type(4))) short s16x4;
typedef __attribute__((ext_vector_type(4))) float f32x4;
typedef __attribute__((ext_vector_type(2))) float f32x2;
typedef __attribute__((ext_vector_type(4))) unsigned u32x4;
typedef __attribute__((ext_vector_type(2))) unsigned u32x2;
typedef __attribute__((ext_vector_type(2))) __bf16 bf16x2_t;
typedef unsigned short u16;
#define LDS_AS __attribute__((address_space(3)))
#define DI __device__ __forceinline__

static constexpr int SEQ = 2048, DM = 1024, NH = 32, FF = 3584;

DI float bf2f(unsigned v) { return __uint_as_float(v << 16); }
DI float bflo(unsigned v) { return __uint_as_float(v << 16); }
DI float bfhi(unsigned v) { return __uint_as_float(v & 0xffff0000u); }
DI unsigned pk2(float a, float b) {
  f32x2 v = {a, b};
  return __builtin_bit_cast(unsigned, __builtin_convertvector(v, bf16x2_t));
}
DI float rbf(float a) { return bflo(pk2(a, a)); }
template <int CTRL> DI float dppf(float v) { return __builtin_bit_cast(float, __builtin_amdgcn_update_dpp(0, __builtin_bit_cast(int, v), CTRL, 0xf, 0xf, true)); }
DI float sum8(float v) { v += dppf<0xB1>(v); v += dppf<0x4E>(v); return v + dppf<0x141>(v); }
DI f32x2 pair16(float v) { const unsigned u = __float_as_uint(v); const auto r = __builtin_amdgcn_permlane16_swap(u, u, false, false); return (f32x2){__uint_as_float(r[0]), __uint_as_float(r[1])}; }
DI f32x2 pair32(float v) { const unsigned u = __float_as_uint(v); const auto r = __builtin_amdgcn_permlane32_swap(u, u, false, false); return (f32x2){__uint_as_float(r[0]), __uint_as_float(r[1])}; }
DI float max4g(float v) { f32x2 t = pair16(v); t = pair32(fmaxf(t[0], t[1])); return fmaxf(t[0], t[1]); }
DI float min4g(float v) { f32x2 t = pair16(v); t = pair32(fminf(t[0], t[1])); return fminf(t[0], t[1]); }
DI float sum4g(float v) { f32x2 t = pair16(v); t = pair32(t[0] + t[1]); return t[0] + t[1]; }
DI float sum64(float v) { v = sum8(v); return sum4g(v + dppf<0x140>(v)); }

template <int I> struct IC { static constexpr int value = I; };
template <int B, int E, class F> DI void static_for(F&& f) {
  if constexpr (B < E) { f(IC<B>{}); static_for<B + 1, E>(f); }
}
template <int N> DI void wait_vm() { asm volatile("s_waitcnt vmcnt(%0)" ::"n"(N) : "memory"); }
DI void wait_lgkm0() { asm volatile("s_waitcnt lgkmcnt(0)" ::: "memory"); }
DI void bar() { __builtin_amdgcn_s_barrier(); }
DI void glds16(const void* src, char* lds_dst) {
  __builtin_amdgcn_global_load_lds((const unsigned*)src, (unsigned*)lds_dst, 16, 0, 0);
}
DI s16x4 trread(const char* p) {
  return __builtin_bit_cast(s16x4, __builtin_amdgcn_ds_read_tr16_b64_v4i16((LDS_AS s16x4*)p));
}
template <int OFF> DI s16x4 lds_tr(unsigned addr) {
  s16x4 r; asm volatile("ds_read_b64_tr_b16 %0, %1 offset:%2" : "=v"(r) : "v"(addr), "n"(OFF) : "memory"); return r;
}
template <int OFF> DI bf16x8 lds_b128(unsigned addr) {
  bf16x8 r; asm volatile("ds_read_b128 %0, %1 offset:%2" : "=v"(r) : "v"(addr), "n"(OFF) : "memory"); return r;
}
DI void st16_wt(void* base, unsigned bytes, unsigned off, u32x4 v) {
  __builtin_amdgcn_raw_buffer_store_b128(v, __builtin_amdgcn_make_buffer_rsrc(base, (short)0, (int)bytes, 0x00020000), (int)off, 0, 16);
}
DI bf16x8 cat8(s16x4 lo, s16x4 hi) { return (bf16x8){lo[0], lo[1], lo[2], lo[3], hi[0], hi[1], hi[2], hi[3]}; }
DI f32x4 mfma16(bf16x8 a, bf16x8 b, f32x4 c) { return __builtin_amdgcn_mfma_f32_16x16x32_bf16(a, b, c, 0, 0, 0); }

DI f32x2 gelu2(f32x2 x) {
  const float c0 = -2.302208198f, c1 = -2.302208198f * 0.044715f;
  const f32x2 a = x * (x * x * c1 + c0);
  const f32x2 d = (f32x2){__builtin_amdgcn_exp2f(a[0]), __builtin_amdgcn_exp2f(a[1])} + 1.0f;
  return x * (f32x2){__builtin_amdgcn_rcpf(d[0]), __builtin_amdgcn_rcpf(d[1])};
}
DI f32x2 rbf2(f32x2 v) { const unsigned p = pk2(v[0], v[1]); return (f32x2){bflo(p), bfhi(p)}; }
DI f32x2 unpk(unsigned p) { return (f32x2){bflo(p), bfhi(p)}; }

DI void feature_map(const bf16x8 (&wf)[2], bf16x8 xf, u32x4& pos, u32x4& neg) {
  f32x2 p[4];
#pragma unroll
  for (int fb = 0; fb < 2; ++fb) {
    const f32x4 d = mfma16(wf[fb], xf, (f32x4){0.f, 0.f, 0.f, 0.f});
    p[fb * 2] = rbf2((f32x2){d[0], d[1]});
    p[fb * 2 + 1] = rbf2((f32x2){d[2], d[3]});
  }
  float mx = fmaxf(p[0][0], p[0][1]), mn = fminf(p[0][0], p[0][1]);
#pragma unroll
  for (int e = 1; e < 4; ++e) { mx = fmaxf(mx, fmaxf(p[e][0], p[e][1])); mn = fminf(mn, fminf(p[e][0], p[e][1])); }
  mx = max4g(mx);
  mn = min4g(mn);
  const float l2e = 1.4426950408889634f;
  f32x2 ep[4], en[4], sp2 = {0.f, 0.f}, sn2 = {0.f, 0.f};
#pragma unroll
  for (int e = 0; e < 4; ++e) {
    const f32x2 a = (p[e] - mx) * l2e, b = (mn - p[e]) * l2e;
    ep[e] = (f32x2){__builtin_amdgcn_exp2f(a[0]), __builtin_amdgcn_exp2f(a[1])}; sp2 += ep[e];
    en[e] = (f32x2){__builtin_amdgcn_exp2f(b[0]), __builtin_amdgcn_exp2f(b[1])}; sn2 += en[e];
  }
  float sp = sp2[0] + sp2[1], sn = sn2[0] + sn2[1];
  sp = sum4g(sp);
  sn = sum4g(sn);
  const float ip = __builtin_amdgcn_rcpf(sp), in_ = __builtin_amdgcn_rcpf(sn);
#pragma unroll
  for (int e = 0; e < 4; ++e) {
    const f32x2 a = ep[e] * ip, b = en[e] * in_;
    pos[e] = pk2(a[0], a[1]); neg[e] = pk2(b[0], b[1]);
  }
}

#define TS(i)
struct GemmArgs {
  const u16* A;
  const u16* B;
  u16* out;
  const u16* res1;
  const u16* res2;
  const float* stats_in;
  float* stats_out;
  const float* colsum;
  const u16* wphi;
  float* kvpart;
  int ln_mode;
  int unused;
};

enum { EPI_QKV = 0, EPI_RES = 1, EPI_GELU_SHIFT = 2, EPI_W2 = 3, EPI_GELU = 4, EPI_W4 = 5 };

struct RowP { float alpha, beta, mhat, dhat; };
DI RowP row_params(float mean, float var, int mode) {
  RowP r;
  const float mh = rbf(mean);
  const float dh = rbf(__builtin_amdgcn_sqrtf(rbf(rbf(var) + 1e-5f)));
  const float i1 = __builtin_amdgcn_rcpf(dh);
  r.mhat = mh; r.dhat = dh;
  if (mode == 2) {
    const float m2 = (mean - mh) * i1;
    const float v2 = var * i1 * i1;
    const float mh2 = rbf(m2);
    const float dh2 = rbf(__builtin_amdgcn_sqrtf(rbf(rbf(v2) + 1e-5f)));
    const float i2 = __builtin_amdgcn_rcpf(dh2);
    r.alpha = i1 * i2;
    r.beta = (mh * i1 + mh2) * i2;
  } else {
    r.alpha = i1;
    r.beta = mh * i1;
  }
  return r;
}

template <int BN, int S, int KD, int EPI>
__global__ __launch_bounds__(512, 2) void gemm_k(GemmArgs g) {
  constexpr int NK = KD / 64;
  constexpr int NPF = BN / 64;
  constexpr bool HALF = (BN % 64) != 0;
  constexpr int NI = BN / 32;
  constexpr int A_BYTES = 16384;
  constexpr int STAGE = A_BYTES + BN * 128;
  constexpr int L = 4 + NPF * 2 + (HALF ? 1 : 0);
  constexpr int NTOT = (EPI == EPI_QKV) ? 3 * DM : (EPI == EPI_GELU ? FF : DM);
  constexpr bool USE_STATS = (EPI == EPI_QKV || EPI == EPI_GELU_SHIFT || EPI == EPI_W2 || EPI == EPI_GELU);
  constexpr bool FOLD = (EPI == EPI_QKV || EPI == EPI_GELU_SHIFT || EPI == EPI_GELU);
  constexpr int NRES = (EPI == EPI_RES || EPI == EPI_W2) ? 1 : (EPI == EPI_W4 ? 2 : 0);
  constexpr int OFF_STATS = S * STAGE;
  constexpr int OFF_RES = OFF_STATS;
  constexpr int OFF_WPHI = OFF_RES + NRES * 16384;
  constexpr int OFF_ROWP = OFF_WPHI + (EPI == EPI_QKV ? 4096 : 0);
  constexpr int OFF_CS = OFF_ROWP + 2048;
  constexpr int OFF_DUMP = OFF_CS + 1024;
  constexpr int NEXTRA = (USE_STATS && BN != 64 ? 8 : 0) + NRES * 4 + (EPI == EPI_QKV ? 1 : 0) + (FOLD ? 1 : 0);

  extern __shared__ __attribute__((aligned(16))) char smem[];
  const int tid = threadIdx.x, lane = tid & 63;
  const int w = __builtin_amdgcn_readfirstlane(tid >> 6);
  const bool prod = w >= 4;
  const int cw = w & 3, wr = cw >> 1, wc = cw & 1;

  const int b = blockIdx.x, xcd = b & 7, j = b >> 3;
  int rp, ct;
  if constexpr (EPI == EPI_QKV || EPI == EPI_GELU) { rp = (xcd >> 2) * 8 + (j >> 2); ct = (xcd & 3) * 4 + (j & 3); }
  else { rp = (xcd >> 1) * 4 + (j >> 3); ct = (xcd & 1) * 8 + (j & 7); }
  const int m0 = rp * 128;
  const int n0 = (EPI == EPI_QKV) ? ct * 64 : ct * BN;
  constexpr int PSTRIDE = (EPI == EPI_QKV) ? DM : 64;
  const int nf = HALF ? n0 + (ct & 1) * 32 : n0;
  const int nh = (ct & 1) ? n0 : n0 + NPF * 64;
  const int g4 = lane >> 4, q4 = (lane >> 2) & 3, pp = lane & 3;
  RowP* rowp = (RowP*)(smem + OFF_ROWP);
  const bool ln_on = USE_STATS && (g.ln_mode != 0 || EPI == EPI_W2);

  f32x4 acc[4][NI];
  TS(0);
  constexpr bool RP_CONS = (BN == 64);
  f32x2 sv[8];
  const int sr = cw * 32 + (lane >> 1), shf = lane & 1;
  auto load_stats = [&]() {
#pragma unroll
    for (int p = 0; p < 8; ++p)
      asm volatile("global_load_dwordx2 %0, %1, off" : "=v"(sv[p]) : "v"(g.stats_in + ((size_t)(shf * 8 + p) * SEQ + m0 + sr) * 2) : "memory");
  };
  float rs_s = 0.f, rs_m2 = 0.f;
  auto row_stats = [&](auto pc) {
    constexpr int piece = decltype(pc)::value;
    if constexpr (piece == 0) {
#pragma unroll
      for (int p = 0; p < 8; ++p) asm volatile("" : "+v"(sv[p]));
      float s = 0.f;
#pragma unroll
      for (int p = 0; p < 8; ++p) s += sv[p][0];
      rs_s = s + dppf<0xB1>(s);
    } else if constexpr (piece == 1) {
      const float mean = rs_s * (1.0f / 1024.0f);
      float m2 = 0.f;
#pragma unroll
      for (int p = 0; p < 8; ++p) { const float d = sv[p][0] * (1.0f / 64.0f) - mean; m2 += sv[p][1] + 64.0f * d * d; }
      rs_m2 = m2 + dppf<0xB1>(m2);
    } else {
      if (ln_on && shf == 0) rowp[sr] = row_params(rs_s * (1.0f / 1024.0f), rs_m2 * (1.0f / 1024.0f), EPI == EPI_W2 ? 1 : g.ln_mode);
      wait_lgkm0();
    }
    __builtin_amdgcn_sched_barrier(0);
  };
  if (__builtin_expect(prod, 1)) {
    const u16* a_src = g.A + (size_t)(m0 + cw * 32 + (lane >> 3)) * KD + (((lane & 7) ^ (lane >> 3)) * 8);
    const u16* b_src[2];
#pragma unroll
    for (int e = 0; e < 2; ++e) {
      const int r = lane >> 3;
      const int f = ((r >> 1) & 1) | (e << 1);
      const int c = ((((lane & 7) >> 1) ^ f) << 1) | (lane & 1);
      b_src[e] = g.B + (size_t)((cw * 2 + e) * 8 + r) * NTOT + nf + c * 8;
    }
    const u16* bh_src = nullptr;
    if constexpr (HALF) {
      const int c = ((((lane >> 1) & 1) ^ ((lane >> 5) & 1)) << 1) | (lane & 1);
      bh_src = g.B + (size_t)(cw * 16 + (lane >> 2)) * NTOT + nh + c * 8;
    }
    auto issue = [&](int kt, int slot) {
      char* base = smem + slot * STAGE;
#pragma unroll
      for (int p = 0; p < NPF; ++p)
#pragma unroll
        for (int e = 0; e < 2; ++e)
          glds16(b_src[e] + (size_t)kt * 64 * NTOT + p * PSTRIDE, base + A_BYTES + p * 8192 + (cw * 2 + e) * 1024);
      if constexpr (HALF) glds16(bh_src + (size_t)kt * 64 * NTOT, base + A_BYTES + NPF * 8192 + cw * 1024);
#pragma unroll
      for (int e = 0; e < 4; ++e) glds16(a_src + (size_t)e * 8 * KD + kt * 64, base + (cw * 4 + e) * 1024);
    };
    TS(6);
    issue(0, 0);
    issue(1, 1);
    wait_vm<L>();
    TS(1);
    bar();
    static_for<2, S>([&](auto ic) { issue(ic.value, ic.value); });
    TS(7);
    if constexpr (NRES >= 1) {
#pragma unroll
      for (int e = 0; e < 4; ++e) {
        const int r = (cw * 4 + e) * 8 + (lane >> 3);
        glds16(g.res1 + (size_t)(m0 + r) * DM + n0 + (lane & 7) * 8, smem + OFF_RES + (cw * 4 + e) * 1024);
      }
    }
    if constexpr (NRES >= 2) {
#pragma unroll
      for (int e = 0; e < 4; ++e) {
        const int r = (cw * 4 + e) * 8 + (lane >> 3);
        glds16(g.res2 + (size_t)(m0 + r) * DM + n0 + (lane & 7) * 8, smem + OFF_RES + 16384 + (cw * 4 + e) * 1024);
      }
    }
    if constexpr (EPI == EPI_QKV) glds16(g.wphi + (size_t)ct * 2048 + cw * 512 + lane * 8, smem + OFF_WPHI + cw * 1024);
    if constexpr (FOLD) {
      int col = cw * 64 + (lane & 15) * 4;
      if (col > BN - 4) col = BN - 4;
      const int gcol = (EPI == EPI_QKV) ? (col >> 6) * DM + n0 + (col & 63) : (HALF && col >= NPF * 64) ? nh + col - NPF * 64 : nf + col;
      if (lane < 16) glds16(g.colsum + gcol, smem + OFF_CS + cw * 256);
    }
    if constexpr (USE_STATS && !RP_CONS) load_stats();
    static_for<0, NK - 1>([&](auto ic) {
      constexpr int kt = ic.value;
      constexpr int young = (S - 2 < NK - 2 - kt) ? S - 2 : NK - 2 - kt;
      constexpr int ex = (kt + 1 <= S - 1 && kt + 1 < NK - 1) ? NEXTRA : 0;
      wait_vm<young * L + ex>();
      bar();
      if constexpr (kt + S < NK) issue(kt + S, kt % S);
      if constexpr (USE_STATS && !RP_CONS && kt >= 4 && kt < 7) row_stats(IC<kt - 4>{});
    });
    TS(2);
    bar();
  } else {
    const int fsw = ((q4 >> 1) & 1) | ((g4 & 1) << 1);
    int a_off[2], b_off[2], bh_off = 0;
#pragma unroll
    for (int k = 0; k < 2; ++k) {
      a_off[k] = (wr * 64 + (lane & 15)) * 128 + (((k * 4 + g4) ^ (lane & 7)) * 16);
      b_off[k] = A_BYTES + (8 * g4 + q4) * 128 + (((2 * k + wc) ^ fsw) * 32) + pp * 8;
    }
    if constexpr (HALF) bh_off = A_BYTES + NPF * 8192 + (8 * g4 + q4) * 64 + ((wc ^ (g4 & 1)) * 32) + pp * 8;
#pragma unroll
    for (int m = 0; m < 4; ++m)
#pragma unroll
      for (int i = 0; i < NI; ++i) acc[m][i] = (f32x4){0.f, 0.f, 0.f, 0.f};
    bf16x8 xa[2][4];
    s16x4 wlo[2][NI], whi[2][NI];
    const unsigned lbase = (unsigned)(size_t)smem;

    auto ldfrag = [&](auto setc, auto slotc, auto kc) {
      constexpr int set = decltype(setc)::value, slot = decltype(slotc)::value, k32 = decltype(kc)::value;
      const unsigned sb = lbase + slot * STAGE;
      const unsigned ba0 = sb + b_off[0], ba1 = sb + b_off[1], bah = sb + bh_off, aa = sb + a_off[k32];
      static_for<0, NI>([&](auto ic) {
        constexpr int i = decltype(ic)::value;
        if constexpr (HALF && i == NI - 1) {
          wlo[set][i] = lds_tr<k32 * 2048>(bah);
          whi[set][i] = lds_tr<k32 * 2048 + 256>(bah);
        } else {
          wlo[set][i] = lds_tr<(i >> 1) * 8192 + k32 * 4096>((i & 1) ? ba1 : ba0);
          whi[set][i] = lds_tr<(i >> 1) * 8192 + k32 * 4096 + 512>((i & 1) ? ba1 : ba0);
        }
      });
      static_for<0, 4>([&](auto mc) { constexpr int m = decltype(mc)::value; xa[set][m] = lds_b128<m * 2048>(aa); });
    };
    auto wait_set = [&](auto setc) {
      constexpr int set = decltype(setc)::value;
      asm volatile("s_waitcnt lgkmcnt(0)" : "+v"(xa[set][0]), "+v"(xa[set][1]), "+v"(xa[set][2]), "+v"(xa[set][3]) :: "memory");
#pragma unroll
      for (int i = 0; i < NI; ++i) asm volatile("" : "+v"(wlo[set][i]), "+v"(whi[set][i]));
      __builtin_amdgcn_sched_barrier(0);
    };
    auto ld1 = [&](auto setc, auto slotc, auto kc, auto rc) {
      constexpr int set = decltype(setc)::value, slot = decltype(slotc)::value, k32 = decltype(kc)::value, r = decltype(rc)::value;
      const unsigned sb = lbase + slot * STAGE;
      if constexpr (r < 2 * NI) {
        constexpr int i = r >> 1, hi = r & 1;
        s16x4 v;
        if constexpr (HALF && i == NI - 1) v = lds_tr<k32 * 2048 + hi * 256>(sb + bh_off);
        else v = lds_tr<(i >> 1) * 8192 + k32 * 4096 + hi * 512>(sb + b_off[i & 1]);
        if constexpr (hi) whi[set][i] = v; else wlo[set][i] = v;
      } else {
        constexpr int m = r - 2 * NI;
        xa[set][m] = lds_b128<m * 2048>(sb + a_off[k32]);
      }
    };
    auto mma_ld = [&](auto setc, auto ldc, auto slotc, auto kc) {
      constexpr int set = decltype(setc)::value;
      constexpr bool ld = decltype(ldc)::value != 0;
      static_for<0, 4 * NI>([&](auto tc) {
        constexpr int t = decltype(tc)::value, i = t >> 2, m = t & 3;
        acc[m][i] = mfma16(cat8(wlo[set][i], whi[set][i]), xa[set][m], acc[m][i]);
        if constexpr (ld && t < 2 * NI + 4) ld1(IC<set ^ 1>{}, slotc, kc, tc);
        __builtin_amdgcn_sched_barrier(0);
      });
    };

    const char* tp = (const char*)g.A;
    unsigned tw = 0;
    bool ton = false;
    int tks = 0;
    if (cw == 0) {
      const int pl = lane >> 4;
      const int bl = (int)(((size_t)g.B >> 7) & 7);
      int off;
      if constexpr (BN == 64) { off = ((xcd & 1) * 8 + ((3 - bl) & 7)) * 128; ton = lane < 16; }
      else if constexpr (EPI == EPI_QKV) { const int c = (3 - bl - (xcd & 3) * 4) & 7; off = pl * 2048 + ((xcd & 3) * 4 + (c & 3)) * 128; ton = c < 4 && lane < 48; }
      else { const int l0 = (xcd & 3) * 14, c = (3 - bl - l0) & 7; off = (l0 + c + pl * 8) * 128; ton = lane < (c < 6 ? 32 : 16); }
      tks = (lane >> 1) & 7;
      if (ton) tp = (const char*)g.B + (size_t)(tks * 64 + j * 2 + (lane & 1)) * NTOT * 2 + off;
      tw = 8 * 64 * NTOT * 2;
    } else if (cw == 1) {
      tks = (3 - (int)(((size_t)g.A >> 7) & 7)) & 7;
      const int row = (BN == 64) ? (xcd >> 1) * 512 + j * 16 + (lane & 15) : (xcd >> 2) * 1024 + j * 32 + (lane & 31);
      ton = lane < (BN == 64 ? 16 : 32);
      tp = (const char*)g.A + (size_t)row * KD * 2 + tks * 128;
      tw = 1024;
    }
    auto touch = [&](auto wc_) {
      constexpr int wi = decltype(wc_)::value;
      if (cw < 2) {
        if (ton && (wi > 0 || tks >= S))
          __builtin_amdgcn_global_load_lds((const unsigned*)(tp + (size_t)(wi * tw)), (unsigned*)(smem + OFF_DUMP), 4, 0, 0);
      }
      __builtin_amdgcn_sched_barrier(0);
    };
    if constexpr (USE_STATS && RP_CONS) load_stats();
    touch(IC<0>{});
    if constexpr (BN == 64) touch(IC<1>{});
    bar();
    TS(1);
    ldfrag(IC<0>{}, IC<0>{}, IC<0>{});
    static_for<0, NK>([&](auto ic) {
      constexpr int kt = ic.value;
      constexpr int slot = kt % S;
      wait_set(IC<0>{});
      if constexpr (BN != 64 && kt == 0) touch(IC<1>{});
      if constexpr (kt % 8 == 4 && (kt + 12) / 8 < NK / 8) touch(IC<(kt + 12) / 8>{});
      if constexpr (USE_STATS && RP_CONS && kt >= 6 && kt < 9) { if constexpr (kt == 6) wait_vm<0>(); row_stats(IC<kt - 6>{}); }
      mma_ld(IC<0>{}, IC<1>{}, IC<slot>{}, IC<1>{});
      wait_set(IC<1>{});
      if constexpr (kt + 1 < NK) {
        bar();
        __builtin_amdgcn_sched_barrier(0);
        mma_ld(IC<1>{}, IC<1>{}, IC<(kt + 1) % S>{}, IC<0>{});
      } else {
        mma_ld(IC<1>{}, IC<0>{}, IC<0>{}, IC<0>{});
      }
    });
    TS(2);
    bar();
  }
  TS(3);
  if constexpr (EPI == EPI_QKV) __builtin_amdgcn_s_waitcnt(0x0F70);

  constexpr int TSTR = BN * 2 + (EPI == EPI_QKV ? 32 : 16);
  char* T = smem;
  u32x4 pv[EPI == EPI_QKV ? 11 : 1];
  if (!prod) {
    float al[4] = {1.f, 1.f, 1.f, 1.f}, be[4] = {0.f, 0.f, 0.f, 0.f};
    if constexpr (FOLD) {
      if (ln_on) {
#pragma unroll
        for (int m = 0; m < 4; ++m) { const RowP r = rowp[wr * 64 + m * 16 + (lane & 15)]; al[m] = r.alpha; be[m] = r.beta; }
      }
    }
    f32x4 csv[NI];
    if constexpr (FOLD) {
#pragma unroll
      for (int i = 0; i < NI; ++i) csv[i] = *(const f32x4*)(smem + OFF_CS + ((2 * i + wc) * 16 + g4 * 4) * 4);
#pragma unroll
      for (int i = 0; i < NI; ++i) csv[i] = ln_on ? csv[i] : (f32x4){0.f, 0.f, 0.f, 0.f};
      __builtin_amdgcn_sched_barrier(0);
    }
#pragma unroll
    for (int i = 0; i < NI; ++i) {
      const int nb = 2 * i + wc;
      const int ncol = nb * 16 + g4 * 4;
      const f32x4 cs = csv[i];
#pragma unroll
      for (int m = 0; m < 4; ++m) {
        u32x2 pkd;
#pragma unroll
        for (int h = 0; h < 2; ++h) {
          f32x2 v = {acc[m][i][2 * h], acc[m][i][2 * h + 1]};
          if constexpr (FOLD) v = v * al[m] - (f32x2){cs[2 * h], cs[2 * h + 1]} * be[m];
          pkd[h] = pk2(v[0], v[1]);
        }
        *(u32x2*)(T + (wr * 64 + m * 16 + (lane & 15)) * TSTR + ncol * 2) = pkd;
      }
    }
  } else if constexpr (EPI == EPI_QKV) {
    if (g.res1) {
      const int t = tid - 256;
      const u16* W4 = (const u16*)g.stats_out + (size_t)b * 14336 + t * 8;
      const u16* Wo = g.res1 + (size_t)b * 4096 + t * 8;
      const u16* W2 = g.res2 + (size_t)b * 4096 + t * 8;
#pragma unroll
      for (int i = 0; i < 2; ++i) { pv[7 + i] = *(const u32x4*)(Wo + i * 2048); pv[9 + i] = *(const u32x4*)(W2 + i * 2048); }
#pragma unroll
      for (int i = 0; i < 7; ++i) pv[i] = *(const u32x4*)(W4 + i * 2048);
    }
  }
  if constexpr (EPI == EPI_QKV) { wait_lgkm0(); bar(); asm volatile("" ::: "memory"); }
  else __syncthreads();
  TS(4);
  auto gelu8 = [&](u32x4 v) {
    u32x4 o;
#pragma unroll
    for (int e = 0; e < 4; ++e) { const f32x2 y = gelu2(unpk(v[e])); o[e] = pk2(y[0], y[1]); }
    return o;
  };
  if constexpr (EPI == EPI_GELU) {
    u32x4 v[NI];
#pragma unroll
    for (int it = 0; it < NI; ++it) {
      const int r = it < NI - 1 ? (it & 1) * 64 + (tid >> 3) : tid >> 2, c = it < NI - 1 ? (it >> 1) * 8 + (tid & 7) : (NI / 2) * 8 + (tid & 3);
      v[it] = *(const u32x4*)(T + r * TSTR + c * 16);
    }
#pragma unroll
    for (int it = 0; it < NI; ++it) {
      const int r = it < NI - 1 ? (it & 1) * 64 + (tid >> 3) : tid >> 2, col = it < NI - 1 ? nf + (it >> 1) * 64 + (tid & 7) * 8 : nh + (tid & 3) * 8;
      st16_wt(g.out, SEQ * FF * 2, ((m0 + r) * FF + col) * 2, gelu8(v[it]));
    }
  } else if constexpr (EPI == EPI_GELU_SHIFT) {
    const int grp = n0 >> 8;
    const int sh = (grp == 0) ? 1 : (grp == 1) ? -1 : (grp == 2) ? 2 : -2;
#pragma unroll
    for (int it = 0; it < 2; ++it) {
      const int r = it * 64 + (tid >> 3), c = tid & 7;
      const u32x4 v = gelu8(*(const u32x4*)(T + r * TSTR + c * 16));
      const int dr = m0 + r + sh;
      if (dr >= 0 && dr < SEQ) st16_wt(g.out, SEQ * DM * 2, (dr * DM + n0 + c * 8) * 2, v);
    }
    if (tid < 16) {
      const int zr = tid >> 3, c = tid & 7;
      const int az = sh > 0 ? sh : -sh;
      if (zr < az) {
        if (sh > 0 && rp == 0) *(u32x4*)(g.out + (size_t)zr * DM + n0 + c * 8) = (u32x4){0u, 0u, 0u, 0u};
        if (sh < 0 && rp == 15) *(u32x4*)(g.out + (size_t)(SEQ - 1 - zr) * DM + n0 + c * 8) = (u32x4){0u, 0u, 0u, 0u};
      }
    }
  } else if constexpr (EPI == EPI_RES || EPI == EPI_W2 || EPI == EPI_W4) {
#pragma unroll
    for (int it = 0; it < 2; ++it) {
      const int r = it * 64 + (tid >> 3), c = tid & 7;
      const u32x4 v = *(const u32x4*)(T + r * TSTR + c * 16);
      const u32x4 x = *(const u32x4*)(smem + OFF_RES + r * 128 + c * 16);
      f32x2 y[4];
      if constexpr (EPI == EPI_RES) {
#pragma unroll
        for (int e = 0; e < 4; ++e) y[e] = unpk(x[e]) + unpk(v[e]);
      } else if constexpr (EPI == EPI_W2) {
        const RowP rr = rowp[r];
#pragma unroll
        for (int e = 0; e < 4; ++e) y[e] = rbf2(rbf2(unpk(x[e]) - rr.mhat) * rr.alpha) + unpk(v[e]);
      } else {
        const u32x4 x1 = *(const u32x4*)(smem + OFF_RES + 16384 + r * 128 + c * 16);
#pragma unroll
        for (int e = 0; e < 4; ++e) y[e] = unpk(x[e]) + rbf2(unpk(x1[e]) + unpk(v[e]));
      }
      u32x4 o;
#pragma unroll
      for (int e = 0; e < 4; ++e) { o[e] = pk2(y[e][0], y[e][1]); y[e] = unpk(o[e]); }
      st16_wt(g.out, SEQ * DM * 2, ((m0 + r) * DM + n0 + c * 8) * 2, o);
      const f32x2 s2 = (y[0] + y[1]) + (y[2] + y[3]);
      const float s = sum8(s2[0] + s2[1]);
      const float mc = s * (1.0f / 64.0f);
      f32x2 q2 = {0.f, 0.f};
#pragma unroll
      for (int e = 0; e < 4; ++e) { const f32x2 d = y[e] - mc; q2 += d * d; }
      const float m2 = sum8(q2[0] + q2[1]);
      if (c == 0) *(f32x2*)(g.stats_out + ((size_t)ct * SEQ + m0 + r) * 2) = (f32x2){s, m2};
    }
  } else {
    constexpr int PKSTR = 160;
    char* PK = smem + 128 * TSTR;
#pragma unroll
    for (int it = 0; it < 2; ++it) {
      const int r = it * 64 + (tid >> 3), c = tid & 7;
      st16_wt(g.out, SEQ * DM * 2, ((m0 + r) * DM + n0 + c * 8) * 2, *(const u32x4*)(T + r * TSTR + c * 16));
    }
    {
      const int hh = w & 1, rq = w >> 1;
      bf16x8 wf[2];
#pragma unroll
      for (int fb = 0; fb < 2; ++fb) {
        const char* p = smem + OFF_WPHI + hh * 2048 + (8 * g4 + q4) * 64 + fb * 32 + pp * 8;
        wf[fb] = cat8(trread(p), trread(p + 256));
      }
#pragma unroll
      for (int mb = 0; mb < 2; ++mb) {
        const int row = rq * 32 + mb * 16 + (lane & 15);
        const bf16x8 xf = *(const bf16x8*)(T + row * TSTR + (64 + hh * 32 + g4 * 8) * 2);
        u32x4 pos, neg;
        feature_map(wf, xf, pos, neg);
        char* dst = PK + (hh * 128 + row) * PKSTR + g4 * 8;
        *(u32x2*)dst = (u32x2){pos[0], pos[1]}; *(u32x2*)(dst + 32) = (u32x2){pos[2], pos[3]};
        *(u32x2*)(dst + 64) = (u32x2){neg[0], neg[1]}; *(u32x2*)(dst + 96) = (u32x2){neg[2], neg[3]};
      }
    }
    __syncthreads();
    {
      const int hh = w >> 2, fa = w & 3;
      f32x4 kv[3];
#pragma unroll
      for (int d = 0; d < 3; ++d) kv[d] = (f32x4){0.f, 0.f, 0.f, 0.f};
      const short one = (short)0x3F80;
      const bf16x8 ones = {one, one, one, one, one, one, one, one};
#pragma unroll
      for (int ks = 0; ks < 4; ++ks) {
        const int row = ks * 32 + 8 * g4 + q4;
        bf16x8 vf[2];
#pragma unroll
        for (int d = 0; d < 2; ++d) {
          const char* p = T + row * TSTR + (128 + hh * 32 + d * 16) * 2 + pp * 8;
          vf[d] = cat8(trread(p), trread(p + 4 * TSTR));
        }
        const char* p = PK + (hh * 128 + row) * PKSTR + (fa * 16) * 2 + pp * 8;
        const bf16x8 pf = cat8(trread(p), trread(p + 4 * PKSTR));
        kv[0] = mfma16(pf, vf[0], kv[0]);
        kv[1] = mfma16(pf, vf[1], kv[1]);
        kv[2] = mfma16(pf, ones, kv[2]);
      }
      constexpr unsigned KVB = 16 * NH * 33 * 64 * 4;
      const int so = (rp * NH + ct * 2 + hh) * (33 * 64) + fa * 16 + g4 * 4;
      st16_wt(g.kvpart, KVB, (so + (lane & 15) * 64) * 4, __builtin_bit_cast(u32x4, kv[0]));
      st16_wt(g.kvpart, KVB, (so + (16 + (lane & 15)) * 64) * 4, __builtin_bit_cast(u32x4, kv[1]));
      if ((lane & 15) == 0) st16_wt(g.kvpart, KVB, (so + 32 * 64) * 4, __builtin_bit_cast(u32x4, kv[2]));
    }
    if (prod && g.res1) {
#pragma unroll
      for (int i = 0; i < 11; ++i) asm volatile("" ::"v"(pv[i]));
    }
  }
  TS(5);
}

__global__ __launch_bounds__(256) void ln0_k(const u16* __restrict__ x, u16* __restrict__ y, const u16* __restrict__ wqkv,
                                             const u16* __restrict__ w1, const u16* __restrict__ w3, float* __restrict__ colsum,
                                             const u16* __restrict__ wo, const u16* __restrict__ w2, const u16* __restrict__ w4) {
  if (blockIdx.x >= SEQ / 4 + 120) {
    const int t = blockIdx.x - (SEQ / 4 + 120);
    const u16* W = t < 32 ? wo + (size_t)t * 32768 : (t < 64 ? w2 + (size_t)(t - 32) * 32768 : w4 + (size_t)(t - 64) * 32768);
#pragma unroll
    for (int i = 0; i < 16; ++i) {
      const u32x4 v = *(const u32x4*)(W + (i * 256 + threadIdx.x) * 8);
      asm volatile("" ::"v"(v));
    }
    return;
  }
  if (blockIdx.x >= SEQ / 4) {
    __shared__ float red[32 * 64];
    const int tid = threadIdx.x;
    const int cg = blockIdx.x - SEQ / 4;
    const u16* W; int N, c0;
    if (cg < 48) { W = wqkv; N = 3 * DM; c0 = cg * 64; }
    else if (cg < 64) { W = w1; N = DM; c0 = (cg - 48) * 64; }
    else { W = w3; N = FF; c0 = (cg - 64) * 64; }
    const int c = tid & 7, rg = tid >> 3;
    f32x2 s[4] = {{0.f, 0.f}, {0.f, 0.f}, {0.f, 0.f}, {0.f, 0.f}};
#pragma unroll 16
    for (int i = 0; i < 32; ++i) {
      const u32x4 v = *(const u32x4*)(W + (size_t)(rg + i * 32) * N + c0 + c * 8);
#pragma unroll
      for (int e = 0; e < 4; ++e) s[e] += unpk(v[e]);
    }
#pragma unroll
    for (int e = 0; e < 4; ++e) *(f32x2*)(red + rg * 64 + c * 8 + 2 * e) = s[e];
    __syncthreads();
    if (tid < 64) {
      float t = 0.f;
#pragma unroll
      for (int i = 0; i < 32; ++i) t += red[i * 64 + tid];
      colsum[cg * 64 + tid] = t;
    }
    return;
  }
  const int lane = threadIdx.x & 63, row = blockIdx.x * 4 + (threadIdx.x >> 6);
  const u16* src = x + (size_t)row * DM;
  const u32x4 a = *(const u32x4*)(src + lane * 8), b = *(const u32x4*)(src + 512 + lane * 8);
  float v[16];
#pragma unroll
  for (int e = 0; e < 4; ++e) { v[2 * e] = bflo(a[e]); v[2 * e + 1] = bfhi(a[e]); v[8 + 2 * e] = bflo(b[e]); v[9 + 2 * e] = bfhi(b[e]); }
  float s = 0.f;
#pragma unroll
  for (int e = 0; e < 16; ++e) s += v[e];
  s = sum64(s);
  const float mean = s * (1.0f / 1024.0f);
  float m2 = 0.f;
#pragma unroll
  for (int e = 0; e < 16; ++e) { const float d = v[e] - mean; m2 += d * d; }
  m2 = sum64(m2);
  const RowP r = row_params(mean, m2 * (1.0f / 1024.0f), 1);
  u32x4 oa, ob;
#pragma unroll
  for (int e = 0; e < 4; ++e) {
    oa[e] = pk2(rbf(v[2 * e] - r.mhat) / r.dhat, rbf(v[2 * e + 1] - r.mhat) / r.dhat);
    ob[e] = pk2(rbf(v[8 + 2 * e] - r.mhat) / r.dhat, rbf(v[9 + 2 * e] - r.mhat) / r.dhat);
  }
  u16* dst = y + (size_t)row * DM;
  *(u32x4*)(dst + lane * 8) = oa;
  *(u32x4*)(dst + 512 + lane * 8) = ob;
}

__global__ __launch_bounds__(512) void attn_o_k(const u16* __restrict__ q, const u16* __restrict__ wphi, const float* __restrict__ kvpart,
                                                u16* __restrict__ o) {
  constexpr int KS = 80;
  __shared__ __attribute__((aligned(16))) char sm[33 * KS * 2 + 8];
  const int tid = threadIdx.x, lane = tid & 63, w = tid >> 6;
  constexpr int EPI = 6;
  TS(0);
  const int h = blockIdx.x & 31, rb = blockIdx.x >> 5;
  const int g4 = lane >> 4, r0 = rb * 256 + w * 32 + (lane & 15);
  bf16x8 qf[2], wf[2];
#pragma unroll
  for (int mb = 0; mb < 2; ++mb) qf[mb] = *(const bf16x8*)(q + (size_t)(r0 + mb * 16) * DM + h * 32 + g4 * 8);
#pragma unroll
  for (int fb = 0; fb < 2; ++fb)
#pragma unroll
    for (int j = 0; j < 8; ++j) wf[fb][j] = (short)wphi[h * 1024 + (g4 * 8 + j) * 32 + fb * 16 + (lane & 15)];
  __builtin_amdgcn_sched_barrier(0);
  f32x4 kp[16], kz[16];
  const float* slab = kvpart + (size_t)h * (33 * 64) + tid * 4;
#pragma unroll
  for (int p = 0; p < 16; ++p) kp[p] = *(const f32x4*)(slab + (size_t)p * NH * (33 * 64));
  const bool zw = __builtin_amdgcn_readfirstlane(w) == 0;
  if (zw) {
#pragma unroll
    for (int p = 0; p < 16; ++p) kz[p] = *(const f32x4*)(slab + (size_t)p * NH * (33 * 64) + 2048 - (tid & 48) * 4);
  }
  __builtin_amdgcn_sched_barrier(0);
  bf16x8 pq[2][2];
#pragma unroll
  for (int mb = 0; mb < 2; ++mb) {
    u32x4 pos, neg;
    feature_map(wf, qf[mb], pos, neg);
    pq[mb][0] = __builtin_bit_cast(bf16x8, pos);
    pq[mb][1] = __builtin_bit_cast(bf16x8, neg);
  }
  __builtin_amdgcn_sched_barrier(0);
  TS(1);
  u16* kvt = (u16*)sm;
  {
    f32x4 t = kp[0];
#pragma unroll
    for (int p = 1; p < 16; ++p) t += kp[p];
    *(u32x2*)(kvt + (tid >> 4) * KS + (tid & 15) * 4) = (u32x2){pk2(t[0], t[1]), pk2(t[2], t[3])};
    if (zw) {
      f32x4 z = kz[0];
#pragma unroll
      for (int p = 1; p < 16; ++p) z += kz[p];
      if (tid < 16) *(u32x2*)(kvt + 32 * KS + tid * 4) = (u32x2){pk2(z[0], z[1]), pk2(z[2], z[3])};
    }
  }
  TS(2);
  __syncthreads();
  TS(3);
  bf16x8 kf[3][2];
#pragma unroll
  for (int db = 0; db < 3; ++db)
#pragma unroll
    for (int ks = 0; ks < 2; ++ks) {
      const int d = (db < 2) ? ((lane >> 2) & 3) * 8 + db * 4 + (lane & 3) : 32;
      const u16* p = kvt + d * KS + ks * 32 + g4 * 4;
      kf[db][ks] = cat8(*(const s16x4*)p, *(const s16x4*)(p + 16));
    }
#pragma unroll
  for (int mb = 0; mb < 2; ++mb) {
    f32x4 a[3];
#pragma unroll
    for (int db = 0; db < 3; ++db) {
      a[db] = mfma16(kf[db][0], pq[mb][0], (f32x4){0.f, 0.f, 0.f, 0.f});
      a[db] = mfma16(kf[db][1], pq[mb][1], a[db]);
    }
    const float id = __builtin_amdgcn_rcpf(rbf(rbf(a[2][0]) + 1e-6f));
    u32x4 ov;
#pragma unroll
    for (int db = 0; db < 2; ++db) {
      const f32x2 lo = rbf2((f32x2){a[db][0], a[db][1]}) * id, hi = rbf2((f32x2){a[db][2], a[db][3]}) * id;
      ov[db * 2] = pk2(lo[0], lo[1]); ov[db * 2 + 1] = pk2(hi[0], hi[1]);
    }
    st16_wt(o, SEQ * DM * 2, ((r0 + mb * 16) * DM + h * 32 + g4 * 8) * 2, ov);
  }
  TS(4);
  TS(5);
}

template <int BN, int S, int KD, int EPI> static constexpr int smem_bytes() {
  constexpr bool USE_STATS = (EPI == EPI_QKV || EPI == EPI_GELU_SHIFT || EPI == EPI_W2 || EPI == EPI_GELU);
  constexpr int NRES = (EPI == EPI_RES || EPI == EPI_W2) ? 1 : (EPI == EPI_W4 ? 2 : 0);
  return S * (16384 + BN * 128) + NRES * 16384 + (EPI == EPI_QKV ? 4096 : 0) + 2048 + 1024 + 256;
}
template <int BN, int S, int KD, int EPI> static void launch_gemm(const GemmArgs& a, hipStream_t st) {
  constexpr int sb = smem_bytes<BN, S, KD, EPI>();
  static_assert(sb <= 163840, "LDS");
  (void)hipFuncSetAttribute((const void*)gemm_k<BN, S, KD, EPI>, hipFuncAttributeMaxDynamicSharedMemorySize, sb);
  gemm_k<BN, S, KD, EPI><<<256, 512, sb, st>>>(a);
}

extern "C" void kernel_launch(void* const* d_in, const int* in_sizes, int n_in, void* d_out, int out_size, void* d_ws,
                              size_t ws_size, hipStream_t stream) {
  const u16* x0 = (const u16*)d_in[0];
  const u16* wqkv = (const u16*)d_in[1];
  const u16* wphi = (const u16*)d_in[2];
  const u16* wo = (const u16*)d_in[3];
  const u16* w1 = (const u16*)d_in[5];
  const u16* w2 = (const u16*)d_in[6];
  const u16* w3 = (const u16*)d_in[7];
  const u16* w4 = (const u16*)d_in[8];
  char* ws = (char*)d_ws;
  const size_t MB = 1 << 20;
  u16* xln0 = (u16*)(ws + 0 * MB);
  u16* phiq = (u16*)(ws + 4 * MB);
  float* kvpart = (float*)(ws + 12 * MB);
  u16* obuf = (u16*)(ws + 17 * MB);
  u16* xa = (u16*)(ws + 21 * MB);
  u16* hsh = (u16*)(ws + 25 * MB);
  u16* x1 = (u16*)(ws + 29 * MB);
  u16* gbuf = (u16*)(ws + 33 * MB);
  u16* xb = (u16*)(ws + 48 * MB);
  float* st_xa = (float*)(ws + 52 * MB);
  float* st_x1 = (float*)(ws + 53 * MB);
  float* st_xb = (float*)(ws + 54 * MB);
  float* colsum = (float*)(ws + 55 * MB);

  ln0_k<<<SEQ / 4 + 120, 256, 0, stream>>>(x0, xln0, wqkv, w1, w3, colsum, wo, w2, w4);
  for (int layer = 0; layer < 2; ++layer) {
    const u16* xin = layer == 0 ? x0 : xb;
    u16* xout = layer == 0 ? xb : (u16*)d_out;
    GemmArgs a;
    a = GemmArgs{layer == 0 ? xln0 : xb, wqkv, phiq, layer == 0 ? wo : nullptr, w2, st_xb, (float*)w4, colsum, wphi, kvpart, layer == 0 ? 0 : 1, 0};
    launch_gemm<192, 3, 1024, EPI_QKV>(a, stream);
    attn_o_k<<<256, 512, 0, stream>>>(phiq, wphi, kvpart, obuf);
    a = GemmArgs{obuf, wo, xa, xin, nullptr, nullptr, st_xa, nullptr, nullptr, nullptr, 0, 0};
    launch_gemm<64, 5, 1024, EPI_RES>(a, stream);
    a = GemmArgs{xa, w1, hsh, nullptr, nullptr, st_xa, nullptr, colsum + 3072, nullptr, nullptr, 2, 0};
    launch_gemm<64, 5, 1024, EPI_GELU_SHIFT>(a, stream);
    a = GemmArgs{hsh, w2, x1, xa, nullptr, st_xa, st_x1, nullptr, nullptr, nullptr, 1, 0};
    launch_gemm<64, 5, 1024, EPI_W2>(a, stream);
    a = GemmArgs{x1, w3, gbuf, nullptr, nullptr, st_x1, nullptr, colsum + 4096, nullptr, nullptr, 1, 0};
    launch_gemm<224, 3, 1024, EPI_GELU>(a, stream);
    a = GemmArgs{gbuf, w4, xout, xa, x1, nullptr, st_xb, nullptr, nullptr, nullptr, 0, 0};
    launch_gemm<64, 5, 3584, EPI_W4>(a, stream);
  }
}
```

```cpp
#include <hip/hip_runtime.h>
#include <hip/hip_bf16.h>
#include <stdint.h>

typedef __attribute__((ext_vector_type(8))) short bf16x8;
typedef __attribute__((ext_vector_type(4))) short s16x4;
typedef __attribute__((ext_vector_type(4))) float f32x4;
typedef __attribute__((ext_vector_type(2))) float f32x2;
typedef __attribute__((ext_vector_type(4))) unsigned u32x4;
typedef __attribute__((ext_vector_type(2))) unsigned u32x2;
typedef __attribute__((ext_vector_type(2))) __bf16 bf16x2_t;
typedef unsigned short u16;
#define LDS_AS __attribute__((address_space(3)))
#define DI __device__ __forceinline__

static constexpr int SEQ = 2048, DM = 1024, NH = 32, FF = 3584;

DI float bf2f(unsigned v) { return __uint_as_float(v << 16); }
DI float bflo(unsigned v) { return __uint_as_float(v << 16); }
DI float bfhi(unsigned v) { return __uint_as_float(v & 0xffff0000u); }
DI unsigned pk2(float a, float b) {
  f32x2 v = {a, b};
  return __builtin_bit_cast(unsigned, __builtin_convertvector(v, bf16x2_t));
}
DI float rbf(float a) { return bflo(pk2(a, a)); }
template <int CTRL> DI float dppf(float v) { return __builtin_bit_cast(float, __builtin_amdgcn_update_dpp(0, __builtin_bit_cast(int, v), CTRL, 0xf, 0xf, true)); }
DI float sum8(float v) { v += dppf<0xB1>(v); v += dppf<0x4E>(v); return v + dppf<0x141>(v); }
DI f32x2 pair16(float v) { const unsigned u = __float_as_uint(v); const auto r = __builtin_amdgcn_permlane16_swap(u, u, false, false); return (f32x2){__uint_as_float(r[0]), __uint_as_float(r[1])}; }
DI f32x2 pair32(float v) { const unsigned u = __float_as_uint(v); const auto r = __builtin_amdgcn_permlane32_swap(u, u, false, false); return (f32x2){__uint_as_float(r[0]), __uint_as_float(r[1])}; }
DI float max4g(float v) { f32x2 t = pair16(v); t = pair32(fmaxf(t[0], t[1])); return fmaxf(t[0], t[1]); }
DI float min4g(float v) { f32x2 t = pair16(v); t = pair32(fminf(t[0], t[1])); return fminf(t[0], t[1]); }
DI float sum4g(float v) { f32x2 t = pair16(v); t = pair32(t[0] + t[1]); return t[0] + t[1]; }
DI float sum64(float v) { v = sum8(v); return sum4g(v + dppf<0x140>(v)); }

template <int I> struct IC { static constexpr int value = I; };
template <int B, int E, class F> DI void static_for(F&& f) {
  if constexpr (B < E) { f(IC<B>{}); static_for<B + 1, E>(f); }
}
template <int N> DI void wait_vm() { asm volatile("s_waitcnt vmcnt(%0)" ::"n"(N) : "memory"); }
DI void wait_lgkm0() { asm volatile("s_waitcnt lgkmcnt(0)" ::: "memory"); }
DI void bar() { __builtin_amdgcn_s_barrier(); }
DI void glds16(const void* src, char* lds_dst) {
  __builtin_amdgcn_global_load_lds((const unsigned*)src, (unsigned*)lds_dst, 16, 0, 0);
}
DI s16x4 trread(const char* p) {
  return __builtin_bit_cast(s16x4, __builtin_amdgcn_ds_read_tr16_b64_v4i16((LDS_AS s16x4*)p));
}
template <int OFF> DI s16x4 lds_tr(unsigned addr) {
  s16x4 r; asm volatile("ds_read_b64_tr_b16 %0, %1 offset:%2" : "=v"(r) : "v"(addr), "n"(OFF) : "memory"); return r;
}
template <int OFF> DI bf16x8 lds_b128(unsigned addr) {
  bf16x8 r; asm volatile("ds_read_b128 %0, %1 offset:%2" : "=v"(r) : "v"(addr), "n"(OFF) : "memory"); return r;
}
template <int OFF> DI f32x2 lds_f2(unsigned addr) {
  f32x2 r; asm volatile("ds_read_b64 %0, %1 offset:%2" : "=v"(r) : "v"(addr), "n"(OFF) : "memory"); return r;
}
template <int OFF> DI f32x4 lds_f4(unsigned addr) {
  f32x4 r; asm volatile("ds_read_b128 %0, %1 offset:%2" : "=v"(r) : "v"(addr), "n"(OFF) : "memory"); return r;
}
DI void st16_wt(void* base, unsigned bytes, unsigned off, u32x4 v) {
  __builtin_amdgcn_raw_buffer_store_b128(v, __builtin_amdgcn_make_buffer_rsrc(base, (short)0, (int)bytes, 0x00020000), (int)off, 0, 16);
}
DI bf16x8 cat8(s16x4 lo, s16x4 hi) { return (bf16x8){lo[0], lo[1], lo[2], lo[3], hi[0], hi[1], hi[2], hi[3]}; }
DI f32x4 mfma16(bf16x8 a, bf16x8 b, f32x4 c) { return __builtin_amdgcn_mfma_f32_16x16x32_bf16(a, b, c, 0, 0, 0); }

DI f32x2 gelu2(f32x2 x) {
  const float c0 = -2.302208198f, c1 = -2.302208198f * 0.044715f;
  const f32x2 a = x * (x * x * c1 + c0);
  const f32x2 d = (f32x2){__builtin_amdgcn_exp2f(a[0]), __builtin_amdgcn_exp2f(a[1])} + 1.0f;
  return x * (f32x2){__builtin_amdgcn_rcpf(d[0]), __builtin_amdgcn_rcpf(d[1])};
}
DI f32x2 rbf2(f32x2 v) { const unsigned p = pk2(v[0], v[1]); return (f32x2){bflo(p), bfhi(p)}; }
DI f32x2 unpk(unsigned p) { return (f32x2){bflo(p), bfhi(p)}; }

DI void feature_map(const bf16x8 (&wf)[2], bf16x8 xf, u32x4& pos, u32x4& neg) {
  f32x2 p[4];
#pragma unroll
  for (int fb = 0; fb < 2; ++fb) {
    const f32x4 d = mfma16(wf[fb], xf, (f32x4){0.f, 0.f, 0.f, 0.f});
    p[fb * 2] = rbf2((f32x2){d[0], d[1]});
    p[fb * 2 + 1] = rbf2((f32x2){d[2], d[3]});
  }
  float mx = fmaxf(p[0][0], p[0][1]), mn = fminf(p[0][0], p[0][1]);
#pragma unroll
  for (int e = 1; e < 4; ++e) { mx = fmaxf(mx, fmaxf(p[e][0], p[e][1])); mn = fminf(mn, fminf(p[e][0], p[e][1])); }
  mx = max4g(mx);
  mn = min4g(mn);
  const float l2e = 1.4426950408889634f;
  f32x2 ep[4], en[4], sp2 = {0.f, 0.f}, sn2 = {0.f, 0.f};
#pragma unroll
  for (int e = 0; e < 4; ++e) {
    const f32x2 a = (p[e] - mx) * l2e, b = (mn - p[e]) * l2e;
    ep[e] = (f32x2){__builtin_amdgcn_exp2f(a[0]), __builtin_amdgcn_exp2f(a[1])}; sp2 += ep[e];
    en[e] = (f32x2){__builtin_amdgcn_exp2f(b[0]), __builtin_amdgcn_exp2f(b[1])}; sn2 += en[e];
  }
  float sp = sp2[0] + sp2[1], sn = sn2[0] + sn2[1];
  sp = sum4g(sp);
  sn = sum4g(sn);
  const float ip = __builtin_amdgcn_rcpf(sp), in_ = __builtin_amdgcn_rcpf(sn);
#pragma unroll
  for (int e = 0; e < 4; ++e) {
    const f32x2 a = ep[e] * ip, b = en[e] * in_;
    pos[e] = pk2(a[0], a[1]); neg[e] = pk2(b[0], b[1]);
  }
}

#define TS(i)
struct GemmArgs {
  const u16* A;
  const u16* B;
  u16* out;
  const u16* res1;
  const u16* res2;
  const float* stats_in;
  float* stats_out;
  const float* colsum;
  const u16* wphi;
  float* kvpart;
  int ln_mode;
  int unused;
};

enum { EPI_QKV = 0, EPI_RES = 1, EPI_GELU_SHIFT = 2, EPI_W2 = 3, EPI_GELU = 4, EPI_W4 = 5 };

struct RowP { float alpha, beta, mhat, dhat; };
DI RowP row_params(float mean, float var, int mode) {
  RowP r;
  const float mh = rbf(mean);
  const float dh = rbf(__builtin_amdgcn_sqrtf(rbf(rbf(var) + 1e-5f)));
  const float i1 = __builtin_amdgcn_rcpf(dh);
  r.mhat = mh; r.dhat = dh;
  if (mode == 2) {
    const float m2 = (mean - mh) * i1;
    const float v2 = var * i1 * i1;
    const float mh2 = rbf(m2);
    const float dh2 = rbf(__builtin_amdgcn_sqrtf(rbf(rbf(v2) + 1e-5f)));
    const float i2 = __builtin_amdgcn_rcpf(dh2);
    r.alpha = i1 * i2;
    r.beta = (mh * i1 + mh2) * i2;
  } else {
    r.alpha = i1;
    r.beta = mh * i1;
  }
  return r;
}

template <int BN, int S, int KD, int EPI>
__global__ __launch_bounds__(512, 2) void gemm_k(GemmArgs g) {
  constexpr int NK = KD / 64;
  constexpr int NPF = BN / 64;
  constexpr bool HALF = (BN % 64) != 0;
  constexpr int NI = BN / 32;
  constexpr int A_BYTES = 16384;
  constexpr int STAGE = A_BYTES + BN * 128;
  constexpr int L = 4 + NPF * 2 + (HALF ? 1 : 0);
  constexpr int NTOT = (EPI == EPI_QKV) ? 3 * DM : (EPI == EPI_GELU ? FF : DM);
  constexpr bool USE_STATS = (EPI == EPI_QKV || EPI == EPI_GELU_SHIFT || EPI == EPI_W2 || EPI == EPI_GELU);
  constexpr bool FOLD = (EPI == EPI_QKV || EPI == EPI_GELU_SHIFT || EPI == EPI_GELU);
  constexpr int NRES = (EPI == EPI_RES || EPI == EPI_W2) ? 1 : (EPI == EPI_W4 ? 2 : 0);
  constexpr int OFF_STATS = S * STAGE;
  constexpr int OFF_RES = OFF_STATS;
  constexpr int OFF_WPHI = OFF_RES + NRES * 16384;
  constexpr int OFF_ROWP = OFF_WPHI + (EPI == EPI_QKV ? 4096 : 0);
  constexpr int OFF_CS = OFF_ROWP + 2048;
  constexpr int OFF_DUMP = OFF_CS + 1024;
  constexpr int NEXTRA = (USE_STATS && BN != 64 ? 8 : 0) + NRES * 4 + (EPI == EPI_QKV ? 1 : 0) + (FOLD ? 1 : 0);

  extern __shared__ __attribute__((aligned(16))) char smem[];
  const int tid = threadIdx.x, lane = tid & 63;
  const int w = __builtin_amdgcn_readfirstlane(tid >> 6);
  const bool prod = w >= 4;
  const int cw = w & 3, wr = cw >> 1, wc = cw & 1;

  const int b = blockIdx.x, xcd = b & 7, j = b >> 3;
  int rp, ct;
  if constexpr (EPI == EPI_QKV || EPI == EPI_GELU) { rp = (xcd >> 2) * 8 + (j >> 2); ct = (xcd & 3) * 4 + (j & 3); }
  else { rp = (xcd >> 1) * 4 + (j >> 3); ct = (xcd & 1) * 8 + (j & 7); }
  const int m0 = rp * 128;
  const int n0 = (EPI == EPI_QKV) ? ct * 64 : ct * BN;
  constexpr int PSTRIDE = (EPI == EPI_QKV) ? DM : 64;
  const int nf = HALF ? n0 + (ct & 1) * 32 : n0;
  const int nh = (ct & 1) ? n0 : n0 + NPF * 64;
  const int g4 = lane >> 4, q4 = (lane >> 2) & 3, pp = lane & 3;
  RowP* rowp = (RowP*)(smem + OFF_ROWP);
  const bool ln_on = USE_STATS && (g.ln_mode != 0 || EPI == EPI_W2);

  f32x4 acc[4][NI];
  f32x2 ab[4];
  f32x4 csv[NI];
  TS(0);
  constexpr bool RP_CONS = (BN == 64);
  f32x2 sv[8];
  const int sr = cw * 32 + (lane >> 1), shf = lane & 1;
  auto load_stats = [&]() {
#pragma unroll
    for (int p = 0; p < 8; ++p)
      asm volatile("global_load_dwordx2 %0, %1, off" : "=v"(sv[p]) : "v"(g.stats_in + ((size_t)(shf * 8 + p) * SEQ + m0 + sr) * 2) : "memory");
  };
  float rs_s = 0.f, rs_m2 = 0.f;
  auto row_stats = [&](auto pc) {
    constexpr int piece = decltype(pc)::value;
    if constexpr (piece == 0) {
#pragma unroll
      for (int p = 0; p < 8; ++p) asm volatile("" : "+v"(sv[p]));
      float s = 0.f;
#pragma unroll
      for (int p = 0; p < 8; ++p) s += sv[p][0];
      rs_s = s + dppf<0xB1>(s);
    } else if constexpr (piece == 1) {
      const float mean = rs_s * (1.0f / 1024.0f);
      float m2 = 0.f;
#pragma unroll
      for (int p = 0; p < 8; ++p) { const float d = sv[p][0] * (1.0f / 64.0f) - mean; m2 += sv[p][1] + 64.0f * d * d; }
      rs_m2 = m2 + dppf<0xB1>(m2);
    } else {
      if (ln_on && shf == 0) rowp[sr] = row_params(rs_s * (1.0f / 1024.0f), rs_m2 * (1.0f / 1024.0f), EPI == EPI_W2 ? 1 : g.ln_mode);
      wait_lgkm0();
    }
    __builtin_amdgcn_sched_barrier(0);
  };
  if (__builtin_expect(prod, 1)) {
    const u16* a_src = g.A + (size_t)(m0 + cw * 32 + (lane >> 3)) * KD + (((lane & 7) ^ (lane >> 3)) * 8);
    const u16* b_src[2];
#pragma unroll
    for (int e = 0; e < 2; ++e) {
      const int r = lane >> 3;
      const int f = ((r >> 1) & 1) | (e << 1);
      const int c = ((((lane & 7) >> 1) ^ f) << 1) | (lane & 1);
      b_src[e] = g.B + (size_t)((cw * 2 + e) * 8 + r) * NTOT + nf + c * 8;
    }
    const u16* bh_src = nullptr;
    if constexpr (HALF) {
      const int c = ((((lane >> 1) & 1) ^ ((lane >> 5) & 1)) << 1) | (lane & 1);
      bh_src = g.B + (size_t)(cw * 16 + (lane >> 2)) * NTOT + nh + c * 8;
    }
    auto issue = [&](int kt, int slot) {
      char* base = smem + slot * STAGE;
#pragma unroll
      for (int p = 0; p < NPF; ++p)
#pragma unroll
        for (int e = 0; e < 2; ++e)
          glds16(b_src[e] + (size_t)kt * 64 * NTOT + p * PSTRIDE, base + A_BYTES + p * 8192 + (cw * 2 + e) * 1024);
      if constexpr (HALF) glds16(bh_src + (size_t)kt * 64 * NTOT, base + A_BYTES + NPF * 8192 + cw * 1024);
#pragma unroll
      for (int e = 0; e < 4; ++e) glds16(a_src + (size_t)e * 8 * KD + kt * 64, base + (cw * 4 + e) * 1024);
    };
    TS(6);
    issue(0, 0);
    issue(1, 1);
    wait_vm<L>();
    TS(1);
    bar();
    static_for<2, S>([&](auto ic) { issue(ic.value, ic.value); });
    TS(7);
    if constexpr (NRES >= 1) {
#pragma unroll
      for (int e = 0; e < 4; ++e) {
        const int r = (cw * 4 + e) * 8 + (lane >> 3);
        glds16(g.res1 + (size_t)(m0 + r) * DM + n0 + (lane & 7) * 8, smem + OFF_RES + (cw * 4 + e) * 1024);
      }
    }
    if constexpr (NRES >= 2) {
#pragma unroll
      for (int e = 0; e < 4; ++e) {
        const int r = (cw * 4 + e) * 8 + (lane >> 3);
        glds16(g.res2 + (size_t)(m0 + r) * DM + n0 + (lane & 7) * 8, smem + OFF_RES + 16384 + (cw * 4 + e) * 1024);
      }
    }
    if constexpr (EPI == EPI_QKV) glds16(g.wphi + (size_t)ct * 2048 + cw * 512 + lane * 8, smem + OFF_WPHI + cw * 1024);
    if constexpr (FOLD) {
      int col = cw * 64 + (lane & 15) * 4;
      if (col > BN - 4) col = BN - 4;
      const int gcol = (EPI == EPI_QKV) ? (col >> 6) * DM + n0 + (col & 63) : (HALF && col >= NPF * 64) ? nh + col - NPF * 64 : nf + col;
      if (lane < 16) glds16(g.colsum + gcol, smem + OFF_CS + cw * 256);
    }
    if constexpr (USE_STATS && !RP_CONS) load_stats();
    static_for<0, NK - 1>([&](auto ic) {
      constexpr int kt = ic.value;
      constexpr int young = (S - 2 < NK - 2 - kt) ? S - 2 : NK - 2 - kt;
      constexpr int ex = (kt + 1 <= S - 1 && kt + 1 < NK - 1) ? NEXTRA : 0;
      wait_vm<young * L + ex>();
      bar();
      if constexpr (kt + S < NK) issue(kt + S, kt % S);
      if constexpr (USE_STATS && !RP_CONS && kt >= 4 && kt < 7) row_stats(IC<kt - 4>{});
    });
    TS(2);
    bar();
  } else {
    const int fsw = ((q4 >> 1) & 1) | ((g4 & 1) << 1);
    int a_off[2], b_off[2], bh_off = 0;
#pragma unroll
    for (int k = 0; k < 2; ++k) {
      a_off[k] = (wr * 64 + (lane & 15)) * 128 + (((k * 4 + g4) ^ (lane & 7)) * 16);
      b_off[k] = A_BYTES + (8 * g4 + q4) * 128 + (((2 * k + wc) ^ fsw) * 32) + pp * 8;
    }
    if constexpr (HALF) bh_off = A_BYTES + NPF * 8192 + (8 * g4 + q4) * 64 + ((wc ^ (g4 & 1)) * 32) + pp * 8;
#pragma unroll
    for (int m = 0; m < 4; ++m)
#pragma unroll
      for (int i = 0; i < NI; ++i) acc[m][i] = (f32x4){0.f, 0.f, 0.f, 0.f};
    bf16x8 xa[2][4];
    s16x4 wlo[2][NI], whi[2][NI];
    const unsigned lbase = (unsigned)(size_t)smem;

    auto ldfrag = [&](auto setc, auto slotc, auto kc) {
      constexpr int set = decltype(setc)::value, slot = decltype(slotc)::value, k32 = decltype(kc)::value;
      const unsigned sb = lbase + slot * STAGE;
      const unsigned ba0 = sb + b_off[0], ba1 = sb + b_off[1], bah = sb + bh_off, aa = sb + a_off[k32];
      static_for<0, NI>([&](auto ic) {
        constexpr int i = decltype(ic)::value;
        if constexpr (HALF && i == NI - 1) {
          wlo[set][i] = lds_tr<k32 * 2048>(bah);
          whi[set][i] = lds_tr<k32 * 2048 + 256>(bah);
        } else {
          wlo[set][i] = lds_tr<(i >> 1) * 8192 + k32 * 4096>((i & 1) ? ba1 : ba0);
          whi[set][i] = lds_tr<(i >> 1) * 8192 + k32 * 4096 + 512>((i & 1) ? ba1 : ba0);
        }
      });
      static_for<0, 4>([&](auto mc) { constexpr int m = decltype(mc)::value; xa[set][m] = lds_b128<m * 2048>(aa); });
    };
    auto wait_set = [&](auto setc) {
      constexpr int set = decltype(setc)::value;
      asm volatile("s_waitcnt lgkmcnt(0)" : "+v"(xa[set][0]), "+v"(xa[set][1]), "+v"(xa[set][2]), "+v"(xa[set][3]) :: "memory");
#pragma unroll
      for (int i = 0; i < NI; ++i) asm volatile("" : "+v"(wlo[set][i]), "+v"(whi[set][i]));
      __builtin_amdgcn_sched_barrier(0);
    };
    auto ld1 = [&](auto setc, auto slotc, auto kc, auto rc) {
      constexpr int set = decltype(setc)::value, slot = decltype(slotc)::value, k32 = decltype(kc)::value, r = decltype(rc)::value;
      const unsigned sb = lbase + slot * STAGE;
      if constexpr (r < 2 * NI) {
        constexpr int i = r >> 1, hi = r & 1;
        s16x4 v;
        if constexpr (HALF && i == NI - 1) v = lds_tr<k32 * 2048 + hi * 256>(sb + bh_off);
        else v = lds_tr<(i >> 1) * 8192 + k32 * 4096 + hi * 512>(sb + b_off[i & 1]);
        if constexpr (hi) whi[set][i] = v; else wlo[set][i] = v;
      } else {
        constexpr int m = r - 2 * NI;
        xa[set][m] = lds_b128<m * 2048>(sb + a_off[k32]);
      }
    };
    auto mma_ld = [&](auto setc, auto ldc, auto slotc, auto kc) {
      constexpr int set = decltype(setc)::value;
      constexpr bool ld = decltype(ldc)::value != 0;
      static_for<0, 4 * NI>([&](auto tc) {
        constexpr int t = decltype(tc)::value, i = t >> 2, m = t & 3;
        acc[m][i] = mfma16(cat8(wlo[set][i], whi[set][i]), xa[set][m], acc[m][i]);
        if constexpr (ld && t < 2 * NI + 4) ld1(IC<set ^ 1>{}, slotc, kc, tc);
        __builtin_amdgcn_sched_barrier(0);
      });
    };

    const char* tp = (const char*)g.A;
    unsigned tw = 0;
    bool ton = false;
    int tks = 0;
    if (cw == 0) {
      const int pl = lane >> 4;
      const int bl = (int)(((size_t)g.B >> 7) & 7);
      int off;
      if constexpr (BN == 64) { off = ((xcd & 1) * 8 + ((3 - bl) & 7)) * 128; ton = lane < 16; }
      else if constexpr (EPI == EPI_QKV) { const int c = (3 - bl - (xcd & 3) * 4) & 7; off = pl * 2048 + ((xcd & 3) * 4 + (c & 3)) * 128; ton = c < 4 && lane < 48; }
      else { const int l0 = (xcd & 3) * 14, c = (3 - bl - l0) & 7; off = (l0 + c + pl * 8) * 128; ton = lane < (c < 6 ? 32 : 16); }
      tks = (lane >> 1) & 7;
      if (ton) tp = (const char*)g.B + (size_t)(tks * 64 + j * 2 + (lane & 1)) * NTOT * 2 + off;
      tw = 8 * 64 * NTOT * 2;
    } else if (cw == 1) {
      tks = (3 - (int)(((size_t)g.A >> 7) & 7)) & 7;
      const int row = (BN == 64) ? (xcd >> 1) * 512 + j * 16 + (lane & 15) : (xcd >> 2) * 1024 + j * 32 + (lane & 31);
      ton = lane < (BN == 64 ? 16 : 32);
      tp = (const char*)g.A + (size_t)row * KD * 2 + tks * 128;
      tw = 1024;
    }
    auto touch = [&](auto wc_) {
      constexpr int wi = decltype(wc_)::value;
      if (cw < 2) {
        if (ton && (wi > 0 || tks >= S))
          __builtin_amdgcn_global_load_lds((const unsigned*)(tp + (size_t)(wi * tw)), (unsigned*)(smem + OFF_DUMP), 4, 0, 0);
      }
      __builtin_amdgcn_sched_barrier(0);
    };
    if constexpr (USE_STATS && RP_CONS) load_stats();
    touch(IC<0>{});
    if constexpr (BN == 64) touch(IC<1>{});
    bar();
    TS(1);
    ldfrag(IC<0>{}, IC<0>{}, IC<0>{});
    static_for<0, NK>([&](auto ic) {
      constexpr int kt = ic.value;
      constexpr int slot = kt % S;
      wait_set(IC<0>{});
      if constexpr (BN != 64 && kt == 0) touch(IC<1>{});
      if constexpr (kt % 8 == 4 && (kt + 12) / 8 < NK / 8) touch(IC<(kt + 12) / 8>{});
      if constexpr (USE_STATS && RP_CONS && kt >= 6 && kt < 9) { if constexpr (kt == 6) wait_vm<0>(); row_stats(IC<kt - 6>{}); }
      mma_ld(IC<0>{}, IC<1>{}, IC<slot>{}, IC<1>{});
      wait_set(IC<1>{});
      if constexpr (kt + 1 < NK) {
        bar();
        __builtin_amdgcn_sched_barrier(0);
        mma_ld(IC<1>{}, IC<1>{}, IC<(kt + 1) % S>{}, IC<0>{});
      } else {
        const unsigned ra = lbase + OFF_ROWP + (wr * 64 + (lane & 15)) * 16, ca = lbase + OFF_CS + (wc * 16 + g4 * 4) * 4;
        static_for<0, 4 * NI>([&](auto tc) {
          constexpr int t = decltype(tc)::value, i = t >> 2, m = t & 3;
          acc[m][i] = mfma16(cat8(wlo[1][i], whi[1][i]), xa[1][m], acc[m][i]);
          if constexpr (FOLD && t < 4) ab[t] = lds_f2<t * 256>(ra);
          if constexpr (FOLD && m == 3) csv[i] = lds_f4<i * 128>(ca);
          __builtin_amdgcn_sched_barrier(0);
        });
      }
    });
    TS(2);
    bar();
    if constexpr (FOLD) {
      asm volatile("s_waitcnt lgkmcnt(0)" : "+v"(ab[0]), "+v"(ab[1]), "+v"(ab[2]), "+v"(ab[3]) :: "memory");
#pragma unroll
      for (int i = 0; i < NI; ++i) asm volatile("" : "+v"(csv[i]));
      if (!ln_on) {
#pragma unroll
        for (int m = 0; m < 4; ++m) ab[m] = (f32x2){1.f, 0.f};
      }
    }
  }
  TS(3);
  if constexpr (EPI == EPI_QKV) __builtin_amdgcn_s_waitcnt(0x0F70);

  constexpr int TSTR = BN * 2 + (EPI == EPI_QKV ? 32 : 16);
  char* T = smem;
  u32x4 pv[EPI == EPI_QKV ? 11 : 1];
  if (!prod) {
#pragma unroll
    for (int i = 0; i < NI; ++i) {
      const int nb = 2 * i + wc;
      const int ncol = nb * 16 + g4 * 4;
      const f32x4 cs = csv[i];
#pragma unroll
      for (int m = 0; m < 4; ++m) {
        u32x2 pkd;
#pragma unroll
        for (int h = 0; h < 2; ++h) {
          f32x2 v = {acc[m][i][2 * h], acc[m][i][2 * h + 1]};
          if constexpr (FOLD) v = v * ab[m][0] - (f32x2){cs[2 * h], cs[2 * h + 1]} * ab[m][1];
          pkd[h] = pk2(v[0], v[1]);
        }
        *(u32x2*)(T + (wr * 64 + m * 16 + (lane & 15)) * TSTR + ncol * 2) = pkd;
      }
    }
  } else if constexpr (EPI == EPI_QKV) {
    if (g.res1) {
      const int t = tid - 256;
      const u16* W4 = (const u16*)g.stats_out + (size_t)b * 14336 + t * 8;
      const u16* Wo = g.res1 + (size_t)b * 4096 + t * 8;
      const u16* W2 = g.res2 + (size_t)b * 4096 + t * 8;
#pragma unroll
      for (int i = 0; i < 2; ++i) { pv[7 + i] = *(const u32x4*)(Wo + i * 2048); pv[9 + i] = *(const u32x4*)(W2 + i * 2048); }
#pragma unroll
      for (int i = 0; i < 7; ++i) pv[i] = *(const u32x4*)(W4 + i * 2048);
    }
  }
  if constexpr (EPI == EPI_QKV) { wait_lgkm0(); bar(); asm volatile("" ::: "memory"); }
  else __syncthreads();
  TS(4);
  auto gelu8 = [&](u32x4 v) {
    u32x4 o;
#pragma unroll
    for (int e = 0; e < 4; ++e) { const f32x2 y = gelu2(unpk(v[e])); o[e] = pk2(y[0], y[1]); }
    return o;
  };
  if constexpr (EPI == EPI_GELU) {
    u32x4 v[NI];
#pragma unroll
    for (int it = 0; it < NI; ++it) {
      const int r = it < NI - 1 ? (it & 1) * 64 + (tid >> 3) : tid >> 2, c = it < NI - 1 ? (it >> 1) * 8 + (tid & 7) : (NI / 2) * 8 + (tid & 3);
      v[it] = *(const u32x4*)(T + r * TSTR + c * 16);
    }
#pragma unroll
    for (int it = 0; it < NI; ++it) {
      const int r = it < NI - 1 ? (it & 1) * 64 + (tid >> 3) : tid >> 2, col = it < NI - 1 ? nf + (it >> 1) * 64 + (tid & 7) * 8 : nh + (tid & 3) * 8;
      st16_wt(g.out, SEQ * FF * 2, ((m0 + r) * FF + col) * 2, gelu8(v[it]));
    }
  } else if constexpr (EPI == EPI_GELU_SHIFT) {
    const int grp = n0 >> 8;
    const int sh = (grp == 0) ? 1 : (grp == 1) ? -1 : (grp == 2) ? 2 : -2;
#pragma unroll
    for (int it = 0; it < 2; ++it) {
      const int r = it * 64 + (tid >> 3), c = tid & 7;
      const u32x4 v = gelu8(*(const u32x4*)(T + r * TSTR + c * 16));
      const int dr = m0 + r + sh;
      if (dr >= 0 && dr < SEQ) st16_wt(g.out, SEQ * DM * 2, (dr * DM + n0 + c * 8) * 2, v);
    }
    if (tid < 16) {
      const int zr = tid >> 3, c = tid & 7;
      const int az = sh > 0 ? sh : -sh;
      if (zr < az) {
        if (sh > 0 && rp == 0) *(u32x4*)(g.out + (size_t)zr * DM + n0 + c * 8) = (u32x4){0u, 0u, 0u, 0u};
        if (sh < 0 && rp == 15) *(u32x4*)(g.out + (size_t)(SEQ - 1 - zr) * DM + n0 + c * 8) = (u32x4){0u, 0u, 0u, 0u};
      }
    }
  } else if constexpr (EPI == EPI_RES || EPI == EPI_W2 || EPI == EPI_W4) {
#pragma unroll
    for (int it = 0; it < 2; ++it) {
      const int r = it * 64 + (tid >> 3), c = tid & 7;
      const u32x4 v = *(const u32x4*)(T + r * TSTR + c * 16);
      const u32x4 x = *(const u32x4*)(smem + OFF_RES + r * 128 + c * 16);
      f32x2 y[4];
      if constexpr (EPI == EPI_RES) {
#pragma unroll
        for (int e = 0; e < 4; ++e) y[e] = unpk(x[e]) + unpk(v[e]);
      } else if constexpr (EPI == EPI_W2) {
        const RowP rr = rowp[r];
#pragma unroll
        for (int e = 0; e < 4; ++e) y[e] = rbf2(rbf2(unpk(x[e]) - rr.mhat) * rr.alpha) + unpk(v[e]);
      } else {
        const u32x4 x1 = *(const u32x4*)(smem + OFF_RES + 16384 + r * 128 + c * 16);
#pragma unroll
        for (int e = 0; e < 4; ++e) y[e] = unpk(x[e]) + rbf2(unpk(x1[e]) + unpk(v[e]));
      }
      u32x4 o;
#pragma unroll
      for (int e = 0; e < 4; ++e) { o[e] = pk2(y[e][0], y[e][1]); y[e] = unpk(o[e]); }
      st16_wt(g.out, SEQ * DM * 2, ((m0 + r) * DM + n0 + c * 8) * 2, o);
      const f32x2 s2 = (y[0] + y[1]) + (y[2] + y[3]);
      const float s = sum8(s2[0] + s2[1]);
      const float mc = s * (1.0f / 64.0f);
      f32x2 q2 = {0.f, 0.f};
#pragma unroll
      for (int e = 0; e < 4; ++e) { const f32x2 d = y[e] - mc; q2 += d * d; }
      const float m2 = sum8(q2[0] + q2[1]);
      if (c == 0) *(f32x2*)(g.stats_out + ((size_t)ct * SEQ + m0 + r) * 2) = (f32x2){s, m2};
    }
  } else {
    constexpr int PKSTR = 160;
    char* PK = smem + 128 * TSTR;
#pragma unroll
    for (int it = 0; it < 2; ++it) {
      const int r = it * 64 + (tid >> 3), c = tid & 7;
      st16_wt(g.out, SEQ * DM * 2, ((m0 + r) * DM + n0 + c * 8) * 2, *(const u32x4*)(T + r * TSTR + c * 16));
    }
    {
      const int hh = w & 1, rq = w >> 1;
      bf16x8 wf[2];
#pragma unroll
      for (int fb = 0; fb < 2; ++fb) {
        const char* p = smem + OFF_WPHI + hh * 2048 + (8 * g4 + q4) * 64 + fb * 32 + pp * 8;
        wf[fb] = cat8(trread(p), trread(p + 256));
      }
#pragma unroll
      for (int mb = 0; mb < 2; ++mb) {
        const int row = rq * 32 + mb * 16 + (lane & 15);
        const bf16x8 xf = *(const bf16x8*)(T + row * TSTR + (64 + hh * 32 + g4 * 8) * 2);
        u32x4 pos, neg;
        feature_map(wf, xf, pos, neg);
        char* dst = PK + (hh * 128 + row) * PKSTR + g4 * 8;
        *(u32x2*)dst = (u32x2){pos[0], pos[1]}; *(u32x2*)(dst + 32) = (u32x2){pos[2], pos[3]};
        *(u32x2*)(dst + 64) = (u32x2){neg[0], neg[1]}; *(u32x2*)(dst + 96) = (u32x2){neg[2], neg[3]};
      }
    }
    __syncthreads();
    {
      const int hh = w >> 2, fa = w & 3;
      f32x4 kv[3];
#pragma unroll
      for (int d = 0; d < 3; ++d) kv[d] = (f32x4){0.f, 0.f, 0.f, 0.f};
      const short one = (short)0x3F80;
      const bf16x8 ones = {one, one, one, one, one, one, one, one};
#pragma unroll
      for (int ks = 0; ks < 4; ++ks) {
        const int row = ks * 32 + 8 * g4 + q4;
        bf16x8 vf[2];
#pragma unroll
        for (int d = 0; d < 2; ++d) {
          const char* p = T + row * TSTR + (128 + hh * 32 + d * 16) * 2 + pp * 8;
          vf[d] = cat8(trread(p), trread(p + 4 * TSTR));
        }
        const char* p = PK + (hh * 128 + row) * PKSTR + (fa * 16) * 2 + pp * 8;
        const bf16x8 pf = cat8(trread(p), trread(p + 4 * PKSTR));
        kv[0] = mfma16(pf, vf[0], kv[0]);
        kv[1] = mfma16(pf, vf[1], kv[1]);
        kv[2] = mfma16(pf, ones, kv[2]);
      }
      constexpr unsigned KVB = 16 * NH * 33 * 64 * 4;
      const int so = (rp * NH + ct * 2 + hh) * (33 * 64) + fa * 16 + g4 * 4;
      st16_wt(g.kvpart, KVB, (so + (lane & 15) * 64) * 4, __builtin_bit_cast(u32x4, kv[0]));
      st16_wt(g.kvpart, KVB, (so + (16 + (lane & 15)) * 64) * 4, __builtin_bit_cast(u32x4, kv[1]));
      if ((lane & 15) == 0) st16_wt(g.kvpart, KVB, (so + 32 * 64) * 4, __builtin_bit_cast(u32x4, kv[2]));
    }
    if (prod && g.res1) {
#pragma unroll
      for (int i = 0; i < 11; ++i) asm volatile("" ::"v"(pv[i]));
    }
  }
  TS(5);
}

__global__ __launch_bounds__(256) void ln0_k(const u16* __restrict__ x, u16* __restrict__ y, const u16* __restrict__ wqkv,
                                             const u16* __restrict__ w1, const u16* __restrict__ w3, float* __restrict__ colsum,
                                             const u16* __restrict__ wo, const u16* __restrict__ w2, const u16* __restrict__ w4) {
  if (blockIdx.x >= SEQ / 4 + 120) {
    const int t = blockIdx.x - (SEQ / 4 + 120);
    const u16* W = t < 32 ? wo + (size_t)t * 32768 : (t < 64 ? w2 + (size_t)(t - 32) * 32768 : w4 + (size_t)(t - 64) * 32768);
#pragma unroll
    for (int i = 0; i < 16; ++i) {
      const u32x4 v = *(const u32x4*)(W + (i * 256 + threadIdx.x) * 8);
      asm volatile("" ::"v"(v));
    }
    return;
  }
  if (blockIdx.x >= SEQ / 4) {
    __shared__ float red[32 * 64];
    const int tid = threadIdx.x;
    const int cg = blockIdx.x - SEQ / 4;
    const u16* W; int N, c0;
    if (cg < 48) { W = wqkv; N = 3 * DM; c0 = cg * 64; }
    else if (cg < 64) { W = w1; N = DM; c0 = (cg - 48) * 64; }
    else { W = w3; N = FF; c0 = (cg - 64) * 64; }
    const int c = tid & 7, rg = tid >> 3;
    f32x2 s[4] = {{0.f, 0.f}, {0.f, 0.f}, {0.f, 0.f}, {0.f, 0.f}};
#pragma unroll 16
    for (int i = 0; i < 32; ++i) {
      const u32x4 v = *(const u32x4*)(W + (size_t)(rg + i * 32) * N + c0 + c * 8);
#pragma unroll
      for (int e = 0; e < 4; ++e) s[e] += unpk(v[e]);
    }
#pragma unroll
    for (int e = 0; e < 4; ++e) *(f32x2*)(red + rg * 64 + c * 8 + 2 * e) = s[e];
    __syncthreads();
    if (tid < 64) {
      float t = 0.f;
#pragma unroll
      for (int i = 0; i < 32; ++i) t += red[i * 64 + tid];
      colsum[cg * 64 + tid] = t;
    }
    return;
  }
  const int lane = threadIdx.x & 63, row = blockIdx.x * 4 + (threadIdx.x >> 6);
  const u16* src = x + (size_t)row * DM;
  const u32x4 a = *(const u32x4*)(src + lane * 8), b = *(const u32x4*)(src + 512 + lane * 8);
  float v[16];
#pragma unroll
  for (int e = 0; e < 4; ++e) { v[2 * e] = bflo(a[e]); v[2 * e + 1] = bfhi(a[e]); v[8 + 2 * e] = bflo(b[e]); v[9 + 2 * e] = bfhi(b[e]); }
  float s = 0.f;
#pragma unroll
  for (int e = 0; e < 16; ++e) s += v[e];
  s = sum64(s);
  const float mean = s * (1.0f / 1024.0f);
  float m2 = 0.f;
#pragma unroll
  for (int e = 0; e < 16; ++e) { const float d = v[e] - mean; m2 += d * d; }
  m2 = sum64(m2);
  const RowP r = row_params(mean, m2 * (1.0f / 1024.0f), 1);
  u32x4 oa, ob;
#pragma unroll
  for (int e = 0; e < 4; ++e) {
    oa[e] = pk2(rbf(v[2 * e] - r.mhat) / r.dhat, rbf(v[2 * e + 1] - r.mhat) / r.dhat);
    ob[e] = pk2(rbf(v[8 + 2 * e] - r.mhat) / r.dhat, rbf(v[9 + 2 * e] - r.mhat) / r.dhat);
  }
  u16* dst = y + (size_t)row * DM;
  *(u32x4*)(dst + lane * 8) = oa;
  *(u32x4*)(dst + 512 + lane * 8) = ob;
}

__global__ __launch_bounds__(512) void attn_o_k(const u16* __restrict__ q, const u16* __restrict__ wphi, const float* __restrict__ kvpart,
                                                u16* __restrict__ o) {
  constexpr int KS = 80;
  __shared__ __attribute__((aligned(16))) char sm[33 * KS * 2 + 8];
  const int tid = threadIdx.x, lane = tid & 63, w = tid >> 6;
  constexpr int EPI = 6;
  TS(0);
  const int h = blockIdx.x & 31, rb = blockIdx.x >> 5;
  const int g4 = lane >> 4, r0 = rb * 256 + w * 32 + (lane & 15);
  bf16x8 qf[2], wf[2];
#pragma unroll
  for (int mb = 0; mb < 2; ++mb) qf[mb] = *(const bf16x8*)(q + (size_t)(r0 + mb * 16) * DM + h * 32 + g4 * 8);
#pragma unroll
  for (int fb = 0; fb < 2; ++fb)
#pragma unroll
    for (int j = 0; j < 8; ++j) wf[fb][j] = (short)wphi[h * 1024 + (g4 * 8 + j) * 32 + fb * 16 + (lane & 15)];
  __builtin_amdgcn_sched_barrier(0);
  f32x4 kp[16], kz[16];
  const float* slab = kvpart + (size_t)h * (33 * 64) + tid * 4;
#pragma unroll
  for (int p = 0; p < 16; ++p) kp[p] = *(const f32x4*)(slab + (size_t)p * NH * (33 * 64));
  const bool zw = __builtin_amdgcn_readfirstlane(w) == 0;
  if (zw) {
#pragma unroll
    for (int p = 0; p < 16; ++p) kz[p] = *(const f32x4*)(slab + (size_t)p * NH * (33 * 64) + 2048 - (tid & 48) * 4);
  }
  __builtin_amdgcn_sched_barrier(0);
  bf16x8 pq[2][2];
#pragma unroll
  for (int mb = 0; mb < 2; ++mb) {
    u32x4 pos, neg;
    feature_map(wf, qf[mb], pos, neg);
    pq[mb][0] = __builtin_bit_cast(bf16x8, pos);
    pq[mb][1] = __builtin_bit_cast(bf16x8, neg);
  }
  __builtin_amdgcn_sched_barrier(0);
  TS(1);
  u16* kvt = (u16*)sm;
  {
    f32x4 t = kp[0];
#pragma unroll
    for (int p = 1; p < 16; ++p) t += kp[p];
    *(u32x2*)(kvt + (tid >> 4) * KS + (tid & 15) * 4) = (u32x2){pk2(t[0], t[1]), pk2(t[2], t[3])};
    if (zw) {
      f32x4 z = kz[0];
#pragma unroll
      for (int p = 1; p < 16; ++p) z += kz[p];
      if (tid < 16) *(u32x2*)(kvt + 32 * KS + tid * 4) = (u32x2){pk2(z[0], z[1]), pk2(z[2], z[3])};
    }
  }
  TS(2);
  __syncthreads();
  TS(3);
  bf16x8 kf[3][2];
#pragma unroll
  for (int db = 0; db < 3; ++db)
#pragma unroll
    for (int ks = 0; ks < 2; ++ks) {
      const int d = (db < 2) ? ((lane >> 2) & 3) * 8 + db * 4 + (lane & 3) : 32;
      const u16* p = kvt + d * KS + ks * 32 + g4 * 4;
      kf[db][ks] = cat8(*(const s16x4*)p, *(const s16x4*)(p + 16));
    }
#pragma unroll
  for (int mb = 0; mb < 2; ++mb) {
    f32x4 a[3];
#pragma unroll
    for (int db = 0; db < 3; ++db) {
      a[db] = mfma16(kf[db][0], pq[mb][0], (f32x4){0.f, 0.f, 0.f, 0.f});
      a[db] = mfma16(kf[db][1], pq[mb][1], a[db]);
    }
    const float id = __builtin_amdgcn_rcpf(rbf(rbf(a[2][0]) + 1e-6f));
    u32x4 ov;
#pragma unroll
    for (int db = 0; db < 2; ++db) {
      const f32x2 lo = rbf2((f32x2){a[db][0], a[db][1]}) * id, hi = rbf2((f32x2){a[db][2], a[db][3]}) * id;
      ov[db * 2] = pk2(lo[0], lo[1]); ov[db * 2 + 1] = pk2(hi[0], hi[1]);
    }
    st16_wt(o, SEQ * DM * 2, ((r0 + mb * 16) * DM + h * 32 + g4 * 8) * 2, ov);
  }
  TS(4);
  TS(5);
}

template <int BN, int S, int KD, int EPI> static constexpr int smem_bytes() {
  constexpr bool USE_STATS = (EPI == EPI_QKV || EPI == EPI_GELU_SHIFT || EPI == EPI_W2 || EPI == EPI_GELU);
  constexpr int NRES = (EPI == EPI_RES || EPI == EPI_W2) ? 1 : (EPI == EPI_W4 ? 2 : 0);
  return S * (16384 + BN * 128) + NRES * 16384 + (EPI == EPI_QKV ? 4096 : 0) + 2048 + 1024 + 256;
}
template <int BN, int S, int KD, int EPI> static void launch_gemm(const GemmArgs& a, hipStream_t st) {
  constexpr int sb = smem_bytes<BN, S, KD, EPI>();
  static_assert(sb <= 163840, "LDS");
  (void)hipFuncSetAttribute((const void*)gemm_k<BN, S, KD, EPI>, hipFuncAttributeMaxDynamicSharedMemorySize, sb);
  gemm_k<BN, S, KD, EPI><<<256, 512, sb, st>>>(a);
}

extern "C" void kernel_launch(void* const* d_in, const int* in_sizes, int n_in, void* d_out, int out_size, void* d_ws,
                              size_t ws_size, hipStream_t stream) {
  const u16* x0 = (const u16*)d_in[0];
  const u16* wqkv = (const u16*)d_in[1];
  const u16* wphi = (const u16*)d_in[2];
  const u16* wo = (const u16*)d_in[3];
  const u16* w1 = (const u16*)d_in[5];
  const u16* w2 = (const u16*)d_in[6];
  const u16* w3 = (const u16*)d_in[7];
  const u16* w4 = (const u16*)d_in[8];
  char* ws = (char*)d_ws;
  const size_t MB = 1 << 20;
  u16* xln0 = (u16*)(ws + 0 * MB);
  u16* phiq = (u16*)(ws + 4 * MB);
  float* kvpart = (float*)(ws + 12 * MB);
  u16* obuf = (u16*)(ws + 17 * MB);
  u16* xa = (u16*)(ws + 21 * MB);
  u16* hsh = (u16*)(ws + 25 * MB);
  u16* x1 = (u16*)(ws + 29 * MB);
  u16* gbuf = (u16*)(ws + 33 * MB);
  u16* xb = (u16*)(ws + 48 * MB);
  float* st_xa = (float*)(ws + 52 * MB);
  float* st_x1 = (float*)(ws + 53 * MB);
  float* st_xb = (float*)(ws + 54 * MB);
  float* colsum = (float*)(ws + 55 * MB);

  ln0_k<<<SEQ / 4 + 120, 256, 0, stream>>>(x0, xln0, wqkv, w1, w3, colsum, wo, w2, w4);
  for (int layer = 0; layer < 2; ++layer) {
    const u16* xin = layer == 0 ? x0 : xb;
    u16* xout = layer == 0 ? xb : (u16*)d_out;
    GemmArgs a;
    a = GemmArgs{layer == 0 ? xln0 : xb, wqkv, phiq, layer == 0 ? wo : nullptr, w2, st_xb, (float*)w4, colsum, wphi, kvpart, layer == 0 ? 0 : 1, 0};
    launch_gemm<192, 3, 1024, EPI_QKV>(a, stream);
    attn_o_k<<<256, 512, 0, stream>>>(phiq, wphi, kvpart, obuf);
    a = GemmArgs{obuf, wo, xa, xin, nullptr, nullptr, st_xa, nullptr, nullptr, nullptr, 0, 0};
    launch_gemm<64, 5, 1024, EPI_RES>(a, stream);
    a = GemmArgs{xa, w1, hsh, nullptr, nullptr, st_xa, nullptr, colsum + 3072, nullptr, nullptr, 2, 0};
    launch_gemm<64, 5, 1024, EPI_GELU_SHIFT>(a, stream);
    a = GemmArgs{hsh, w2, x1, xa, nullptr, st_xa, st_x1, nullptr, nullptr, nullptr, 1, 0};
    launch_gemm<64, 5, 1024, EPI_W2>(a, stream);
    a = GemmArgs{x1, w3, gbuf, nullptr, nullptr, st_x1, nullptr, colsum + 4096, nullptr, nullptr, 1, 0};
    launch_gemm<224, 3, 1024, EPI_GELU>(a, stream);
    a = GemmArgs{gbuf, w4, xout, xa, x1, nullptr, st_xb, nullptr, nullptr, nullptr, 0, 0};
    launch_gemm<64, 5, 3584, EPI_W4>(a, stream);
  }
}
```

```cpp
#include <hip/hip_runtime.h>
#include <hip/hip_bf16.h>
#include <stdint.h>

typedef __attribute__((ext_vector_type(8))) short bf16x8;
typedef __attribute__((ext_vector_type(4))) short s16x4;
typedef __attribute__((ext_vector_type(4))) float f32x4;
typedef __attribute__((ext_vector_type(2))) float f32x2;
typedef __attribute__((ext_vector_type(4))) unsigned u32x4;
typedef __attribute__((ext_vector_type(2))) unsigned u32x2;
typedef __attribute__((ext_vector_type(2))) __bf16 bf16x2_t;
typedef unsigned short u16;
#define LDS_AS __attribute__((address_space(3)))
#define DI __device__ __forceinline__

static constexpr int SEQ = 2048, DM = 1024, NH = 32, FF = 3584;

DI float bf2f(unsigned v) { return __uint_as_float(v << 16); }
DI float bflo(unsigned v) { return __uint_as_float(v << 16); }
DI float bfhi(unsigned v) { return __uint_as_float(v & 0xffff0000u); }
DI unsigned pk2(float a, float b) {
  f32x2 v = {a, b};
  return __builtin_bit_cast(unsigned, __builtin_convertvector(v, bf16x2_t));
}
DI float rbf(float a) { return bflo(pk2(a, a)); }
template <int CTRL> DI float dppf(float v) { return __builtin_bit_cast(float, __builtin_amdgcn_update_dpp(0, __builtin_bit_cast(int, v), CTRL, 0xf, 0xf, true)); }
DI float sum8(float v) { v += dppf<0xB1>(v); v += dppf<0x4E>(v); return v + dppf<0x141>(v); }
DI f32x2 pair16(float v) { const unsigned u = __float_as_uint(v); const auto r = __builtin_amdgcn_permlane16_swap(u, u, false, false); return (f32x2){__uint_as_float(r[0]), __uint_as_float(r[1])}; }
DI f32x2 pair32(float v) { const unsigned u = __float_as_uint(v); const auto r = __builtin_amdgcn_permlane32_swap(u, u, false, false); return (f32x2){__uint_as_float(r[0]), __uint_as_float(r[1])}; }
DI float max4g(float v) { f32x2 t = pair16(v); t = pair32(fmaxf(t[0], t[1])); return fmaxf(t[0], t[1]); }
DI float min4g(float v) { f32x2 t = pair16(v); t = pair32(fminf(t[0], t[1])); return fminf(t[0], t[1]); }
DI float sum4g(float v) { f32x2 t = pair16(v); t = pair32(t[0] + t[1]); return t[0] + t[1]; }
DI float sum64(float v) { v = sum8(v); return sum4g(v + dppf<0x140>(v)); }

template <int I> struct IC { static constexpr int value = I; };
template <int B, int E, class F> DI void static_for(F&& f) {
  if constexpr (B < E) { f(IC<B>{}); static_for<B + 1, E>(f); }
}
template <int N> DI void wait_vm() { asm volatile("s_waitcnt vmcnt(%0)" ::"n"(N) : "memory"); }
DI void wait_lgkm0() { asm volatile("s_waitcnt lgkmcnt(0)" ::: "memory"); }
DI void bar() { __builtin_amdgcn_s_barrier(); }
DI void glds16(const void* src, char* lds_dst) {
  __builtin_amdgcn_global_load_lds((const unsigned*)src, (unsigned*)lds_dst, 16, 0, 0);
}
DI s16x4 trread(const char* p) {
  return __builtin_bit_cast(s16x4, __builtin_amdgcn_ds_read_tr16_b64_v4i16((LDS_AS s16x4*)p));
}
template <int OFF> DI s16x4 lds_tr(unsigned addr) {
  s16x4 r; asm volatile("ds_read_b64_tr_b16 %0, %1 offset:%2" : "=v"(r) : "v"(addr), "n"(OFF) : "memory"); return r;
}
template <int OFF> DI bf16x8 lds_b128(unsigned addr) {
  bf16x8 r; asm volatile("ds_read_b128 %0, %1 offset:%2" : "=v"(r) : "v"(addr), "n"(OFF) : "memory"); return r;
}
template <int OFF> DI f32x2 lds_f2(unsigned addr) {
  f32x2 r; asm volatile("ds_read_b64 %0, %1 offset:%2" : "=v"(r) : "v"(addr), "n"(OFF) : "memory"); return r;
}
template <int OFF> DI f32x4 lds_f4(unsigned addr) {
  f32x4 r; asm volatile("ds_read_b128 %0, %1 offset:%2" : "=v"(r) : "v"(addr), "n"(OFF) : "memory"); return r;
}
DI void st16_wt(void* base, unsigned bytes, unsigned off, u32x4 v) {
  __builtin_amdgcn_raw_buffer_store_b128(v, __builtin_amdgcn_make_buffer_rsrc(base, (short)0, (int)bytes, 0x00020000), (int)off, 0, 16);
}
DI bf16x8 cat8(s16x4 lo, s16x4 hi) { return (bf16x8){lo[0], lo[1], lo[2], lo[3], hi[0], hi[1], hi[2], hi[3]}; }
DI f32x4 mfma16(bf16x8 a, bf16x8 b, f32x4 c) { return __builtin_amdgcn_mfma_f32_16x16x32_bf16(a, b, c, 0, 0, 0); }

DI f32x2 gelu2(f32x2 x) {
  const float c0 = -2.302208198f, c1 = -2.302208198f * 0.044715f;
  const f32x2 a = x * (x * x * c1 + c0);
  const f32x2 d = (f32x2){__builtin_amdgcn_exp2f(a[0]), __builtin_amdgcn_exp2f(a[1])} + 1.0f;
  return x * (f32x2){__builtin_amdgcn_rcpf(d[0]), __builtin_amdgcn_rcpf(d[1])};
}
DI f32x2 rbf2(f32x2 v) { const unsigned p = pk2(v[0], v[1]); return (f32x2){bflo(p), bfhi(p)}; }
DI f32x2 unpk(unsigned p) { return (f32x2){bflo(p), bfhi(p)}; }

DI void feature_map(const bf16x8 (&wf)[2], bf16x8 xf, u32x4& pos, u32x4& neg) {
  f32x2 p[4];
#pragma unroll
  for (int fb = 0; fb < 2; ++fb) {
    const f32x4 d = mfma16(wf[fb], xf, (f32x4){0.f, 0.f, 0.f, 0.f});
    p[fb * 2] = rbf2((f32x2){d[0], d[1]});
    p[fb * 2 + 1] = rbf2((f32x2){d[2], d[3]});
  }
  float mx = fmaxf(p[0][0], p[0][1]), mn = fminf(p[0][0], p[0][1]);
#pragma unroll
  for (int e = 1; e < 4; ++e) { mx = fmaxf(mx, fmaxf(p[e][0], p[e][1])); mn = fminf(mn, fminf(p[e][0], p[e][1])); }
  mx = max4g(mx);
  mn = min4g(mn);
  const float l2e = 1.4426950408889634f;
  f32x2 ep[4], en[4], sp2 = {0.f, 0.f}, sn2 = {0.f, 0.f};
#pragma unroll
  for (int e = 0; e < 4; ++e) {
    const f32x2 a = (p[e] - mx) * l2e, b = (mn - p[e]) * l2e;
    ep[e] = (f32x2){__builtin_amdgcn_exp2f(a[0]), __builtin_amdgcn_exp2f(a[1])}; sp2 += ep[e];
    en[e] = (f32x2){__builtin_amdgcn_exp2f(b[0]), __builtin_amdgcn_exp2f(b[1])}; sn2 += en[e];
  }
  float sp = sp2[0] + sp2[1], sn = sn2[0] + sn2[1];
  sp = sum4g(sp);
  sn = sum4g(sn);
  const float ip = __builtin_amdgcn_rcpf(sp), in_ = __builtin_amdgcn_rcpf(sn);
#pragma unroll
  for (int e = 0; e < 4; ++e) {
    const f32x2 a = ep[e] * ip, b = en[e] * in_;
    pos[e] = pk2(a[0], a[1]); neg[e] = pk2(b[0], b[1]);
  }
}

#define TS(i)
struct GemmArgs {
  const u16* A;
  const u16* B;
  u16* out;
  const u16* res1;
  const u16* res2;
  const float* stats_in;
  float* stats_out;
  const float* colsum;
  const u16* wphi;
  float* kvpart;
  int ln_mode;
  int unused;
};

enum { EPI_QKV = 0, EPI_RES = 1, EPI_GELU_SHIFT = 2, EPI_W2 = 3, EPI_GELU = 4, EPI_W4 = 5 };

struct RowP { float alpha, beta, mhat, dhat; };
DI RowP row_params(float mean, float var, int mode) {
  RowP r;
  const float mh = rbf(mean);
  const float dh = rbf(__builtin_amdgcn_sqrtf(rbf(rbf(var) + 1e-5f)));
  const float i1 = __builtin_amdgcn_rcpf(dh);
  r.mhat = mh; r.dhat = dh;
  if (mode == 2) {
    const float m2 = (mean - mh) * i1;
    const float v2 = var * i1 * i1;
    const float mh2 = rbf(m2);
    const float dh2 = rbf(__builtin_amdgcn_sqrtf(rbf(rbf(v2) + 1e-5f)));
    const float i2 = __builtin_amdgcn_rcpf(dh2);
    r.alpha = i1 * i2;
    r.beta = (mh * i1 + mh2) * i2;
  } else {
    r.alpha = i1;
    r.beta = mh * i1;
  }
  return r;
}

template <int BN, int S, int KD, int EPI>
__global__ __launch_bounds__(512, 2) void gemm_k(GemmArgs g) {
  constexpr int NK = KD / 64;
  constexpr int NPF = BN / 64;
  constexpr bool HALF = (BN % 64) != 0;
  constexpr int NI = BN / 32;
  constexpr int A_BYTES = 16384;
  constexpr int STAGE = A_BYTES + BN * 128;
  constexpr int L = 4 + NPF * 2 + (HALF ? 1 : 0);
  constexpr int NTOT = (EPI == EPI_QKV) ? 3 * DM : (EPI == EPI_GELU ? FF : DM);
  constexpr bool USE_STATS = (EPI == EPI_QKV || EPI == EPI_GELU_SHIFT || EPI == EPI_W2 || EPI == EPI_GELU);
  constexpr bool FOLD = (EPI == EPI_QKV || EPI == EPI_GELU_SHIFT || EPI == EPI_GELU);
  constexpr int NRES = (EPI == EPI_RES || EPI == EPI_W2) ? 1 : (EPI == EPI_W4 ? 2 : 0);
  constexpr int OFF_STATS = S * STAGE;
  constexpr int OFF_RES = OFF_STATS;
  constexpr int OFF_WPHI = OFF_RES + NRES * 16384;
  constexpr int OFF_ROWP = OFF_WPHI + (EPI == EPI_QKV ? 4096 : 0);
  constexpr int OFF_CS = OFF_ROWP + 2048;
  constexpr int OFF_DUMP = OFF_CS + 1024;
  constexpr int NEXTRA = (USE_STATS && BN != 64 ? 8 : 0) + NRES * 4 + (EPI == EPI_QKV ? 1 : 0) + (FOLD ? 1 : 0);

  extern __shared__ __attribute__((aligned(16))) char smem[];
  const int tid = threadIdx.x, lane = tid & 63;
  const int w = __builtin_amdgcn_readfirstlane(tid >> 6);
  const bool prod = w >= 4;
  const int cw = w & 3, wr = cw >> 1, wc = cw & 1;

  const int b = blockIdx.x, xcd = b & 7, j = b >> 3;
  int rp, ct;
  if constexpr (EPI == EPI_QKV || EPI == EPI_GELU) { rp = (xcd >> 2) * 8 + (j >> 2); ct = (xcd & 3) * 4 + (j & 3); }
  else { rp = (xcd >> 1) * 4 + (j >> 3); ct = (xcd & 1) * 8 + (j & 7); }
  const int m0 = rp * 128;
  const int n0 = (EPI == EPI_QKV) ? ct * 64 : ct * BN;
  constexpr int PSTRIDE = (EPI == EPI_QKV) ? DM : 64;
  const int nf = HALF ? n0 + (ct & 1) * 32 : n0;
  const int nh = (ct & 1) ? n0 : n0 + NPF * 64;
  const int g4 = lane >> 4, q4 = (lane >> 2) & 3, pp = lane & 3;
  RowP* rowp = (RowP*)(smem + OFF_ROWP);
  const bool ln_on = USE_STATS && (g.ln_mode != 0 || EPI == EPI_W2);

  f32x4 acc[4][NI];
  f32x2 ab[4];
  f32x4 csv[NI];
  TS(0);
  constexpr bool RP_CONS = (BN == 64);
  f32x2 sv[8];
  const int sr = cw * 32 + (lane >> 1), shf = lane & 1;
  auto load_stats = [&]() {
#pragma unroll
    for (int p = 0; p < 8; ++p)
      asm volatile("global_load_dwordx2 %0, %1, off" : "=v"(sv[p]) : "v"(g.stats_in + ((size_t)(shf * 8 + p) * SEQ + m0 + sr) * 2) : "memory");
  };
  float rs_s = 0.f, rs_m2 = 0.f;
  auto row_stats = [&](auto pc) {
    constexpr int piece = decltype(pc)::value;
    if constexpr (piece == 0) {
#pragma unroll
      for (int p = 0; p < 8; ++p) asm volatile("" : "+v"(sv[p]));
      float s = 0.f;
#pragma unroll
      for (int p = 0; p < 8; ++p) s += sv[p][0];
      rs_s = s + dppf<0xB1>(s);
    } else if constexpr (piece == 1) {
      const float mean = rs_s * (1.0f / 1024.0f);
      float m2 = 0.f;
#pragma unroll
      for (int p = 0; p < 8; ++p) { const float d = sv[p][0] * (1.0f / 64.0f) - mean; m2 += sv[p][1] + 64.0f * d * d; }
      rs_m2 = m2 + dppf<0xB1>(m2);
    } else {
      if (ln_on && shf == 0) rowp[sr] = row_params(rs_s * (1.0f / 1024.0f), rs_m2 * (1.0f / 1024.0f), EPI == EPI_W2 ? 1 : g.ln_mode);
      wait_lgkm0();
    }
    __builtin_amdgcn_sched_barrier(0);
  };
  if (__builtin_expect(prod, 1)) {
    const u16* a_src = g.A + (size_t)(m0 + cw * 32 + (lane >> 3)) * KD + (((lane & 7) ^ (lane >> 3)) * 8);
    const u16* b_src[2];
#pragma unroll
    for (int e = 0; e < 2; ++e) {
      const int r = lane >> 3;
      const int f = ((r >> 1) & 1) | (e << 1);
      const int c = ((((lane & 7) >> 1) ^ f) << 1) | (lane & 1);
      b_src[e] = g.B + (size_t)((cw * 2 + e) * 8 + r) * NTOT + nf + c * 8;
    }
    const u16* bh_src = nullptr;
    if constexpr (HALF) {
      const int c = ((((lane >> 1) & 1) ^ ((lane >> 5) & 1)) << 1) | (lane & 1);
      bh_src = g.B + (size_t)(cw * 16 + (lane >> 2)) * NTOT + nh + c * 8;
    }
    auto issue = [&](int kt, int slot) {
      char* base = smem + slot * STAGE;
#pragma unroll
      for (int p = 0; p < NPF; ++p)
#pragma unroll
        for (int e = 0; e < 2; ++e)
          glds16(b_src[e] + (size_t)kt * 64 * NTOT + p * PSTRIDE, base + A_BYTES + p * 8192 + (cw * 2 + e) * 1024);
      if constexpr (HALF) glds16(bh_src + (size_t)kt * 64 * NTOT, base + A_BYTES + NPF * 8192 + cw * 1024);
#pragma unroll
      for (int e = 0; e < 4; ++e) glds16(a_src + (size_t)e * 8 * KD + kt * 64, base + (cw * 4 + e) * 1024);
    };
    TS(6);
    issue(0, 0);
    issue(1, 1);
    wait_vm<L>();
    TS(1);
    bar();
    static_for<2, S>([&](auto ic) { issue(ic.value, ic.value); });
    TS(7);
    if constexpr (NRES >= 1) {
#pragma unroll
      for (int e = 0; e < 4; ++e) {
        const int r = (cw * 4 + e) * 8 + (lane >> 3);
        glds16(g.res1 + (size_t)(m0 + r) * DM + n0 + (lane & 7) * 8, smem + OFF_RES + (cw * 4 + e) * 1024);
      }
    }
    if constexpr (NRES >= 2) {
#pragma unroll
      for (int e = 0; e < 4; ++e) {
        const int r = (cw * 4 + e) * 8 + (lane >> 3);
        glds16(g.res2 + (size_t)(m0 + r) * DM + n0 + (lane & 7) * 8, smem + OFF_RES + 16384 + (cw * 4 + e) * 1024);
      }
    }
    if constexpr (EPI == EPI_QKV) glds16(g.wphi + (size_t)ct * 2048 + cw * 512 + lane * 8, smem + OFF_WPHI + cw * 1024);
    if constexpr (FOLD) {
      int col = cw * 64 + (lane & 15) * 4;
      if (col > BN - 4) col = BN - 4;
      const int gcol = (EPI == EPI_QKV) ? (col >> 6) * DM + n0 + (col & 63) : (HALF && col >= NPF * 64) ? nh + col - NPF * 64 : nf + col;
      if (lane < 16) glds16(g.colsum + gcol, smem + OFF_CS + cw * 256);
    }
    if constexpr (USE_STATS && !RP_CONS) load_stats();
    static_for<0, NK - 1>([&](auto ic) {
      constexpr int kt = ic.value;
      constexpr int young = (S - 2 < NK - 2 - kt) ? S - 2 : NK - 2 - kt;
      constexpr int ex = (kt + 1 <= S - 1 && kt + 1 < NK - 1) ? NEXTRA : 0;
      wait_vm<young * L + ex>();
      bar();
      if constexpr (kt + S < NK) issue(kt + S, kt % S);
      if constexpr (USE_STATS && !RP_CONS && kt >= 4 && kt < 7) row_stats(IC<kt - 4>{});
    });
    TS(2);
    bar();
  } else {
    const int fsw = ((q4 >> 1) & 1) | ((g4 & 1) << 1);
    int a_off[2], b_off[2], bh_off = 0;
#pragma unroll
    for (int k = 0; k < 2; ++k) {
      a_off[k] = (wr * 64 + (lane & 15)) * 128 + (((k * 4 + g4) ^ (lane & 7)) * 16);
      b_off[k] = A_BYTES + (8 * g4 + q4) * 128 + (((2 * k + wc) ^ fsw) * 32) + pp * 8;
    }
    if constexpr (HALF) bh_off = A_BYTES + NPF * 8192 + (8 * g4 + q4) * 64 + ((wc ^ (g4 & 1)) * 32) + pp * 8;
#pragma unroll
    for (int m = 0; m < 4; ++m)
#pragma unroll
      for (int i = 0; i < NI; ++i) acc[m][i] = (f32x4){0.f, 0.f, 0.f, 0.f};
    bf16x8 xa[2][4];
    s16x4 wlo[2][NI], whi[2][NI];
    const unsigned lbase = (unsigned)(size_t)smem;

    auto ldfrag = [&](auto setc, auto slotc, auto kc) {
      constexpr int set = decltype(setc)::value, slot = decltype(slotc)::value, k32 = decltype(kc)::value;
      const unsigned sb = lbase + slot * STAGE;
      const unsigned ba0 = sb + b_off[0], ba1 = sb + b_off[1], bah = sb + bh_off, aa = sb + a_off[k32];
      static_for<0, NI>([&](auto ic) {
        constexpr int i = decltype(ic)::value;
        if constexpr (HALF && i == NI - 1) {
          wlo[set][i] = lds_tr<k32 * 2048>(bah);
          whi[set][i] = lds_tr<k32 * 2048 + 256>(bah);
        } else {
          wlo[set][i] = lds_tr<(i >> 1) * 8192 + k32 * 4096>((i & 1) ? ba1 : ba0);
          whi[set][i] = lds_tr<(i >> 1) * 8192 + k32 * 4096 + 512>((i & 1) ? ba1 : ba0);
        }
      });
      static_for<0, 4>([&](auto mc) { constexpr int m = decltype(mc)::value; xa[set][m] = lds_b128<m * 2048>(aa); });
    };
    auto wait_set = [&](auto setc) {
      constexpr int set = decltype(setc)::value;
      asm volatile("s_waitcnt lgkmcnt(0)" : "+v"(xa[set][0]), "+v"(xa[set][1]), "+v"(xa[set][2]), "+v"(xa[set][3]) :: "memory");
#pragma unroll
      for (int i = 0; i < NI; ++i) asm volatile("" : "+v"(wlo[set][i]), "+v"(whi[set][i]));
      __builtin_amdgcn_sched_barrier(0);
    };
    auto ld1 = [&](auto setc, auto slotc, auto kc, auto rc) {
      constexpr int set = decltype(setc)::value, slot = decltype(slotc)::value, k32 = decltype(kc)::value, r = decltype(rc)::value;
      const unsigned sb = lbase + slot * STAGE;
      if constexpr (r < 2 * NI) {
        constexpr int i = r >> 1, hi = r & 1;
        s16x4 v;
        if constexpr (HALF && i == NI - 1) v = lds_tr<k32 * 2048 + hi * 256>(sb + bh_off);
        else v = lds_tr<(i >> 1) * 8192 + k32 * 4096 + hi * 512>(sb + b_off[i & 1]);
        if constexpr (hi) whi[set][i] = v; else wlo[set][i] = v;
      } else {
        constexpr int m = r - 2 * NI;
        xa[set][m] = lds_b128<m * 2048>(sb + a_off[k32]);
      }
    };
    auto mma_ld = [&](auto setc, auto ldc, auto slotc, auto kc) {
      constexpr int set = decltype(setc)::value;
      constexpr bool ld = decltype(ldc)::value != 0;
      static_for<0, 4 * NI>([&](auto tc) {
        constexpr int t = decltype(tc)::value, i = t >> 2, m = t & 3;
        acc[m][i] = mfma16(cat8(wlo[set][i], whi[set][i]), xa[set][m], acc[m][i]);
        if constexpr (ld && t < 2 * NI + 4) ld1(IC<set ^ 1>{}, slotc, kc, tc);
        __builtin_amdgcn_sched_barrier(0);
      });
    };

    const char* tp = (const char*)g.A;
    unsigned tw = 0;
    bool ton = false;
    int tks = 0;
    if (cw == 0) {
      const int pl = lane >> 4;
      const int bl = (int)(((size_t)g.B >> 7) & 7);
      int off;
      if constexpr (BN == 64) { off = ((xcd & 1) * 8 + ((3 - bl) & 7)) * 128; ton = lane < 16; }
      else if constexpr (EPI == EPI_QKV) { const int c = (3 - bl - (xcd & 3) * 4) & 7; off = pl * 2048 + ((xcd & 3) * 4 + (c & 3)) * 128; ton = c < 4 && lane < 48; }
      else { const int l0 = (xcd & 3) * 14, c = (3 - bl - l0) & 7; off = (l0 + c + pl * 8) * 128; ton = lane < (c < 6 ? 32 : 16); }
      tks = (lane >> 1) & 7;
      if (ton) tp = (const char*)g.B + (size_t)(tks * 64 + j * 2 + (lane & 1)) * NTOT * 2 + off;
      tw = 8 * 64 * NTOT * 2;
    } else if (cw == 1) {
      tks = (3 - (int)(((size_t)g.A >> 7) & 7)) & 7;
      const int row = (BN == 64) ? (xcd >> 1) * 512 + j * 16 + (lane & 15) : (xcd >> 2) * 1024 + j * 32 + (lane & 31);
      ton = lane < (BN == 64 ? 16 : 32);
      tp = (const char*)g.A + (size_t)row * KD * 2 + tks * 128;
      tw = 1024;
    }
    auto touch = [&](auto wc_) {
      constexpr int wi = decltype(wc_)::value;
      if (cw < 2) {
        if (ton && (wi > 0 || tks >= S))
          __builtin_amdgcn_global_load_lds((const unsigned*)(tp + (size_t)(wi * tw)), (unsigned*)(smem + OFF_DUMP), 4, 0, 0);
      }
      __builtin_amdgcn_sched_barrier(0);
    };
    if constexpr (USE_STATS && RP_CONS) load_stats();
    touch(IC<0>{});
    if constexpr (BN == 64) touch(IC<1>{});
    bar();
    TS(1);
    ldfrag(IC<0>{}, IC<0>{}, IC<0>{});
    static_for<0, NK>([&](auto ic) {
      constexpr int kt = ic.value;
      constexpr int slot = kt % S;
      wait_set(IC<0>{});
      if constexpr (BN != 64 && kt == 0) touch(IC<1>{});
      if constexpr (kt % 8 == 4 && (kt + 12) / 8 < NK / 8) touch(IC<(kt + 12) / 8>{});
      if constexpr (USE_STATS && RP_CONS && kt >= 6 && kt < 9) { if constexpr (kt == 6) wait_vm<0>(); row_stats(IC<kt - 6>{}); }
      mma_ld(IC<0>{}, IC<1>{}, IC<slot>{}, IC<1>{});
      wait_set(IC<1>{});
      if constexpr (kt + 1 < NK) {
        bar();
        __builtin_amdgcn_sched_barrier(0);
        mma_ld(IC<1>{}, IC<1>{}, IC<(kt + 1) % S>{}, IC<0>{});
      } else {
        const unsigned ra = lbase + OFF_ROWP + (wr * 64 + (lane & 15)) * 16, ca = lbase + OFF_CS + (wc * 16 + g4 * 4) * 4;
        static_for<0, 4 * NI>([&](auto tc) {
          constexpr int t = decltype(tc)::value, i = t >> 2, m = t & 3;
          acc[m][i] = mfma16(cat8(wlo[1][i], whi[1][i]), xa[1][m], acc[m][i]);
          if constexpr (FOLD && t < 4) ab[t] = lds_f2<t * 256>(ra);
          if constexpr (FOLD && m == 3) csv[i] = lds_f4<i * 128>(ca);
          __builtin_amdgcn_sched_barrier(0);
        });
      }
    });
    TS(2);
    bar();
    if constexpr (FOLD) {
      asm volatile("s_waitcnt lgkmcnt(0)" : "+v"(ab[0]), "+v"(ab[1]), "+v"(ab[2]), "+v"(ab[3]) :: "memory");
#pragma unroll
      for (int i = 0; i < NI; ++i) asm volatile("" : "+v"(csv[i]));
      if (!ln_on) {
#pragma unroll
        for (int m = 0; m < 4; ++m) ab[m] = (f32x2){1.f, 0.f};
      }
    }
  }
  TS(3);
  if constexpr (EPI == EPI_QKV) __builtin_amdgcn_s_waitcnt(0x0F70);

  constexpr int TSTR = BN * 2 + (EPI == EPI_QKV ? 32 : 16);
  char* T = smem;
  u32x4 pv[EPI == EPI_QKV ? 11 : 1];
  if (!prod) {
#pragma unroll
    for (int i = 0; i < NI; ++i) {
      const int nb = 2 * i + wc;
      const int ncol = nb * 16 + g4 * 4;
      const f32x4 cs = csv[i];
#pragma unroll
      for (int m = 0; m < 4; ++m) {
        u32x2 pkd;
#pragma unroll
        for (int h = 0; h < 2; ++h) {
          f32x2 v = {acc[m][i][2 * h], acc[m][i][2 * h + 1]};
          if constexpr (FOLD) v = v * ab[m][0] - (f32x2){cs[2 * h], cs[2 * h + 1]} * ab[m][1];
          pkd[h] = pk2(v[0], v[1]);
        }
        *(u32x2*)(T + (wr * 64 + m * 16 + (lane & 15)) * TSTR + ncol * 2) = pkd;
      }
    }
  } else if constexpr (EPI == EPI_QKV) {
    if (g.res1) {
      const int t = tid - 256;
      const u16* W4 = (const u16*)g.stats_out + (size_t)b * 14336 + t * 8;
      const u16* Wo = g.res1 + (size_t)b * 4096 + t * 8;
      const u16* W2 = g.res2 + (size_t)b * 4096 + t * 8;
#pragma unroll
      for (int i = 0; i < 2; ++i) { pv[7 + i] = *(const u32x4*)(Wo + i * 2048); pv[9 + i] = *(const u32x4*)(W2 + i * 2048); }
#pragma unroll
      for (int i = 0; i < 7; ++i) pv[i] = *(const u32x4*)(W4 + i * 2048);
    }
  }
  if constexpr (EPI == EPI_QKV) { wait_lgkm0(); bar(); asm volatile("" ::: "memory"); }
  else __syncthreads();
  TS(4);
  auto gelu8 = [&](u32x4 v) {
    u32x4 o;
#pragma unroll
    for (int e = 0; e < 4; ++e) { const f32x2 y = gelu2(unpk(v[e])); o[e] = pk2(y[0], y[1]); }
    return o;
  };
  if constexpr (EPI == EPI_GELU) {
    u32x4 v[NI];
#pragma unroll
    for (int it = 0; it < NI; ++it) {
      const int r = it < NI - 1 ? (it & 1) * 64 + (tid >> 3) : tid >> 2, c = it < NI - 1 ? (it >> 1) * 8 + (tid & 7) : (NI / 2) * 8 + (tid & 3);
      v[it] = *(const u32x4*)(T + r * TSTR + c * 16);
    }
#pragma unroll
    for (int it = 0; it < NI; ++it) {
      const int r = it < NI - 1 ? (it & 1) * 64 + (tid >> 3) : tid >> 2, col = it < NI - 1 ? nf + (it >> 1) * 64 + (tid & 7) * 8 : nh + (tid & 3) * 8;
      st16_wt(g.out, SEQ * FF * 2, ((m0 + r) * FF + col) * 2, gelu8(v[it]));
    }
  } else if constexpr (EPI == EPI_GELU_SHIFT) {
    const int grp = n0 >> 8;
    const int sh = (grp == 0) ? 1 : (grp == 1) ? -1 : (grp == 2) ? 2 : -2;
#pragma unroll
    for (int it = 0; it < 2; ++it) {
      const int r = it * 64 + (tid >> 3), c = tid & 7;
      const u32x4 v = gelu8(*(const u32x4*)(T + r * TSTR + c * 16));
      const int dr = m0 + r + sh;
      if (dr >= 0 && dr < SEQ) st16_wt(g.out, SEQ * DM * 2, (dr * DM + n0 + c * 8) * 2, v);
    }
    if (tid < 16) {
      const int zr = tid >> 3, c = tid & 7;
      const int az = sh > 0 ? sh : -sh;
      if (zr < az) {
        if (sh > 0 && rp == 0) *(u32x4*)(g.out + (size_t)zr * DM + n0 + c * 8) = (u32x4){0u, 0u, 0u, 0u};
        if (sh < 0 && rp == 15) *(u32x4*)(g.out + (size_t)(SEQ - 1 - zr) * DM + n0 + c * 8) = (u32x4){0u, 0u, 0u, 0u};
      }
    }
  } else if constexpr (EPI == EPI_RES || EPI == EPI_W2 || EPI == EPI_W4) {
#pragma unroll
    for (int it = 0; it < 2; ++it) {
      const int r = it * 64 + (tid >> 3), c = tid & 7;
      const u32x4 v = *(const u32x4*)(T + r * TSTR + c * 16);
      const u32x4 x = *(const u32x4*)(smem + OFF_RES + r * 128 + c * 16);
      f32x2 y[4];
      if constexpr (EPI == EPI_RES) {
#pragma unroll
        for (int e = 0; e < 4; ++e) y[e] = unpk(x[e]) + unpk(v[e]);
      } else if constexpr (EPI == EPI_W2) {
        const RowP rr = rowp[r];
#pragma unroll
        for (int e = 0; e < 4; ++e) y[e] = rbf2(rbf2(unpk(x[e]) - rr.mhat) * rr.alpha) + unpk(v[e]);
      } else {
        const u32x4 x1 = *(const u32x4*)(smem + OFF_RES + 16384 + r * 128 + c * 16);
#pragma unroll
        for (int e = 0; e < 4; ++e) y[e] = unpk(x[e]) + rbf2(unpk(x1[e]) + unpk(v[e]));
      }
      u32x4 o;
#pragma unroll
      for (int e = 0; e < 4; ++e) { o[e] = pk2(y[e][0], y[e][1]); y[e] = unpk(o[e]); }
      st16_wt(g.out, SEQ * DM * 2, ((m0 + r) * DM + n0 + c * 8) * 2, o);
      const f32x2 s2 = (y[0] + y[1]) + (y[2] + y[3]);
      const float s = sum8(s2[0] + s2[1]);
      const float mc = s * (1.0f / 64.0f);
      f32x2 q2 = {0.f, 0.f};
#pragma unroll
      for (int e = 0; e < 4; ++e) { const f32x2 d = y[e] - mc; q2 += d * d; }
      const float m2 = sum8(q2[0] + q2[1]);
      if (c == 0) *(f32x2*)(g.stats_out + ((size_t)ct * SEQ + m0 + r) * 2) = (f32x2){s, m2};
    }
  } else {
    constexpr int PKSTR = 160;
    char* PK = smem + 128 * TSTR;
#pragma unroll
    for (int it = 0; it < 2; ++it) {
      const int r = it * 64 + (tid >> 3), c = tid & 7;
      st16_wt(g.out, SEQ * DM * 2, ((m0 + r) * DM + n0 + c * 8) * 2, *(const u32x4*)(T + r * TSTR + c * 16));
    }
    {
      const int hh = w & 1, rq = w >> 1;
      bf16x8 wf[2];
#pragma unroll
      for (int fb = 0; fb < 2; ++fb) {
        const char* p = smem + OFF_WPHI + hh * 2048 + (8 * g4 + q4) * 64 + fb * 32 + pp * 8;
        wf[fb] = cat8(trread(p), trread(p + 256));
      }
#pragma unroll
      for (int mb = 0; mb < 2; ++mb) {
        const int row = rq * 32 + mb * 16 + (lane & 15);
        const bf16x8 xf = *(const bf16x8*)(T + row * TSTR + (64 + hh * 32 + g4 * 8) * 2);
        u32x4 pos, neg;
        feature_map(wf, xf, pos, neg);
        char* dst = PK + (hh * 128 + row) * PKSTR + g4 * 8;
        *(u32x2*)dst = (u32x2){pos[0], pos[1]}; *(u32x2*)(dst + 32) = (u32x2){pos[2], pos[3]};
        *(u32x2*)(dst + 64) = (u32x2){neg[0], neg[1]}; *(u32x2*)(dst + 96) = (u32x2){neg[2], neg[3]};
      }
    }
    __syncthreads();
    {
      const int hh = w >> 2, fa = w & 3;
      f32x4 kv[3];
#pragma unroll
      for (int d = 0; d < 3; ++d) kv[d] = (f32x4){0.f, 0.f, 0.f, 0.f};
      const short one = (short)0x3F80;
      const bf16x8 ones = {one, one, one, one, one, one, one, one};
#pragma unroll
      for (int ks = 0; ks < 4; ++ks) {
        const int row = ks * 32 + 8 * g4 + q4;
        bf16x8 vf[2];
#pragma unroll
        for (int d = 0; d < 2; ++d) {
          const char* p = T + row * TSTR + (128 + hh * 32 + d * 16) * 2 + pp * 8;
          vf[d] = cat8(trread(p), trread(p + 4 * TSTR));
        }
        const char* p = PK + (hh * 128 + row) * PKSTR + (fa * 16) * 2 + pp * 8;
        const bf16x8 pf = cat8(trread(p), trread(p + 4 * PKSTR));
        kv[0] = mfma16(pf, vf[0], kv[0]);
        kv[1] = mfma16(pf, vf[1], kv[1]);
        kv[2] = mfma16(pf, ones, kv[2]);
      }
      constexpr unsigned KVB = 16 * NH * 33 * 64 * 4;
      const int so = (rp * NH + ct * 2 + hh) * (33 * 64) + fa * 16 + g4 * 4;
      st16_wt(g.kvpart, KVB, (so + (lane & 15) * 64) * 4, __builtin_bit_cast(u32x4, kv[0]));
      st16_wt(g.kvpart, KVB, (so + (16 + (lane & 15)) * 64) * 4, __builtin_bit_cast(u32x4, kv[1]));
      if ((lane & 15) == 0) st16_wt(g.kvpart, KVB, (so + 32 * 64) * 4, __builtin_bit_cast(u32x4, kv[2]));
    }
    if (prod && g.res1) {
#pragma unroll
      for (int i = 0; i < 11; ++i) asm volatile("" ::"v"(pv[i]));
    }
  }
  TS(5);
}

__global__ __launch_bounds__(256) void ln0_k(const u16* __restrict__ x, u16* __restrict__ y, const u16* __restrict__ wqkv,
                                             const u16* __restrict__ w1, const u16* __restrict__ w3, float* __restrict__ colsum,
                                             const u16* __restrict__ wo, const u16* __restrict__ w2, const u16* __restrict__ w4) {
  if (blockIdx.x >= SEQ / 4 + 120) {
    const int t = blockIdx.x - (SEQ / 4 + 120);
    const u16* W = t < 32 ? wo + (size_t)t * 32768 : (t < 64 ? w2 + (size_t)(t - 32) * 32768 : w4 + (size_t)(t - 64) * 32768);
#pragma unroll
    for (int i = 0; i < 16; ++i) {
      const u32x4 v = *(const u32x4*)(W + (i * 256 + threadIdx.x) * 8);
      asm volatile("" ::"v"(v));
    }
    return;
  }
  if (blockIdx.x >= SEQ / 4) {
    __shared__ float red[32 * 64];
    const int tid = threadIdx.x;
    const int cg = blockIdx.x - SEQ / 4;
    const u16* W; int N, c0;
    if (cg < 48) { W = wqkv; N = 3 * DM; c0 = cg * 64; }
    else if (cg < 64) { W = w1; N = DM; c0 = (cg - 48) * 64; }
    else { W = w3; N = FF; c0 = (cg - 64) * 64; }
    const int c = tid & 7, rg = tid >> 3;
    f32x2 s[4] = {{0.f, 0.f}, {0.f, 0.f}, {0.f, 0.f}, {0.f, 0.f}};
#pragma unroll 16
    for (int i = 0; i < 32; ++i) {
      const u32x4 v = *(const u32x4*)(W + (size_t)(rg + i * 32) * N + c0 + c * 8);
#pragma unroll
      for (int e = 0; e < 4; ++e) s[e] += unpk(v[e]);
    }
#pragma unroll
    for (int e = 0; e < 4; ++e) *(f32x2*)(red + rg * 64 + c * 8 + 2 * e) = s[e];
    __syncthreads();
    if (tid < 64) {
      float t = 0.f;
#pragma unroll
      for (int i = 0; i < 32; ++i) t += red[i * 64 + tid];
      colsum[cg * 64 + tid] = t;
    }
    return;
  }
  const int lane = threadIdx.x & 63, row = blockIdx.x * 4 + (threadIdx.x >> 6);
  const u16* src = x + (size_t)row * DM;
  const u32x4 a = *(const u32x4*)(src + lane * 8), b = *(const u32x4*)(src + 512 + lane * 8);
  float v[16];
#pragma unroll
  for (int e = 0; e < 4; ++e) { v[2 * e] = bflo(a[e]); v[2 * e + 1] = bfhi(a[e]); v[8 + 2 * e] = bflo(b[e]); v[9 + 2 * e] = bfhi(b[e]); }
  float s = 0.f;
#pragma unroll
  for (int e = 0; e < 16; ++e) s += v[e];
  s = sum64(s);
  const float mean = s * (1.0f / 1024.0f);
  float m2 = 0.f;
#pragma unroll
  for (int e = 0; e < 16; ++e) { const float d = v[e] - mean; m2 += d * d; }
  m2 = sum64(m2);
  const RowP r = row_params(mean, m2 * (1.0f / 1024.0f), 1);
  u32x4 oa, ob;
#pragma unroll
  for (int e = 0; e < 4; ++e) {
    oa[e] = pk2(rbf(v[2 * e] - r.mhat) / r.dhat, rbf(v[2 * e + 1] - r.mhat) / r.dhat);
    ob[e] = pk2(rbf(v[8 + 2 * e] - r.mhat) / r.dhat, rbf(v[9 + 2 * e] - r.mhat) / r.dhat);
  }
  u16* dst = y + (size_t)row * DM;
  *(u32x4*)(dst + lane * 8) = oa;
  *(u32x4*)(dst + 512 + lane * 8) = ob;
}

__global__ __launch_bounds__(512) void attn_o_k(const u16* __restrict__ q, const u16* __restrict__ wphi, const float* __restrict__ kvpart,
                                                u16* __restrict__ o) {
  constexpr int KS = 80;
  __shared__ __attribute__((aligned(16))) char sm[33 * KS * 2 + 8];
  const int tid = threadIdx.x, lane = tid & 63, w = tid >> 6;
  constexpr int EPI = 6;
  TS(0);
  const int h = blockIdx.x & 31, rb = blockIdx.x >> 5;
  const int g4 = lane >> 4, r0 = rb * 256 + w * 32 + (lane & 15);
  bf16x8 qf[2], wf[2];
#pragma unroll
  for (int mb = 0; mb < 2; ++mb) qf[mb] = *(const bf16x8*)(q + (size_t)(r0 + mb * 16) * DM + h * 32 + g4 * 8);
#pragma unroll
  for (int fb = 0; fb < 2; ++fb)
#pragma unroll
    for (int j = 0; j < 8; ++j) wf[fb][j] = (short)wphi[h * 1024 + (g4 * 8 + j) * 32 + fb * 16 + (lane & 15)];
  __builtin_amdgcn_sched_barrier(0);
  bar();
  __builtin_amdgcn_sched_barrier(0);
  f32x4 kp[16], kz[4];
  const float* slab = kvpart + (size_t)h * (33 * 64) + tid * 4;
  const bool zw = __builtin_amdgcn_readfirstlane(w) == 0;
  if (zw) {
#pragma unroll
    for (int p = 0; p < 4; ++p) kz[p] = *(const f32x4*)(kvpart + ((size_t)(p * 4 + g4) * NH + h) * (33 * 64) + 2048 + (lane & 15) * 4);
  }
#pragma unroll
  for (int p = 0; p < 16; ++p) kp[p] = *(const f32x4*)(slab + (size_t)p * NH * (33 * 64));
  __builtin_amdgcn_sched_barrier(0);
  bf16x8 pq[2][2];
#pragma unroll
  for (int mb = 0; mb < 2; ++mb) {
    u32x4 pos, neg;
    feature_map(wf, qf[mb], pos, neg);
    pq[mb][0] = __builtin_bit_cast(bf16x8, pos);
    pq[mb][1] = __builtin_bit_cast(bf16x8, neg);
  }
  __builtin_amdgcn_sched_barrier(0);
  TS(1);
  u16* kvt = (u16*)sm;
  {
    f32x4 t = kp[0];
#pragma unroll
    for (int p = 1; p < 16; ++p) t += kp[p];
    *(u32x2*)(kvt + (tid >> 4) * KS + (tid & 15) * 4) = (u32x2){pk2(t[0], t[1]), pk2(t[2], t[3])};
    if (zw) {
      f32x4 z = (kz[0] + kz[1]) + (kz[2] + kz[3]);
#pragma unroll
      for (int e = 0; e < 4; ++e) z[e] = sum4g(z[e]);
      if (tid < 16) *(u32x2*)(kvt + 32 * KS + tid * 4) = (u32x2){pk2(z[0], z[1]), pk2(z[2], z[3])};
    }
  }
  TS(2);
  __syncthreads();
  TS(3);
  bf16x8 kf[3][2];
#pragma unroll
  for (int db = 0; db < 3; ++db)
#pragma unroll
    for (int ks = 0; ks < 2; ++ks) {
      const int d = (db < 2) ? ((lane >> 2) & 3) * 8 + db * 4 + (lane & 3) : 32;
      const u16* p = kvt + d * KS + ks * 32 + g4 * 4;
      kf[db][ks] = cat8(*(const s16x4*)p, *(const s16x4*)(p + 16));
    }
#pragma unroll
  for (int mb = 0; mb < 2; ++mb) {
    f32x4 a[3];
#pragma unroll
    for (int db = 0; db < 3; ++db) {
      a[db] = mfma16(kf[db][0], pq[mb][0], (f32x4){0.f, 0.f, 0.f, 0.f});
      a[db] = mfma16(kf[db][1], pq[mb][1], a[db]);
    }
    const float id = __builtin_amdgcn_rcpf(rbf(rbf(a[2][0]) + 1e-6f));
    u32x4 ov;
#pragma unroll
    for (int db = 0; db < 2; ++db) {
      const f32x2 lo = rbf2((f32x2){a[db][0], a[db][1]}) * id, hi = rbf2((f32x2){a[db][2], a[db][3]}) * id;
      ov[db * 2] = pk2(lo[0], lo[1]); ov[db * 2 + 1] = pk2(hi[0], hi[1]);
    }
    st16_wt(o, SEQ * DM * 2, ((r0 + mb * 16) * DM + h * 32 + g4 * 8) * 2, ov);
  }
  TS(4);
  TS(5);
}

template <int BN, int S, int KD, int EPI> static constexpr int smem_bytes() {
  constexpr bool USE_STATS = (EPI == EPI_QKV || EPI == EPI_GELU_SHIFT || EPI == EPI_W2 || EPI == EPI_GELU);
  constexpr int NRES = (EPI == EPI_RES || EPI == EPI_W2) ? 1 : (EPI == EPI_W4 ? 2 : 0);
  return S * (16384 + BN * 128) + NRES * 16384 + (EPI == EPI_QKV ? 4096 : 0) + 2048 + 1024 + 256;
}
template <int BN, int S, int KD, int EPI> static void launch_gemm(const GemmArgs& a, hipStream_t st) {
  constexpr int sb = smem_bytes<BN, S, KD, EPI>();
  static_assert(sb <= 163840, "LDS");
  (void)hipFuncSetAttribute((const void*)gemm_k<BN, S, KD, EPI>, hipFuncAttributeMaxDynamicSharedMemorySize, sb);
  gemm_k<BN, S, KD, EPI><<<256, 512, sb, st>>>(a);
}

extern "C" void kernel_launch(void* const* d_in, const int* in_sizes, int n_in, void* d_out, int out_size, void* d_ws,
                              size_t ws_size, hipStream_t stream) {
  const u16* x0 = (const u16*)d_in[0];
  const u16* wqkv = (const u16*)d_in[1];
  const u16* wphi = (const u16*)d_in[2];
  const u16* wo = (const u16*)d_in[3];
  const u16* w1 = (const u16*)d_in[5];
  const u16* w2 = (const u16*)d_in[6];
  const u16* w3 = (const u16*)d_in[7];
  const u16* w4 = (const u16*)d_in[8];
  char* ws = (char*)d_ws;
  const size_t MB = 1 << 20;
  u16* xln0 = (u16*)(ws + 0 * MB);
  u16* phiq = (u16*)(ws + 4 * MB);
  float* kvpart = (float*)(ws + 12 * MB);
  u16* obuf = (u16*)(ws + 17 * MB);
  u16* xa = (u16*)(ws + 21 * MB);
  u16* hsh = (u16*)(ws + 25 * MB);
  u16* x1 = (u16*)(ws + 29 * MB);
  u16* gbuf = (u16*)(ws + 33 * MB);
  u16* xb = (u16*)(ws + 48 * MB);
  float* st_xa = (float*)(ws + 52 * MB);
  float* st_x1 = (float*)(ws + 53 * MB);
  float* st_xb = (float*)(ws + 54 * MB);
  float* colsum = (float*)(ws + 55 * MB);

  ln0_k<<<SEQ / 4 + 120, 256, 0, stream>>>(x0, xln0, wqkv, w1, w3, colsum, wo, w2, w4);
  for (int layer = 0; layer < 2; ++layer) {
    const u16* xin = layer == 0 ? x0 : xb;
    u16* xout = layer == 0 ? xb : (u16*)d_out;
    GemmArgs a;
    a = GemmArgs{layer == 0 ? xln0 : xb, wqkv, phiq, layer == 0 ? wo : nullptr, w2, st_xb, (float*)w4, colsum, wphi, kvpart, layer == 0 ? 0 : 1, 0};
    launch_gemm<192, 3, 1024, EPI_QKV>(a, stream);
    attn_o_k<<<256, 512, 0, stream>>>(phiq, wphi, kvpart, obuf);
    a = GemmArgs{obuf, wo, xa, xin, nullptr, nullptr, st_xa, nullptr, nullptr, nullptr, 0, 0};
    launch_gemm<64, 5, 1024, EPI_RES>(a, stream);
    a = GemmArgs{xa, w1, hsh, nullptr, nullptr, st_xa, nullptr, colsum + 3072, nullptr, nullptr, 2, 0};
    launch_gemm<64, 5, 1024, EPI_GELU_SHIFT>(a, stream);
    a = GemmArgs{hsh, w2, x1, xa, nullptr, st_xa, st_x1, nullptr, nullptr, nullptr, 1, 0};
    launch_gemm<64, 5, 1024, EPI_W2>(a, stream);
    a = GemmArgs{x1, w3, gbuf, nullptr, nullptr, st_x1, nullptr, colsum + 4096, nullptr, nullptr, 1, 0};
    launch_gemm<224, 3, 1024, EPI_GELU>(a, stream);
    a = GemmArgs{gbuf, w4, xout, xa, x1, nullptr, st_xb, nullptr, nullptr, nullptr, 0, 0};
    launch_gemm<64, 5, 3584, EPI_W4>(a, stream);
  }
}
```

```cpp
#include <hip/hip_runtime.h>
#include <hip/hip_bf16.h>
#include <stdint.h>

typedef __attribute__((ext_vector_type(8))) short bf16x8;
typedef __attribute__((ext_vector_type(4))) short s16x4;
typedef __attribute__((ext_vector_type(4))) float f32x4;
typedef __attribute__((ext_vector_type(2))) float f32x2;
typedef __attribute__((ext_vector_type(4))) unsigned u32x4;
typedef __attribute__((ext_vector_type(2))) unsigned u32x2;
typedef __attribute__((ext_vector_type(2))) __bf16 bf16x2_t;
typedef unsigned short u16;
#define LDS_AS __attribute__((address_space(3)))
#define DI __device__ __forceinline__

static constexpr int SEQ = 2048, DM = 1024, NH = 32, FF = 3584;

DI float bf2f(unsigned v) { return __uint_as_float(v << 16); }
DI float bflo(unsigned v) { return __uint_as_float(v << 16); }
DI float bfhi(unsigned v) { return __uint_as_float(v & 0xffff0000u); }
DI unsigned pk2(float a, float b) {
  f32x2 v = {a, b};
  return __builtin_bit_cast(unsigned, __builtin_convertvector(v, bf16x2_t));
}
DI float rbf(float a) { return bflo(pk2(a, a)); }
template <int CTRL> DI float dppf(float v) { return __builtin_bit_cast(float, __builtin_amdgcn_update_dpp(0, __builtin_bit_cast(int, v), CTRL, 0xf, 0xf, true)); }
DI float sum8(float v) { v += dppf<0xB1>(v); v += dppf<0x4E>(v); return v + dppf<0x141>(v); }
DI f32x2 pair16(float v) { const unsigned u = __float_as_uint(v); const auto r = __builtin_amdgcn_permlane16_swap(u, u, false, false); return (f32x2){__uint_as_float(r[0]), __uint_as_float(r[1])}; }
DI f32x2 pair32(float v) { const unsigned u = __float_as_uint(v); const auto r = __builtin_amdgcn_permlane32_swap(u, u, false, false); return (f32x2){__uint_as_float(r[0]), __uint_as_float(r[1])}; }
DI float max4g(float v) { f32x2 t = pair16(v); t = pair32(fmaxf(t[0], t[1])); return fmaxf(t[0], t[1]); }
DI float min4g(float v) { f32x2 t = pair16(v); t = pair32(fminf(t[0], t[1])); return fminf(t[0], t[1]); }
DI float sum4g(float v) { f32x2 t = pair16(v); t = pair32(t[0] + t[1]); return t[0] + t[1]; }
DI float sum64(float v) { v = sum8(v); return sum4g(v + dppf<0x140>(v)); }

template <int I> struct IC { static constexpr int value = I; };
template <int B, int E, class F> DI void static_for(F&& f) {
  if constexpr (B < E) { f(IC<B>{}); static_for<B + 1, E>(f); }
}
template <int N> DI void wait_vm() { asm volatile("s_waitcnt vmcnt(%0)" ::"n"(N) : "memory"); }
DI void wait_lgkm0() { asm volatile("s_waitcnt lgkmcnt(0)" ::: "memory"); }
DI void bar() { __builtin_amdgcn_s_barrier(); }
DI void glds16(const void* src, char* lds_dst) {
  __builtin_amdgcn_global_load_lds((const unsigned*)src, (unsigned*)lds_dst, 16, 0, 0);
}
DI s16x4 trread(const char* p) {
  return __builtin_bit_cast(s16x4, __builtin_amdgcn_ds_read_tr16_b64_v4i16((LDS_AS s16x4*)p));
}
template <int OFF> DI s16x4 lds_tr(unsigned addr) {
  s16x4 r; asm volatile("ds_read_b64_tr_b16 %0, %1 offset:%2" : "=v"(r) : "v"(addr), "n"(OFF) : "memory"); return r;
}
template <int OFF> DI bf16x8 lds_b128(unsigned addr) {
  bf16x8 r; asm volatile("ds_read_b128 %0, %1 offset:%2" : "=v"(r) : "v"(addr), "n"(OFF) : "memory"); return r;
}
template <int OFF> DI f32x2 lds_f2(unsigned addr) {
  f32x2 r; asm volatile("ds_read_b64 %0, %1 offset:%2" : "=v"(r) : "v"(addr), "n"(OFF) : "memory"); return r;
}
template <int OFF> DI f32x4 lds_f4(unsigned addr) {
  f32x4 r; asm volatile("ds_read_b128 %0, %1 offset:%2" : "=v"(r) : "v"(addr), "n"(OFF) : "memory"); return r;
}
DI void st16_wt(void* base, unsigned bytes, unsigned off, u32x4 v) {
  __builtin_amdgcn_raw_buffer_store_b128(v, __builtin_amdgcn_make_buffer_rsrc(base, (short)0, (int)bytes, 0x00020000), (int)off, 0, 16);
}
DI bf16x8 cat8(s16x4 lo, s16x4 hi) { return (bf16x8){lo[0], lo[1], lo[2], lo[3], hi[0], hi[1], hi[2], hi[3]}; }
DI f32x4 mfma16(bf16x8 a, bf16x8 b, f32x4 c) { return __builtin_amdgcn_mfma_f32_16x16x32_bf16(a, b, c, 0, 0, 0); }

DI f32x2 gelu2(f32x2 x) {
  const float c0 = -2.302208198f, c1 = -2.302208198f * 0.044715f;
  const f32x2 a = x * (x * x * c1 + c0);
  const f32x2 d = (f32x2){__builtin_amdgcn_exp2f(a[0]), __builtin_amdgcn_exp2f(a[1])} + 1.0f;
  return x * (f32x2){__builtin_amdgcn_rcpf(d[0]), __builtin_amdgcn_rcpf(d[1])};
}
DI f32x2 rbf2(f32x2 v) { const unsigned p = pk2(v[0], v[1]); return (f32x2){bflo(p), bfhi(p)}; }
DI f32x2 unpk(unsigned p) { return (f32x2){bflo(p), bfhi(p)}; }

DI void feature_map(const bf16x8 (&wf)[2], bf16x8 xf, u32x4& pos, u32x4& neg) {
  f32x2 p[4];
#pragma unroll
  for (int fb = 0; fb < 2; ++fb) {
    const f32x4 d = mfma16(wf[fb], xf, (f32x4){0.f, 0.f, 0.f, 0.f});
    p[fb * 2] = rbf2((f32x2){d[0], d[1]});
    p[fb * 2 + 1] = rbf2((f32x2){d[2], d[3]});
  }
  float mx = fmaxf(p[0][0], p[0][1]), mn = fminf(p[0][0], p[0][1]);
#pragma unroll
  for (int e = 1; e < 4; ++e) { mx = fmaxf(mx, fmaxf(p[e][0], p[e][1])); mn = fminf(mn, fminf(p[e][0], p[e][1])); }
  mx = max4g(mx);
  mn = min4g(mn);
  const float l2e = 1.4426950408889634f;
  f32x2 ep[4], en[4], sp2 = {0.f, 0.f}, sn2 = {0.f, 0.f};
#pragma unroll
  for (int e = 0; e < 4; ++e) {
    const f32x2 a = (p[e] - mx) * l2e, b = (mn - p[e]) * l2e;
    ep[e] = (f32x2){__builtin_amdgcn_exp2f(a[0]), __builtin_amdgcn_exp2f(a[1])}; sp2 += ep[e];
    en[e] = (f32x2){__builtin_amdgcn_exp2f(b[0]), __builtin_amdgcn_exp2f(b[1])}; sn2 += en[e];
  }
  float sp = sp2[0] + sp2[1], sn = sn2[0] + sn2[1];
  sp = sum4g(sp);
  sn = sum4g(sn);
  const float ip = __builtin_amdgcn_rcpf(sp), in_ = __builtin_amdgcn_rcpf(sn);
#pragma unroll
  for (int e = 0; e < 4; ++e) {
    const f32x2 a = ep[e] * ip, b = en[e] * in_;
    pos[e] = pk2(a[0], a[1]); neg[e] = pk2(b[0], b[1]);
  }
}

#define TS(i)
struct GemmArgs {
  const u16* A;
  const u16* B;
  u16* out;
  const u16* res1;
  const u16* res2;
  const float* stats_in;
  float* stats_out;
  const float* colsum;
  const u16* wphi;
  float* kvpart;
  int ln_mode;
  int unused;
};

enum { EPI_QKV = 0, EPI_RES = 1, EPI_GELU_SHIFT = 2, EPI_W2 = 3, EPI_GELU = 4, EPI_W4 = 5 };

struct RowP { float alpha, beta, mhat, dhat; };
DI RowP row_params(float mean, float var, int mode) {
  RowP r;
  const float mh = rbf(mean);
  const float dh = rbf(__builtin_amdgcn_sqrtf(rbf(rbf(var) + 1e-5f)));
  const float i1 = __builtin_amdgcn_rcpf(dh);
  r.mhat = mh; r.dhat = dh;
  if (mode == 2) {
    const float m2 = (mean - mh) * i1;
    const float v2 = var * i1 * i1;
    const float mh2 = rbf(m2);
    const float dh2 = rbf(__builtin_amdgcn_sqrtf(rbf(rbf(v2) + 1e-5f)));
    const float i2 = __builtin_amdgcn_rcpf(dh2);
    r.alpha = i1 * i2;
    r.beta = (mh * i1 + mh2) * i2;
  } else {
    r.alpha = i1;
    r.beta = mh * i1;
  }
  return r;
}

template <int BN, int S, int KD, int EPI>
__global__ __launch_bounds__(512, 2) void gemm_k(GemmArgs g) {
  constexpr int NK = KD / 64;
  constexpr int NPF = BN / 64;
  constexpr bool HALF = (BN % 64) != 0;
  constexpr int NI = BN / 32;
  constexpr int A_BYTES = 16384;
  constexpr int STAGE = A_BYTES + BN * 128;
  constexpr int L = 4 + NPF * 2 + (HALF ? 1 : 0);
  constexpr int NTOT = (EPI == EPI_QKV) ? 3 * DM : (EPI == EPI_GELU ? FF : DM);
  constexpr bool USE_STATS = (EPI == EPI_QKV || EPI == EPI_GELU_SHIFT || EPI == EPI_W2 || EPI == EPI_GELU);
  constexpr bool FOLD = (EPI == EPI_QKV || EPI == EPI_GELU_SHIFT || EPI == EPI_GELU);
  constexpr int NRES = (EPI == EPI_RES || EPI == EPI_W2) ? 1 : (EPI == EPI_W4 ? 2 : 0);
  constexpr int OFF_STATS = S * STAGE;
  constexpr int OFF_RES = OFF_STATS;
  constexpr int OFF_WPHI = OFF_RES + NRES * 16384;
  constexpr int OFF_ROWP = OFF_WPHI + (EPI == EPI_QKV ? 4096 : 0);
  constexpr int OFF_CS = OFF_ROWP + 2048;
  constexpr int OFF_DUMP = OFF_CS + 1024;
  constexpr int NEXTRA = (USE_STATS && BN != 64 ? 8 : 0) + NRES * 4 + (EPI == EPI_QKV ? 1 : 0) + (FOLD ? 1 : 0);

  constexpr int PR0 = NK > 16 ? 5 : (S == 3 ? 7 : NK - 1), PRN = PR0 < NK - 1 ? (NK - S - PR0) / S : 0;
  constexpr int CR0 = 0, CRN = (NK - 1) / S;
  static_assert(!(USE_STATS && BN == 64) || S == 5, "row statistics sit in the second group of five stages");
  extern __shared__ __attribute__((aligned(16))) char smem[];
  const int tid = threadIdx.x, lane = tid & 63;
  const int w = __builtin_amdgcn_readfirstlane(tid >> 6);
  const bool prod = w >= 4;
  const int cw = w & 3, wr = cw >> 1, wc = cw & 1;

  const int b = blockIdx.x, xcd = b & 7, j = b >> 3;
  int rp, ct;
  if constexpr (EPI == EPI_QKV || EPI == EPI_GELU) { rp = (xcd >> 2) * 8 + (j >> 2); ct = (xcd & 3) * 4 + (j & 3); }
  else { rp = (xcd >> 1) * 4 + (j >> 3); ct = (xcd & 1) * 8 + (j & 7); }
  const int m0 = rp * 128;
  const int n0 = (EPI == EPI_QKV) ? ct * 64 : ct * BN;
  constexpr int PSTRIDE = (EPI == EPI_QKV) ? DM : 64;
  const int nf = HALF ? n0 + (ct & 1) * 32 : n0;
  const int nh = (ct & 1) ? n0 : n0 + NPF * 64;
  const int g4 = lane >> 4, q4 = (lane >> 2) & 3, pp = lane & 3;
  RowP* rowp = (RowP*)(smem + OFF_ROWP);
  const bool ln_on = USE_STATS && (g.ln_mode != 0 || EPI == EPI_W2);

  f32x4 acc[4][NI];
  f32x2 ab[4];
  f32x4 csv[NI];
  TS(0);
  constexpr bool RP_CONS = (BN == 64);
  f32x2 sv[8];
  const int sr = cw * 32 + (lane >> 1), shf = lane & 1;
  auto load_stats = [&]() {
#pragma unroll
    for (int p = 0; p < 8; ++p)
      asm volatile("global_load_dwordx2 %0, %1, off" : "=v"(sv[p]) : "v"(g.stats_in + ((size_t)(shf * 8 + p) * SEQ + m0 + sr) * 2) : "memory");
  };
  float rs_s = 0.f, rs_m2 = 0.f;
  auto row_stats = [&](auto pc) {
    constexpr int piece = decltype(pc)::value;
    if constexpr (piece == 0) {
#pragma unroll
      for (int p = 0; p < 8; ++p) asm volatile("" : "+v"(sv[p]));
      float s = 0.f;
#pragma unroll
      for (int p = 0; p < 8; ++p) s += sv[p][0];
      rs_s = s + dppf<0xB1>(s);
    } else if constexpr (piece == 1) {
      const float mean = rs_s * (1.0f / 1024.0f);
      float m2 = 0.f;
#pragma unroll
      for (int p = 0; p < 8; ++p) { const float d = sv[p][0] * (1.0f / 64.0f) - mean; m2 += sv[p][1] + 64.0f * d * d; }
      rs_m2 = m2 + dppf<0xB1>(m2);
    } else {
      if (ln_on && shf == 0) rowp[sr] = row_params(rs_s * (1.0f / 1024.0f), rs_m2 * (1.0f / 1024.0f), EPI == EPI_W2 ? 1 : g.ln_mode);
      wait_lgkm0();
    }
    __builtin_amdgcn_sched_barrier(0);
  };
  if (__builtin_expect(prod, 1)) {
    const u16* a_src = g.A + (size_t)(m0 + cw * 32 + (lane >> 3)) * KD + (((lane & 7) ^ (lane >> 3)) * 8);
    const u16* b_src[2];
#pragma unroll
    for (int e = 0; e < 2; ++e) {
      const int r = lane >> 3;
      const int f = ((r >> 1) & 1) | (e << 1);
      const int c = ((((lane & 7) >> 1) ^ f) << 1) | (lane & 1);
      b_src[e] = g.B + (size_t)((cw * 2 + e) * 8 + r) * NTOT + nf + c * 8;
    }
    const u16* bh_src = nullptr;
    if constexpr (HALF) {
      const int c = ((((lane >> 1) & 1) ^ ((lane >> 5) & 1)) << 1) | (lane & 1);
      bh_src = g.B + (size_t)(cw * 16 + (lane >> 2)) * NTOT + nh + c * 8;
    }
    auto issue = [&](int kt, int slot) {
      char* base = smem + slot * STAGE;
#pragma unroll
      for (int p = 0; p < NPF; ++p)
#pragma unroll
        for (int e = 0; e < 2; ++e)
          glds16(b_src[e] + (size_t)kt * 64 * NTOT + p * PSTRIDE, base + A_BYTES + p * 8192 + (cw * 2 + e) * 1024);
      if constexpr (HALF) glds16(bh_src + (size_t)kt * 64 * NTOT, base + A_BYTES + NPF * 8192 + cw * 1024);
#pragma unroll
      for (int e = 0; e < 4; ++e) glds16(a_src + (size_t)e * 8 * KD + kt * 64, base + (cw * 4 + e) * 1024);
    };
    TS(6);
    issue(0, 0);
    issue(1, 1);
    wait_vm<L>();
    TS(1);
    bar();
    static_for<2, S>([&](auto ic) { issue(ic.value, ic.value); });
    TS(7);
    if constexpr (NRES >= 1) {
#pragma unroll
      for (int e = 0; e < 4; ++e) {
        const int r = (cw * 4 + e) * 8 + (lane >> 3);
        glds16(g.res1 + (size_t)(m0 + r) * DM + n0 + (lane & 7) * 8, smem + OFF_RES + (cw * 4 + e) * 1024);
      }
    }
    if constexpr (NRES >= 2) {
#pragma unroll
      for (int e = 0; e < 4; ++e) {
        const int r = (cw * 4 + e) * 8 + (lane >> 3);
        glds16(g.res2 + (size_t)(m0 + r) * DM + n0 + (lane & 7) * 8, smem + OFF_RES + 16384 + (cw * 4 + e) * 1024);
      }
    }
    if constexpr (EPI == EPI_QKV) glds16(g.wphi + (size_t)ct * 2048 + cw * 512 + lane * 8, smem + OFF_WPHI + cw * 1024);
    if constexpr (FOLD) {
      int col = cw * 64 + (lane & 15) * 4;
      if (col > BN - 4) col = BN - 4;
      const int gcol = (EPI == EPI_QKV) ? (col >> 6) * DM + n0 + (col & 63) : (HALF && col >= NPF * 64) ? nh + col - NPF * 64 : nf + col;
      if (lane < 16) glds16(g.colsum + gcol, smem + OFF_CS + cw * 256);
    }
    if constexpr (USE_STATS && !RP_CONS) load_stats();
    auto pstep = [&](auto ic) {
      constexpr int kt = ic.value;
      constexpr int young = (S - 2 < NK - 2 - kt) ? S - 2 : NK - 2 - kt;
      constexpr int ex = (kt + 1 <= S - 1 && kt + 1 < NK - 1) ? NEXTRA : 0;
      wait_vm<young * L + ex>();
      bar();
      if constexpr (kt + S < NK) issue(kt + S, kt % S);
      if constexpr (USE_STATS && !RP_CONS && kt >= 4 && kt < 7) row_stats(IC<kt - 4>{});
    };
    static_for<0, PR0>(pstep);
    if constexpr (PRN > 0) {
#pragma unroll 1
      for (int k0 = PR0; k0 < PR0 + PRN * S; k0 += S)
        static_for<0, S>([&](auto uc) {
          wait_vm<(S - 2) * L>();
          bar();
          issue(k0 + uc.value + S, (PR0 + uc.value) % S);
        });
    }
    static_for<PR0 + PRN * S, NK - 1>(pstep);
    TS(2);
    bar();
  } else {
    const int fsw = ((q4 >> 1) & 1) | ((g4 & 1) << 1);
    int a_off[2], b_off[2], bh_off = 0;
#pragma unroll
    for (int k = 0; k < 2; ++k) {
      a_off[k] = (wr * 64 + (lane & 15)) * 128 + (((k * 4 + g4) ^ (lane & 7)) * 16);
      b_off[k] = A_BYTES + (8 * g4 + q4) * 128 + (((2 * k + wc) ^ fsw) * 32) + pp * 8;
    }
    if constexpr (HALF) bh_off = A_BYTES + NPF * 8192 + (8 * g4 + q4) * 64 + ((wc ^ (g4 & 1)) * 32) + pp * 8;
#pragma unroll
    for (int m = 0; m < 4; ++m)
#pragma unroll
      for (int i = 0; i < NI; ++i) acc[m][i] = (f32x4){0.f, 0.f, 0.f, 0.f};
    bf16x8 xa[2][4];
    s16x4 wlo[2][NI], whi[2][NI];
    const unsigned lbase = (unsigned)(size_t)smem;

    auto ldfrag = [&](auto setc, auto slotc, auto kc) {
      constexpr int set = decltype(setc)::value, slot = decltype(slotc)::value, k32 = decltype(kc)::value;
      const unsigned sb = lbase + slot * STAGE;
      const unsigned ba0 = sb + b_off[0], ba1 = sb + b_off[1], bah = sb + bh_off, aa = sb + a_off[k32];
      static_for<0, NI>([&](auto ic) {
        constexpr int i = decltype(ic)::value;
        if constexpr (HALF && i == NI - 1) {
          wlo[set][i] = lds_tr<k32 * 2048>(bah);
          whi[set][i] = lds_tr<k32 * 2048 + 256>(bah);
        } else {
          wlo[set][i] = lds_tr<(i >> 1) * 8192 + k32 * 4096>((i & 1) ? ba1 : ba0);
          whi[set][i] = lds_tr<(i >> 1) * 8192 + k32 * 4096 + 512>((i & 1) ? ba1 : ba0);
        }
      });
      static_for<0, 4>([&](auto mc) { constexpr int m = decltype(mc)::value; xa[set][m] = lds_b128<m * 2048>(aa); });
    };
    auto wait_set = [&](auto setc) {
      constexpr int set = decltype(setc)::value;
      asm volatile("s_waitcnt lgkmcnt(0)" : "+v"(xa[set][0]), "+v"(xa[set][1]), "+v"(xa[set][2]), "+v"(xa[set][3]) :: "memory");
#pragma unroll
      for (int i = 0; i < NI; ++i) asm volatile("" : "+v"(wlo[set][i]), "+v"(whi[set][i]));
      __builtin_amdgcn_sched_barrier(0);
    };
    auto ld1 = [&](auto setc, auto slotc, auto kc, auto rc) {
      constexpr int set = decltype(setc)::value, slot = decltype(slotc)::value, k32 = decltype(kc)::value, r = decltype(rc)::value;
      const unsigned sb = lbase + slot * STAGE;
      if constexpr (r < 2 * NI) {
        constexpr int i = r >> 1, hi = r & 1;
        s16x4 v;
        if constexpr (HALF && i == NI - 1) v = lds_tr<k32 * 2048 + hi * 256>(sb + bh_off);
        else v = lds_tr<(i >> 1) * 8192 + k32 * 4096 + hi * 512>(sb + b_off[i & 1]);
        if constexpr (hi) whi[set][i] = v; else wlo[set][i] = v;
      } else {
        constexpr int m = r - 2 * NI;
        xa[set][m] = lds_b128<m * 2048>(sb + a_off[k32]);
      }
    };
    auto mma_ld = [&](auto setc, auto ldc, auto slotc, auto kc) {
      constexpr int set = decltype(setc)::value;
      constexpr bool ld = decltype(ldc)::value != 0;
      static_for<0, 4 * NI>([&](auto tc) {
        constexpr int t = decltype(tc)::value, i = t >> 2, m = t & 3;
        acc[m][i] = mfma16(cat8(wlo[set][i], whi[set][i]), xa[set][m], acc[m][i]);
        if constexpr (ld && t < 2 * NI + 4) ld1(IC<set ^ 1>{}, slotc, kc, tc);
        __builtin_amdgcn_sched_barrier(0);
      });
    };

    const char* tp = (const char*)g.A;
    unsigned tw = 0;
    bool ton = false;
    int tks = 0;
    if (cw == 0) {
      const int pl = lane >> 4;
      const int bl = (int)(((size_t)g.B >> 7) & 7);
      int off;
      if constexpr (BN == 64) { off = ((xcd & 1) * 8 + ((3 - bl) & 7)) * 128; ton = lane < 16; }
      else if constexpr (EPI == EPI_QKV) { const int c = (3 - bl - (xcd & 3) * 4) & 7; off = pl * 2048 + ((xcd & 3) * 4 + (c & 3)) * 128; ton = c < 4 && lane < 48; }
      else { const int l0 = (xcd & 3) * 14, c = (3 - bl - l0) & 7; off = (l0 + c + pl * 8) * 128; ton = lane < (c < 6 ? 32 : 16); }
      tks = (lane >> 1) & 7;
      if (ton) tp = (const char*)g.B + (size_t)(tks * 64 + j * 2 + (lane & 1)) * NTOT * 2 + off;
      tw = 8 * 64 * NTOT * 2;
    } else if (cw == 1) {
      tks = (3 - (int)(((size_t)g.A >> 7) & 7)) & 7;
      const int row = (BN == 64) ? (xcd >> 1) * 512 + j * 16 + (lane & 15) : (xcd >> 2) * 1024 + j * 32 + (lane & 31);
      ton = lane < (BN == 64 ? 16 : 32);
      tp = (const char*)g.A + (size_t)row * KD * 2 + tks * 128;
      tw = 1024;
    }
    auto touch = [&](auto wc_) {
      constexpr int wi = decltype(wc_)::value;
      if (cw < 2) {
        if (ton && (wi > 0 || tks >= S))
          __builtin_amdgcn_global_load_lds((const unsigned*)(tp + (size_t)(wi * tw)), (unsigned*)(smem + OFF_DUMP), 4, 0, 0);
      }
      __builtin_amdgcn_sched_barrier(0);
    };
    if constexpr (USE_STATS && RP_CONS) load_stats();
    touch(IC<0>{});
    if constexpr (BN == 64) touch(IC<1>{});
    bar();
    TS(1);
    ldfrag(IC<0>{}, IC<0>{}, IC<0>{});
    if constexpr (BN != 64) touch(IC<1>{});
    auto cstep = [&](auto ic) {
      constexpr int kt = ic.value;
      constexpr int slot = kt % S;
      wait_set(IC<0>{});
      if constexpr (BN != 64 && kt == 0) touch(IC<1>{});
      if constexpr (kt % 8 == 4 && (kt + 12) / 8 < NK / 8) touch(IC<(kt + 12) / 8>{});
      if constexpr (USE_STATS && RP_CONS && kt >= 6 && kt < 9) { if constexpr (kt == 6) wait_vm<0>(); row_stats(IC<kt - 6>{}); }
      mma_ld(IC<0>{}, IC<1>{}, IC<slot>{}, IC<1>{});
      wait_set(IC<1>{});
      if constexpr (kt + 1 < NK) {
        bar();
        __builtin_amdgcn_sched_barrier(0);
        mma_ld(IC<1>{}, IC<1>{}, IC<(kt + 1) % S>{}, IC<0>{});
      } else {
        const unsigned ra = lbase + OFF_ROWP + (wr * 64 + (lane & 15)) * 16, ca = lbase + OFF_CS + (wc * 16 + g4 * 4) * 4;
        static_for<0, 4 * NI>([&](auto tc) {
          constexpr int t = decltype(tc)::value, i = t >> 2, m = t & 3;
          acc[m][i] = mfma16(cat8(wlo[1][i], whi[1][i]), xa[1][m], acc[m][i]);
          if constexpr (FOLD && t < 4) ab[t] = lds_f2<t * 256>(ra);
          if constexpr (FOLD && m == 3) csv[i] = lds_f4<i * 128>(ca);
          __builtin_amdgcn_sched_barrier(0);
        });
      }
    };
    static_for<0, CR0>(cstep);
    if constexpr (CRN > 0) {
#pragma unroll 1
      for (int k0 = CR0; k0 < CR0 + CRN * S; k0 += S)
        static_for<0, S>([&](auto uc) {
          constexpr int slot = (CR0 + uc.value) % S;
          const int kt = k0 + uc.value;
          wait_set(IC<0>{});
          if constexpr (NK > 16) {
            if ((kt & 7) == 4 && kt + 12 < NK && cw < 2) {
              if (ton) __builtin_amdgcn_global_load_lds((const unsigned*)(tp + (size_t)((unsigned)((kt + 12) >> 3) * tw)), (unsigned*)(smem + OFF_DUMP), 4, 0, 0);
            }
          }
          if constexpr (USE_STATS && RP_CONS && uc.value >= 1 && uc.value < 4) {
            if (k0 == 5) { if constexpr (uc.value == 1) wait_vm<0>(); row_stats(IC<uc.value - 1>{}); }
          }
          __builtin_amdgcn_sched_barrier(0);
          mma_ld(IC<0>{}, IC<1>{}, IC<slot>{}, IC<1>{});
          wait_set(IC<1>{});
          bar();
          __builtin_amdgcn_sched_barrier(0);
          mma_ld(IC<1>{}, IC<1>{}, IC<(slot + 1) % S>{}, IC<0>{});
        });
    }
    static_for<CR0 + CRN * S, NK>(cstep);
    TS(2);
    bar();
    if constexpr (FOLD) {
      asm volatile("s_waitcnt lgkmcnt(0)" : "+v"(ab[0]), "+v"(ab[1]), "+v"(ab[2]), "+v"(ab[3]) :: "memory");
#pragma unroll
      for (int i = 0; i < NI; ++i) asm volatile("" : "+v"(csv[i]));
      if (!ln_on) {
#pragma unroll
        for (int m = 0; m < 4; ++m) ab[m] = (f32x2){1.f, 0.f};
      }
    }
  }
  TS(3);
  if constexpr (EPI == EPI_QKV) __builtin_amdgcn_s_waitcnt(0x0F70);

  constexpr int TSTR = BN * 2 + (EPI == EPI_QKV ? 32 : 16);
  char* T = smem;
  u32x4 pv[EPI == EPI_QKV ? 11 : 1];
  if (!prod) {
#pragma unroll
    for (int i = 0; i < NI; ++i) {
      const int nb = 2 * i + wc;
      const int ncol = nb * 16 + g4 * 4;
      const f32x4 cs = csv[i];
#pragma unroll
      for (int m = 0; m < 4; ++m) {
        u32x2 pkd;
#pragma unroll
        for (int h = 0; h < 2; ++h) {
          f32x2 v = {acc[m][i][2 * h], acc[m][i][2 * h + 1]};
          if constexpr (FOLD) v = v * ab[m][0] - (f32x2){cs[2 * h], cs[2 * h + 1]} * ab[m][1];
          pkd[h] = pk2(v[0], v[1]);
        }
        *(u32x2*)(T + (wr * 64 + m * 16 + (lane & 15)) * TSTR + ncol * 2) = pkd;
      }
    }
  } else if constexpr (EPI == EPI_QKV) {
    if (g.res1) {
      const int t = tid - 256;
      const u16* W4 = (const u16*)g.stats_out + (size_t)b * 14336 + t * 8;
      const u16* Wo = g.res1 + (size_t)b * 4096 + t * 8;
      const u16* W2 = g.res2 + (size_t)b * 4096 + t * 8;
#pragma unroll
      for (int i = 0; i < 2; ++i) { pv[7 + i] = *(const u32x4*)(Wo + i * 2048); pv[9 + i] = *(const u32x4*)(W2 + i * 2048); }
#pragma unroll
      for (int i = 0; i < 7; ++i) pv[i] = *(const u32x4*)(W4 + i * 2048);
    }
  }
  if constexpr (EPI == EPI_QKV) { wait_lgkm0(); bar(); asm volatile("" ::: "memory"); }
  else __syncthreads();
  TS(4);
  auto gelu8 = [&](u32x4 v) {
    u32x4 o;
#pragma unroll
    for (int e = 0; e < 4; ++e) { const f32x2 y = gelu2(unpk(v[e])); o[e] = pk2(y[0], y[1]); }
    return o;
  };
  if constexpr (EPI == EPI_GELU) {
    u32x4 v[NI];
#pragma unroll
    for (int it = 0; it < NI; ++it) {
      const int r = it < NI - 1 ? (it & 1) * 64 + (tid >> 3) : tid >> 2, c = it < NI - 1 ? (it >> 1) * 8 + (tid & 7) : (NI / 2) * 8 + (tid & 3);
      v[it] = *(const u32x4*)(T + r * TSTR + c * 16);
    }
#pragma unroll
    for (int it = 0; it < NI; ++it) {
      const int r = it < NI - 1 ? (it & 1) * 64 + (tid >> 3) : tid >> 2, col = it < NI - 1 ? nf + (it >> 1) * 64 + (tid & 7) * 8 : nh + (tid & 3) * 8;
      st16_wt(g.out, SEQ * FF * 2, ((m0 + r) * FF + col) * 2, gelu8(v[it]));
    }
  } else if constexpr (EPI == EPI_GELU_SHIFT) {
    const int grp = n0 >> 8;
    const int sh = (grp == 0) ? 1 : (grp == 1) ? -1 : (grp == 2) ? 2 : -2;
#pragma unroll
    for (int it = 0; it < 2; ++it) {
      const int r = it * 64 + (tid >> 3), c = tid & 7;
      const u32x4 v = gelu8(*(const u32x4*)(T + r * TSTR + c * 16));
      const int dr = m0 + r + sh;
      if (dr >= 0 && dr < SEQ) st16_wt(g.out, SEQ * DM * 2, (dr * DM + n0 + c * 8) * 2, v);
    }
    if (tid < 16) {
      const int zr = tid >> 3, c = tid & 7;
      const int az = sh > 0 ? sh : -sh;
      if (zr < az) {
        if (sh > 0 && rp == 0) *(u32x4*)(g.out + (size_t)zr * DM + n0 + c * 8) = (u32x4){0u, 0u, 0u, 0u};
        if (sh < 0 && rp == 15) *(u32x4*)(g.out + (size_t)(SEQ - 1 - zr) * DM + n0 + c * 8) = (u32x4){0u, 0u, 0u, 0u};
      }
    }
  } else if constexpr (EPI == EPI_RES || EPI == EPI_W2 || EPI == EPI_W4) {
#pragma unroll
    for (int it = 0; it < 2; ++it) {
      const int r = it * 64 + (tid >> 3), c = tid & 7;
      const u32x4 v = *(const u32x4*)(T + r * TSTR + c * 16);
      const u32x4 x = *(const u32x4*)(smem + OFF_RES + r * 128 + c * 16);
      f32x2 y[4];
      if constexpr (EPI == EPI_RES) {
#pragma unroll
        for (int e = 0; e < 4; ++e) y[e] = unpk(x[e]) + unpk(v[e]);
      } else if constexpr (EPI == EPI_W2) {
        const RowP rr = rowp[r];
#pragma unroll
        for (int e = 0; e < 4; ++e) y[e] = rbf2(rbf2(unpk(x[e]) - rr.mhat) * rr.alpha) + unpk(v[e]);
      } else {
        const u32x4 x1 = *(const u32x4*)(smem + OFF_RES + 16384 + r * 128 + c * 16);
#pragma unroll
        for (int e = 0; e < 4; ++e) y[e] = unpk(x[e]) + rbf2(unpk(x1[e]) + unpk(v[e]));
      }
      u32x4 o;
#pragma unroll
      for (int e = 0; e < 4; ++e) { o[e] = pk2(y[e][0], y[e][1]); y[e] = unpk(o[e]); }
      st16_wt(g.out, SEQ * DM * 2, ((m0 + r) * DM + n0 + c * 8) * 2, o);
      const f32x2 s2 = (y[0] + y[1]) + (y[2] + y[3]);
      const float s = sum8(s2[0] + s2[1]);
      const float mc = s * (1.0f / 64.0f);
      f32x2 q2 = {0.f, 0.f};
#pragma unroll
      for (int e = 0; e < 4; ++e) { const f32x2 d = y[e] - mc; q2 += d * d; }
      const float m2 = sum8(q2[0] + q2[1]);
      if (c == 0) *(f32x2*)(g.stats_out + ((size_t)ct * SEQ + m0 + r) * 2) = (f32x2){s, m2};
    }
  } else {
    constexpr int PKSTR = 160;
    char* PK = smem + 128 * TSTR;
#pragma unroll
    for (int it = 0; it < 2; ++it) {
      const int r = it * 64 + (tid >> 3), c = tid & 7;
      st16_wt(g.out, SEQ * DM * 2, ((m0 + r) * DM + n0 + c * 8) * 2, *(const u32x4*)(T + r * TSTR + c * 16));
    }
    {
      const int hh = w & 1, rq = w >> 1;
      bf16x8 wf[2];
#pragma unroll
      for (int fb = 0; fb < 2; ++fb) {
        const char* p = smem + OFF_WPHI + hh * 2048 + (8 * g4 + q4) * 64 + fb * 32 + pp * 8;
        wf[fb] = cat8(trread(p), trread(p + 256));
      }
#pragma unroll
      for (int mb = 0; mb < 2; ++mb) {
        const int row = rq * 32 + mb * 16 + (lane & 15);
        const bf16x8 xf = *(const bf16x8*)(T + row * TSTR + (64 + hh * 32 + g4 * 8) * 2);
        u32x4 pos, neg;
        feature_map(wf, xf, pos, neg);
        char* dst = PK + (hh * 128 + row) * PKSTR + g4 * 8;
        *(u32x2*)dst = (u32x2){pos[0], pos[1]}; *(u32x2*)(dst + 32) = (u32x2){pos[2], pos[3]};
        *(u32x2*)(dst + 64) = (u32x2){neg[0], neg[1]}; *(u32x2*)(dst + 96) = (u32x2){neg[2], neg[3]};
      }
    }
    __syncthreads();
    {
      const int hh = w >> 2, fa = w & 3;
      f32x4 kv[3];
#pragma unroll
      for (int d = 0; d < 3; ++d) kv[d] = (f32x4){0.f, 0.f, 0.f, 0.f};
      const short one = (short)0x3F80;
      const bf16x8 ones = {one, one, one, one, one, one, one, one};
#pragma unroll
      for (int ks = 0; ks < 4; ++ks) {
        const int row = ks * 32 + 8 * g4 + q4;
        bf16x8 vf[2];
#pragma unroll
        for (int d = 0; d < 2; ++d) {
          const char* p = T + row * TSTR + (128 + hh * 32 + d * 16) * 2 + pp * 8;
          vf[d] = cat8(trread(p), trread(p + 4 * TSTR));
        }
        const char* p = PK + (hh * 128 + row) * PKSTR + (fa * 16) * 2 + pp * 8;
        const bf16x8 pf = cat8(trread(p), trread(p + 4 * PKSTR));
        kv[0] = mfma16(pf, vf[0], kv[0]);
        kv[1] = mfma16(pf, vf[1], kv[1]);
        kv[2] = mfma16(pf, ones, kv[2]);
      }
      constexpr unsigned KVB = 16 * NH * 33 * 64 * 4;
      const int so = (rp * NH + ct * 2 + hh) * (33 * 64) + fa * 16 + g4 * 4;
      st16_wt(g.kvpart, KVB, (so + (lane & 15) * 64) * 4, __builtin_bit_cast(u32x4, kv[0]));
      st16_wt(g.kvpart, KVB, (so + (16 + (lane & 15)) * 64) * 4, __builtin_bit_cast(u32x4, kv[1]));
      if ((lane & 15) == 0) st16_wt(g.kvpart, KVB, (so + 32 * 64) * 4, __builtin_bit_cast(u32x4, kv[2]));
    }
    if (prod && g.res1) {
#pragma unroll
      for (int i = 0; i < 11; ++i) asm volatile("" ::"v"(pv[i]));
    }
  }
  TS(5);
}

__global__ __launch_bounds__(256) void ln0_k(const u16* __restrict__ x, u16* __restrict__ y, const u16* __restrict__ wqkv,
                                             const u16* __restrict__ w1, const u16* __restrict__ w3, float* __restrict__ colsum,
                                             const u16* __restrict__ wo, const u16* __restrict__ w2, const u16* __restrict__ w4) {
  if (blockIdx.x >= SEQ / 4 + 120) {
    const int t = blockIdx.x - (SEQ / 4 + 120);
    const u16* W = t < 32 ? wo + (size_t)t * 32768 : (t < 64 ? w2 + (size_t)(t - 32) * 32768 : w4 + (size_t)(t - 64) * 32768);
#pragma unroll
    for (int i = 0; i < 16; ++i) {
      const u32x4 v = *(const u32x4*)(W + (i * 256 + threadIdx.x) * 8);
      asm volatile("" ::"v"(v));
    }
    return;
  }
  if (blockIdx.x >= SEQ / 4) {
    __shared__ float red[32 * 64];
    const int tid = threadIdx.x;
    const int cg = blockIdx.x - SEQ / 4;
    const u16* W; int N, c0;
    if (cg < 48) { W = wqkv; N = 3 * DM; c0 = cg * 64; }
    else if (cg < 64) { W = w1; N = DM; c0 = (cg - 48) * 64; }
    else { W = w3; N = FF; c0 = (cg - 64) * 64; }
    const int c = tid & 7, rg = tid >> 3;
    f32x2 s[4] = {{0.f, 0.f}, {0.f, 0.f}, {0.f, 0.f}, {0.f, 0.f}};
#pragma unroll 16
    for (int i = 0; i < 32; ++i) {
      const u32x4 v = *(const u32x4*)(W + (size_t)(rg + i * 32) * N + c0 + c * 8);
#pragma unroll
      for (int e = 0; e < 4; ++e) s[e] += unpk(v[e]);
    }
#pragma unroll
    for (int e = 0; e < 4; ++e) *(f32x2*)(red + rg * 64 + c * 8 + 2 * e) = s[e];
    __syncthreads();
    if (tid < 64) {
      float t = 0.f;
#pragma unroll
      for (int i = 0; i < 32; ++i) t += red[i * 64 + tid];
      colsum[cg * 64 + tid] = t;
    }
    return;
  }
  const int lane = threadIdx.x & 63, row = blockIdx.x * 4 + (threadIdx.x >> 6);
  const u16* src = x + (size_t)row * DM;
  const u32x4 a = *(const u32x4*)(src + lane * 8), b = *(const u32x4*)(src + 512 + lane * 8);
  float v[16];
#pragma unroll
  for (int e = 0; e < 4; ++e) { v[2 * e] = bflo(a[e]); v[2 * e + 1] = bfhi(a[e]); v[8 + 2 * e] = bflo(b[e]); v[9 + 2 * e] = bfhi(b[e]); }
  float s = 0.f;
#pragma unroll
  for (int e = 0; e < 16; ++e) s += v[e];
  s = sum64(s);
  const float mean = s * (1.0f / 1024.0f);
  float m2 = 0.f;
#pragma unroll
  for (int e = 0; e < 16; ++e) { const float d = v[e] - mean; m2 += d * d; }
  m2 = sum64(m2);
  const RowP r = row_params(mean, m2 * (1.0f / 1024.0f), 1);
  u32x4 oa, ob;
#pragma unroll
  for (int e = 0; e < 4; ++e) {
    oa[e] = pk2(rbf(v[2 * e] - r.mhat) / r.dhat, rbf(v[2 * e + 1] - r.mhat) / r.dhat);
    ob[e] = pk2(rbf(v[8 + 2 * e] - r.mhat) / r.dhat, rbf(v[9 + 2 * e] - r.mhat) / r.dhat);
  }
  u16* dst = y + (size_t)row * DM;
  *(u32x4*)(dst + lane * 8) = oa;
  *(u32x4*)(dst + 512 + lane * 8) = ob;
}

__global__ __launch_bounds__(512) void attn_o_k(const u16* __restrict__ q, const u16* __restrict__ wphi, const float* __restrict__ kvpart,
                                                u16* __restrict__ o) {
  constexpr int KS = 80;
  __shared__ __attribute__((aligned(16))) char sm[33 * KS * 2 + 8];
  const int tid = threadIdx.x, lane = tid & 63, w = tid >> 6;
  constexpr int EPI = 6;
  TS(0);
  const int h = blockIdx.x & 31, rb = blockIdx.x >> 5;
  const int g4 = lane >> 4, r0 = rb * 256 + w * 32 + (lane & 15);
  bf16x8 qf[2], wf[2];
#pragma unroll
  for (int mb = 0; mb < 2; ++mb) qf[mb] = *(const bf16x8*)(q + (size_t)(r0 + mb * 16) * DM + h * 32 + g4 * 8);
#pragma unroll
  for (int fb = 0; fb < 2; ++fb)
#pragma unroll
    for (int j = 0; j < 8; ++j) wf[fb][j] = (short)wphi[h * 1024 + (g4 * 8 + j) * 32 + fb * 16 + (lane & 15)];
  __builtin_amdgcn_sched_barrier(0);
  bar();
  __builtin_amdgcn_sched_barrier(0);
  f32x4 kp[16], kz[4];
  const float* slab = kvpart + (size_t)h * (33 * 64) + tid * 4;
  const bool zw = __builtin_amdgcn_readfirstlane(w) == 0;
  if (zw) {
#pragma unroll
    for (int p = 0; p < 4; ++p) kz[p] = *(const f32x4*)(kvpart + ((size_t)(p * 4 + g4) * NH + h) * (33 * 64) + 2048 + (lane & 15) * 4);
  }
#pragma unroll
  for (int p = 0; p < 16; ++p) kp[p] = *(const f32x4*)(slab + (size_t)p * NH * (33 * 64));
  __builtin_amdgcn_sched_barrier(0);
  bf16x8 pq[2][2];
#pragma unroll
  for (int mb = 0; mb < 2; ++mb) {
    u32x4 pos, neg;
    feature_map(wf, qf[mb], pos, neg);
    pq[mb][0] = __builtin_bit_cast(bf16x8, pos);
    pq[mb][1] = __builtin_bit_cast(bf16x8, neg);
  }
  __builtin_amdgcn_sched_barrier(0);
  TS(1);
  u16* kvt = (u16*)sm;
  {
    f32x4 t = kp[0];
#pragma unroll
    for (int p = 1; p < 16; ++p) t += kp[p];
    *(u32x2*)(kvt + (tid >> 4) * KS + (tid & 15) * 4) = (u32x2){pk2(t[0], t[1]), pk2(t[2], t[3])};
    if (zw) {
      f32x4 z = (kz[0] + kz[1]) + (kz[2] + kz[3]);
#pragma unroll
      for (int e = 0; e < 4; ++e) z[e] = sum4g(z[e]);
      if (tid < 16) *(u32x2*)(kvt + 32 * KS + tid * 4) = (u32x2){pk2(z[0], z[1]), pk2(z[2], z[3])};
    }
  }
  TS(2);
  __syncthreads();
  TS(3);
  bf16x8 kf[3][2];
#pragma unroll
  for (int db = 0; db < 3; ++db)
#pragma unroll
    for (int ks = 0; ks < 2; ++ks) {
      const int d = (db < 2) ? ((lane >> 2) & 3) * 8 + db * 4 + (lane & 3) : 32;
      const u16* p = kvt + d * KS + ks * 32 + g4 * 4;
      kf[db][ks] = cat8(*(const s16x4*)p, *(const s16x4*)(p + 16));
    }
#pragma unroll
  for (int mb = 0; mb < 2; ++mb) {
    f32x4 a[3];
#pragma unroll
    for (int db = 0; db < 3; ++db) {
      a[db] = mfma16(kf[db][0], pq[mb][0], (f32x4){0.f, 0.f, 0.f, 0.f});
      a[db] = mfma16(kf[db][1], pq[mb][1], a[db]);
    }
    const float id = __builtin_amdgcn_rcpf(rbf(rbf(a[2][0]) + 1e-6f));
    u32x4 ov;
#pragma unroll
    for (int db = 0; db < 2; ++db) {
      const f32x2 lo = rbf2((f32x2){a[db][0], a[db][1]}) * id, hi = rbf2((f32x2){a[db][2], a[db][3]}) * id;
      ov[db * 2] = pk2(lo[0], lo[1]); ov[db * 2 + 1] = pk2(hi[0], hi[1]);
    }
    st16_wt(o, SEQ * DM * 2, ((r0 + mb * 16) * DM + h * 32 + g4 * 8) * 2, ov);
  }
  TS(4);
  TS(5);
}

template <int BN, int S, int KD, int EPI> static constexpr int smem_bytes() {
  constexpr bool USE_STATS = (EPI == EPI_QKV || EPI == EPI_GELU_SHIFT || EPI == EPI_W2 || EPI == EPI_GELU);
  constexpr int NRES = (EPI == EPI_RES || EPI == EPI_W2) ? 1 : (EPI == EPI_W4 ? 2 : 0);
  return S * (16384 + BN * 128) + NRES * 16384 + (EPI == EPI_QKV ? 4096 : 0) + 2048 + 1024 + 256;
}
template <int BN, int S, int KD, int EPI> static void launch_gemm(const GemmArgs& a, hipStream_t st) {
  constexpr int sb = smem_bytes<BN, S, KD, EPI>();
  static_assert(sb <= 163840, "LDS");
  (void)hipFuncSetAttribute((const void*)gemm_k<BN, S, KD, EPI>, hipFuncAttributeMaxDynamicSharedMemorySize, sb);
  gemm_k<BN, S, KD, EPI><<<256, 512, sb, st>>>(a);
}

extern "C" void kernel_launch(void* const* d_in, const int* in_sizes, int n_in, void* d_out, int out_size, void* d_ws,
                              size_t ws_size, hipStream_t stream) {
  const u16* x0 = (const u16*)d_in[0];
  const u16* wqkv = (const u16*)d_in[1];
  const u16* wphi = (const u16*)d_in[2];
  const u16* wo = (const u16*)d_in[3];
  const u16* w1 = (const u16*)d_in[5];
  const u16* w2 = (const u16*)d_in[6];
  const u16* w3 = (const u16*)d_in[7];
  const u16* w4 = (const u16*)d_in[8];
  char* ws = (char*)d_ws;
  const size_t MB = 1 << 20;
  u16* xln0 = (u16*)(ws + 0 * MB);
  u16* phiq = (u16*)(ws + 4 * MB);
  float* kvpart = (float*)(ws + 12 * MB);
  u16* obuf = (u16*)(ws + 17 * MB);
  u16* xa = (u16*)(ws + 21 * MB);
  u16* hsh = (u16*)(ws + 25 * MB);
  u16* x1 = (u16*)(ws + 29 * MB);
  u16* gbuf = (u16*)(ws + 33 * MB);
  u16* xb = (u16*)(ws + 48 * MB);
  float* st_xa = (float*)(ws + 52 * MB);
  float* st_x1 = (float*)(ws + 53 * MB);
  float* st_xb = (float*)(ws + 54 * MB);
  float* colsum = (float*)(ws + 55 * MB);

  ln0_k<<<SEQ / 4 + 120, 256, 0, stream>>>(x0, xln0, wqkv, w1, w3, colsum, wo, w2, w4);
  for (int layer = 0; layer < 2; ++layer) {
    const u16* xin = layer == 0 ? x0 : xb;
    u16* xout = layer == 0 ? xb : (u16*)d_out;
    GemmArgs a;
    a = GemmArgs{layer == 0 ? xln0 : xb, wqkv, phiq, layer == 0 ? wo : nullptr, w2, st_xb, (float*)w4, colsum, wphi, kvpart, layer == 0 ? 0 : 1, 0};
    launch_gemm<192, 3, 1024, EPI_QKV>(a, stream);
    attn_o_k<<<256, 512, 0, stream>>>(phiq, wphi, kvpart, obuf);
    a = GemmArgs{obuf, wo, xa, xin, nullptr, nullptr, st_xa, nullptr, nullptr, nullptr, 0, 0};
    launch_gemm<64, 5, 1024, EPI_RES>(a, stream);
    a = GemmArgs{xa, w1, hsh, nullptr, nullptr, st_xa, nullptr, colsum + 3072, nullptr, nullptr, 2, 0};
    launch_gemm<64, 5, 1024, EPI_GELU_SHIFT>(a, stream);
    a = GemmArgs{hsh, w2, x1, xa, nullptr, st_xa, st_x1, nullptr, nullptr, nullptr, 1, 0};
    launch_gemm<64, 5, 1024, EPI_W2>(a, stream);
    a = GemmArgs{x1, w3, gbuf, nullptr, nullptr, st_x1, nullptr, colsum + 4096, nullptr, nullptr, 1, 0};
    launch_gemm<224, 3, 1024, EPI_GELU>(a, stream);
    a = GemmArgs{gbuf, w4, xout, xa, x1, nullptr, st_xb, nullptr, nullptr, nullptr, 0, 0};
    launch_gemm<64, 5, 3584, EPI_W4>(a, stream);
  }
}
```

```cpp
#include <hip/hip_runtime.h>
#include <hip/hip_bf16.h>
#include <stdint.h>

typedef __attribute__((ext_vector_type(8))) short bf16x8;
typedef __attribute__((ext_vector_type(4))) short s16x4;
typedef __attribute__((ext_vector_type(4))) float f32x4;
typedef __attribute__((ext_vector_type(2))) float f32x2;
typedef __attribute__((ext_vector_type(4))) unsigned u32x4;
typedef __attribute__((ext_vector_type(2))) unsigned u32x2;
typedef __attribute__((ext_vector_type(2))) __bf16 bf16x2_t;
typedef unsigned short u16;
#define LDS_AS __attribute__((address_space(3)))
#define DI __device__ __forceinline__

static constexpr int SEQ = 2048, DM = 1024, NH = 32, FF = 3584;

DI float bf2f(unsigned v) { return __uint_as_float(v << 16); }
DI float bflo(unsigned v) { return __uint_as_float(v << 16); }
DI float bfhi(unsigned v) { return __uint_as_float(v & 0xffff0000u); }
DI unsigned pk2(float a, float b) {
  f32x2 v = {a, b};
  return __builtin_bit_cast(unsigned, __builtin_convertvector(v, bf16x2_t));
}
DI float rbf(float a) { return bflo(pk2(a, a)); }
template <int CTRL> DI float dppf(float v) { return __builtin_bit_cast(float, __builtin_amdgcn_update_dpp(0, __builtin_bit_cast(int, v), CTRL, 0xf, 0xf, true)); }
DI float sum8(float v) { v += dppf<0xB1>(v); v += dppf<0x4E>(v); return v + dppf<0x141>(v); }
DI f32x2 pair16(float v) { const unsigned u = __float_as_uint(v); const auto r = __builtin_amdgcn_permlane16_swap(u, u, false, false); return (f32x2){__uint_as_float(r[0]), __uint_as_float(r[1])}; }
DI f32x2 pair32(float v) { const unsigned u = __float_as_uint(v); const auto r = __builtin_amdgcn_permlane32_swap(u, u, false, false); return (f32x2){__uint_as_float(r[0]), __uint_as_float(r[1])}; }
DI float max4g(float v) { f32x2 t = pair16(v); t = pair32(fmaxf(t[0], t[1])); return fmaxf(t[0], t[1]); }
DI float min4g(float v) { f32x2 t = pair16(v); t = pair32(fminf(t[0], t[1])); return fminf(t[0], t[1]); }
DI float sum4g(float v) { f32x2 t = pair16(v); t = pair32(t[0] + t[1]); return t[0] + t[1]; }
DI float sum64(float v) { v = sum8(v); return sum4g(v + dppf<0x140>(v)); }

template <int I> struct IC { static constexpr int value = I; };
template <int B, int E, class F> DI void static_for(F&& f) {
  if constexpr (B < E) { f(IC<B>{}); static_for<B + 1, E>(f); }
}
template <int N> DI void wait_vm() { asm volatile("s_waitcnt vmcnt(%0)" ::"n"(N) : "memory"); }
DI void wait_lgkm0() { asm volatile("s_waitcnt lgkmcnt(0)" ::: "memory"); }
DI void bar() { __builtin_amdgcn_s_barrier(); }
DI void glds16(const void* src, char* lds_dst) {
  __builtin_amdgcn_global_load_lds((const unsigned*)src, (unsigned*)lds_dst, 16, 0, 0);
}
DI s16x4 trread(const char* p) {
  return __builtin_bit_cast(s16x4, __builtin_amdgcn_ds_read_tr16_b64_v4i16((LDS_AS s16x4*)p));
}
template <int OFF> DI s16x4 lds_tr(unsigned addr) {
  s16x4 r; asm volatile("ds_read_b64_tr_b16 %0, %1 offset:%2" : "=v"(r) : "v"(addr), "n"(OFF) : "memory"); return r;
}
template <int OFF> DI bf16x8 lds_b128(unsigned addr) {
  bf16x8 r; asm volatile("ds_read_b128 %0, %1 offset:%2" : "=v"(r) : "v"(addr), "n"(OFF) : "memory"); return r;
}
template <int OFF> DI f32x2 lds_f2(unsigned addr) {
  f32x2 r; asm volatile("ds_read_b64 %0, %1 offset:%2" : "=v"(r) : "v"(addr), "n"(OFF) : "memory"); return r;
}
template <int OFF> DI f32x4 lds_f4(unsigned addr) {
  f32x4 r; asm volatile("ds_read_b128 %0, %1 offset:%2" : "=v"(r) : "v"(addr), "n"(OFF) : "memory"); return r;
}
DI void st16_wt(void* base, unsigned bytes, unsigned off, u32x4 v) {
  __builtin_amdgcn_raw_buffer_store_b128(v, __builtin_amdgcn_make_buffer_rsrc(base, (short)0, (int)bytes, 0x00020000), (int)off, 0, 16);
}
DI bf16x8 cat8(s16x4 lo, s16x4 hi) { return (bf16x8){lo[0], lo[1], lo[2], lo[3], hi[0], hi[1], hi[2], hi[3]}; }
DI f32x4 mfma16(bf16x8 a, bf16x8 b, f32x4 c) { return __builtin_amdgcn_mfma_f32_16x16x32_bf16(a, b, c, 0, 0, 0); }

DI f32x2 gelu2(f32x2 x) {
  const float c0 = -2.302208198f, c1 = -2.302208198f * 0.044715f;
  const f32x2 a = x * (x * x * c1 + c0);
  const f32x2 d = (f32x2){__builtin_amdgcn_exp2f(a[0]), __builtin_amdgcn_exp2f(a[1])} + 1.0f;
  return x * (f32x2){__builtin_amdgcn_rcpf(d[0]), __builtin_amdgcn_rcpf(d[1])};
}
DI f32x2 rbf2(f32x2 v) { const unsigned p = pk2(v[0], v[1]); return (f32x2){bflo(p), bfhi(p)}; }
DI f32x2 unpk(unsigned p) { return (f32x2){bflo(p), bfhi(p)}; }

DI void feature_map(const bf16x8 (&wf)[2], bf16x8 xf, u32x4& pos, u32x4& neg) {
  f32x2 p[4];
#pragma unroll
  for (int fb = 0; fb < 2; ++fb) {
    const f32x4 d = mfma16(wf[fb], xf, (f32x4){0.f, 0.f, 0.f, 0.f});
    p[fb * 2] = rbf2((f32x2){d[0], d[1]});
    p[fb * 2 + 1] = rbf2((f32x2){d[2], d[3]});
  }
  float mx = fmaxf(p[0][0], p[0][1]), mn = fminf(p[0][0], p[0][1]);
#pragma unroll
  for (int e = 1; e < 4; ++e) { mx = fmaxf(mx, fmaxf(p[e][0], p[e][1])); mn = fminf(mn, fminf(p[e][0], p[e][1])); }
  mx = max4g(mx);
  mn = min4g(mn);
  const float l2e = 1.4426950408889634f;
  f32x2 ep[4], en[4], sp2 = {0.f, 0.f}, sn2 = {0.f, 0.f};
#pragma unroll
  for (int e = 0; e < 4; ++e) {
    const f32x2 a = (p[e] - mx) * l2e, b = (mn - p[e]) * l2e;
    ep[e] = (f32x2){__builtin_amdgcn_exp2f(a[0]), __builtin_amdgcn_exp2f(a[1])}; sp2 += ep[e];
    en[e] = (f32x2){__builtin_amdgcn_exp2f(b[0]), __builtin_amdgcn_exp2f(b[1])}; sn2 += en[e];
  }
  float sp = sp2[0] + sp2[1], sn = sn2[0] + sn2[1];
  sp = sum4g(sp);
  sn = sum4g(sn);
  const float ip = __builtin_amdgcn_rcpf(sp), in_ = __builtin_amdgcn_rcpf(sn);
#pragma unroll
  for (int e = 0; e < 4; ++e) {
    const f32x2 a = ep[e] * ip, b = en[e] * in_;
    pos[e] = pk2(a[0], a[1]); neg[e] = pk2(b[0], b[1]);
  }
}

#define TS(i)
struct GemmArgs {
  const u16* A;
  const u16* B;
  u16* out;
  const u16* res1;
  const u16* res2;
  const float* stats_in;
  float* stats_out;
  const float* colsum;
  const u16* wphi;
  float* kvpart;
  int ln_mode;
  int unused;
};

enum { EPI_QKV = 0, EPI_RES = 1, EPI_GELU_SHIFT = 2, EPI_W2 = 3, EPI_GELU = 4, EPI_W4 = 5 };

struct RowP { float alpha, beta, mhat, dhat; };
DI RowP row_params(float mean, float var, int mode) {
  RowP r;
  const float mh = rbf(mean);
  const float dh = rbf(__builtin_amdgcn_sqrtf(rbf(rbf(var) + 1e-5f)));
  const float i1 = __builtin_amdgcn_rcpf(dh);
  r.mhat = mh; r.dhat = dh;
  if (mode == 2) {
    const float m2 = (mean - mh) * i1;
    const float v2 = var * i1 * i1;
    const float mh2 = rbf(m2);
    const float dh2 = rbf(__builtin_amdgcn_sqrtf(rbf(rbf(v2) + 1e-5f)));
    const float i2 = __builtin_amdgcn_rcpf(dh2);
    r.alpha = i1 * i2;
    r.beta = (mh * i1 + mh2) * i2;
  } else {
    r.alpha = i1;
    r.beta = mh * i1;
  }
  return r;
}

template <int BN, int S, int KD, int EPI>
__global__ __launch_bounds__(512, 2) void gemm_k(GemmArgs g) {
  constexpr int NK = KD / 64;
  constexpr int NPF = BN / 64;
  constexpr bool HALF = (BN % 64) != 0;
  constexpr int NI = BN / 32;
  constexpr int A_BYTES = 16384;
  constexpr int STAGE = A_BYTES + BN * 128;
  constexpr int L = 4 + NPF * 2 + (HALF ? 1 : 0);
  constexpr int NTOT = (EPI == EPI_QKV) ? 3 * DM : (EPI == EPI_GELU ? FF : DM);
  constexpr bool USE_STATS = (EPI == EPI_QKV || EPI == EPI_GELU_SHIFT || EPI == EPI_W2 || EPI == EPI_GELU);
  constexpr bool FOLD = (EPI == EPI_QKV || EPI == EPI_GELU_SHIFT || EPI == EPI_GELU);
  constexpr int NRES = (EPI == EPI_RES || EPI == EPI_W2) ? 1 : (EPI == EPI_W4 ? 2 : 0);
  constexpr int OFF_STATS = S * STAGE;
  constexpr int OFF_RES = OFF_STATS;
  constexpr int OFF_WPHI = OFF_RES + NRES * 16384;
  constexpr int OFF_ROWP = OFF_WPHI + (EPI == EPI_QKV ? 4096 : 0);
  constexpr int OFF_CS = OFF_ROWP + 2048;
  constexpr int OFF_DUMP = OFF_CS + 1024;
  constexpr int NEXTRA = (USE_STATS && BN != 64 ? 8 : 0) + NRES * 4 + (EPI == EPI_QKV ? 1 : 0) + (FOLD ? 1 : 0);

  constexpr int PR0 = NK > 16 ? 5 : (S == 3 ? 7 : NK - 1), PRN = PR0 < NK - 1 ? (NK - S - PR0) / S : 0;
  constexpr int CR0 = 0, CRN = (NK - 1) / S;
  static_assert(!(USE_STATS && BN == 64) || S == 5, "row statistics sit in the second group of five stages");
  extern __shared__ __attribute__((aligned(16))) char smem[];
  const int tid = threadIdx.x, lane = tid & 63;
  const int w = __builtin_amdgcn_readfirstlane(tid >> 6);
  const bool prod = w >= 4;
  const int cw = w & 3, wr = cw >> 1, wc = cw & 1;

  const int b = blockIdx.x, xcd = b & 7, j = b >> 3;
  int rp, ct;
  if constexpr (EPI == EPI_QKV || EPI == EPI_GELU) { rp = (xcd >> 2) * 8 + (j >> 2); ct = (xcd & 3) * 4 + (j & 3); }
  else { rp = (xcd >> 1) * 4 + (j >> 3); ct = (xcd & 1) * 8 + (j & 7); }
  const int m0 = rp * 128;
  const int n0 = (EPI == EPI_QKV) ? ct * 64 : ct * BN;
  constexpr int PSTRIDE = (EPI == EPI_QKV) ? DM : 64;
  const int nf = HALF ? n0 + (ct & 1) * 32 : n0;
  const int nh = (ct & 1) ? n0 : n0 + NPF * 64;
  const int g4 = lane >> 4, q4 = (lane >> 2) & 3, pp = lane & 3;
  RowP* rowp = (RowP*)(smem + OFF_ROWP);
  const bool ln_on = USE_STATS && (g.ln_mode != 0 || EPI == EPI_W2);

  f32x4 acc[4][NI];
  f32x2 ab[4];
  f32x4 csv[NI];
  TS(0);
  constexpr bool RP_CONS = (BN == 64);
  f32x2 sv[8];
  const int sr = cw * 32 + (lane >> 1), shf = lane & 1;
  auto load_stats = [&]() {
#pragma unroll
    for (int p = 0; p < 8; ++p)
      asm volatile("global_load_dwordx2 %0, %1, off" : "=v"(sv[p]) : "v"(g.stats_in + ((size_t)(shf * 8 + p) * SEQ + m0 + sr) * 2) : "memory");
  };
  float rs_s = 0.f, rs_m2 = 0.f;
  auto row_stats = [&](auto pc) {
    constexpr int piece = decltype(pc)::value;
    if constexpr (piece == 0) {
#pragma unroll
      for (int p = 0; p < 8; ++p) asm volatile("" : "+v"(sv[p]));
      float s = 0.f;
#pragma unroll
      for (int p = 0; p < 8; ++p) s += sv[p][0];
      rs_s = s + dppf<0xB1>(s);
    } else if constexpr (piece == 1) {
      const float mean = rs_s * (1.0f / 1024.0f);
      float m2 = 0.f;
#pragma unroll
      for (int p = 0; p < 8; ++p) { const float d = sv[p][0] * (1.0f / 64.0f) - mean; m2 += sv[p][1] + 64.0f * d * d; }
      rs_m2 = m2 + dppf<0xB1>(m2);
    } else {
      if (ln_on && shf == 0) rowp[sr] = row_params(rs_s * (1.0f / 1024.0f), rs_m2 * (1.0f / 1024.0f), EPI == EPI_W2 ? 1 : g.ln_mode);
      wait_lgkm0();
    }
    __builtin_amdgcn_sched_barrier(0);
  };
  if (__builtin_expect(prod, 1)) {
    const u16* a_src = g.A + (size_t)(m0 + cw * 32 + (lane >> 3)) * KD + (((lane & 7) ^ (lane >> 3)) * 8);
    const u16* b_src[2];
#pragma unroll
    for (int e = 0; e < 2; ++e) {
      const int r = lane >> 3;
      const int f = ((r >> 1) & 1) | (e << 1);
      const int c = ((((lane & 7) >> 1) ^ f) << 1) | (lane & 1);
      b_src[e] = g.B + (size_t)((cw * 2 + e) * 8 + r) * NTOT + nf + c * 8;
    }
    const u16* bh_src = nullptr;
    if constexpr (HALF) {
      const int c = ((((lane >> 1) & 1) ^ ((lane >> 5) & 1)) << 1) | (lane & 1);
      bh_src = g.B + (size_t)(cw * 16 + (lane >> 2)) * NTOT + nh + c * 8;
    }
    auto issue = [&](int kt, int slot) {
      char* base = smem + slot * STAGE;
#pragma unroll
      for (int p = 0; p < NPF; ++p)
#pragma unroll
        for (int e = 0; e < 2; ++e)
          glds16(b_src[e] + (size_t)kt * 64 * NTOT + p * PSTRIDE, base + A_BYTES + p * 8192 + (cw * 2 + e) * 1024);
      if constexpr (HALF) glds16(bh_src + (size_t)kt * 64 * NTOT, base + A_BYTES + NPF * 8192 + cw * 1024);
#pragma unroll
      for (int e = 0; e < 4; ++e) glds16(a_src + (size_t)e * 8 * KD + kt * 64, base + (cw * 4 + e) * 1024);
    };
    TS(6);
    issue(0, 0);
    issue(1, 1);
    wait_vm<L>();
    TS(1);
    bar();
    static_for<2, S>([&](auto ic) { issue(ic.value, ic.value); });
    TS(7);
    if constexpr (NRES >= 1) {
#pragma unroll
      for (int e = 0; e < 4; ++e) {
        const int r = (cw * 4 + e) * 8 + (lane >> 3);
        glds16(g.res1 + (size_t)(m0 + r) * DM + n0 + (lane & 7) * 8, smem + OFF_RES + (cw * 4 + e) * 1024);
      }
    }
    if constexpr (NRES >= 2) {
#pragma unroll
      for (int e = 0; e < 4; ++e) {
        const int r = (cw * 4 + e) * 8 + (lane >> 3);
        glds16(g.res2 + (size_t)(m0 + r) * DM + n0 + (lane & 7) * 8, smem + OFF_RES + 16384 + (cw * 4 + e) * 1024);
      }
    }
    if constexpr (EPI == EPI_QKV) glds16(g.wphi + (size_t)ct * 2048 + cw * 512 + lane * 8, smem + OFF_WPHI + cw * 1024);
    if constexpr (FOLD) {
      int col = cw * 64 + (lane & 15) * 4;
      if (col > BN - 4) col = BN - 4;
      const int gcol = (EPI == EPI_QKV) ? (col >> 6) * DM + n0 + (col & 63) : (HALF && col >= NPF * 64) ? nh + col - NPF * 64 : nf + col;
      if (lane < 16) glds16(g.colsum + gcol, smem + OFF_CS + cw * 256);
    }
    if constexpr (USE_STATS && !RP_CONS) load_stats();
    auto pstep = [&](auto ic) {
      constexpr int kt = ic.value;
      constexpr int young = (S - 2 < NK - 2 - kt) ? S - 2 : NK - 2 - kt;
      constexpr int ex = (kt + 1 <= S - 1 && kt + 1 < NK - 1) ? NEXTRA : 0;
      wait_vm<young * L + ex>();
      bar();
      if constexpr (kt + S < NK) issue(kt + S, kt % S);
    };
    static_for<0, S - 1>(pstep);
    {
      int slot = S - 1;
#pragma unroll 1
      for (int kt = S - 1; kt < NK - S; ++kt) {
        wait_vm<(S - 2) * L>();
        bar();
        issue(kt + S, slot);
        slot = slot == S - 1 ? 0 : slot + 1;
        if constexpr (USE_STATS && !RP_CONS) {
          if (kt == 4) row_stats(IC<0>{});
          else if (kt == 5) row_stats(IC<1>{});
          else if (kt == 6) row_stats(IC<2>{});
        }
      }
    }
    static_for<NK - S, NK - 1>(pstep);
    TS(2);
    bar();
  } else {
    const int fsw = ((q4 >> 1) & 1) | ((g4 & 1) << 1);
    int a_off[2], b_off[2], bh_off = 0;
#pragma unroll
    for (int k = 0; k < 2; ++k) {
      a_off[k] = (wr * 64 + (lane & 15)) * 128 + (((k * 4 + g4) ^ (lane & 7)) * 16);
      b_off[k] = A_BYTES + (8 * g4 + q4) * 128 + (((2 * k + wc) ^ fsw) * 32) + pp * 8;
    }
    if constexpr (HALF) bh_off = A_BYTES + NPF * 8192 + (8 * g4 + q4) * 64 + ((wc ^ (g4 & 1)) * 32) + pp * 8;
#pragma unroll
    for (int m = 0; m < 4; ++m)
#pragma unroll
      for (int i = 0; i < NI; ++i) acc[m][i] = (f32x4){0.f, 0.f, 0.f, 0.f};
    bf16x8 xa[2][4];
    s16x4 wlo[2][NI], whi[2][NI];
    const unsigned lbase = (unsigned)(size_t)smem;

    auto ldfrag = [&](auto setc, auto slotc, auto kc) {
      constexpr int set = decltype(setc)::value, slot = decltype(slotc)::value, k32 = decltype(kc)::value;
      const unsigned sb = lbase + slot * STAGE;
      const unsigned ba0 = sb + b_off[0], ba1 = sb + b_off[1], bah = sb + bh_off, aa = sb + a_off[k32];
      static_for<0, NI>([&](auto ic) {
        constexpr int i = decltype(ic)::value;
        if constexpr (HALF && i == NI - 1) {
          wlo[set][i] = lds_tr<k32 * 2048>(bah);
          whi[set][i] = lds_tr<k32 * 2048 + 256>(bah);
        } else {
          wlo[set][i] = lds_tr<(i >> 1) * 8192 + k32 * 4096>((i & 1) ? ba1 : ba0);
          whi[set][i] = lds_tr<(i >> 1) * 8192 + k32 * 4096 + 512>((i & 1) ? ba1 : ba0);
        }
      });
      static_for<0, 4>([&](auto mc) { constexpr int m = decltype(mc)::value; xa[set][m] = lds_b128<m * 2048>(aa); });
    };
    auto wait_set = [&](auto setc) {
      constexpr int set = decltype(setc)::value;
      asm volatile("s_waitcnt lgkmcnt(0)" : "+v"(xa[set][0]), "+v"(xa[set][1]), "+v"(xa[set][2]), "+v"(xa[set][3]) :: "memory");
#pragma unroll
      for (int i = 0; i < NI; ++i) asm volatile("" : "+v"(wlo[set][i]), "+v"(whi[set][i]));
      __builtin_amdgcn_sched_barrier(0);
    };
    auto ld1 = [&](auto setc, auto slotc, auto kc, auto rc) {
      constexpr int set = decltype(setc)::value, slot = decltype(slotc)::value, k32 = decltype(kc)::value, r = decltype(rc)::value;
      const unsigned sb = lbase + slot * STAGE;
      if constexpr (r < 2 * NI) {
        constexpr int i = r >> 1, hi = r & 1;
        s16x4 v;
        if constexpr (HALF && i == NI - 1) v = lds_tr<k32 * 2048 + hi * 256>(sb + bh_off);
        else v = lds_tr<(i >> 1) * 8192 + k32 * 4096 + hi * 512>(sb + b_off[i & 1]);
        if constexpr (hi) whi[set][i] = v; else wlo[set][i] = v;
      } else {
        constexpr int m = r - 2 * NI;
        xa[set][m] = lds_b128<m * 2048>(sb + a_off[k32]);
      }
    };
    auto mma_ld = [&](auto setc, auto ldc, auto slotc, auto kc) {
      constexpr int set = decltype(setc)::value;
      constexpr bool ld = decltype(ldc)::value != 0;
      static_for<0, 4 * NI>([&](auto tc) {
        constexpr int t = decltype(tc)::value, i = t >> 2, m = t & 3;
        acc[m][i] = mfma16(cat8(wlo[set][i], whi[set][i]), xa[set][m], acc[m][i]);
        if constexpr (ld && t < 2 * NI + 4) ld1(IC<set ^ 1>{}, slotc, kc, tc);
        __builtin_amdgcn_sched_barrier(0);
      });
    };

    const char* tp = (const char*)g.A;
    unsigned tw = 0;
    bool ton = false;
    int tks = 0;
    if (cw == 0) {
      const int pl = lane >> 4;
      const int bl = (int)(((size_t)g.B >> 7) & 7);
      int off;
      if constexpr (BN == 64) { off = ((xcd & 1) * 8 + ((3 - bl) & 7)) * 128; ton = lane < 16; }
      else if constexpr (EPI == EPI_QKV) { const int c = (3 - bl - (xcd & 3) * 4) & 7; off = pl * 2048 + ((xcd & 3) * 4 + (c & 3)) * 128; ton = c < 4 && lane < 48; }
      else { const int l0 = (xcd & 3) * 14, c = (3 - bl - l0) & 7; off = (l0 + c + pl * 8) * 128; ton = lane < (c < 6 ? 32 : 16); }
      tks = (lane >> 1) & 7;
      if (ton) tp = (const char*)g.B + (size_t)(tks * 64 + j * 2 + (lane & 1)) * NTOT * 2 + off;
      tw = 8 * 64 * NTOT * 2;
    } else if (cw == 1) {
      tks = (3 - (int)(((size_t)g.A >> 7) & 7)) & 7;
      const int row = (BN == 64) ? (xcd >> 1) * 512 + j * 16 + (lane & 15) : (xcd >> 2) * 1024 + j * 32 + (lane & 31);
      ton = lane < (BN == 64 ? 16 : 32);
      tp = (const char*)g.A + (size_t)row * KD * 2 + tks * 128;
      tw = 1024;
    }
    auto touch = [&](auto wc_) {
      constexpr int wi = decltype(wc_)::value;
      if (cw < 2) {
        if (ton && (wi > 0 || tks >= S))
          __builtin_amdgcn_global_load_lds((const unsigned*)(tp + (size_t)(wi * tw)), (unsigned*)(smem + OFF_DUMP), 4, 0, 0);
      }
      __builtin_amdgcn_sched_barrier(0);
    };
    if constexpr (USE_STATS && RP_CONS) load_stats();
    touch(IC<0>{});
    if constexpr (BN == 64) touch(IC<1>{});
#pragma unroll
    for (int m = 0; m < 4; ++m)
#pragma unroll
      for (int i = 0; i < (NI > 6 ? 6 : NI); ++i) asm volatile("" : "+v"(acc[m][i]));
    bar();
    TS(1);
    ldfrag(IC<0>{}, IC<0>{}, IC<0>{});
    if constexpr (BN != 64) touch(IC<1>{});
    auto cstep = [&](auto ic) {
      constexpr int kt = ic.value;
      constexpr int slot = kt % S;
      wait_set(IC<0>{});
      if constexpr (BN != 64 && kt == 0) touch(IC<1>{});
      if constexpr (kt % 8 == 4 && (kt + 12) / 8 < NK / 8) touch(IC<(kt + 12) / 8>{});
      if constexpr (USE_STATS && RP_CONS && kt >= 6 && kt < 9) { if constexpr (kt == 6) wait_vm<0>(); row_stats(IC<kt - 6>{}); }
      mma_ld(IC<0>{}, IC<1>{}, IC<slot>{}, IC<1>{});
      wait_set(IC<1>{});
      if constexpr (kt + 1 < NK) {
        bar();
        __builtin_amdgcn_sched_barrier(0);
        mma_ld(IC<1>{}, IC<1>{}, IC<(kt + 1) % S>{}, IC<0>{});
      } else {
        const unsigned ra = lbase + OFF_ROWP + (wr * 64 + (lane & 15)) * 16, ca = lbase + OFF_CS + (wc * 16 + g4 * 4) * 4;
        static_for<0, 4 * NI>([&](auto tc) {
          constexpr int t = decltype(tc)::value, i = t >> 2, m = t & 3;
          acc[m][i] = mfma16(cat8(wlo[1][i], whi[1][i]), xa[1][m], acc[m][i]);
          if constexpr (FOLD && t < 4) ab[t] = lds_f2<t * 256>(ra);
          if constexpr (FOLD && m == 3) csv[i] = lds_f4<i * 128>(ca);
          __builtin_amdgcn_sched_barrier(0);
        });
      }
    };
    static_for<0, CR0>(cstep);
    if constexpr (CRN > 0) {
#pragma unroll 1
      for (int k0 = CR0; k0 < CR0 + CRN * S; k0 += S)
        static_for<0, S>([&](auto uc) {
          constexpr int slot = (CR0 + uc.value) % S;
          const int kt = k0 + uc.value;
          wait_set(IC<0>{});
          if constexpr (NK > 16) {
            if ((kt & 7) == 4 && kt + 12 < NK && cw < 2) {
              if (ton) __builtin_amdgcn_global_load_lds((const unsigned*)(tp + (size_t)((unsigned)((kt + 12) >> 3) * tw)), (unsigned*)(smem + OFF_DUMP), 4, 0, 0);
            }
          }
          if constexpr (USE_STATS && RP_CONS && uc.value >= 1 && uc.value < 4) {
            if (k0 == 5) { if constexpr (uc.value == 1) wait_vm<0>(); row_stats(IC<uc.value - 1>{}); }
          }
          __builtin_amdgcn_sched_barrier(0);
          mma_ld(IC<0>{}, IC<1>{}, IC<slot>{}, IC<1>{});
          wait_set(IC<1>{});
          bar();
          __builtin_amdgcn_sched_barrier(0);
          mma_ld(IC<1>{}, IC<1>{}, IC<(slot + 1) % S>{}, IC<0>{});
        });
    }
    static_for<CR0 + CRN * S, NK>(cstep);
    TS(2);
    bar();
    if constexpr (FOLD) {
      asm volatile("s_waitcnt lgkmcnt(0)" : "+v"(ab[0]), "+v"(ab[1]), "+v"(ab[2]), "+v"(ab[3]) :: "memory");
#pragma unroll
      for (int i = 0; i < NI; ++i) asm volatile("" : "+v"(csv[i]));
      if (!ln_on) {
#pragma unroll
        for (int m = 0; m < 4; ++m) ab[m] = (f32x2){1.f, 0.f};
      }
    }
  }
  TS(3);
  if constexpr (EPI == EPI_QKV) __builtin_amdgcn_s_waitcnt(0x0F70);

  constexpr int TSTR = BN * 2 + (EPI == EPI_QKV ? 32 : 16);
  char* T = smem;
  u32x4 pv[EPI == EPI_QKV ? 11 : 1];
  if (!prod) {
#pragma unroll
    for (int i = 0; i < NI; ++i) {
      const int nb = 2 * i + wc;
      const int ncol = nb * 16 + g4 * 4;
      const f32x4 cs = csv[i];
#pragma unroll
      for (int m = 0; m < 4; ++m) {
        u32x2 pkd;
#pragma unroll
        for (int h = 0; h < 2; ++h) {
          f32x2 v = {acc[m][i][2 * h], acc[m][i][2 * h + 1]};
          if constexpr (FOLD) v = v * ab[m][0] - (f32x2){cs[2 * h], cs[2 * h + 1]} * ab[m][1];
          pkd[h] = pk2(v[0], v[1]);
        }
        *(u32x2*)(T + (wr * 64 + m * 16 + (lane & 15)) * TSTR + ncol * 2) = pkd;
      }
    }
  } else if constexpr (EPI == EPI_QKV) {
    if (g.res1) {
      const int t = tid - 256;
      const u16* W4 = (const u16*)g.stats_out + (size_t)b * 14336 + t * 8;
      const u16* Wo = g.res1 + (size_t)b * 4096 + t * 8;
      const u16* W2 = g.res2 + (size_t)b * 4096 + t * 8;
#pragma unroll
      for (int i = 0; i < 2; ++i) { pv[7 + i] = *(const u32x4*)(Wo + i * 2048); pv[9 + i] = *(const u32x4*)(W2 + i * 2048); }
#pragma unroll
      for (int i = 0; i < 7; ++i) pv[i] = *(const u32x4*)(W4 + i * 2048);
    }
  }
  if constexpr (EPI == EPI_QKV) { wait_lgkm0(); bar(); asm volatile("" ::: "memory"); }
  else __syncthreads();
  TS(4);
  auto gelu8 = [&](u32x4 v) {
    u32x4 o;
#pragma unroll
    for (int e = 0; e < 4; ++e) { const f32x2 y = gelu2(unpk(v[e])); o[e] = pk2(y[0], y[1]); }
    return o;
  };
  if constexpr (EPI == EPI_GELU) {
    u32x4 v[NI];
#pragma unroll
    for (int it = 0; it < NI; ++it) {
      const int r = it < NI - 1 ? (it & 1) * 64 + (tid >> 3) : tid >> 2, c = it < NI - 1 ? (it >> 1) * 8 + (tid & 7) : (NI / 2) * 8 + (tid & 3);
      v[it] = *(const u32x4*)(T + r * TSTR + c * 16);
    }
#pragma unroll
    for (int it = 0; it < NI; ++it) {
      const int r = it < NI - 1 ? (it & 1) * 64 + (tid >> 3) : tid >> 2, col = it < NI - 1 ? nf + (it >> 1) * 64 + (tid & 7) * 8 : nh + (tid & 3) * 8;
      st16_wt(g.out, SEQ * FF * 2, ((m0 + r) * FF + col) * 2, gelu8(v[it]));
    }
  } else if constexpr (EPI == EPI_GELU_SHIFT) {
    const int grp = n0 >> 8;
    const int sh = (grp == 0) ? 1 : (grp == 1) ? -1 : (grp == 2) ? 2 : -2;
#pragma unroll
    for (int it = 0; it < 2; ++it) {
      const int r = it * 64 + (tid >> 3), c = tid & 7;
      const u32x4 v = gelu8(*(const u32x4*)(T + r * TSTR + c * 16));
      const int dr = m0 + r + sh;
      if (dr >= 0 && dr < SEQ) st16_wt(g.out, SEQ * DM * 2, (dr * DM + n0 + c * 8) * 2, v);
    }
    if (tid < 16) {
      const int zr = tid >> 3, c = tid & 7;
      const int az = sh > 0 ? sh : -sh;
      if (zr < az) {
        if (sh > 0 && rp == 0) *(u32x4*)(g.out + (size_t)zr * DM + n0 + c * 8) = (u32x4){0u, 0u, 0u, 0u};
        if (sh < 0 && rp == 15) *(u32x4*)(g.out + (size_t)(SEQ - 1 - zr) * DM + n0 + c * 8) = (u32x4){0u, 0u, 0u, 0u};
      }
    }
  } else if constexpr (EPI == EPI_RES || EPI == EPI_W2 || EPI == EPI_W4) {
#pragma unroll
    for (int it = 0; it < 2; ++it) {
      const int r = it * 64 + (tid >> 3), c = tid & 7;
      const u32x4 v = *(const u32x4*)(T + r * TSTR + c * 16);
      const u32x4 x = *(const u32x4*)(smem + OFF_RES + r * 128 + c * 16);
      f32x2 y[4];
      if constexpr (EPI == EPI_RES) {
#pragma unroll
        for (int e = 0; e < 4; ++e) y[e] = unpk(x[e]) + unpk(v[e]);
      } else if constexpr (EPI == EPI_W2) {
        const RowP rr = rowp[r];
#pragma unroll
        for (int e = 0; e < 4; ++e) y[e] = rbf2(rbf2(unpk(x[e]) - rr.mhat) * rr.alpha) + unpk(v[e]);
      } else {
        const u32x4 x1 = *(const u32x4*)(smem + OFF_RES + 16384 + r * 128 + c * 16);
#pragma unroll
        for (int e = 0; e < 4; ++e) y[e] = unpk(x[e]) + rbf2(unpk(x1[e]) + unpk(v[e]));
      }
      u32x4 o;
#pragma unroll
      for (int e = 0; e < 4; ++e) { o[e] = pk2(y[e][0], y[e][1]); y[e] = unpk(o[e]); }
      st16_wt(g.out, SEQ * DM * 2, ((m0 + r) * DM + n0 + c * 8) * 2, o);
      const f32x2 s2 = (y[0] + y[1]) + (y[2] + y[3]);
      const float s = sum8(s2[0] + s2[1]);
      const float mc = s * (1.0f / 64.0f);
      f32x2 q2 = {0.f, 0.f};
#pragma unroll
      for (int e = 0; e < 4; ++e) { const f32x2 d = y[e] - mc; q2 += d * d; }
      const float m2 = sum8(q2[0] + q2[1]);
      if (c == 0) *(f32x2*)(g.stats_out + ((size_t)ct * SEQ + m0 + r) * 2) = (f32x2){s, m2};
    }
  } else {
    constexpr int PKSTR = 160;
    char* PK = smem + 128 * TSTR;
#pragma unroll
    for (int it = 0; it < 2; ++it) {
      const int r = it * 64 + (tid >> 3), c = tid & 7;
      st16_wt(g.out, SEQ * DM * 2, ((m0 + r) * DM + n0 + c * 8) * 2, *(const u32x4*)(T + r * TSTR + c * 16));
    }
    {
      const int hh = w & 1, rq = w >> 1;
      bf16x8 wf[2];
#pragma unroll
      for (int fb = 0; fb < 2; ++fb) {
        const char* p = smem + OFF_WPHI + hh * 2048 + (8 * g4 + q4) * 64 + fb * 32 + pp * 8;
        wf[fb] = cat8(trread(p), trread(p + 256));
      }
#pragma unroll
      for (int mb = 0; mb < 2; ++mb) {
        const int row = rq * 32 + mb * 16 + (lane & 15);
        const bf16x8 xf = *(const bf16x8*)(T + row * TSTR + (64 + hh * 32 + g4 * 8) * 2);
        u32x4 pos, neg;
        feature_map(wf, xf, pos, neg);
        char* dst = PK + (hh * 128 + row) * PKSTR + g4 * 8;
        *(u32x2*)dst = (u32x2){pos[0], pos[1]}; *(u32x2*)(dst + 32) = (u32x2){pos[2], pos[3]};
        *(u32x2*)(dst + 64) = (u32x2){neg[0], neg[1]}; *(u32x2*)(dst + 96) = (u32x2){neg[2], neg[3]};
      }
    }
    __syncthreads();
    {
      const int hh = w >> 2, fa = w & 3;
      f32x4 kv[3];
#pragma unroll
      for (int d = 0; d < 3; ++d) kv[d] = (f32x4){0.f, 0.f, 0.f, 0.f};
      const short one = (short)0x3F80;
      const bf16x8 ones = {one, one, one, one, one, one, one, one};
#pragma unroll
      for (int ks = 0; ks < 4; ++ks) {
        const int row = ks * 32 + 8 * g4 + q4;
        bf16x8 vf[2];
#pragma unroll
        for (int d = 0; d < 2; ++d) {
          const char* p = T + row * TSTR + (128 + hh * 32 + d * 16) * 2 + pp * 8;
          vf[d] = cat8(trread(p), trread(p + 4 * TSTR));
        }
        const char* p = PK + (hh * 128 + row) * PKSTR + (fa * 16) * 2 + pp * 8;
        const bf16x8 pf = cat8(trread(p), trread(p + 4 * PKSTR));
        kv[0] = mfma16(pf, vf[0], kv[0]);
        kv[1] = mfma16(pf, vf[1], kv[1]);
        kv[2] = mfma16(pf, ones, kv[2]);
      }
      constexpr unsigned KVB = 16 * NH * 33 * 64 * 4;
      const int so = (rp * NH + ct * 2 + hh) * (33 * 64) + fa * 16 + g4 * 4;
      st16_wt(g.kvpart, KVB, (so + (lane & 15) * 64) * 4, __builtin_bit_cast(u32x4, kv[0]));
      st16_wt(g.kvpart, KVB, (so + (16 + (lane & 15)) * 64) * 4, __builtin_bit_cast(u32x4, kv[1]));
      if ((lane & 15) == 0) st16_wt(g.kvpart, KVB, (so + 32 * 64) * 4, __builtin_bit_cast(u32x4, kv[2]));
    }
    if (prod && g.res1) {
#pragma unroll
      for (int i = 0; i < 11; ++i) asm volatile("" ::"v"(pv[i]));
    }
  }
  TS(5);
}

__global__ __launch_bounds__(256) void ln0_k(const u16* __restrict__ x, u16* __restrict__ y, const u16* __restrict__ wqkv,
                                             const u16* __restrict__ w1, const u16* __restrict__ w3, float* __restrict__ colsum,
                                             const u16* __restrict__ wphi, u16* __restrict__ wphit) {
  if (blockIdx.x >= SEQ / 4 + 120) {
    const int t = (blockIdx.x - (SEQ / 4 + 120)) * 256 + threadIdx.x;
    const int hd = t >> 7, fb = (t >> 6) & 1, ln = t & 63;
    const u16* src = wphi + hd * 1024 + (ln >> 4) * 256 + fb * 16 + (ln & 15);
    u32x4 v;
#pragma unroll
    for (int e = 0; e < 4; ++e) v[e] = (unsigned)src[e * 64] | ((unsigned)src[e * 64 + 32] << 16);
    *(u32x4*)(wphit + t * 8) = v;
    return;
  }
  if (blockIdx.x >= SEQ / 4) {
    __shared__ float red[32 * 64];
    const int tid = threadIdx.x;
    const int cg = blockIdx.x - SEQ / 4;
    const u16* W; int N, c0;
    if (cg < 48) { W = wqkv; N = 3 * DM; c0 = cg * 64; }
    else if (cg < 64) { W = w1; N = DM; c0 = (cg - 48) * 64; }
    else { W = w3; N = FF; c0 = (cg - 64) * 64; }
    const int c = tid & 7, rg = tid >> 3;
    f32x2 s[4] = {{0.f, 0.f}, {0.f, 0.f}, {0.f, 0.f}, {0.f, 0.f}};
#pragma unroll 16
    for (int i = 0; i < 32; ++i) {
      const u32x4 v = *(const u32x4*)(W + (size_t)(rg + i * 32) * N + c0 + c * 8);
#pragma unroll
      for (int e = 0; e < 4; ++e) s[e] += unpk(v[e]);
    }
#pragma unroll
    for (int e = 0; e < 4; ++e) *(f32x2*)(red + rg * 64 + c * 8 + 2 * e) = s[e];
    __syncthreads();
    if (tid < 64) {
      float t = 0.f;
#pragma unroll
      for (int i = 0; i < 32; ++i) t += red[i * 64 + tid];
      colsum[cg * 64 + tid] = t;
    }
    return;
  }
  const int lane = threadIdx.x & 63, row = blockIdx.x * 4 + (threadIdx.x >> 6);
  const u16* src = x + (size_t)row * DM;
  const u32x4 a = *(const u32x4*)(src + lane * 8), b = *(const u32x4*)(src + 512 + lane * 8);
  float v[16];
#pragma unroll
  for (int e = 0; e < 4; ++e) { v[2 * e] = bflo(a[e]); v[2 * e + 1] = bfhi(a[e]); v[8 + 2 * e] = bflo(b[e]); v[9 + 2 * e] = bfhi(b[e]); }
  float s = 0.f;
#pragma unroll
  for (int e = 0; e < 16; ++e) s += v[e];
  s = sum64(s);
  const float mean = s * (1.0f / 1024.0f);
  float m2 = 0.f;
#pragma unroll
  for (int e = 0; e < 16; ++e) { const float d = v[e] - mean; m2 += d * d; }
  m2 = sum64(m2);
  const RowP r = row_params(mean, m2 * (1.0f / 1024.0f), 1);
  u32x4 oa, ob;
#pragma unroll
  for (int e = 0; e < 4; ++e) {
    oa[e] = pk2(rbf(v[2 * e] - r.mhat) / r.dhat, rbf(v[2 * e + 1] - r.mhat) / r.dhat);
    ob[e] = pk2(rbf(v[8 + 2 * e] - r.mhat) / r.dhat, rbf(v[9 + 2 * e] - r.mhat) / r.dhat);
  }
  u16* dst = y + (size_t)row * DM;
  *(u32x4*)(dst + lane * 8) = oa;
  *(u32x4*)(dst + 512 + lane * 8) = ob;
}

__global__ __launch_bounds__(512) void attn_o_k(const u16* __restrict__ q, const u16* __restrict__ wphi, const float* __restrict__ kvpart,
                                                u16* __restrict__ o) {
  constexpr int KS = 80;
  __shared__ __attribute__((aligned(16))) char sm[33 * KS * 2 + 8];
  const int tid = threadIdx.x, lane = tid & 63, w = tid >> 6;
  constexpr int EPI = 6;
  TS(0);
  const int h = blockIdx.x & 31, rb = blockIdx.x >> 5;
  const int g4 = lane >> 4, r0 = rb * 256 + w * 32 + (lane & 15);
  bf16x8 qf[2], wf[2];
#pragma unroll
  for (int mb = 0; mb < 2; ++mb) qf[mb] = *(const bf16x8*)(q + (size_t)(r0 + mb * 16) * DM + h * 32 + g4 * 8);
#pragma unroll
  for (int fb = 0; fb < 2; ++fb) wf[fb] = *(const bf16x8*)(wphi + ((h * 2 + fb) * 64 + lane) * 8);
  __builtin_amdgcn_sched_barrier(0);
  bar();
  __builtin_amdgcn_sched_barrier(0);
  f32x4 kp[16], kz[4];
  const float* slab = kvpart + (size_t)h * (33 * 64) + tid * 4;
  const bool zw = __builtin_amdgcn_readfirstlane(w) == 0;
  if (zw) {
#pragma unroll
    for (int p = 0; p < 4; ++p) kz[p] = *(const f32x4*)(kvpart + ((size_t)(p * 4 + g4) * NH + h) * (33 * 64) + 2048 + (lane & 15) * 4);
  }
#pragma unroll
  for (int p = 0; p < 16; ++p) kp[p] = *(const f32x4*)(slab + (size_t)p * NH * (33 * 64));
  __builtin_amdgcn_sched_barrier(0);
  bf16x8 pq[2][2];
#pragma unroll
  for (int mb = 0; mb < 2; ++mb) {
    u32x4 pos, neg;
    feature_map(wf, qf[mb], pos, neg);
    pq[mb][0] = __builtin_bit_cast(bf16x8, pos);
    pq[mb][1] = __builtin_bit_cast(bf16x8, neg);
  }
  __builtin_amdgcn_sched_barrier(0);
  TS(1);
  u16* kvt = (u16*)sm;
  {
    f32x4 t = kp[0];
#pragma unroll
    for (int p = 1; p < 16; ++p) t += kp[p];
    *(u32x2*)(kvt + (tid >> 4) * KS + (tid & 15) * 4) = (u32x2){pk2(t[0], t[1]), pk2(t[2], t[3])};
    if (zw) {
      f32x4 z = (kz[0] + kz[1]) + (kz[2] + kz[3]);
#pragma unroll
      for (int e = 0; e < 4; ++e) z[e] = sum4g(z[e]);
      if (tid < 16) *(u32x2*)(kvt + 32 * KS + tid * 4) = (u32x2){pk2(z[0], z[1]), pk2(z[2], z[3])};
    }
  }
  TS(2);
  __syncthreads();
  TS(3);
  bf16x8 kf[3][2];
#pragma unroll
  for (int db = 0; db < 3; ++db)
#pragma unroll
    for (int ks = 0; ks < 2; ++ks) {
      const int d = (db < 2) ? ((lane >> 2) & 3) * 8 + db * 4 + (lane & 3) : 32;
      const u16* p = kvt + d * KS + ks * 32 + g4 * 4;
      kf[db][ks] = cat8(*(const s16x4*)p, *(const s16x4*)(p + 16));
    }
#pragma unroll
  for (int mb = 0; mb < 2; ++mb) {
    f32x4 a[3];
#pragma unroll
    for (int db = 0; db < 3; ++db) {
      a[db] = mfma16(kf[db][0], pq[mb][0], (f32x4){0.f, 0.f, 0.f, 0.f});
      a[db] = mfma16(kf[db][1], pq[mb][1], a[db]);
    }
    const float id = __builtin_amdgcn_rcpf(rbf(rbf(a[2][0]) + 1e-6f));
    u32x4 ov;
#pragma unroll
    for (int db = 0; db < 2; ++db) {
      const f32x2 lo = rbf2((f32x2){a[db][0], a[db][1]}) * id, hi = rbf2((f32x2){a[db][2], a[db][3]}) * id;
      ov[db * 2] = pk2(lo[0], lo[1]); ov[db * 2 + 1] = pk2(hi[0], hi[1]);
    }
    st16_wt(o, SEQ * DM * 2, ((r0 + mb * 16) * DM + h * 32 + g4 * 8) * 2, ov);
  }
  TS(4);
  TS(5);
}

template <int BN, int S, int KD, int EPI> static constexpr int smem_bytes() {
  constexpr bool USE_STATS = (EPI == EPI_QKV || EPI == EPI_GELU_SHIFT || EPI == EPI_W2 || EPI == EPI_GELU);
  constexpr int NRES = (EPI == EPI_RES || EPI == EPI_W2) ? 1 : (EPI == EPI_W4 ? 2 : 0);
  return S * (16384 + BN * 128) + NRES * 16384 + (EPI == EPI_QKV ? 4096 : 0) + 2048 + 1024 + 256;
}
template <int BN, int S, int KD, int EPI> static void launch_gemm(const GemmArgs& a, hipStream_t st) {
  constexpr int sb = smem_bytes<BN, S, KD, EPI>();
  static_assert(sb <= 163840, "LDS");
  (void)hipFuncSetAttribute((const void*)gemm_k<BN, S, KD, EPI>, hipFuncAttributeMaxDynamicSharedMemorySize, sb);
  gemm_k<BN, S, KD, EPI><<<256, 512, sb, st>>>(a);
}

extern "C" void kernel_launch(void* const* d_in, const int* in_sizes, int n_in, void* d_out, int out_size, void* d_ws,
                              size_t ws_size, hipStream_t stream) {
  const u16* x0 = (const u16*)d_in[0];
  const u16* wqkv = (const u16*)d_in[1];
  const u16* wphi = (const u16*)d_in[2];
  const u16* wo = (const u16*)d_in[3];
  const u16* w1 = (const u16*)d_in[5];
  const u16* w2 = (const u16*)d_in[6];
  const u16* w3 = (const u16*)d_in[7];
  const u16* w4 = (const u16*)d_in[8];
  char* ws = (char*)d_ws;
  const size_t MB = 1 << 20;
  u16* xln0 = (u16*)(ws + 0 * MB);
  u16* phiq = (u16*)(ws + 4 * MB);
  float* kvpart = (float*)(ws + 12 * MB);
  u16* obuf = (u16*)(ws + 17 * MB);
  u16* xa = (u16*)(ws + 21 * MB);
  u16* hsh = (u16*)(ws + 25 * MB);
  u16* x1 = (u16*)(ws + 29 * MB);
  u16* gbuf = (u16*)(ws + 33 * MB);
  u16* xb = (u16*)(ws + 48 * MB);
  float* st_xa = (float*)(ws + 52 * MB);
  float* st_x1 = (float*)(ws + 53 * MB);
  float* st_xb = (float*)(ws + 54 * MB);
  float* colsum = (float*)(ws + 55 * MB);

  u16* wphit = (u16*)(ws + 55 * MB + 65536);
  ln0_k<<<SEQ / 4 + 136, 256, 0, stream>>>(x0, xln0, wqkv, w1, w3, colsum, wphi, wphit);
  for (int layer = 0; layer < 2; ++layer) {
    const u16* xin = layer == 0 ? x0 : xb;
    u16* xout = layer == 0 ? xb : (u16*)d_out;
    GemmArgs a;
    a = GemmArgs{layer == 0 ? xln0 : xb, wqkv, phiq, layer == 0 ? wo : nullptr, w2, st_xb, (float*)w4, colsum, wphi, kvpart, layer == 0 ? 0 : 1, 0};
    launch_gemm<192, 3, 1024, EPI_QKV>(a, stream);
    attn_o_k<<<256, 512, 0, stream>>>(phiq, wphit, kvpart, obuf);
    a = GemmArgs{obuf, wo, xa, xin, nullptr, nullptr, st_xa, nullptr, nullptr, nullptr, 0, 0};
    launch_gemm<64, 5, 1024, EPI_RES>(a, stream);
    a = GemmArgs{xa, w1, hsh, nullptr, nullptr, st_xa, nullptr, colsum + 3072, nullptr, nullptr, 2, 0};
    launch_gemm<64, 5, 1024, EPI_GELU_SHIFT>(a, stream);
    a = GemmArgs{hsh, w2, x1, xa, nullptr, st_xa, st_x1, nullptr, nullptr, nullptr, 1, 0};
    launch_gemm<64, 5, 1024, EPI_W2>(a, stream);
    a = GemmArgs{x1, w3, gbuf, nullptr, nullptr, st_x1, nullptr, colsum + 4096, nullptr, nullptr, 1, 0};
    launch_gemm<224, 3, 1024, EPI_GELU>(a, stream);
    a = GemmArgs{gbuf, w4, xout, xa, x1, nullptr, st_xb, nullptr, nullptr, nullptr, 0, 0};
    launch_gemm<64, 5, 3584, EPI_W4>(a, stream);
  }
}
```

```cpp
#include <hip/hip_runtime.h>
#include <hip/hip_bf16.h>
#include <stdint.h>

typedef __attribute__((ext_vector_type(8))) short bf16x8;
typedef __attribute__((ext_vector_type(4))) short s16x4;
typedef __attribute__((ext_vector_type(4))) float f32x4;
typedef __attribute__((ext_vector_type(2))) float f32x2;
typedef __attribute__((ext_vector_type(4))) unsigned u32x4;
typedef __attribute__((ext_vector_type(2))) unsigned u32x2;
typedef __attribute__((ext_vector_type(2))) __bf16 bf16x2_t;
typedef unsigned short u16;
#define LDS_AS __attribute__((address_space(3)))
#define DI __device__ __forceinline__

static constexpr int SEQ = 2048, DM = 1024, NH = 32, FF = 3584;

DI float bf2f(unsigned v) { return __uint_as_float(v << 16); }
DI float bflo(unsigned v) { return __uint_as_float(v << 16); }
DI float bfhi(unsigned v) { return __uint_as_float(v & 0xffff0000u); }
DI unsigned pk2(float a, float b) {
  f32x2 v = {a, b};
  return __builtin_bit_cast(unsigned, __builtin_convertvector(v, bf16x2_t));
}
DI float rbf(float a) { return bflo(pk2(a, a)); }
template <int CTRL> DI float dppf(float v) { return __builtin_bit_cast(float, __builtin_amdgcn_update_dpp(0, __builtin_bit_cast(int, v), CTRL, 0xf, 0xf, true)); }
DI float sum8(float v) { v += dppf<0xB1>(v); v += dppf<0x4E>(v); return v + dppf<0x141>(v); }
DI f32x2 pair16(float v) { const unsigned u = __float_as_uint(v); const auto r = __builtin_amdgcn_permlane16_swap(u, u, false, false); return (f32x2){__uint_as_float(r[0]), __uint_as_float(r[1])}; }
DI f32x2 pair32(float v) { const unsigned u = __float_as_uint(v); const auto r = __builtin_amdgcn_permlane32_swap(u, u, false, false); return (f32x2){__uint_as_float(r[0]), __uint_as_float(r[1])}; }
DI float max4g(float v) { f32x2 t = pair16(v); t = pair32(fmaxf(t[0], t[1])); return fmaxf(t[0], t[1]); }
DI float min4g(float v) { f32x2 t = pair16(v); t = pair32(fminf(t[0], t[1])); return fminf(t[0], t[1]); }
DI float sum4g(float v) { f32x2 t = pair16(v); t = pair32(t[0] + t[1]); return t[0] + t[1]; }
DI float sum64(float v) { v = sum8(v); return sum4g(v + dppf<0x140>(v)); }

template <int I> struct IC { static constexpr int value = I; };
template <int B, int E, class F> DI void static_for(F&& f) {
  if constexpr (B < E) { f(IC<B>{}); static_for<B + 1, E>(f); }
}
template <int N> DI void wait_vm() { asm volatile("s_waitcnt vmcnt(%0)" ::"n"(N) : "memory"); }
DI void wait_lgkm0() { asm volatile("s_waitcnt lgkmcnt(0)" ::: "memory"); }
DI void bar() { __builtin_amdgcn_s_barrier(); }
DI void glds16(const void* src, char* lds_dst) {
  __builtin_amdgcn_global_load_lds((const unsigned*)src, (unsigned*)lds_dst, 16, 0, 0);
}
DI s16x4 trread(const char* p) {
  return __builtin_bit_cast(s16x4, __builtin_amdgcn_ds_read_tr16_b64_v4i16((LDS_AS s16x4*)p));
}
template <int OFF> DI s16x4 lds_tr(unsigned addr) {
  s16x4 r; asm volatile("ds_read_b64_tr_b16 %0, %1 offset:%2" : "=v"(r) : "v"(addr), "n"(OFF) : "memory"); return r;
}
template <int OFF> DI bf16x8 lds_b128(unsigned addr) {
  bf16x8 r; asm volatile("ds_read_b128 %0, %1 offset:%2" : "=v"(r) : "v"(addr), "n"(OFF) : "memory"); return r;
}
template <int OFF> DI f32x2 lds_f2(unsigned addr) {
  f32x2 r; asm volatile("ds_read_b64 %0, %1 offset:%2" : "=v"(r) : "v"(addr), "n"(OFF) : "memory"); return r;
}
template <int OFF> DI f32x4 lds_f4(unsigned addr) {
  f32x4 r; asm volatile("ds_read_b128 %0, %1 offset:%2" : "=v"(r) : "v"(addr), "n"(OFF) : "memory"); return r;
}
DI void st16_wt(void* base, unsigned bytes, unsigned off, u32x4 v) {
  __builtin_amdgcn_raw_buffer_store_b128(v, __builtin_amdgcn_make_buffer_rsrc(base, (short)0, (int)bytes, 0x00020000), (int)off, 0, 16);
}
DI bf16x8 cat8(s16x4 lo, s16x4 hi) { return (bf16x8){lo[0], lo[1], lo[2], lo[3], hi[0], hi[1], hi[2], hi[3]}; }
DI f32x4 mfma16(bf16x8 a, bf16x8 b, f32x4 c) { return __builtin_amdgcn_mfma_f32_16x16x32_bf16(a, b, c, 0, 0, 0); }

DI f32x2 gelu2(f32x2 x) {
  const float c0 = -2.302208198f, c1 = -2.302208198f * 0.044715f;
  const f32x2 a = x * (x * x * c1 + c0);
  const f32x2 d = (f32x2){__builtin_amdgcn_exp2f(a[0]), __builtin_amdgcn_exp2f(a[1])} + 1.0f;
  return x * (f32x2){__builtin_amdgcn_rcpf(d[0]), __builtin_amdgcn_rcpf(d[1])};
}
DI f32x2 rbf2(f32x2 v) { const unsigned p = pk2(v[0], v[1]); return (f32x2){bflo(p), bfhi(p)}; }
DI f32x2 unpk(unsigned p) { return (f32x2){bflo(p), bfhi(p)}; }

DI void feature_map(const bf16x8 (&wf)[2], bf16x8 xf, u32x4& pos, u32x4& neg) {
  f32x2 p[4];
#pragma unroll
  for (int fb = 0; fb < 2; ++fb) {
    const f32x4 d = mfma16(wf[fb], xf, (f32x4){0.f, 0.f, 0.f, 0.f});
    p[fb * 2] = rbf2((f32x2){d[0], d[1]});
    p[fb * 2 + 1] = rbf2((f32x2){d[2], d[3]});
  }
  float mx = fmaxf(p[0][0], p[0][1]), mn = fminf(p[0][0], p[0][1]);
#pragma unroll
  for (int e = 1; e < 4; ++e) { mx = fmaxf(mx, fmaxf(p[e][0], p[e][1])); mn = fminf(mn, fminf(p[e][0], p[e][1])); }
  mx = max4g(mx);
  mn = min4g(mn);
  const float l2e = 1.4426950408889634f;
  f32x2 ep[4], en[4], sp2 = {0.f, 0.f}, sn2 = {0.f, 0.f};
#pragma unroll
  for (int e = 0; e < 4; ++e) {
    const f32x2 a = (p[e] - mx) * l2e, b = (mn - p[e]) * l2e;
    ep[e] = (f32x2){__builtin_amdgcn_exp2f(a[0]), __builtin_amdgcn_exp2f(a[1])}; sp2 += ep[e];
    en[e] = (f32x2){__builtin_amdgcn_exp2f(b[0]), __builtin_amdgcn_exp2f(b[1])}; sn2 += en[e];
  }
  float sp = sp2[0] + sp2[1], sn = sn2[0] + sn2[1];
  sp = sum4g(sp);
  sn = sum4g(sn);
  const float ip = __builtin_amdgcn_rcpf(sp), in_ = __builtin_amdgcn_rcpf(sn);
#pragma unroll
  for (int e = 0; e < 4; ++e) {
    const f32x2 a = ep[e] * ip, b = en[e] * in_;
    pos[e] = pk2(a[0], a[1]); neg[e] = pk2(b[0], b[1]);
  }
}

#define TS(i)
struct GemmArgs {
  const u16* A;
  const u16* B;
  u16* out;
  const u16* res1;
  const u16* res2;
  const float* stats_in;
  float* stats_out;
  const float* colsum;
  const u16* wphi;
  float* kvpart;
  int ln_mode;
  int unused;
};

enum { EPI_QKV = 0, EPI_RES = 1, EPI_GELU_SHIFT = 2, EPI_W2 = 3, EPI_GELU = 4, EPI_W4 = 5 };

struct RowP { float alpha, beta, mhat, dhat; };
DI RowP row_params(float mean, float var, int mode) {
  RowP r;
  const float mh = rbf(mean);
  const float dh = rbf(__builtin_amdgcn_sqrtf(rbf(rbf(var) + 1e-5f)));
  const float i1 = __builtin_amdgcn_rcpf(dh);
  r.mhat = mh; r.dhat = dh;
  if (mode == 2) {
    const float m2 = (mean - mh) * i1;
    const float v2 = var * i1 * i1;
    const float mh2 = rbf(m2);
    const float dh2 = rbf(__builtin_amdgcn_sqrtf(rbf(rbf(v2) + 1e-5f)));
    const float i2 = __builtin_amdgcn_rcpf(dh2);
    r.alpha = i1 * i2;
    r.beta = (mh * i1 + mh2) * i2;
  } else {
    r.alpha = i1;
    r.beta = mh * i1;
  }
  return r;
}

template <int BN, int S, int KD, int EPI>
__global__ __launch_bounds__(512, 2) void gemm_k(GemmArgs g) {
  constexpr int NK = KD / 64;
  constexpr int NPF = BN / 64;
  constexpr bool HALF = (BN % 64) != 0;
  constexpr int NI = BN / 32;
  constexpr int A_BYTES = 16384;
  constexpr int STAGE = A_BYTES + BN * 128;
  constexpr int L = 4 + NPF * 2 + (HALF ? 1 : 0);
  constexpr int NTOT = (EPI == EPI_QKV) ? 3 * DM : (EPI == EPI_GELU ? FF : DM);
  constexpr bool USE_STATS = (EPI == EPI_QKV || EPI == EPI_GELU_SHIFT || EPI == EPI_W2 || EPI == EPI_GELU);
  constexpr bool FOLD = (EPI == EPI_QKV || EPI == EPI_GELU_SHIFT || EPI == EPI_GELU);
  constexpr int NRES = (EPI == EPI_RES || EPI == EPI_W2) ? 1 : (EPI == EPI_W4 ? 2 : 0);
  constexpr int OFF_STATS = S * STAGE;
  constexpr int OFF_RES = OFF_STATS;
  constexpr int OFF_WPHI = OFF_RES + NRES * 16384;
  constexpr int OFF_ROWP = OFF_WPHI + (EPI == EPI_QKV ? 4096 : 0);
  constexpr int OFF_CS = OFF_ROWP + 2048;
  constexpr int OFF_DUMP = OFF_CS + 1024;
  constexpr int NEXTRA = (USE_STATS && BN != 64 ? 8 : 0) + NRES * 4 + (EPI == EPI_QKV ? 1 : 0) + (FOLD ? 1 : 0);

  constexpr int PR0 = NK > 16 ? 5 : (S == 3 ? 7 : NK - 1), PRN = PR0 < NK - 1 ? (NK - S - PR0) / S : 0;
  constexpr int CR0 = 0, CRN = (NK - 1) / S;
  static_assert(!(USE_STATS && BN == 64) || S == 5, "row statistics sit in the second group of five stages");
  extern __shared__ __attribute__((aligned(16))) char smem[];
  const int tid = threadIdx.x, lane = tid & 63;
  const int w = __builtin_amdgcn_readfirstlane(tid >> 6);
  const bool prod = w >= 4;
  const int cw = w & 3, wr = cw >> 1, wc = cw & 1;

  const int b = blockIdx.x, xcd = b & 7, j = b >> 3;
  int rp, ct;
  if constexpr (EPI == EPI_QKV || EPI == EPI_GELU) { rp = (xcd >> 2) * 8 + (j >> 2); ct = (xcd & 3) * 4 + (j & 3); }
  else { rp = (xcd >> 1) * 4 + (j >> 3); ct = (xcd & 1) * 8 + (j & 7); }
  const int m0 = rp * 128;
  const int n0 = (EPI == EPI_QKV) ? ct * 64 : ct * BN;
  constexpr int PSTRIDE = (EPI == EPI_QKV) ? DM : 64;
  const int nf = HALF ? n0 + (ct & 1) * 32 : n0;
  const int nh = (ct & 1) ? n0 : n0 + NPF * 64;
  const int g4 = lane >> 4, q4 = (lane >> 2) & 3, pp = lane & 3;
  RowP* rowp = (RowP*)(smem + OFF_ROWP);
  const bool ln_on = USE_STATS && (g.ln_mode != 0 || EPI == EPI_W2);

  f32x4 acc[4][NI];
  f32x2 ab[4];
  f32x4 csv[NI];
  TS(0);
  constexpr bool RP_CONS = (BN == 64);
  f32x2 sv[8];
  const int sr = cw * 32 + (lane >> 1), shf = lane & 1;
  auto load_stats = [&]() {
#pragma unroll
    for (int p = 0; p < 8; ++p)
      asm volatile("global_load_dwordx2 %0, %1, off" : "=v"(sv[p]) : "v"(g.stats_in + ((size_t)(shf * 8 + p) * SEQ + m0 + sr) * 2) : "memory");
  };
  float rs_s = 0.f, rs_m2 = 0.f;
  auto row_stats = [&](auto pc) {
    constexpr int piece = decltype(pc)::value;
    if constexpr (piece == 0) {
#pragma unroll
      for (int p = 0; p < 8; ++p) asm volatile("" : "+v"(sv[p]));
      float s = 0.f;
#pragma unroll
      for (int p = 0; p < 8; ++p) s += sv[p][0];
      rs_s = s + dppf<0xB1>(s);
    } else if constexpr (piece == 1) {
      const float mean = rs_s * (1.0f / 1024.0f);
      float m2 = 0.f;
#pragma unroll
      for (int p = 0; p < 8; ++p) { const float d = sv[p][0] * (1.0f / 64.0f) - mean; m2 += sv[p][1] + 64.0f * d * d; }
      rs_m2 = m2 + dppf<0xB1>(m2);
    } else {
      if (ln_on && shf == 0) rowp[sr] = row_params(rs_s * (1.0f / 1024.0f), rs_m2 * (1.0f / 1024.0f), EPI == EPI_W2 ? 1 : g.ln_mode);
      wait_lgkm0();
    }
    __builtin_amdgcn_sched_barrier(0);
  };
  if (__builtin_expect(prod, 1)) {
    const u16* a_src = g.A + (size_t)(m0 + cw * 32 + (lane >> 3)) * KD + (((lane & 7) ^ (lane >> 3)) * 8);
    const u16* b_src[2];
#pragma unroll
    for (int e = 0; e < 2; ++e) {
      const int r = lane >> 3;
      const int f = ((r >> 1) & 1) | (e << 1);
      const int c = ((((lane & 7) >> 1) ^ f) << 1) | (lane & 1);
      b_src[e] = g.B + (size_t)((cw * 2 + e) * 8 + r) * NTOT + nf + c * 8;
    }
    const u16* bh_src = nullptr;
    if constexpr (HALF) {
      const int c = ((((lane >> 1) & 1) ^ ((lane >> 5) & 1)) << 1) | (lane & 1);
      bh_src = g.B + (size_t)(cw * 16 + (lane >> 2)) * NTOT + nh + c * 8;
    }
    auto issue = [&](int kt, int slot) {
      char* base = smem + slot * STAGE;
#pragma unroll
      for (int p = 0; p < NPF; ++p)
#pragma unroll
        for (int e = 0; e < 2; ++e)
          glds16(b_src[e] + (size_t)kt * 64 * NTOT + p * PSTRIDE, base + A_BYTES + p * 8192 + (cw * 2 + e) * 1024);
      if constexpr (HALF) glds16(bh_src + (size_t)kt * 64 * NTOT, base + A_BYTES + NPF * 8192 + cw * 1024);
#pragma unroll
      for (int e = 0; e < 4; ++e) glds16(a_src + (size_t)e * 8 * KD + kt * 64, base + (cw * 4 + e) * 1024);
    };
    TS(6);
    issue(0, 0);
    issue(1, 1);
    wait_vm<L>();
    TS(1);
    bar();
    static_for<2, S>([&](auto ic) { issue(ic.value, ic.value); });
    TS(7);
    if constexpr (NRES >= 1) {
#pragma unroll
      for (int e = 0; e < 4; ++e) {
        const int r = (cw * 4 + e) * 8 + (lane >> 3);
        glds16(g.res1 + (size_t)(m0 + r) * DM + n0 + (lane & 7) * 8, smem + OFF_RES + (cw * 4 + e) * 1024);
      }
    }
    if constexpr (NRES >= 2) {
#pragma unroll
      for (int e = 0; e < 4; ++e) {
        const int r = (cw * 4 + e) * 8 + (lane >> 3);
        glds16(g.res2 + (size_t)(m0 + r) * DM + n0 + (lane & 7) * 8, smem + OFF_RES + 16384 + (cw * 4 + e) * 1024);
      }
    }
    if constexpr (EPI == EPI_QKV) glds16(g.wphi + (size_t)ct * 2048 + cw * 512 + lane * 8, smem + OFF_WPHI + cw * 1024);
    if constexpr (FOLD) {
      int col = cw * 64 + (lane & 15) * 4;
      if (col > BN - 4) col = BN - 4;
      const int gcol = (EPI == EPI_QKV) ? (col >> 6) * DM + n0 + (col & 63) : (HALF && col >= NPF * 64) ? nh + col - NPF * 64 : nf + col;
      if (lane < 16) glds16(g.colsum + gcol, smem + OFF_CS + cw * 256);
    }
    if constexpr (USE_STATS && !RP_CONS) load_stats();
    auto pstep = [&](auto ic) {
      constexpr int kt = ic.value;
      constexpr int young = (S - 2 < NK - 2 - kt) ? S - 2 : NK - 2 - kt;
      constexpr int ex = (kt + 1 <= S - 1 && kt + 1 < NK - 1) ? NEXTRA : 0;
      wait_vm<young * L + ex>();
      bar();
      if constexpr (kt + S < NK) issue(kt + S, kt % S);
    };
    static_for<0, S - 1>(pstep);
    {
      int slot = S - 1;
#pragma unroll 1
      for (int kt = S - 1; kt < NK - S; ++kt) {
        wait_vm<(S - 2) * L>();
        bar();
        issue(kt + S, slot);
        slot = slot == S - 1 ? 0 : slot + 1;
        if constexpr (USE_STATS && !RP_CONS) {
          if (kt == 4) row_stats(IC<0>{});
          else if (kt == 5) row_stats(IC<1>{});
          else if (kt == 6) row_stats(IC<2>{});
        }
      }
    }
    static_for<NK - S, NK - 1>(pstep);
    TS(2);
    bar();
  } else {
    const int fsw = ((q4 >> 1) & 1) | ((g4 & 1) << 1);
    int a_off[2], b_off[2], bh_off = 0;
#pragma unroll
    for (int k = 0; k < 2; ++k) {
      a_off[k] = (wr * 64 + (lane & 15)) * 128 + (((k * 4 + g4) ^ (lane & 7)) * 16);
      b_off[k] = A_BYTES + (8 * g4 + q4) * 128 + (((2 * k + wc) ^ fsw) * 32) + pp * 8;
    }
    if constexpr (HALF) bh_off = A_BYTES + NPF * 8192 + (8 * g4 + q4) * 64 + ((wc ^ (g4 & 1)) * 32) + pp * 8;
#pragma unroll
    for (int m = 0; m < 4; ++m)
#pragma unroll
      for (int i = 0; i < NI; ++i) acc[m][i] = (f32x4){0.f, 0.f, 0.f, 0.f};
    bf16x8 xa[2][4];
    s16x4 wlo[2][NI], whi[2][NI];
    const unsigned lbase = (unsigned)(size_t)smem;

    auto ldfrag = [&](auto setc, auto slotc, auto kc) {
      constexpr int set = decltype(setc)::value, slot = decltype(slotc)::value, k32 = decltype(kc)::value;
      const unsigned sb = lbase + slot * STAGE;
      const unsigned ba0 = sb + b_off[0], ba1 = sb + b_off[1], bah = sb + bh_off, aa = sb + a_off[k32];
      static_for<0, NI>([&](auto ic) {
        constexpr int i = decltype(ic)::value;
        if constexpr (HALF && i == NI - 1) {
          wlo[set][i] = lds_tr<k32 * 2048>(bah);
          whi[set][i] = lds_tr<k32 * 2048 + 256>(bah);
        } else {
          wlo[set][i] = lds_tr<(i >> 1) * 8192 + k32 * 4096>((i & 1) ? ba1 : ba0);
          whi[set][i] = lds_tr<(i >> 1) * 8192 + k32 * 4096 + 512>((i & 1) ? ba1 : ba0);
        }
      });
      static_for<0, 4>([&](auto mc) { constexpr int m = decltype(mc)::value; xa[set][m] = lds_b128<m * 2048>(aa); });
    };
    auto wait_set = [&](auto setc) {
      constexpr int set = decltype(setc)::value;
      asm volatile("s_waitcnt lgkmcnt(0)" : "+v"(xa[set][0]), "+v"(xa[set][1]), "+v"(xa[set][2]), "+v"(xa[set][3]) :: "memory");
#pragma unroll
      for (int i = 0; i < NI; ++i) asm volatile("" : "+v"(wlo[set][i]), "+v"(whi[set][i]));
      __builtin_amdgcn_sched_barrier(0);
    };
    auto ld1 = [&](auto setc, auto slotc, auto kc, auto rc) {
      constexpr int set = decltype(setc)::value, slot = decltype(slotc)::value, k32 = decltype(kc)::value, r = decltype(rc)::value;
      const unsigned sb = lbase + slot * STAGE;
      if constexpr (r < 2 * NI) {
        constexpr int i = r >> 1, hi = r & 1;
        s16x4 v;
        if constexpr (HALF && i == NI - 1) v = lds_tr<k32 * 2048 + hi * 256>(sb + bh_off);
        else v = lds_tr<(i >> 1) * 8192 + k32 * 4096 + hi * 512>(sb + b_off[i & 1]);
        if constexpr (hi) whi[set][i] = v; else wlo[set][i] = v;
      } else {
        constexpr int m = r - 2 * NI;
        xa[set][m] = lds_b128<m * 2048>(sb + a_off[k32]);
      }
    };
    auto mma_ld = [&](auto setc, auto ldc, auto slotc, auto kc) {
      constexpr int set = decltype(setc)::value;
      constexpr bool ld = decltype(ldc)::value != 0;
      static_for<0, 4 * NI>([&](auto tc) {
        constexpr int t = decltype(tc)::value, i = t >> 2, m = t & 3;
        acc[m][i] = mfma16(cat8(wlo[set][i], whi[set][i]), xa[set][m], acc[m][i]);
        if constexpr (ld && t < 2 * NI + 4) ld1(IC<set ^ 1>{}, slotc, kc, tc);
        __builtin_amdgcn_sched_barrier(0);
      });
    };

    const char* tp = (const char*)g.A;
    unsigned tw = 0;
    bool ton = false;
    int tks = 0;
    if (cw == 0) {
      const int pl = lane >> 4;
      const int bl = (int)(((size_t)g.B >> 7) & 7);
      int off;
      if constexpr (BN == 64) { off = ((xcd & 1) * 8 + ((3 - bl) & 7)) * 128; ton = lane < 16; }
      else if constexpr (EPI == EPI_QKV) { const int c = (3 - bl - (xcd & 3) * 4) & 7; off = pl * 2048 + ((xcd & 3) * 4 + (c & 3)) * 128; ton = c < 4 && lane < 48; }
      else { const int l0 = (xcd & 3) * 14, c = (3 - bl - l0) & 7; off = (l0 + c + pl * 8) * 128; ton = lane < (c < 6 ? 32 : 16); }
      tks = (lane >> 1) & 7;
      if (ton) tp = (const char*)g.B + (size_t)(tks * 64 + j * 2 + (lane & 1)) * NTOT * 2 + off;
      tw = 8 * 64 * NTOT * 2;
    } else if (cw == 1) {
      tks = (3 - (int)(((size_t)g.A >> 7) & 7)) & 7;
      const int row = (BN == 64) ? (xcd >> 1) * 512 + j * 16 + (lane & 15) : (xcd >> 2) * 1024 + j * 32 + (lane & 31);
      ton = lane < (BN == 64 ? 16 : 32);
      tp = (const char*)g.A + (size_t)row * KD * 2 + tks * 128;
      tw = 1024;
    }
    auto touch = [&](auto wc_) {
      constexpr int wi = decltype(wc_)::value;
      if (cw < 2) {
        if (ton && (wi > 0 || tks >= S))
          __builtin_amdgcn_global_load_lds((const unsigned*)(tp + (size_t)(wi * tw)), (unsigned*)(smem + OFF_DUMP), 4, 0, 0);
      }
      __builtin_amdgcn_sched_barrier(0);
    };
    if constexpr (USE_STATS && RP_CONS) load_stats();
    touch(IC<0>{});
    if constexpr (BN == 64) touch(IC<1>{});
#pragma unroll
    for (int m = 0; m < 4; ++m)
#pragma unroll
      for (int i = 0; i < (NI > 6 ? 6 : NI); ++i) asm volatile("" : "+v"(acc[m][i]));
    bar();
    TS(1);
    ldfrag(IC<0>{}, IC<0>{}, IC<0>{});
    if constexpr (BN != 64) touch(IC<1>{});
    auto cstep = [&](auto ic) {
      constexpr int kt = ic.value;
      constexpr int slot = kt % S;
      wait_set(IC<0>{});
      if constexpr (BN != 64 && kt == 0) touch(IC<1>{});
      if constexpr (kt % 8 == 4 && (kt + 12) / 8 < NK / 8) touch(IC<(kt + 12) / 8>{});
      if constexpr (USE_STATS && RP_CONS && kt >= 6 && kt < 9) { if constexpr (kt == 6) wait_vm<0>(); row_stats(IC<kt - 6>{}); }
      mma_ld(IC<0>{}, IC<1>{}, IC<slot>{}, IC<1>{});
      wait_set(IC<1>{});
      if constexpr (kt + 1 < NK) {
        bar();
        __builtin_amdgcn_sched_barrier(0);
        mma_ld(IC<1>{}, IC<1>{}, IC<(kt + 1) % S>{}, IC<0>{});
      } else {
        const unsigned ra = lbase + OFF_ROWP + (wr * 64 + (lane & 15)) * 16, ca = lbase + OFF_CS + (wc * 16 + g4 * 4) * 4;
        static_for<0, 4 * NI>([&](auto tc) {
          constexpr int t = decltype(tc)::value, i = t >> 2, m = t & 3;
          acc[m][i] = mfma16(cat8(wlo[1][i], whi[1][i]), xa[1][m], acc[m][i]);
          if constexpr (FOLD && t < 4) ab[t] = lds_f2<t * 256>(ra);
          if constexpr (FOLD && m == 3) csv[i] = lds_f4<i * 128>(ca);
          __builtin_amdgcn_sched_barrier(0);
        });
      }
    };
    static_for<0, CR0>(cstep);
    if constexpr (CRN > 0) {
#pragma unroll 1
      for (int k0 = CR0; k0 < CR0 + CRN * S; k0 += S)
        static_for<0, S>([&](auto uc) {
          constexpr int slot = (CR0 + uc.value) % S;
          const int kt = k0 + uc.value;
          wait_set(IC<0>{});
          if constexpr (NK > 16) {
            if ((kt & 7) == 4 && kt + 12 < NK && cw < 2) {
              if (ton) __builtin_amdgcn_global_load_lds((const unsigned*)(tp + (size_t)((unsigned)((kt + 12) >> 3) * tw)), (unsigned*)(smem + OFF_DUMP), 4, 0, 0);
            }
          }
          if constexpr (USE_STATS && RP_CONS && uc.value >= 1 && uc.value < 4) {
            if (k0 == 5) { if constexpr (uc.value == 1) wait_vm<0>(); row_stats(IC<uc.value - 1>{}); }
          }
          __builtin_amdgcn_sched_barrier(0);
          mma_ld(IC<0>{}, IC<1>{}, IC<slot>{}, IC<1>{});
          wait_set(IC<1>{});
          bar();
          __builtin_amdgcn_sched_barrier(0);
          mma_ld(IC<1>{}, IC<1>{}, IC<(slot + 1) % S>{}, IC<0>{});
        });
    }
    static_for<CR0 + CRN * S, NK>(cstep);
    TS(2);
    bar();
    if constexpr (FOLD) {
      asm volatile("s_waitcnt lgkmcnt(0)" : "+v"(ab[0]), "+v"(ab[1]), "+v"(ab[2]), "+v"(ab[3]) :: "memory");
#pragma unroll
      for (int i = 0; i < NI; ++i) asm volatile("" : "+v"(csv[i]));
      if (!ln_on) {
#pragma unroll
        for (int m = 0; m < 4; ++m) ab[m] = (f32x2){1.f, 0.f};
      }
    }
  }
  TS(3);
  if constexpr (EPI == EPI_QKV) __builtin_amdgcn_s_waitcnt(0x0F70);

  constexpr int TSTR = BN * 2 + (EPI == EPI_QKV ? 32 : 16);
  char* T = smem;
  u32x4 pv[EPI == EPI_QKV ? 11 : 1];
  if (!prod) {
#pragma unroll
    for (int i = 0; i < NI; ++i) {
      const int nb = 2 * i + wc;
      const int ncol = nb * 16 + g4 * 4;
      const f32x4 cs = csv[i];
#pragma unroll
      for (int m = 0; m < 4; ++m) {
        u32x2 pkd;
#pragma unroll
        for (int h = 0; h < 2; ++h) {
          f32x2 v = {acc[m][i][2 * h], acc[m][i][2 * h + 1]};
          if constexpr (FOLD) v = v * ab[m][0] - (f32x2){cs[2 * h], cs[2 * h + 1]} * ab[m][1];
          pkd[h] = pk2(v[0], v[1]);
        }
        *(u32x2*)(T + (wr * 64 + m * 16 + (lane & 15)) * TSTR + ncol * 2) = pkd;
      }
    }
  } else if constexpr (EPI == EPI_QKV) {
    if (g.res1) {
      const int t = tid - 256;
      const u16* W4 = (const u16*)g.stats_out + (size_t)b * 14336 + t * 8;
      const u16* Wo = g.res1 + (size_t)b * 4096 + t * 8;
      const u16* W2 = g.res2 + (size_t)b * 4096 + t * 8;
#pragma unroll
      for (int i = 0; i < 2; ++i) { pv[7 + i] = *(const u32x4*)(Wo + i * 2048); pv[9 + i] = *(const u32x4*)(W2 + i * 2048); }
#pragma unroll
      for (int i = 0; i < 7; ++i) pv[i] = *(const u32x4*)(W4 + i * 2048);
    }
  }
  if constexpr (EPI == EPI_QKV) { wait_lgkm0(); bar(); asm volatile("" ::: "memory"); }
  else __syncthreads();
  TS(4);
  auto gelu8 = [&](u32x4 v) {
    u32x4 o;
#pragma unroll
    for (int e = 0; e < 4; ++e) { const f32x2 y = gelu2(unpk(v[e])); o[e] = pk2(y[0], y[1]); }
    return o;
  };
  if constexpr (EPI == EPI_GELU) {
    u32x4 v[NI];
#pragma unroll
    for (int it = 0; it < NI; ++it) {
      const int r = it < NI - 1 ? (it & 1) * 64 + (tid >> 3) : tid >> 2, c = it < NI - 1 ? (it >> 1) * 8 + (tid & 7) : (NI / 2) * 8 + (tid & 3);
      v[it] = *(const u32x4*)(T + r * TSTR + c * 16);
    }
#pragma unroll
    for (int it = 0; it < NI; ++it) {
      const int r = it < NI - 1 ? (it & 1) * 64 + (tid >> 3) : tid >> 2, col = it < NI - 1 ? nf + (it >> 1) * 64 + (tid & 7) * 8 : nh + (tid & 3) * 8;
      st16_wt(g.out, SEQ * FF * 2, ((m0 + r) * FF + col) * 2, gelu8(v[it]));
    }
  } else if constexpr (EPI == EPI_GELU_SHIFT) {
    const int grp = n0 >> 8;
    const int sh = (grp == 0) ? 1 : (grp == 1) ? -1 : (grp == 2) ? 2 : -2;
#pragma unroll
    for (int it = 0; it < 2; ++it) {
      const int r = it * 64 + (tid >> 3), c = tid & 7;
      const u32x4 v = gelu8(*(const u32x4*)(T + r * TSTR + c * 16));
      const int dr = m0 + r + sh;
      if (dr >= 0 && dr < SEQ) st16_wt(g.out, SEQ * DM * 2, (dr * DM + n0 + c * 8) * 2, v);
    }
    if (tid < 16) {
      const int zr = tid >> 3, c = tid & 7;
      const int az = sh > 0 ? sh : -sh;
      if (zr < az) {
        if (sh > 0 && rp == 0) *(u32x4*)(g.out + (size_t)zr * DM + n0 + c * 8) = (u32x4){0u, 0u, 0u, 0u};
        if (sh < 0 && rp == 15) *(u32x4*)(g.out + (size_t)(SEQ - 1 - zr) * DM + n0 + c * 8) = (u32x4){0u, 0u, 0u, 0u};
      }
    }
  } else if constexpr (EPI == EPI_RES || EPI == EPI_W2 || EPI == EPI_W4) {
#pragma unroll
    for (int it = 0; it < 2; ++it) {
      const int r = it * 64 + (tid >> 3), c = tid & 7;
      const u32x4 v = *(const u32x4*)(T + r * TSTR + c * 16);
      const u32x4 x = *(const u32x4*)(smem + OFF_RES + r * 128 + c * 16);
      f32x2 y[4];
      if constexpr (EPI == EPI_RES) {
#pragma unroll
        for (int e = 0; e < 4; ++e) y[e] = unpk(x[e]) + unpk(v[e]);
      } else if constexpr (EPI == EPI_W2) {
        const RowP rr = rowp[r];
#pragma unroll
        for (int e = 0; e < 4; ++e) y[e] = rbf2(rbf2(unpk(x[e]) - rr.mhat) * rr.alpha) + unpk(v[e]);
      } else {
        const u32x4 x1 = *(const u32x4*)(smem + OFF_RES + 16384 + r * 128 + c * 16);
#pragma unroll
        for (int e = 0; e < 4; ++e) y[e] = unpk(x[e]) + rbf2(unpk(x1[e]) + unpk(v[e]));
      }
      u32x4 o;
#pragma unroll
      for (int e = 0; e < 4; ++e) { o[e] = pk2(y[e][0], y[e][1]); y[e] = unpk(o[e]); }
      st16_wt(g.out, SEQ * DM * 2, ((m0 + r) * DM + n0 + c * 8) * 2, o);
      const f32x2 s2 = (y[0] + y[1]) + (y[2] + y[3]);
      const float s = sum8(s2[0] + s2[1]);
      const float mc = s * (1.0f / 64.0f);
      f32x2 q2 = {0.f, 0.f};
#pragma unroll
      for (int e = 0; e < 4; ++e) { const f32x2 d = y[e] - mc; q2 += d * d; }
      const float m2 = sum8(q2[0] + q2[1]);
      if (c == 0) *(f32x2*)(g.stats_out + ((size_t)ct * SEQ + m0 + r) * 2) = (f32x2){s, m2};
    }
  } else {
    constexpr int PKSTR = 160;
    char* PK = smem + 128 * TSTR;
#pragma unroll
    for (int it = 0; it < 2; ++it) {
      const int r = it * 64 + (tid >> 3), c = tid & 7;
      st16_wt(g.out, SEQ * DM * 2, ((m0 + r) * DM + n0 + c * 8) * 2, *(const u32x4*)(T + r * TSTR + c * 16));
    }
    {
      const int hh = w & 1, rq = w >> 1;
      bf16x8 wf[2];
#pragma unroll
      for (int fb = 0; fb < 2; ++fb) {
        const char* p = smem + OFF_WPHI + hh * 2048 + (8 * g4 + q4) * 64 + fb * 32 + pp * 8;
        wf[fb] = cat8(trread(p), trread(p + 256));
      }
#pragma unroll
      for (int mb = 0; mb < 2; ++mb) {
        const int row = rq * 32 + mb * 16 + (lane & 15);
        const bf16x8 xf = *(const bf16x8*)(T + row * TSTR + (64 + hh * 32 + g4 * 8) * 2);
        u32x4 pos, neg;
        feature_map(wf, xf, pos, neg);
        char* dst = PK + (hh * 128 + row) * PKSTR + g4 * 8;
        *(u32x2*)dst = (u32x2){pos[0], pos[1]}; *(u32x2*)(dst + 32) = (u32x2){pos[2], pos[3]};
        *(u32x2*)(dst + 64) = (u32x2){neg[0], neg[1]}; *(u32x2*)(dst + 96) = (u32x2){neg[2], neg[3]};
      }
    }
    __syncthreads();
    {
      const int hh = w >> 2, fa = w & 3;
      f32x4 kv[3];
#pragma unroll
      for (int d = 0; d < 3; ++d) kv[d] = (f32x4){0.f, 0.f, 0.f, 0.f};
      const short one = (short)0x3F80;
      const bf16x8 ones = {one, one, one, one, one, one, one, one};
#pragma unroll
      for (int ks = 0; ks < 4; ++ks) {
        const int row = ks * 32 + 8 * g4 + q4;
        bf16x8 vf[2];
#pragma unroll
        for (int d = 0; d < 2; ++d) {
          const char* p = T + row * TSTR + (128 + hh * 32 + d * 16) * 2 + pp * 8;
          vf[d] = cat8(trread(p), trread(p + 4 * TSTR));
        }
        const char* p = PK + (hh * 128 + row) * PKSTR + (fa * 16) * 2 + pp * 8;
        const bf16x8 pf = cat8(trread(p), trread(p + 4 * PKSTR));
        kv[0] = mfma16(pf, vf[0], kv[0]);
        kv[1] = mfma16(pf, vf[1], kv[1]);
        kv[2] = mfma16(pf, ones, kv[2]);
      }
      constexpr unsigned KVB = 16 * NH * 33 * 64 * 4;
      const int so = (rp * NH + ct * 2 + hh) * (33 * 64) + fa * 16 + g4 * 4;
      st16_wt(g.kvpart, KVB, (so + (lane & 15) * 64) * 4, __builtin_bit_cast(u32x4, kv[0]));
      st16_wt(g.kvpart, KVB, (so + (16 + (lane & 15)) * 64) * 4, __builtin_bit_cast(u32x4, kv[1]));
      if ((lane & 15) == 0) st16_wt(g.kvpart, KVB, (so + 32 * 64) * 4, __builtin_bit_cast(u32x4, kv[2]));
    }
    if (prod && g.res1) {
#pragma unroll
      for (int i = 0; i < 11; ++i) asm volatile("" ::"v"(pv[i]));
    }
  }
  TS(5);
}

__global__ __launch_bounds__(1024) void ln0_k(const u16* __restrict__ x, float* __restrict__ stats, const u16* __restrict__ wqkv,
                                              const u16* __restrict__ w1, const u16* __restrict__ w3, float* __restrict__ colsum,
                                              const u16* __restrict__ wphi, u16* __restrict__ wphit) {
  const int tid = threadIdx.x, lane = tid & 63;
  if (blockIdx.x >= 248) {
    const int t = (blockIdx.x - 248) * 1024 + tid;
    const int hd = t >> 7, fb = (t >> 6) & 1;
    const u16* src = wphi + hd * 1024 + (lane >> 4) * 256 + fb * 16 + (lane & 15);
    u32x4 v;
#pragma unroll
    for (int e = 0; e < 4; ++e) v[e] = (unsigned)src[e * 64] | ((unsigned)src[e * 64 + 32] << 16);
    *(u32x4*)(wphit + t * 8) = v;
    return;
  }
  if (blockIdx.x >= 128) {
    __shared__ float red[16 * 64];
    const int cg = blockIdx.x - 128;
    const u16* W; int N, c0;
    if (cg < 48) { W = wqkv; N = 3 * DM; c0 = cg * 64; }
    else if (cg < 64) { W = w1; N = DM; c0 = (cg - 48) * 64; }
    else { W = w3; N = FF; c0 = (cg - 64) * 64; }
    const int c = tid & 7, rg = tid >> 3;
    u32x4 v[8];
#pragma unroll
    for (int i = 0; i < 8; ++i) v[i] = *(const u32x4*)(W + (size_t)(rg + i * 128) * N + c0 + c * 8);
    f32x2 s[4] = {{0.f, 0.f}, {0.f, 0.f}, {0.f, 0.f}, {0.f, 0.f}};
#pragma unroll
    for (int i = 0; i < 8; ++i)
#pragma unroll
      for (int e = 0; e < 4; ++e) s[e] += unpk(v[i][e]);
#pragma unroll
    for (int e = 0; e < 4; ++e)
#pragma unroll
      for (int hf = 0; hf < 2; ++hf) { const float t = s[e][hf]; s[e][hf] = sum4g(t + dppf<0x128>(t)); }
    if (lane < 8) {
#pragma unroll
      for (int e = 0; e < 4; ++e) *(f32x2*)(red + (tid >> 6) * 64 + c * 8 + 2 * e) = s[e];
    }
    __syncthreads();
    if (tid < 64) {
      float t = 0.f;
#pragma unroll
      for (int i = 0; i < 16; ++i) t += red[i * 64 + tid];
      colsum[cg * 64 + tid] = t;
    }
    return;
  }
  const int row = blockIdx.x * 16 + (tid >> 6);
  const u16* src = x + (size_t)row * DM;
  const u32x4 a = *(const u32x4*)(src + lane * 8), b = *(const u32x4*)(src + 512 + lane * 8);
#pragma unroll
  for (int hf = 0; hf < 2; ++hf) {
    const u32x4 u = hf ? b : a;
    f32x2 y[4];
#pragma unroll
    for (int e = 0; e < 4; ++e) y[e] = unpk(u[e]);
    const f32x2 s2 = (y[0] + y[1]) + (y[2] + y[3]);
    const float s = sum8(s2[0] + s2[1]);
    const float mc = s * (1.0f / 64.0f);
    f32x2 q2 = {0.f, 0.f};
#pragma unroll
    for (int e = 0; e < 4; ++e) { const f32x2 dl = y[e] - mc; q2 += dl * dl; }
    const float m2 = sum8(q2[0] + q2[1]);
    if ((lane & 7) == 0) *(f32x2*)(stats + ((size_t)(hf * 8 + (lane >> 3)) * SEQ + row) * 2) = (f32x2){s, m2};
  }
}

__global__ __launch_bounds__(1024) void attn_o_k(const u16* __restrict__ q, const u16* __restrict__ wphi, const float* __restrict__ kvpart,
                                                u16* __restrict__ o) {
  constexpr int KS = 80;
  __shared__ __attribute__((aligned(16))) char sm[33 * KS * 2 + 8];
  const int tid = threadIdx.x & 511, lane = tid & 63, w = tid >> 6;
  const bool ldw = __builtin_amdgcn_readfirstlane(threadIdx.x >> 9) != 0;
  constexpr int EPI = 6;
  const int h = blockIdx.x & 31, rb = blockIdx.x >> 5;
  const int g4 = lane >> 4, r0 = rb * 256 + w * 32 + (lane & 15);
  u16* kvt = (u16*)sm;
  if (ldw) {
    f32x4 kp[16], kz[4];
    const float* slab = kvpart + (size_t)h * (33 * 64) + tid * 4;
    const bool zw = __builtin_amdgcn_readfirstlane(w) == 0;
    if (zw) {
#pragma unroll
      for (int p = 0; p < 4; ++p) kz[p] = *(const f32x4*)(kvpart + ((size_t)(p * 4 + g4) * NH + h) * (33 * 64) + 2048 + (lane & 15) * 4);
    }
#pragma unroll
    for (int p = 0; p < 16; ++p) kp[p] = *(const f32x4*)(slab + (size_t)p * NH * (33 * 64));
    __builtin_amdgcn_sched_barrier(0);
    f32x4 t = kp[0];
#pragma unroll
    for (int p = 1; p < 16; ++p) t += kp[p];
    *(u32x2*)(kvt + (tid >> 4) * KS + (tid & 15) * 4) = (u32x2){pk2(t[0], t[1]), pk2(t[2], t[3])};
    if (zw) {
      f32x4 z = (kz[0] + kz[1]) + (kz[2] + kz[3]);
#pragma unroll
      for (int e = 0; e < 4; ++e) z[e] = sum4g(z[e]);
      if (tid < 16) *(u32x2*)(kvt + 32 * KS + tid * 4) = (u32x2){pk2(z[0], z[1]), pk2(z[2], z[3])};
    }
    __syncthreads();
    return;
  }
  TS(0);
  bf16x8 qf[2], wf[2];
#pragma unroll
  for (int mb = 0; mb < 2; ++mb) qf[mb] = *(const bf16x8*)(q + (size_t)(r0 + mb * 16) * DM + h * 32 + g4 * 8);
#pragma unroll
  for (int fb = 0; fb < 2; ++fb) wf[fb] = *(const bf16x8*)(wphi + ((h * 2 + fb) * 64 + lane) * 8);
  bf16x8 pq[2][2];
#pragma unroll
  for (int mb = 0; mb < 2; ++mb) {
    u32x4 pos, neg;
    feature_map(wf, qf[mb], pos, neg);
    pq[mb][0] = __builtin_bit_cast(bf16x8, pos);
    pq[mb][1] = __builtin_bit_cast(bf16x8, neg);
  }
  __builtin_amdgcn_sched_barrier(0);
  TS(1);
  TS(2);
  __syncthreads();
  TS(3);
  bf16x8 kf[3][2];
#pragma unroll
  for (int db = 0; db < 3; ++db)
#pragma unroll
    for (int ks = 0; ks < 2; ++ks) {
      const int d = (db < 2) ? ((lane >> 2) & 3) * 8 + db * 4 + (lane & 3) : 32;
      const u16* p = kvt + d * KS + ks * 32 + g4 * 4;
      kf[db][ks] = cat8(*(const s16x4*)p, *(const s16x4*)(p + 16));
    }
#pragma unroll
  for (int mb = 0; mb < 2; ++mb) {
    f32x4 a[3];
#pragma unroll
    for (int db = 0; db < 3; ++db) {
      a[db] = mfma16(kf[db][0], pq[mb][0], (f32x4){0.f, 0.f, 0.f, 0.f});
      a[db] = mfma16(kf[db][1], pq[mb][1], a[db]);
    }
    const float id = __builtin_amdgcn_rcpf(rbf(rbf(a[2][0]) + 1e-6f));
    u32x4 ov;
#pragma unroll
    for (int db = 0; db < 2; ++db) {
      const f32x2 lo = rbf2((f32x2){a[db][0], a[db][1]}) * id, hi = rbf2((f32x2){a[db][2], a[db][3]}) * id;
      ov[db * 2] = pk2(lo[0], lo[1]); ov[db * 2 + 1] = pk2(hi[0], hi[1]);
    }
    st16_wt(o, SEQ * DM * 2, ((r0 + mb * 16) * DM + h * 32 + g4 * 8) * 2, ov);
  }
  TS(4);
  TS(5);
}

template <int BN, int S, int KD, int EPI> static constexpr int smem_bytes() {
  constexpr bool USE_STATS = (EPI == EPI_QKV || EPI == EPI_GELU_SHIFT || EPI == EPI_W2 || EPI == EPI_GELU);
  constexpr int NRES = (EPI == EPI_RES || EPI == EPI_W2) ? 1 : (EPI == EPI_W4 ? 2 : 0);
  return S * (16384 + BN * 128) + NRES * 16384 + (EPI == EPI_QKV ? 4096 : 0) + 2048 + 1024 + 256;
}
template <int BN, int S, int KD, int EPI> static void launch_gemm(const GemmArgs& a, hipStream_t st) {
  constexpr int sb = smem_bytes<BN, S, KD, EPI>();
  static_assert(sb <= 163840, "LDS");
  (void)hipFuncSetAttribute((const void*)gemm_k<BN, S, KD, EPI>, hipFuncAttributeMaxDynamicSharedMemorySize, sb);
  gemm_k<BN, S, KD, EPI><<<256, 512, sb, st>>>(a);
}

extern "C" void kernel_launch(void* const* d_in, const int* in_sizes, int n_in, void* d_out, int out_size, void* d_ws,
                              size_t ws_size, hipStream_t stream) {
  const u16* x0 = (const u16*)d_in[0];
  const u16* wqkv = (const u16*)d_in[1];
  const u16* wphi = (const u16*)d_in[2];
  const u16* wo = (const u16*)d_in[3];
  const u16* w1 = (const u16*)d_in[5];
  const u16* w2 = (const u16*)d_in[6];
  const u16* w3 = (const u16*)d_in[7];
  const u16* w4 = (const u16*)d_in[8];
  char* ws = (char*)d_ws;
  const size_t MB = 1 << 20;
  u16* phiq = (u16*)(ws + 4 * MB);
  float* kvpart = (float*)(ws + 12 * MB);
  u16* obuf = (u16*)(ws + 17 * MB);
  u16* xa = (u16*)(ws + 21 * MB);
  u16* hsh = (u16*)(ws + 25 * MB);
  u16* x1 = (u16*)(ws + 29 * MB);
  u16* gbuf = (u16*)(ws + 33 * MB);
  u16* xb = (u16*)(ws + 48 * MB);
  float* st_xa = (float*)(ws + 52 * MB);
  float* st_x1 = (float*)(ws + 53 * MB);
  float* st_xb = (float*)(ws + 54 * MB);
  float* colsum = (float*)(ws + 55 * MB);

  u16* wphit = (u16*)(ws + 55 * MB + 65536);
  ln0_k<<<252, 1024, 0, stream>>>(x0, st_xb, wqkv, w1, w3, colsum, wphi, wphit);
  for (int layer = 0; layer < 2; ++layer) {
    const u16* xin = layer == 0 ? x0 : xb;
    u16* xout = layer == 0 ? xb : (u16*)d_out;
    GemmArgs a;
    a = GemmArgs{xin, wqkv, phiq, layer == 0 ? wo : nullptr, w2, st_xb, (float*)w4, colsum, wphi, kvpart, 1, 0};
    launch_gemm<192, 3, 1024, EPI_QKV>(a, stream);
    attn_o_k<<<256, 1024, 0, stream>>>(phiq, wphit, kvpart, obuf);
    a = GemmArgs{obuf, wo, xa, xin, nullptr, nullptr, st_xa, nullptr, nullptr, nullptr, 0, 0};
    launch_gemm<64, 5, 1024, EPI_RES>(a, stream);
    a = GemmArgs{xa, w1, hsh, nullptr, nullptr, st_xa, nullptr, colsum + 3072, nullptr, nullptr, 2, 0};
    launch_gemm<64, 5, 1024, EPI_GELU_SHIFT>(a, stream);
    a = GemmArgs{hsh, w2, x1, xa, nullptr, st_xa, st_x1, nullptr, nullptr, nullptr, 1, 0};
    launch_gemm<64, 5, 1024, EPI_W2>(a, stream);
    a = GemmArgs{x1, w3, gbuf, nullptr, nullptr, st_x1, nullptr, colsum + 4096, nullptr, nullptr, 1, 0};
    launch_gemm<224, 3, 1024, EPI_GELU>(a, stream);
    a = GemmArgs{gbuf, w4, xout, xa, x1, nullptr, st_xb, nullptr, nullptr, nullptr, 0, 0};
    launch_gemm<64, 5, 3584, EPI_W4>(a, stream);
  }
}
```

```cpp
#include <hip/hip_runtime.h>
#include <hip/hip_bf16.h>
#include <stdint.h>

typedef __attribute__((ext_vector_type(8))) short bf16x8;
typedef __attribute__((ext_vector_type(4))) short s16x4;
typedef __attribute__((ext_vector_type(4))) float f32x4;
typedef __attribute__((ext_vector_type(2))) float f32x2;
typedef __attribute__((ext_vector_type(4))) unsigned u32x4;
typedef __attribute__((ext_vector_type(2))) unsigned u32x2;
typedef __attribute__((ext_vector_type(2))) __bf16 bf16x2_t;
typedef unsigned short u16;
#define LDS_AS __attribute__((address_space(3)))
#define DI __device__ __forceinline__

static constexpr int SEQ = 2048, DM = 1024, NH = 32, FF = 3584;

DI float bf2f(unsigned v) { return __uint_as_float(v << 16); }
DI float bflo(unsigned v) { return __uint_as_float(v << 16); }
DI float bfhi(unsigned v) { return __uint_as_float(v & 0xffff0000u); }
DI unsigned pk2(float a, float b) {
  f32x2 v = {a, b};
  return __builtin_bit_cast(unsigned, __builtin_convertvector(v, bf16x2_t));
}
DI float rbf(float a) { return bflo(pk2(a, a)); }
template <int CTRL> DI float dppf(float v) { return __builtin_bit_cast(float, __builtin_amdgcn_update_dpp(0, __builtin_bit_cast(int, v), CTRL, 0xf, 0xf, true)); }
DI float sum8(float v) { v += dppf<0xB1>(v); v += dppf<0x4E>(v); return v + dppf<0x141>(v); }
DI f32x2 pair16(float v) { const unsigned u = __float_as_uint(v); const auto r = __builtin_amdgcn_permlane16_swap(u, u, false, false); return (f32x2){__uint_as_float(r[0]), __uint_as_float(r[1])}; }
DI f32x2 pair32(float v) { const unsigned u = __float_as_uint(v); const auto r = __builtin_amdgcn_permlane32_swap(u, u, false, false); return (f32x2){__uint_as_float(r[0]), __uint_as_float(r[1])}; }
DI float max4g(float v) { f32x2 t = pair16(v); t = pair32(fmaxf(t[0], t[1])); return fmaxf(t[0], t[1]); }
DI float min4g(float v) { f32x2 t = pair16(v); t = pair32(fminf(t[0], t[1])); return fminf(t[0], t[1]); }
DI float sum4g(float v) { f32x2 t = pair16(v); t = pair32(t[0] + t[1]); return t[0] + t[1]; }

template <int I> struct IC { static constexpr int value = I; };
template <int B, int E, class F> DI void static_for(F&& f) {
  if constexpr (B < E) { f(IC<B>{}); static_for<B + 1, E>(f); }
}
template <int N> DI void wait_vm() { asm volatile("s_waitcnt vmcnt(%0)" ::"n"(N) : "memory"); }
DI void wait_lgkm0() { asm volatile("s_waitcnt lgkmcnt(0)" ::: "memory"); }
DI void bar() { __builtin_amdgcn_s_barrier(); }
DI void glds16(const void* src, char* lds_dst) {
  __builtin_amdgcn_global_load_lds((const unsigned*)src, (unsigned*)lds_dst, 16, 0, 0);
}
DI s16x4 trread(const char* p) {
  return __builtin_bit_cast(s16x4, __builtin_amdgcn_ds_read_tr16_b64_v4i16((LDS_AS s16x4*)p));
}
template <int OFF> DI s16x4 lds_tr(unsigned addr) {
  s16x4 r; asm volatile("ds_read_b64_tr_b16 %0, %1 offset:%2" : "=v"(r) : "v"(addr), "n"(OFF) : "memory"); return r;
}
template <int OFF> DI bf16x8 lds_b128(unsigned addr) {
  bf16x8 r; asm volatile("ds_read_b128 %0, %1 offset:%2" : "=v"(r) : "v"(addr), "n"(OFF) : "memory"); return r;
}
template <int OFF> DI f32x2 lds_f2(unsigned addr) {
  f32x2 r; asm volatile("ds_read_b64 %0, %1 offset:%2" : "=v"(r) : "v"(addr), "n"(OFF) : "memory"); return r;
}
template <int OFF> DI f32x4 lds_f4(unsigned addr) {
  f32x4 r; asm volatile("ds_read_b128 %0, %1 offset:%2" : "=v"(r) : "v"(addr), "n"(OFF) : "memory"); return r;
}
DI void st16_wt(void* base, unsigned bytes, unsigned off, u32x4 v) {
  __builtin_amdgcn_raw_buffer_store_b128(v, __builtin_amdgcn_make_buffer_rsrc(base, (short)0, (int)bytes, 0x00020000), (int)off, 0, 16);
}
DI bf16x8 cat8(s16x4 lo, s16x4 hi) { return (bf16x8){lo[0], lo[1], lo[2], lo[3], hi[0], hi[1], hi[2], hi[3]}; }
DI f32x4 mfma16(bf16x8 a, bf16x8 b, f32x4 c) { return __builtin_amdgcn_mfma_f32_16x16x32_bf16(a, b, c, 0, 0, 0); }

DI f32x2 gelu2(f32x2 x) {
  const float c0 = -2.302208198f, c1 = -2.302208198f * 0.044715f;
  const f32x2 a = x * (x * x * c1 + c0);
  const f32x2 d = (f32x2){__builtin_amdgcn_exp2f(a[0]), __builtin_amdgcn_exp2f(a[1])} + 1.0f;
  return x * (f32x2){__builtin_amdgcn_rcpf(d[0]), __builtin_amdgcn_rcpf(d[1])};
}
DI f32x2 rbf2(f32x2 v) { const unsigned p = pk2(v[0], v[1]); return (f32x2){bflo(p), bfhi(p)}; }
DI f32x2 unpk(unsigned p) { return (f32x2){bflo(p), bfhi(p)}; }

DI void feature_map(const bf16x8 (&wf)[2], bf16x8 xf, u32x4& pos, u32x4& neg) {
  f32x2 p[4];
#pragma unroll
  for (int fb = 0; fb < 2; ++fb) {
    const f32x4 d = mfma16(wf[fb], xf, (f32x4){0.f, 0.f, 0.f, 0.f});
    p[fb * 2] = rbf2((f32x2){d[0], d[1]});
    p[fb * 2 + 1] = rbf2((f32x2){d[2], d[3]});
  }
  float mx = fmaxf(p[0][0], p[0][1]), mn = fminf(p[0][0], p[0][1]);
#pragma unroll
  for (int e = 1; e < 4; ++e) { mx = fmaxf(mx, fmaxf(p[e][0], p[e][1])); mn = fminf(mn, fminf(p[e][0], p[e][1])); }
  mx = max4g(mx);
  mn = min4g(mn);
  const float l2e = 1.4426950408889634f;
  f32x2 ep[4], en[4], sp2 = {0.f, 0.f}, sn2 = {0.f, 0.f};
#pragma unroll
  for (int e = 0; e < 4; ++e) {
    const f32x2 a = (p[e] - mx) * l2e, b = (mn - p[e]) * l2e;
    ep[e] = (f32x2){__builtin_amdgcn_exp2f(a[0]), __builtin_amdgcn_exp2f(a[1])}; sp2 += ep[e];
    en[e] = (f32x2){__builtin_amdgcn_exp2f(b[0]), __builtin_amdgcn_exp2f(b[1])}; sn2 += en[e];
  }
  float sp = sp2[0] + sp2[1], sn = sn2[0] + sn2[1];
  sp = sum4g(sp);
  sn = sum4g(sn);
  const float ip = __builtin_amdgcn_rcpf(sp), in_ = __builtin_amdgcn_rcpf(sn);
#pragma unroll
  for (int e = 0; e < 4; ++e) {
    const f32x2 a = ep[e] * ip, b = en[e] * in_;
    pos[e] = pk2(a[0], a[1]); neg[e] = pk2(b[0], b[1]);
  }
}

struct GemmArgs {
  const u16* A;
  const u16* B;
  u16* out;
  const u16* res1;
  const u16* res2;
  const float* stats_in;
  float* stats_out;
  const float* colsum;
  const u16* wphi;
  float* kvpart;
  int ln_mode;
  int unused;
};

enum { EPI_QKV = 0, EPI_RES = 1, EPI_GELU_SHIFT = 2, EPI_W2 = 3, EPI_GELU = 4, EPI_W4 = 5 };

struct RowP { float alpha, beta, mhat, dhat; };
DI RowP row_params(float mean, float var, int mode) {
  RowP r;
  const float mh = rbf(mean);
  const float dh = rbf(__builtin_amdgcn_sqrtf(rbf(rbf(var) + 1e-5f)));
  const float i1 = __builtin_amdgcn_rcpf(dh);
  r.mhat = mh; r.dhat = dh;
  if (mode == 2) {
    const float m2 = (mean - mh) * i1;
    const float v2 = var * i1 * i1;
    const float mh2 = rbf(m2);
    const float dh2 = rbf(__builtin_amdgcn_sqrtf(rbf(rbf(v2) + 1e-5f)));
    const float i2 = __builtin_amdgcn_rcpf(dh2);
    r.alpha = i1 * i2;
    r.beta = (mh * i1 + mh2) * i2;
  } else {
    r.alpha = i1;
    r.beta = mh * i1;
  }
  return r;
}

template <int BN, int S, int KD, int EPI>
__global__ __launch_bounds__(512, 2) void gemm_k(GemmArgs g) {
  constexpr int NK = KD / 64;
  constexpr int NPF = BN / 64;
  constexpr bool HALF = (BN % 64) != 0;
  constexpr int NI = BN / 32;
  constexpr int A_BYTES = 16384;
  constexpr int STAGE = A_BYTES + BN * 128;
  constexpr int L = 4 + NPF * 2 + (HALF ? 1 : 0);
  constexpr int NTOT = (EPI == EPI_QKV) ? 3 * DM : (EPI == EPI_GELU ? FF : DM);
  constexpr bool USE_STATS = (EPI == EPI_QKV || EPI == EPI_GELU_SHIFT || EPI == EPI_W2 || EPI == EPI_GELU);
  constexpr bool FOLD = (EPI == EPI_QKV || EPI == EPI_GELU_SHIFT || EPI == EPI_GELU);
  constexpr int NRES = (EPI == EPI_RES || EPI == EPI_W2) ? 1 : (EPI == EPI_W4 ? 2 : 0);
  constexpr int OFF_STATS = S * STAGE;
  constexpr int OFF_RES = OFF_STATS;
  constexpr int OFF_WPHI = OFF_RES + NRES * 16384;
  constexpr int OFF_ROWP = OFF_WPHI + (EPI == EPI_QKV ? 4096 : 0);
  constexpr int OFF_CS = OFF_ROWP + 2048;
  constexpr int OFF_DUMP = OFF_CS + 1024;
  constexpr int NEXTRA = (USE_STATS && BN != 64 ? 8 : 0) + NRES * 4 + (EPI == EPI_QKV ? 1 : 0) + (FOLD ? 1 : 0);

  constexpr int PR0 = NK > 16 ? 5 : (S == 3 ? 7 : NK - 1), PRN = PR0 < NK - 1 ? (NK - S - PR0) / S : 0;
  constexpr int CR0 = 0, CRN = (NK - 1) / S;
  static_assert(!(USE_STATS && BN == 64) || S == 5, "row statistics sit in the second group of five stages");
  extern __shared__ __attribute__((aligned(16))) char smem[];
  const int tid = threadIdx.x, lane = tid & 63;
  const int w = __builtin_amdgcn_readfirstlane(tid >> 6);
  const bool prod = w >= 4;
  const int cw = w & 3, wr = cw >> 1, wc = cw & 1;

  const int b = blockIdx.x, xcd = b & 7, j = b >> 3;
  int rp, ct;
  if constexpr (EPI == EPI_QKV || EPI == EPI_GELU) { rp = (xcd >> 2) * 8 + (j >> 2); ct = (xcd & 3) * 4 + (j & 3); }
  else { rp = (xcd >> 1) * 4 + (j >> 3); ct = (xcd & 1) * 8 + (j & 7); }
  const int m0 = rp * 128;
  const int n0 = (EPI == EPI_QKV) ? ct * 64 : ct * BN;
  constexpr int PSTRIDE = (EPI == EPI_QKV) ? DM : 64;
  const int nf = HALF ? n0 + (ct & 1) * 32 : n0;
  const int nh = (ct & 1) ? n0 : n0 + NPF * 64;
  const int g4 = lane >> 4, q4 = (lane >> 2) & 3, pp = lane & 3;
  RowP* rowp = (RowP*)(smem + OFF_ROWP);
  const bool ln_on = USE_STATS && (g.ln_mode != 0 || EPI == EPI_W2);

  f32x4 acc[4][NI];
  f32x2 ab[4];
  f32x4 csv[NI];
  constexpr bool RP_CONS = (BN == 64);
  f32x2 sv[8];
  const int sr = cw * 32 + (lane >> 1), shf = lane & 1;
  auto load_stats = [&]() {
#pragma unroll
    for (int p = 0; p < 8; ++p)
      asm volatile("global_load_dwordx2 %0, %1, off" : "=v"(sv[p]) : "v"(g.stats_in + ((size_t)(shf * 8 + p) * SEQ + m0 + sr) * 2) : "memory");
  };
  float rs_s = 0.f, rs_m2 = 0.f;
  auto row_stats = [&](auto pc) {
    constexpr int piece = decltype(pc)::value;
    if constexpr (piece == 0) {
#pragma unroll
      for (int p = 0; p < 8; ++p) asm volatile("" : "+v"(sv[p]));
      float s = 0.f;
#pragma unroll
      for (int p = 0; p < 8; ++p) s += sv[p][0];
      rs_s = s + dppf<0xB1>(s);
    } else if constexpr (piece == 1) {
      const float mean = rs_s * (1.0f / 1024.0f);
      float m2 = 0.f;
#pragma unroll
      for (int p = 0; p < 8; ++p) { const float d = sv[p][0] * (1.0f / 64.0f) - mean; m2 += sv[p][1] + 64.0f * d * d; }
      rs_m2 = m2 + dppf<0xB1>(m2);
    } else {
      if (ln_on && shf == 0) rowp[sr] = row_params(rs_s * (1.0f / 1024.0f), rs_m2 * (1.0f / 1024.0f), EPI == EPI_W2 ? 1 : g.ln_mode);
      wait_lgkm0();
    }
    __builtin_amdgcn_sched_barrier(0);
  };
  if (__builtin_expect(prod, 1)) {
    const u16* a_src = g.A + (size_t)(m0 + cw * 32 + (lane >> 3)) * KD + (((lane & 7) ^ (lane >> 3)) * 8);
    const u16* b_src[2];
#pragma unroll
    for (int e = 0; e < 2; ++e) {
      const int r = lane >> 3;
      const int f = ((r >> 1) & 1) | (e << 1);
      const int c = ((((lane & 7) >> 1) ^ f) << 1) | (lane & 1);
      b_src[e] = g.B + (size_t)((cw * 2 + e) * 8 + r) * NTOT + nf + c * 8;
    }
    const u16* bh_src = nullptr;
    if constexpr (HALF) {
      const int c = ((((lane >> 1) & 1) ^ ((lane >> 5) & 1)) << 1) | (lane & 1);
      bh_src = g.B + (size_t)(cw * 16 + (lane >> 2)) * NTOT + nh + c * 8;
    }
    auto issue = [&](int kt, int slot) {
      char* base = smem + slot * STAGE;
#pragma unroll
      for (int p = 0; p < NPF; ++p)
#pragma unroll
        for (int e = 0; e < 2; ++e)
          glds16(b_src[e] + (size_t)kt * 64 * NTOT + p * PSTRIDE, base + A_BYTES + p * 8192 + (cw * 2 + e) * 1024);
      if constexpr (HALF) glds16(bh_src + (size_t)kt * 64 * NTOT, base + A_BYTES + NPF * 8192 + cw * 1024);
#pragma unroll
      for (int e = 0; e < 4; ++e) glds16(a_src + (size_t)e * 8 * KD + kt * 64, base + (cw * 4 + e) * 1024);
    };
    issue(0, 0);
    issue(1, 1);
    wait_vm<L>();
    bar();
    static_for<2, S>([&](auto ic) { issue(ic.value, ic.value); });
    if constexpr (NRES >= 1) {
#pragma unroll
      for (int e = 0; e < 4; ++e) {
        const int r = (cw * 4 + e) * 8 + (lane >> 3);
        glds16(g.res1 + (size_t)(m0 + r) * DM + n0 + (lane & 7) * 8, smem + OFF_RES + (cw * 4 + e) * 1024);
      }
    }
    if constexpr (NRES >= 2) {
#pragma unroll
      for (int e = 0; e < 4; ++e) {
        const int r = (cw * 4 + e) * 8 + (lane >> 3);
        glds16(g.res2 + (size_t)(m0 + r) * DM + n0 + (lane & 7) * 8, smem + OFF_RES + 16384 + (cw * 4 + e) * 1024);
      }
    }
    if constexpr (EPI == EPI_QKV) glds16(g.wphi + (size_t)ct * 2048 + cw * 512 + lane * 8, smem + OFF_WPHI + cw * 1024);
    if constexpr (FOLD) {
      int col = cw * 64 + (lane & 15) * 4;
      if (col > BN - 4) col = BN - 4;
      const int gcol = (EPI == EPI_QKV) ? (col >> 6) * DM + n0 + (col & 63) : (HALF && col >= NPF * 64) ? nh + col - NPF * 64 : nf + col;
      if (lane < 16) glds16(g.colsum + gcol, smem + OFF_CS + cw * 256);
    }
    if constexpr (USE_STATS && !RP_CONS) load_stats();
    auto pstep = [&](auto ic) {
      constexpr int kt = ic.value;
      constexpr int young = (S - 2 < NK - 2 - kt) ? S - 2 : NK - 2 - kt;
      constexpr int ex = (kt + 1 <= S - 1 && kt + 1 < NK - 1) ? NEXTRA : 0;
      wait_vm<young * L + ex>();
      bar();
      if constexpr (kt + S < NK) issue(kt + S, kt % S);
    };
    static_for<0, S - 1>(pstep);
    {
      int slot = S - 1;
#pragma unroll 1
      for (int kt = S - 1; kt < NK - S; ++kt) {
        wait_vm<(S - 2) * L>();
        bar();
        issue(kt + S, slot);
        slot = slot == S - 1 ? 0 : slot + 1;
        if constexpr (USE_STATS && !RP_CONS) {
          if (kt == 4) row_stats(IC<0>{});
          else if (kt == 5) row_stats(IC<1>{});
          else if (kt == 6) row_stats(IC<2>{});
        }
      }
    }
    static_for<NK - S, NK - 1>(pstep);
    bar();
  } else {
    const int fsw = ((q4 >> 1) & 1) | ((g4 & 1) << 1);
    int a_off[2], b_off[2], bh_off = 0;
#pragma unroll
    for (int k = 0; k < 2; ++k) {
      a_off[k] = (wr * 64 + (lane & 15)) * 128 + (((k * 4 + g4) ^ (lane & 7)) * 16);
      b_off[k] = A_BYTES + (8 * g4 + q4) * 128 + (((2 * k + wc) ^ fsw) * 32) + pp * 8;
    }
    if constexpr (HALF) bh_off = A_BYTES + NPF * 8192 + (8 * g4 + q4) * 64 + ((wc ^ (g4 & 1)) * 32) + pp * 8;
#pragma unroll
    for (int m = 0; m < 4; ++m)
#pragma unroll
      for (int i = 0; i < NI; ++i) acc[m][i] = (f32x4){0.f, 0.f, 0.f, 0.f};
    bf16x8 xa[2][4];
    s16x4 wlo[2][NI], whi[2][NI];
    const unsigned lbase = (unsigned)(size_t)smem;

    auto ldfrag = [&](auto setc, auto slotc, auto kc) {
      constexpr int set = decltype(setc)::value, slot = decltype(slotc)::value, k32 = decltype(kc)::value;
      const unsigned sb = lbase + slot * STAGE;
      const unsigned ba0 = sb + b_off[0], ba1 = sb + b_off[1], bah = sb + bh_off, aa = sb + a_off[k32];
      static_for<0, NI>([&](auto ic) {
        constexpr int i = decltype(ic)::value;
        if constexpr (HALF && i == NI - 1) {
          wlo[set][i] = lds_tr<k32 * 2048>(bah);
          whi[set][i] = lds_tr<k32 * 2048 + 256>(bah);
        } else {
          wlo[set][i] = lds_tr<(i >> 1) * 8192 + k32 * 4096>((i & 1) ? ba1 : ba0);
          whi[set][i] = lds_tr<(i >> 1) * 8192 + k32 * 4096 + 512>((i & 1) ? ba1 : ba0);
        }
      });
      static_for<0, 4>([&](auto mc) { constexpr int m = decltype(mc)::value; xa[set][m] = lds_b128<m * 2048>(aa); });
    };
    auto wait_set = [&](auto setc) {
      constexpr int set = decltype(setc)::value;
      asm volatile("s_waitcnt lgkmcnt(0)" : "+v"(xa[set][0]), "+v"(xa[set][1]), "+v"(xa[set][2]), "+v"(xa[set][3]) :: "memory");
#pragma unroll
      for (int i = 0; i < NI; ++i) asm volatile("" : "+v"(wlo[set][i]), "+v"(whi[set][i]));
      __builtin_amdgcn_sched_barrier(0);
    };
    auto ld1 = [&](auto setc, auto slotc, auto kc, auto rc) {
      constexpr int set = decltype(setc)::value, slot = decltype(slotc)::value, k32 = decltype(kc)::value, r = decltype(rc)::value;
      const unsigned sb = lbase + slot * STAGE;
      if constexpr (r < 2 * NI) {
        constexpr int i = r >> 1, hi = r & 1;
        s16x4 v;
        if constexpr (HALF && i == NI - 1) v = lds_tr<k32 * 2048 + hi * 256>(sb + bh_off);
        else v = lds_tr<(i >> 1) * 8192 + k32 * 4096 + hi * 512>(sb + b_off[i & 1]);
        if constexpr (hi) whi[set][i] = v; else wlo[set][i] = v;
      } else {
        constexpr int m = r - 2 * NI;
        xa[set][m] = lds_b128<m * 2048>(sb + a_off[k32]);
      }
    };
    auto mma_ld = [&](auto setc, auto ldc, auto slotc, auto kc) {
      constexpr int set = decltype(setc)::value;
      constexpr bool ld = decltype(ldc)::value != 0;
      static_for<0, 4 * NI>([&](auto tc) {
        constexpr int t = decltype(tc)::value, i = t >> 2, m = t & 3;
        acc[m][i] = mfma16(cat8(wlo[set][i], whi[set][i]), xa[set][m], acc[m][i]);
        if constexpr (ld && t < 2 * NI + 4) ld1(IC<set ^ 1>{}, slotc, kc, tc);
        __builtin_amdgcn_sched_barrier(0);
      });
    };

    const char* tp = (const char*)g.A;
    unsigned tw = 0;
    bool ton = false;
    int tks = 0;
    if (cw == 0) {
      const int pl = lane >> 4;
      const int bl = (int)(((size_t)g.B >> 7) & 7);
      int off;
      if constexpr (BN == 64) { off = ((xcd & 1) * 8 + ((3 - bl) & 7)) * 128; ton = lane < 16; }
      else if constexpr (EPI == EPI_QKV) { const int c = (3 - bl - (xcd & 3) * 4) & 7; off = pl * 2048 + ((xcd & 3) * 4 + (c & 3)) * 128; ton = c < 4 && lane < 48; }
      else { const int l0 = (xcd & 3) * 14, c = (3 - bl - l0) & 7; off = (l0 + c + pl * 8) * 128; ton = lane < (c < 6 ? 32 : 16); }
      tks = (lane >> 1) & 7;
      if (ton) tp = (const char*)g.B + (size_t)(tks * 64 + j * 2 + (lane & 1)) * NTOT * 2 + off;
      tw = 8 * 64 * NTOT * 2;
    } else if (cw == 1) {
      tks = (3 - (int)(((size_t)g.A >> 7) & 7)) & 7;
      const int row = (BN == 64) ? (xcd >> 1) * 512 + j * 16 + (lane & 15) : (xcd >> 2) * 1024 + j * 32 + (lane & 31);
      ton = lane < (BN == 64 ? 16 : 32);
      tp = (const char*)g.A + (size_t)row * KD * 2 + tks * 128;
      tw = 1024;
    }
    auto touch = [&](auto wc_) {
      constexpr int wi = decltype(wc_)::value;
      if (cw < 2) {
        if (ton && (wi > 0 || tks >= S))
          __builtin_amdgcn_global_load_lds((const unsigned*)(tp + (size_t)(wi * tw)), (unsigned*)(smem + OFF_DUMP), 4, 0, 0);
      }
      __builtin_amdgcn_sched_barrier(0);
    };
    if constexpr (USE_STATS && RP_CONS) load_stats();
    touch(IC<0>{});
    if constexpr (BN == 64) touch(IC<1>{});
#pragma unroll
    for (int m = 0; m < 4; ++m)
#pragma unroll
      for (int i = 0; i < (NI > 6 ? 6 : NI); ++i) asm volatile("" : "+v"(acc[m][i]));
    bar();
    ldfrag(IC<0>{}, IC<0>{}, IC<0>{});
    if constexpr (BN != 64) touch(IC<1>{});
    auto cstep = [&](auto ic) {
      constexpr int kt = ic.value;
      constexpr int slot = kt % S;
      wait_set(IC<0>{});
      if constexpr (BN != 64 && kt == 0) touch(IC<1>{});
      if constexpr (kt % 8 == 4 && (kt + 12) / 8 < NK / 8) touch(IC<(kt + 12) / 8>{});
      if constexpr (USE_STATS && RP_CONS && kt >= 6 && kt < 9) { if constexpr (kt == 6) wait_vm<0>(); row_stats(IC<kt - 6>{}); }
      mma_ld(IC<0>{}, IC<1>{}, IC<slot>{}, IC<1>{});
      wait_set(IC<1>{});
      if constexpr (kt + 1 < NK) {
        bar();
        __builtin_amdgcn_sched_barrier(0);
        mma_ld(IC<1>{}, IC<1>{}, IC<(kt + 1) % S>{}, IC<0>{});
      } else {
        const unsigned ra = lbase + OFF_ROWP + (wr * 64 + (lane & 15)) * 16, ca = lbase + OFF_CS + (wc * 16 + g4 * 4) * 4;
        static_for<0, 4 * NI>([&](auto tc) {
          constexpr int t = decltype(tc)::value, i = t >> 2, m = t & 3;
          acc[m][i] = mfma16(cat8(wlo[1][i], whi[1][i]), xa[1][m], acc[m][i]);
          if constexpr (FOLD && t < 4) ab[t] = lds_f2<t * 256>(ra);
          if constexpr (FOLD && m == 3) csv[i] = lds_f4<i * 128>(ca);
          __builtin_amdgcn_sched_barrier(0);
        });
      }
    };
    static_for<0, CR0>(cstep);
    if constexpr (CRN > 0) {
#pragma unroll 1
      for (int k0 = CR0; k0 < CR0 + CRN * S; k0 += S)
        static_for<0, S>([&](auto uc) {
          constexpr int slot = (CR0 + uc.value) % S;
          const int kt = k0 + uc.value;
          wait_set(IC<0>{});
          if constexpr (NK > 16) {
            if ((kt & 7) == 4 && kt + 12 < NK && cw < 2) {
              if (ton) __builtin_amdgcn_global_load_lds((const unsigned*)(tp + (size_t)((unsigned)((kt + 12) >> 3) * tw)), (unsigned*)(smem + OFF_DUMP), 4, 0, 0);
            }
          }
          if constexpr (USE_STATS && RP_CONS && uc.value >= 1 && uc.value < 4) {
            if (k0 == 5) { if constexpr (uc.value == 1) wait_vm<0>(); row_stats(IC<uc.value - 1>{}); }
          }
          __builtin_amdgcn_sched_barrier(0);
          mma_ld(IC<0>{}, IC<1>{}, IC<slot>{}, IC<1>{});
          wait_set(IC<1>{});
          bar();
          __builtin_amdgcn_sched_barrier(0);
          mma_ld(IC<1>{}, IC<1>{}, IC<(slot + 1) % S>{}, IC<0>{});
        });
    }
    static_for<CR0 + CRN * S, NK>(cstep);
    bar();
    if constexpr (FOLD) {
      asm volatile("s_waitcnt lgkmcnt(0)" : "+v"(ab[0]), "+v"(ab[1]), "+v"(ab[2]), "+v"(ab[3]) :: "memory");
#pragma unroll
      for (int i = 0; i < NI; ++i) asm volatile("" : "+v"(csv[i]));
      if (!ln_on) {
#pragma unroll
        for (int m = 0; m < 4; ++m) ab[m] = (f32x2){1.f, 0.f};
      }
    }
  }
  if constexpr (EPI == EPI_QKV) __builtin_amdgcn_s_waitcnt(0x0F70);

  constexpr int TSTR = BN * 2 + (EPI == EPI_QKV ? 32 : 16);
  char* T = smem;
  u32x4 pv[EPI == EPI_QKV ? 11 : 1];
  if (!prod) {
#pragma unroll
    for (int i = 0; i < NI; ++i) {
      const int nb = 2 * i + wc;
      const int ncol = nb * 16 + g4 * 4;
      const f32x4 cs = csv[i];
#pragma unroll
      for (int m = 0; m < 4; ++m) {
        u32x2 pkd;
#pragma unroll
        for (int h = 0; h < 2; ++h) {
          f32x2 v = {acc[m][i][2 * h], acc[m][i][2 * h + 1]};
          if constexpr (FOLD) v = v * ab[m][0] - (f32x2){cs[2 * h], cs[2 * h + 1]} * ab[m][1];
          pkd[h] = pk2(v[0], v[1]);
        }
        *(u32x2*)(T + (wr * 64 + m * 16 + (lane & 15)) * TSTR + ncol * 2) = pkd;
      }
    }
  } else if constexpr (EPI == EPI_QKV) {
    if (g.res1) {
      const int t = tid - 256;
      const u16* W4 = (const u16*)g.stats_out + (size_t)b * 14336 + t * 8;
      const u16* Wo = g.res1 + (size_t)b * 4096 + t * 8;
      const u16* W2 = g.res2 + (size_t)b * 4096 + t * 8;
#pragma unroll
      for (int i = 0; i < 2; ++i) { pv[7 + i] = *(const u32x4*)(Wo + i * 2048); pv[9 + i] = *(const u32x4*)(W2 + i * 2048); }
#pragma unroll
      for (int i = 0; i < 7; ++i) pv[i] = *(const u32x4*)(W4 + i * 2048);
    }
  }
  if constexpr (EPI == EPI_QKV) { wait_lgkm0(); bar(); asm volatile("" ::: "memory"); }
  else __syncthreads();
  auto gelu8 = [&](u32x4 v) {
    u32x4 o;
#pragma unroll
    for (int e = 0; e < 4; ++e) { const f32x2 y = gelu2(unpk(v[e])); o[e] = pk2(y[0], y[1]); }
    return o;
  };
  if constexpr (EPI == EPI_GELU) {
    u32x4 v[NI];
#pragma unroll
    for (int it = 0; it < NI; ++it) {
      const int r = it < NI - 1 ? (it & 1) * 64 + (tid >> 3) : tid >> 2, c = it < NI - 1 ? (it >> 1) * 8 + (tid & 7) : (NI / 2) * 8 + (tid & 3);
      v[it] = *(const u32x4*)(T + r * TSTR + c * 16);
    }
#pragma unroll
    for (int it = 0; it < NI; ++it) {
      const int r = it < NI - 1 ? (it & 1) * 64 + (tid >> 3) : tid >> 2, col = it < NI - 1 ? nf + (it >> 1) * 64 + (tid & 7) * 8 : nh + (tid & 3) * 8;
      st16_wt(g.out, SEQ * FF * 2, ((m0 + r) * FF + col) * 2, gelu8(v[it]));
    }
  } else if constexpr (EPI == EPI_GELU_SHIFT) {
    const int grp = n0 >> 8;
    const int sh = (grp == 0) ? 1 : (grp == 1) ? -1 : (grp == 2) ? 2 : -2;
#pragma unroll
    for (int it = 0; it < 2; ++it) {
      const int r = it * 64 + (tid >> 3), c = tid & 7;
      const u32x4 v = gelu8(*(const u32x4*)(T + r * TSTR + c * 16));
      const int dr = m0 + r + sh;
      if (dr >= 0 && dr < SEQ) st16_wt(g.out, SEQ * DM * 2, (dr * DM + n0 + c * 8) * 2, v);
    }
    if (tid < 16) {
      const int zr = tid >> 3, c = tid & 7;
      const int az = sh > 0 ? sh : -sh;
      if (zr < az) {
        if (sh > 0 && rp == 0) *(u32x4*)(g.out + (size_t)zr * DM + n0 + c * 8) = (u32x4){0u, 0u, 0u, 0u};
        if (sh < 0 && rp == 15) *(u32x4*)(g.out + (size_t)(SEQ - 1 - zr) * DM + n0 + c * 8) = (u32x4){0u, 0u, 0u, 0u};
      }
    }
  } else if constexpr (EPI == EPI_RES || EPI == EPI_W2 || EPI == EPI_W4) {
#pragma unroll
    for (int it = 0; it < 2; ++it) {
      const int r = it * 64 + (tid >> 3), c = tid & 7;
      const u32x4 v = *(const u32x4*)(T + r * TSTR + c * 16);
      const u32x4 x = *(const u32x4*)(smem + OFF_RES + r * 128 + c * 16);
      f32x2 y[4];
      if constexpr (EPI == EPI_RES) {
#pragma unroll
        for (int e = 0; e < 4; ++e) y[e] = unpk(x[e]) + unpk(v[e]);
      } else if constexpr (EPI == EPI_W2) {
        const RowP rr = rowp[r];
#pragma unroll
        for (int e = 0; e < 4; ++e) y[e] = rbf2(rbf2(unpk(x[e]) - rr.mhat) * rr.alpha) + unpk(v[e]);
      } else {
        const u32x4 x1 = *(const u32x4*)(smem + OFF_RES + 16384 + r * 128 + c * 16);
#pragma unroll
        for (int e = 0; e < 4; ++e) y[e] = unpk(x[e]) + rbf2(unpk(x1[e]) + unpk(v[e]));
      }
      u32x4 o;
#pragma unroll
      for (int e = 0; e < 4; ++e) { o[e] = pk2(y[e][0], y[e][1]); y[e] = unpk(o[e]); }
      st16_wt(g.out, SEQ * DM * 2, ((m0 + r) * DM + n0 + c * 8) * 2, o);
      const f32x2 s2 = (y[0] + y[1]) + (y[2] + y[3]);
      const float s = sum8(s2[0] + s2[1]);
      const float mc = s * (1.0f / 64.0f);
      f32x2 q2 = {0.f, 0.f};
#pragma unroll
      for (int e = 0; e < 4; ++e) { const f32x2 d = y[e] - mc; q2 += d * d; }
      const float m2 = sum8(q2[0] + q2[1]);
      if (c == 0) *(f32x2*)(g.stats_out + ((size_t)ct * SEQ + m0 + r) * 2) = (f32x2){s, m2};
    }
  } else {
    constexpr int PKSTR = 160;
    char* PK = smem + 128 * TSTR;
#pragma unroll
    for (int it = 0; it < 2; ++it) {
      const int r = it * 64 + (tid >> 3), c = tid & 7;
      st16_wt(g.out, SEQ * DM * 2, ((m0 + r) * DM + n0 + c * 8) * 2, *(const u32x4*)(T + r * TSTR + c * 16));
    }
    {
      const int hh = w & 1, rq = w >> 1;
      bf16x8 wf[2];
#pragma unroll
      for (int fb = 0; fb < 2; ++fb) {
        const char* p = smem + OFF_WPHI + hh * 2048 + (8 * g4 + q4) * 64 + fb * 32 + pp * 8;
        wf[fb] = cat8(trread(p), trread(p + 256));
      }
#pragma unroll
      for (int mb = 0; mb < 2; ++mb) {
        const int row = rq * 32 + mb * 16 + (lane & 15);
        const bf16x8 xf = *(const bf16x8*)(T + row * TSTR + (64 + hh * 32 + g4 * 8) * 2);
        u32x4 pos, neg;
        feature_map(wf, xf, pos, neg);
        char* dst = PK + (hh * 128 + row) * PKSTR + g4 * 8;
        *(u32x2*)dst = (u32x2){pos[0], pos[1]}; *(u32x2*)(dst + 32) = (u32x2){pos[2], pos[3]};
        *(u32x2*)(dst + 64) = (u32x2){neg[0], neg[1]}; *(u32x2*)(dst + 96) = (u32x2){neg[2], neg[3]};
      }
    }
    __syncthreads();
    {
      const int hh = w >> 2, fa = w & 3;
      f32x4 kv[3];
#pragma unroll
      for (int d = 0; d < 3; ++d) kv[d] = (f32x4){0.f, 0.f, 0.f, 0.f};
      const short one = (short)0x3F80;
      const bf16x8 ones = {one, one, one, one, one, one, one, one};
#pragma unroll
      for (int ks = 0; ks < 4; ++ks) {
        const int row = ks * 32 + 8 * g4 + q4;
        bf16x8 vf[2];
#pragma unroll
        for (int d = 0; d < 2; ++d) {
          const char* p = T + row * TSTR + (128 + hh * 32 + d * 16) * 2 + pp * 8;
          vf[d] = cat8(trread(p), trread(p + 4 * TSTR));
        }
        const char* p = PK + (hh * 128 + row) * PKSTR + (fa * 16) * 2 + pp * 8;
        const bf16x8 pf = cat8(trread(p), trread(p + 4 * PKSTR));
        kv[0] = mfma16(pf, vf[0], kv[0]);
        kv[1] = mfma16(pf, vf[1], kv[1]);
        kv[2] = mfma16(pf, ones, kv[2]);
      }
      constexpr unsigned KVB = 16 * NH * 33 * 64 * 4;
      const int so = (rp * NH + ct * 2 + hh) * (33 * 64) + fa * 16 + g4 * 4;
      st16_wt(g.kvpart, KVB, (so + (lane & 15) * 64) * 4, __builtin_bit_cast(u32x4, kv[0]));
      st16_wt(g.kvpart, KVB, (so + (16 + (lane & 15)) * 64) * 4, __builtin_bit_cast(u32x4, kv[1]));
      if ((lane & 15) == 0) st16_wt(g.kvpart, KVB, (so + 32 * 64) * 4, __builtin_bit_cast(u32x4, kv[2]));
    }
    if (prod && g.res1) {
#pragma unroll
      for (int i = 0; i < 11; ++i) asm volatile("" ::"v"(pv[i]));
    }
  }
}

__global__ __launch_bounds__(1024) void ln0_k(const u16* __restrict__ x, float* __restrict__ stats, const u16* __restrict__ wqkv,
                                              const u16* __restrict__ w1, const u16* __restrict__ w3, float* __restrict__ colsum,
                                              const u16* __restrict__ wphi, u16* __restrict__ wphit) {
  const int tid = threadIdx.x, lane = tid & 63;
  if (blockIdx.x >= 248) {
    const int t = (blockIdx.x - 248) * 1024 + tid;
    const int hd = t >> 7, fb = (t >> 6) & 1;
    const u16* src = wphi + hd * 1024 + (lane >> 4) * 256 + fb * 16 + (lane & 15);
    u32x4 v;
#pragma unroll
    for (int e = 0; e < 4; ++e) v[e] = (unsigned)src[e * 64] | ((unsigned)src[e * 64 + 32] << 16);
    *(u32x4*)(wphit + t * 8) = v;
    return;
  }
  if (blockIdx.x >= 128) {
    __shared__ float red[16 * 64];
    const int cg = blockIdx.x - 128;
    const u16* W; int N, c0;
    if (cg < 48) { W = wqkv; N = 3 * DM; c0 = cg * 64; }
    else if (cg < 64) { W = w1; N = DM; c0 = (cg - 48) * 64; }
    else { W = w3; N = FF; c0 = (cg - 64) * 64; }
    const int c = tid & 7, rg = tid >> 3;
    u32x4 v[8];
#pragma unroll
    for (int i = 0; i < 8; ++i) v[i] = *(const u32x4*)(W + (size_t)(rg + i * 128) * N + c0 + c * 8);
    f32x2 s[4] = {{0.f, 0.f}, {0.f, 0.f}, {0.f, 0.f}, {0.f, 0.f}};
#pragma unroll
    for (int i = 0; i < 8; ++i)
#pragma unroll
      for (int e = 0; e < 4; ++e) s[e] += unpk(v[i][e]);
#pragma unroll
    for (int e = 0; e < 4; ++e)
#pragma unroll
      for (int hf = 0; hf < 2; ++hf) { const float t = s[e][hf]; s[e][hf] = sum4g(t + dppf<0x128>(t)); }
    if (lane < 8) {
#pragma unroll
      for (int e = 0; e < 4; ++e) *(f32x2*)(red + (tid >> 6) * 64 + c * 8 + 2 * e) = s[e];
    }
    __syncthreads();
    if (tid < 64) {
      float t = 0.f;
#pragma unroll
      for (int i = 0; i < 16; ++i) t += red[i * 64 + tid];
      colsum[cg * 64 + tid] = t;
    }
    return;
  }
  const int row = blockIdx.x * 16 + (tid >> 6);
  const u16* src = x + (size_t)row * DM;
  const u32x4 a = *(const u32x4*)(src + lane * 8), b = *(const u32x4*)(src + 512 + lane * 8);
#pragma unroll
  for (int hf = 0; hf < 2; ++hf) {
    const u32x4 u = hf ? b : a;
    f32x2 y[4];
#pragma unroll
    for (int e = 0; e < 4; ++e) y[e] = unpk(u[e]);
    const f32x2 s2 = (y[0] + y[1]) + (y[2] + y[3]);
    const float s = sum8(s2[0] + s2[1]);
    const float mc = s * (1.0f / 64.0f);
    f32x2 q2 = {0.f, 0.f};
#pragma unroll
    for (int e = 0; e < 4; ++e) { const f32x2 dl = y[e] - mc; q2 += dl * dl; }
    const float m2 = sum8(q2[0] + q2[1]);
    if ((lane & 7) == 0) *(f32x2*)(stats + ((size_t)(hf * 8 + (lane >> 3)) * SEQ + row) * 2) = (f32x2){s, m2};
  }
}

__global__ __launch_bounds__(1024) void attn_o_k(const u16* __restrict__ q, const u16* __restrict__ wphi, const float* __restrict__ kvpart,
                                                u16* __restrict__ o) {
  constexpr int KS = 80;
  __shared__ __attribute__((aligned(16))) char sm[33 * KS * 2 + 8];
  const int tid = threadIdx.x & 511, lane = tid & 63, w = tid >> 6;
  const bool ldw = __builtin_amdgcn_readfirstlane(threadIdx.x >> 9) != 0;
  const int h = blockIdx.x & 31, rb = blockIdx.x >> 5;
  const int g4 = lane >> 4, r0 = rb * 256 + w * 32 + (lane & 15);
  u16* kvt = (u16*)sm;
  if (ldw) {
    f32x4 kp[16], kz[4];
    const float* slab = kvpart + (size_t)h * (33 * 64) + tid * 4;
    const bool zw = __builtin_amdgcn_readfirstlane(w) == 0;
    if (zw) {
#pragma unroll
      for (int p = 0; p < 4; ++p) kz[p] = *(const f32x4*)(kvpart + ((size_t)(p * 4 + g4) * NH + h) * (33 * 64) + 2048 + (lane & 15) * 4);
    }
#pragma unroll
    for (int p = 0; p < 16; ++p) kp[p] = *(const f32x4*)(slab + (size_t)p * NH * (33 * 64));
    __builtin_amdgcn_sched_barrier(0);
    f32x4 t = kp[0];
#pragma unroll
    for (int p = 1; p < 16; ++p) t += kp[p];
    *(u32x2*)(kvt + (tid >> 4) * KS + (tid & 15) * 4) = (u32x2){pk2(t[0], t[1]), pk2(t[2], t[3])};
    if (zw) {
      f32x4 z = (kz[0] + kz[1]) + (kz[2] + kz[3]);
#pragma unroll
      for (int e = 0; e < 4; ++e) z[e] = sum4g(z[e]);
      if (tid < 16) *(u32x2*)(kvt + 32 * KS + tid * 4) = (u32x2){pk2(z[0], z[1]), pk2(z[2], z[3])};
    }
    __syncthreads();
    return;
  }
  bf16x8 qf[2], wf[2];
#pragma unroll
  for (int mb = 0; mb < 2; ++mb) qf[mb] = *(const bf16x8*)(q + (size_t)(r0 + mb * 16) * DM + h * 32 + g4 * 8);
#pragma unroll
  for (int fb = 0; fb < 2; ++fb) wf[fb] = *(const bf16x8*)(wphi + ((h * 2 + fb) * 64 + lane) * 8);
  bf16x8 pq[2][2];
#pragma unroll
  for (int mb = 0; mb < 2; ++mb) {
    u32x4 pos, neg;
    feature_map(wf, qf[mb], pos, neg);
    pq[mb][0] = __builtin_bit_cast(bf16x8, pos);
    pq[mb][1] = __builtin_bit_cast(bf16x8, neg);
  }
  __builtin_amdgcn_sched_barrier(0);
  __syncthreads();
  bf16x8 kf[3][2];
#pragma unroll
  for (int db = 0; db < 3; ++db)
#pragma unroll
    for (int ks = 0; ks < 2; ++ks) {
      const int d = (db < 2) ? ((lane >> 2) & 3) * 8 + db * 4 + (lane & 3) : 32;
      const u16* p = kvt + d * KS + ks * 32 + g4 * 4;
      kf[db][ks] = cat8(*(const s16x4*)p, *(const s16x4*)(p + 16));
    }
#pragma unroll
  for (int mb = 0; mb < 2; ++mb) {
    f32x4 a[3];
#pragma unroll
    for (int db = 0; db < 3; ++db) {
      a[db] = mfma16(kf[db][0], pq[mb][0], (f32x4){0.f, 0.f, 0.f, 0.f});
      a[db] = mfma16(kf[db][1], pq[mb][1], a[db]);
    }
    const float id = __builtin_amdgcn_rcpf(rbf(rbf(a[2][0]) + 1e-6f));
    u32x4 ov;
#pragma unroll
    for (int db = 0; db < 2; ++db) {
      const f32x2 lo = rbf2((f32x2){a[db][0], a[db][1]}) * id, hi = rbf2((f32x2){a[db][2], a[db][3]}) * id;
      ov[db * 2] = pk2(lo[0], lo[1]); ov[db * 2 + 1] = pk2(hi[0], hi[1]);
    }
    st16_wt(o, SEQ * DM * 2, ((r0 + mb * 16) * DM + h * 32 + g4 * 8) * 2, ov);
  }
}

template <int BN, int S, int KD, int EPI> static constexpr int smem_bytes() {
  constexpr bool USE_STATS = (EPI == EPI_QKV || EPI == EPI_GELU_SHIFT || EPI == EPI_W2 || EPI == EPI_GELU);
  constexpr int NRES = (EPI == EPI_RES || EPI == EPI_W2) ? 1 : (EPI == EPI_W4 ? 2 : 0);
  return S * (16384 + BN * 128) + NRES * 16384 + (EPI == EPI_QKV ? 4096 : 0) + 2048 + 1024 + 256;
}
template <int BN, int S, int KD, int EPI> static void launch_gemm(const GemmArgs& a, hipStream_t st) {
  constexpr int sb = smem_bytes<BN, S, KD, EPI>();
  static_assert(sb <= 163840, "LDS");
  (void)hipFuncSetAttribute((const void*)gemm_k<BN, S, KD, EPI>, hipFuncAttributeMaxDynamicSharedMemorySize, sb);
  gemm_k<BN, S, KD, EPI><<<256, 512, sb, st>>>(a);
}

extern "C" void kernel_launch(void* const* d_in, const int* in_sizes, int n_in, void* d_out, int out_size, void* d_ws,
                              size_t ws_size, hipStream_t stream) {
  const u16* x0 = (const u16*)d_in[0];
  const u16* wqkv = (const u16*)d_in[1];
  const u16* wphi = (const u16*)d_in[2];
  const u16* wo = (const u16*)d_in[3];
  const u16* w1 = (const u16*)d_in[5];
  const u16* w2 = (const u16*)d_in[6];
  const u16* w3 = (const u16*)d_in[7];
  const u16* w4 = (const u16*)d_in[8];
  char* ws = (char*)d_ws;
  const size_t MB = 1 << 20;
  u16* phiq = (u16*)(ws + 4 * MB);
  float* kvpart = (float*)(ws + 12 * MB);
  u16* obuf = (u16*)(ws + 17 * MB);
  u16* xa = (u16*)(ws + 21 * MB);
  u16* hsh = (u16*)(ws + 25 * MB);
  u16* x1 = (u16*)(ws + 29 * MB);
  u16* gbuf = (u16*)(ws + 33 * MB);
  u16* xb = (u16*)(ws + 48 * MB);
  float* st_xa = (float*)(ws + 52 * MB);
  float* st_x1 = (float*)(ws + 53 * MB);
  float* st_xb = (float*)(ws + 54 * MB);
  float* colsum = (float*)(ws + 55 * MB);

  u16* wphit = (u16*)(ws + 55 * MB + 65536);
  ln0_k<<<252, 1024, 0, stream>>>(x0, st_xb, wqkv, w1, w3, colsum, wphi, wphit);
  for (int layer = 0; layer < 2; ++layer) {
    const u16* xin = layer == 0 ? x0 : xb;
    u16* xout = layer == 0 ? xb : (u16*)d_out;
    GemmArgs a;
    a = GemmArgs{xin, wqkv, phiq, layer == 0 ? wo : nullptr, w2, st_xb, (float*)w4, colsum, wphi, kvpart, 1, 0};
    launch_gemm<192, 3, 1024, EPI_QKV>(a, stream);
    attn_o_k<<<256, 1024, 0, stream>>>(phiq, wphit, kvpart, obuf);
    a = GemmArgs{obuf, wo, xa, xin, nullptr, nullptr, st_xa, nullptr, nullptr, nullptr, 0, 0};
    launch_gemm<64, 5, 1024, EPI_RES>(a, stream);
    a = GemmArgs{xa, w1, hsh, nullptr, nullptr, st_xa, nullptr, colsum + 3072, nullptr, nullptr, 2, 0};
    launch_gemm<64, 5, 1024, EPI_GELU_SHIFT>(a, stream);
    a = GemmArgs{hsh, w2, x1, xa, nullptr, st_xa, st_x1, nullptr, nullptr, nullptr, 1, 0};
    launch_gemm<64, 5, 1024, EPI_W2>(a, stream);
    a = GemmArgs{x1, w3, gbuf, nullptr, nullptr, st_x1, nullptr, colsum + 4096, nullptr, nullptr, 1, 0};
    launch_gemm<224, 3, 1024, EPI_GELU>(a, stream);
    a = GemmArgs{gbuf, w4, xout, xa, x1, nullptr, st_xb, nullptr, nullptr, nullptr, 0, 0};
    launch_gemm<64, 5, 3584, EPI_W4>(a, stream);
  }
}
```
